```python
import torch

def _softmax(x, axis=-1):
    return torch.softmax(x, dim=axis)

def _gelu(x):
    return torch.nn.functional.gelu(x, approximate='tanh')

def _silu(x):
    return torch.nn.functional.silu(x)

def _sigmoid(x):
    return torch.sigmoid(x)

def _layernorm(x, axis=-1, eps=1e-05):
    m = x.mean(dim=axis, keepdim=True)
    v = x.var(dim=axis, keepdim=True, unbiased=False)
    return (x - m) / torch.sqrt(v + eps)

def _scatter_add(tgt, idx, src, axis=0):
    return tgt.clone().index_add_(axis, idx, src)

def _segment_sum(src, seg, n):
    out = torch.zeros((n,) + src.shape[1:], dtype=src.dtype)
    return out.index_add_(0, seg, src)

def _pad(x, pw):
    flat = tuple((v for pair in reversed(pw) for v in pair))
    return torch.nn.functional.pad(x, flat)
kernel_name = 'adaLN_cond__compressed_kv_cla__dense_activation_silu__prompt'

def setup_inputs():

    _w = lambda a: (lambda t: t.to(torch.bfloat16) if t.is_floating_point() else t)(torch.as_tensor(a))

    def _A_setup():
        B, H, Sq, Sk, D, M = (1, 32, 2048, 2048, 128, 640)
        Dc = 4 * D
        return {'x': _w((lambda *s: torch.randn(*s, dtype=torch.bfloat16) * 0.05)(B, Sq, M)), 'c_kv': _w((lambda *s: torch.randn(*s, dtype=torch.bfloat16) * 0.8)(B, Sk, Dc)), 'w_q': _w((lambda *s: torch.randn(*s, dtype=torch.bfloat16) * 1.6)(M, H, D) * M ** (-0.5)), 'w_uk': _w((lambda *s: torch.randn(*s, dtype=torch.bfloat16) * 1.6)(Dc, H, D) * Dc ** (-0.5)), 'w_uv': _w((lambda *s: torch.randn(*s, dtype=torch.bfloat16) * 0.8)(Dc, H, D) * Dc ** (-0.5)), 'w_o': _w((lambda *s: torch.randn(*s, dtype=torch.bfloat16) * 0.8)(H, D, M) * (H * D) ** (-0.5))}
    _A_in = _A_setup()

    def _B_setup():
        B, S, D, F = (1, 2048, 640, 1728)
        return {'x': _w((lambda *s: torch.randn(*s, dtype=torch.bfloat16) * 0.05)(B, S, D)), 'w1': _w((lambda *s: torch.randn(*s, dtype=torch.bfloat16) * 1.6)(D, F) * D ** (-0.5)), 'w2': _w((lambda *s: torch.randn(*s, dtype=torch.bfloat16) * 0.2)(F, D) * F ** (-0.5))}
    _B_in = _B_setup()
    return {'A_x': _A_in['x'], 'A_c_kv': _A_in['c_kv'], 'A_w_q': _A_in['w_q'], 'A_w_uk': _A_in['w_uk'], 'A_w_uv': _A_in['w_uv'], 'A_w_o': _A_in['w_o'], 'B_x': _B_in['x'], 'B_w1': _B_in['w1'], 'B_w2': _B_in['w2'], 'c': _w((lambda *s: torch.randn(*s, dtype=torch.bfloat16) * 0.1)(1, 640) * 8.0), 'w_mod': _w((lambda *s: torch.randn(*s, dtype=torch.bfloat16) * 0.1)(640, 6 * 640) * 640 ** (-0.5) * 8.0)}

def reference(A_x, A_c_kv, A_w_q, A_w_uk, A_w_uv, A_w_o, B_x, B_w1, B_w2, c, w_mod):

    def _A_ref(x, c_kv, w_q, w_uk, w_uv, w_o):
        B, Sq, M = (x.shape[0], x.shape[1], x.shape[2])
        Sk = c_kv.shape[1]
        H, D = (w_q.shape[1], w_q.shape[2])
        q = torch.einsum('bqm,mhd->bhqd', x, w_q)
        k = torch.einsum('bkc,chd->bhkd', c_kv, w_uk)
        v = torch.einsum('bkc,chd->bhkd', c_kv, w_uv)
        scores = torch.einsum('bhqd,bhkd->bhqk', q, k) * D ** (-0.5)
        off = Sk - Sq
        qi = torch.arange(Sq)[:, None]
        kj = torch.arange(Sk)[None, :]
        cmask = kj <= qi + off
        scores = torch.where(cmask[None, None], scores, -1000000000.0)
        p = _softmax(scores, axis=-1)
        ctx = torch.einsum('bhqk,bhkd->bhqd', p, v)
        return torch.einsum('bhqd,hdm->bqm', ctx, w_o)

    def _B_ref(x, w1, w2):
        h = torch.einsum('bsd,df->bsf', x, w1)
        h = _silu(h)
        out = torch.einsum('bsf,fd->bsd', h, w2)
        return out
    x = A_x
    mod = (_silu(c) @ w_mod).reshape(c.shape[0], 6, 1, -1)
    sa, ca, ga, sb, cb, gb = (mod[:, 0].clone(), mod[:, 1].clone(), mod[:, 2].clone(), mod[:, 3].clone(), mod[:, 4].clone(), mod[:, 5].clone())
    h = _layernorm(x, axis=-1) * (1 + ca) + sa
    x = x + ga * _A_ref(h, A_c_kv, A_w_q, A_w_uk, A_w_uv, A_w_o)
    h = _layernorm(x, axis=-1) * (1 + cb) + sb
    x = x + gb * _B_ref(h, B_w1, B_w2)
    return x
```

```cpp
#include <hip/hip_runtime.h>
#include <hip/hip_bf16.h>
typedef unsigned short u16;
typedef short bf16x8 __attribute__((ext_vector_type(8)));
typedef float f32x4 __attribute__((ext_vector_type(4)));
typedef unsigned u32x4 __attribute__((ext_vector_type(4)));
__device__ __forceinline__ float bf2f(u16 x) { return __uint_as_float((unsigned)x << 16); }
__device__ __forceinline__ u16 f2bf(float f) { unsigned u = __float_as_uint(f); u += 0x7fffu + ((u >> 16) & 1u); return (u16)(u >> 16); }
constexpr int S = 2048, M = 640, H = 32, D = 128, DC = 512, F = 1728;

__global__ void k_mod(const u16* c, const u16* wm, float* mod) {
  __shared__ float red[4][64];
  int col = blockIdx.x * 64 + (threadIdx.x & 63), part = threadIdx.x >> 6;
  float s = 0;
  for (int k = part * 160; k < part * 160 + 160; ++k) { float v = bf2f(c[k]); v = bf2f(f2bf(v / (1.f + __expf(-v)))); s += v * bf2f(wm[k * 3840 + col]); }
  red[part][threadIdx.x & 63] = s; __syncthreads();
  if (part == 0) mod[col] = bf2f(f2bf(red[0][threadIdx.x] + red[1][threadIdx.x] + red[2][threadIdx.x] + red[3][threadIdx.x]));
}
__global__ void k_ln(const u16* x, const float* shift, const float* scale, u16* h) {
  int row = blockIdx.x * 4 + (threadIdx.x >> 6), lane = threadIdx.x & 63;
  float v[10], s = 0;
  for (int i = 0; i < 10; ++i) { v[i] = bf2f(x[row * M + i * 64 + lane]); s += v[i]; }
  for (int o = 32; o; o >>= 1) s += __shfl_xor(s, o);
  float m = s / M, q = 0;
  for (int i = 0; i < 10; ++i) { v[i] -= m; q += v[i] * v[i]; }
  for (int o = 32; o; o >>= 1) q += __shfl_xor(q, o);
  float r = rsqrtf(q / M + 1e-5f);
  for (int i = 0; i < 10; ++i) h[row * M + i * 64 + lane] = f2bf(v[i] * r * (1.f + scale[i * 64 + lane]) + shift[i * 64 + lane]);
}
template <int EPI>
__global__ __launch_bounds__(256) void k_gemm(const u16* A, const u16* B, u16* C, int N, int K, const u16* res, const float* gate) {
  __shared__ __attribute__((aligned(16))) u16 sa[64][40], sb[64][40];
  int bm = blockIdx.y * 64, bn = blockIdx.x * 64, t = threadIdx.x, w = t >> 6, lane = t & 63, fr = lane & 15, fq = lane >> 4;
  int wm = (w >> 1) * 32, wn = (w & 1) * 32;
  f32x4 acc[2][2] = {};
  for (int k0 = 0; k0 < K; k0 += 32) {
    { int r = t >> 2, c = (t & 3) * 8; *(u32x4*)&sa[r][c] = *(const u32x4*)&A[(size_t)(bm + r) * K + k0 + c]; }
    { int kk = t >> 3, c = (t & 7) * 8; u32x4 v = *(const u32x4*)&B[(size_t)(k0 + kk) * N + bn + c]; const u16* p = (const u16*)&v; for (int j = 0; j < 8; ++j) sb[c + j][kk] = p[j]; }
    __syncthreads();
    bf16x8 a[2], b[2];
    for (int i = 0; i < 2; ++i) { a[i] = *(const bf16x8*)&sa[wm + i * 16 + fr][fq * 8]; b[i] = *(const bf16x8*)&sb[wn + i * 16 + fr][fq * 8]; }
    for (int i = 0; i < 2; ++i) for (int j = 0; j < 2; ++j) acc[i][j] = __builtin_amdgcn_mfma_f32_16x16x32_bf16(a[i], b[j], acc[i][j], 0, 0, 0);
    __syncthreads();
  }
  for (int i = 0; i < 2; ++i) for (int j = 0; j < 2; ++j) for (int r = 0; r < 4; ++r) {
    int row = bm + wm + i * 16 + fq * 4 + r, col = bn + wn + j * 16 + fr; float v = acc[i][j][r];
    if (EPI == 1) v = v / (1.f + __expf(-v));
    if (EPI == 2) v = bf2f(res[(size_t)row * N + col]) + gate[col] * v;
    C[(size_t)row * N + col] = f2bf(v);
  }
}
__global__ __launch_bounds__(256) void k_attn(const u16* Q, const u16* Kx, const u16* V, u16* O) {
  __shared__ __attribute__((aligned(16))) u16 sv[128][40];
  int h = blockIdx.y, t = threadIdx.x, w = t >> 6, lane = t & 63, fr = lane & 15, fq = lane >> 4;
  int qb = (31 - blockIdx.x) * 64, q0 = qb + w * 16;
  bf16x8 qf[4];
  for (int s = 0; s < 4; ++s) qf[s] = *(const bf16x8*)&Q[(size_t)(q0 + fr) * 4096 + h * 128 + s * 32 + fq * 8];
  f32x4 o[8] = {}; float m = -1e30f, l = 0;
  const float c = 0.08838834764831845f * 1.4426950408889634f;
  for (int k0 = 0; k0 < qb + 64; k0 += 32) {
    __syncthreads();
    for (int i = 0; i < 2; ++i) { int idx = t + i * 256, kk = idx >> 4, cc = (idx & 15) * 8; u32x4 v = *(const u32x4*)&V[(size_t)(k0 + kk) * 4096 + h * 128 + cc]; const u16* p = (const u16*)&v;
      int kp = ((kk & 15) >> 2) * 8 + (kk >> 4) * 4 + (kk & 3); for (int j = 0; j < 8; ++j) sv[cc + j][kp] = p[j]; }
    f32x4 s[2] = {};
    for (int kb = 0; kb < 2; ++kb) for (int ds = 0; ds < 4; ++ds) {
      bf16x8 kf = *(const bf16x8*)&Kx[(size_t)(k0 + kb * 16 + fr) * 4096 + h * 128 + ds * 32 + fq * 8];
      s[kb] = __builtin_amdgcn_mfma_f32_16x16x32_bf16(kf, qf[ds], s[kb], 0, 0, 0); }
    float mx = m;
    for (int kb = 0; kb < 2; ++kb) for (int r = 0; r < 4; ++r) { int key = k0 + kb * 16 + fq * 4 + r; float v = s[kb][r] * c; if (key > q0 + fr) v = -__builtin_inff(); s[kb][r] = v; mx = fmaxf(mx, v); }
    mx = fmaxf(mx, __shfl_xor(mx, 16)); mx = fmaxf(mx, __shfl_xor(mx, 32));
    float al = exp2f(m - mx); m = mx; float ps = 0; bf16x8 p;
    for (int kb = 0; kb < 2; ++kb) for (int r = 0; r < 4; ++r) { float e = exp2f(s[kb][r] - mx); u16 b = f2bf(e); ps += bf2f(b); p[kb * 4 + r] = (short)b; }
    l = l * al + ps;
    __syncthreads();
    for (int db = 0; db < 8; ++db) { o[db] *= al; bf16x8 vf = *(const bf16x8*)&sv[db * 16 + fr][fq * 8]; o[db] = __builtin_amdgcn_mfma_f32_16x16x32_bf16(vf, p, o[db], 0, 0, 0); }
  }
  l += __shfl_xor(l, 16); l += __shfl_xor(l, 32);
  float il = 1.f / l;
  for (int db = 0; db < 8; ++db) for (int r = 0; r < 4; ++r) O[(size_t)(q0 + fr) * 4096 + h * 128 + db * 16 + fq * 4 + r] = f2bf(o[db][r] * il);
}
#define NDUMMY 0
__global__ void k_dummy(float* p) { if (threadIdx.x == 0) p[blockIdx.x] = 1.f; }
extern "C" void kernel_launch(void* const* d_in, const int* in_sizes, int n_in, void* d_out, int out_size, void* d_ws, size_t ws_size, hipStream_t stream) {
  const u16 *x = (const u16*)d_in[0], *ckv = (const u16*)d_in[1], *wq = (const u16*)d_in[2], *wuk = (const u16*)d_in[3], *wuv = (const u16*)d_in[4], *wo = (const u16*)d_in[5];
  const u16 *w1 = (const u16*)d_in[7], *w2 = (const u16*)d_in[8], *c = (const u16*)d_in[9], *wm = (const u16*)d_in[10];
  char* ws = (char*)d_ws; float* mod = (float*)ws;
  u16* h = (u16*)(ws + (1 << 20)); u16* q = (u16*)(ws + (8 << 20)); u16* k = (u16*)(ws + (32 << 20)); u16* v = (u16*)(ws + (56 << 20));
  u16* ctx = (u16*)(ws + (80 << 20)); u16* x1 = (u16*)(ws + (104 << 20)); u16* f = (u16*)(ws + (112 << 20)); u16* out = (u16*)d_out;
  for (int i = 0; i < NDUMMY; ++i) k_dummy<<<256, 256, 0, stream>>>((float*)(ws + (200 << 20)));
  k_mod<<<60, 256, 0, stream>>>(c, wm, mod);
  k_ln<<<512, 256, 0, stream>>>(x, mod, mod + 640, h);
  k_gemm<0><<<dim3(64, 32), 256, 0, stream>>>(h, wq, q, 4096, 640, nullptr, nullptr);
  k_gemm<0><<<dim3(64, 32), 256, 0, stream>>>(ckv, wuk, k, 4096, 512, nullptr, nullptr);
  k_gemm<0><<<dim3(64, 32), 256, 0, stream>>>(ckv, wuv, v, 4096, 512, nullptr, nullptr);
  k_attn<<<dim3(32, 32), 256, 0, stream>>>(q, k, v, ctx);
  k_gemm<2><<<dim3(10, 32), 256, 0, stream>>>(ctx, wo, x1, 640, 4096, x, mod + 1280);
  k_ln<<<512, 256, 0, stream>>>(x1, mod + 1920, mod + 2560, h);
  k_gemm<1><<<dim3(27, 32), 256, 0, stream>>>(h, w1, f, 1728, 640, nullptr, nullptr);
  k_gemm<2><<<dim3(10, 32), 256, 0, stream>>>(f, w2, out, 640, 1728, x1, mod + 3200);
}
```

```cpp
#include <hip/hip_runtime.h>
#define CLOBBERS "v1","v2","v3","v4","v5","v6","v7","v8","v9","v10","v11","v12","v13","v14","v15","v16","v17","v18","v19","v20","v21","v22","v23","v24","v25","v26","v27","v28","v29","v30","v31","v32","v33","v34","v35","v36","v37","v38","v39","v40","v41","v42","v43","v44","v45","v46","v47","v48","v49","v50","v51","v52","v53","v54","v55","v56","v57","v58","v59","v60","v61","v62","v63","v64","v65","v66","v67","v68","v69","v70","v71","v72","v73","v74","v75","v76","v77","v78","v79","v80","v81","v82","v83","v84","v85","v86","v87","v88","v89","v90","v91","v92","v93","v94","v95","v96","v97","v98","v99","v100","v101","v102","v103","v104","v105","v106","v107","v108","v109","v110","v111","v112","v113","v114","v115","v116","v117","v118","v119","v120","v121","v122","v123","v124","v125","v126","v127","v128","v129","v130","v131","v132","v133","v134","v135","v136","v137","v138","v139","v140","v141","v142","v143","v144","v145","v146","v147","v148","v149","v150","v151","v152","v153","v154","v155","v156","v157","v158","v159","v160","v161","v162","v163","v164","v165","v166","v167","v168","v169","v170","v171","v172","v173","v174","v175","v176","v177","v178","v179","v180","v181","v182","v183","v184","v185","v186","v187","v188","v189","v190","v191","v192","v193","v194","v195","v196","v197","v198","v199","v200","v201","v202","v203","v204","v205","v206","v207","v208","v209","v210","v211","v212","v213","v214","v215","v216","v217","v218","v219","v220","v221","v222","v223","v224","v225","v226","v227","v228","v229","v230","v231","v232","v233","v234","v235","v236","v237","v238","v239","v240","v241","v242","v243","v244","v245","v246","v247","v248","v249","v250","v251","v252","v253","v254","v255","a0","a1","a2","a3","a4","a5","a6","a7","a8","a9","a10","a11","a12","a13","a14","a15","a16","a17","a18","a19","a20","a21","a22","a23","a24","a25","a26","a27","a28","a29","a30","a31","a32","a33","a34","a35","a36","a37","a38","a39","a40","a41","a42","a43","a44","a45","a46","a47","a48","a49","a50","a51","a52","a53","a54","a55","a56","a57","a58","a59","a60","a61","a62","a63","a64","a65","a66","a67","a68","a69","a70","a71","a72","a73","a74","a75","a76","a77","a78","a79","a80","a81","a82","a83","a84","a85","a86","a87","a88","a89","a90","a91","a92","a93","a94","a95","a96","a97","a98","a99","a100","a101","a102","a103","a104","a105","a106","a107","a108","a109","a110","a111","a112","a113","a114","a115","a116","a117","a118","a119","a120","a121","a122","a123","a124","a125","a126","a127","a128","a129","a130","a131","a132","a133","a134","a135","a136","a137","a138","a139","a140","a141","a142","a143","a144","a145","a146","a147","a148","a149","a150","a151","a152","a153","a154","a155","a156","a157","a158","a159","a160","a161","a162","a163","a164","a165","a166","a167","a168","a169","a170","a171","a172","a173","a174","a175","a176","a177","a178","a179","a180","a181","a182","a183","a184","a185","a186","a187","a188","a189","a190","a191","a192","a193","a194","a195","a196","a197","a198","a199","a200","a201","a202","a203","a204","a205","a206","a207","a208","a209","a210","a211","a212","a213","a214","a215","a216","a217","a218","a219","a220","a221","a222","a223","a224","a225","a226","a227","a228","a229","a230","a231","a232","a233","a234","a235","a236","a237","a238","a239","a240","a241","a242","a243","a244","a245","a246","a247","a248","a249","a250","a251","a252","a253","a254","a255","s4","s5","s6","s7","s8","s9","s10","s11","s12","s13","s14","s15","s16","s17","s18","s19","s20","s21","s22","s23","s24","s25","s26","s27","s28","s29","s30","s31","s33","s34","s35","s36","s37","s38","s39","s40","s41","s42","s43","s44","s45","s46","s47","s48","s49","s50","s51","s52","s53","s54","s55","s56","s57","s58","s59","s60","s61","s62","s63","s64","s65","s66","s67","s68","s69","s70","s71","s72","s73","s74","s75","s76","s77","s78","s79","s80","s81","s82","s83","s84","s85","s86","s87","s88","s89","s90","s91","s92","s93","s94","s95","s96","s97","s98","s99","s100","s101","vcc","scc","memory"
__global__ __launch_bounds__(256, 1) void adaln_block(const void* x, const void* ckv, const void* wq, const void* wuk, const void* wuv, const void* wo, const void* w1, const void* w2, const void* c, const void* wmod, const void* out, const void* ws) {
  __shared__ __attribute__((aligned(1024))) char lds[163840];
  asm volatile(
"s_getpc_b64 s[76:77]\n"
".Ladaln_block_C_PC:\n"
"s_load_dwordx16 s[24:39], s[0:1], 0\n"
"s_load_dwordx8 s[40:47], s[0:1], 64\n"
"v_lshrrev_b32 v1, 6, v0\n"
"v_and_b32 v0, 63, v0\n"
"v_readfirstlane_b32 s3, v1\n"
"v_lshlrev_b32 v1, 4, v0\n"
"s_and_b32 s50, s2, 7\n"
"s_lshr_b32 s51, s2, 6\n"
"s_lshl2_add_u32 s48, s50, s51\n"
"s_bfe_u32 s49, s2, 0x30003\n"
"s_mov_b32 s20, s76\n"
"s_and_b32 s21, s77, 0xffff\n"
"s_mov_b32 s22, .Ladaln_block_C_END-.Ladaln_block_C_PC\n"
"s_mov_b32 s23, 0x20000\n"
"s_lshr_b32 s83, s2, 3\n"
"s_lshl2_add_u32 s83, s83, s3\n"
"s_lshl_b32 s83, s83, 10\n"
"v_lshrrev_b32 v99, 3, v0\n"
"v_min_u32 v99, 1, v99\n"
"v_lshlrev_b32 v99, 30, v99\n"
"v_lshl_add_u32 v98, v0, 7, v99\n"
"buffer_load_dword v99, v98, s[20:23], s83 offen\n"
"v_lshrrev_b32 v24, 3, v0\n"
"v_lshrrev_b32 v25, 4, v0\n"
"v_and_b32 v26, 7, v0\n"
"v_xor_b32 v26, v26, v25\n"
"v_lshlrev_b32 v26, 4, v26\n"
"v_lshl_add_u32 v2, v24, 10, v26\n"
"v_xor_b32 v27, 64, v2\n"
"v_add_u32 v3, 0x1c00, v27\n"
"v_add_u32 v4, 0x3800, v2\n"
"v_add_u32 v5, 0x5400, v27\n"
"s_lshl_b32 s54, s3, 13\n"
"s_lshl_b32 s78, s3, 12\n"
"s_lshl2_add_u32 s50, s49, s3\n"
"s_lshl_b32 s50, s50, 16\n"
"v_add_u32 v2, s50, v2\n"
"v_add_u32 v3, s50, v3\n"
"v_add_u32 v4, s50, v4\n"
"v_add_u32 v5, s50, v5\n"
"s_add_u32 s55, s48, s49\n"
"s_and_b32 s55, s55, 1\n"
"s_lshl_b32 s50, s55, 7\n"
"s_add_u32 s51, s50, 0x8000\n"
"s_lshl_b32 s52, s55, 19\n"
"s_mov_b32 s53, s52\n"
"s_waitcnt lgkmcnt(0)\n"
"s_mov_b32 s8, s26\n"
"s_and_b32 s9, s27, 0xffff\n"
"s_mov_b32 s10, 0x200000\n"
"s_mov_b32 s11, 0x20000\n"
"s_add_u32 m0, s54, 0\n"
"s_nop 0\n"
"buffer_load_dwordx4 v2, s[8:11], s50 offen lds\n"
"buffer_load_dwordx4 v3, s[8:11], s50 offen offset:1024 lds\n"
"buffer_load_dwordx4 v4, s[8:11], s50 offen offset:2048 lds\n"
"buffer_load_dwordx4 v5, s[8:11], s50 offen offset:3072 lds\n"
"v_and_b32 v26, 15, v0\n"
"v_lshlrev_b32 v27, 1, v25\n"
"v_xor_b32 v26, v26, v27\n"
"v_lshlrev_b32 v26, 4, v26\n"
"v_lshl_add_u32 v6, v25, 13, v26\n"
"v_add_u32 v7, 0x7c00, v6\n"
"v_xor_b32 v27, 0x80, v6\n"
"v_add_u32 v8, 0xf800, v27\n"
"v_add_u32 v9, 0x17400, v27\n"
"s_lshl_b32 s60, s3, 17\n"
"s_lshl_b32 s59, s48, 8\n"
"s_add_u32 s60, s60, s59\n"
"v_add_u32 v6, s60, v6\n"
"v_add_u32 v7, s60, v7\n"
"v_add_u32 v8, s60, v8\n"
"v_add_u32 v9, s60, v9\n"
"s_mov_b32 s12, s30\n"
"s_and_b32 s13, s31, 0xffff\n"
"s_mov_b32 s14, 0x400000\n"
"s_mov_b32 s15, 0x20000\n"
"s_lshl_b32 s31, s55, 8\n"
"s_sub_u32 s26, 0x80, s31\n"
"s_add_u32 s27, 0x80, s31\n"
"s_lshl_b32 s30, s26, 12\n"
"s_lshl_b32 s31, s27, 12\n"
"s_add_u32 s50, s50, s26\n"
"s_add_u32 m0, s78, 0x10000\n"
"s_nop 0\n"
"buffer_load_dwordx4 v6, s[12:15], s52 offen lds\n"
"buffer_load_dwordx4 v7, s[12:15], s52 offen offset:1024 lds\n"
"buffer_load_dwordx4 v8, s[12:15], s52 offen offset:2048 lds\n"
"buffer_load_dwordx4 v9, s[12:15], s52 offen offset:3072 lds\n"
"s_add_u32 s52, s52, s30\n"
"s_add_u32 s4, s46, 0x3fffff\n"
"s_addc_u32 s5, s47, 0\n"
"s_andn2_b32 s4, s4, 0x3fffff\n"
"s_and_b32 s5, s5, 0xffff\n"
"s_mov_b32 s6, 0x7800000\n"
"s_mov_b32 s7, 0x20000\n"
"s_mov_b32 s100, 0\n"
"s_cmp_lg_u32 s49, 0\n"
"s_cbranch_scc1 .Ladaln_block_N_SKIP\n"
"s_lshl2_add_u32 s61, s48, s3\n"
"s_mul_i32 s61, s61, 0xc000\n"
"s_add_u32 s61, s61, 0x7000000\n"
"s_and_b32 s62, s48, 1\n"
"s_lshl_b32 s62, s62, 13\n"
"s_lshr_b32 s63, s61, 8\n"
"s_and_b32 s63, s63, 0x2000\n"
"s_xor_b32 s61, s61, s63\n"
"s_xor_b32 s61, s61, s62\n"
"s_memtime s[64:65]\n"
"s_waitcnt lgkmcnt(0)\n"
"s_load_dword s63, s[4:5], s61\n"
"s_waitcnt lgkmcnt(0)\n"
"s_memtime s[66:67]\n"
"s_waitcnt lgkmcnt(0)\n"
"s_sub_u32 s61, s66, s64\n"
"s_getreg_b32 s62, hwreg(HW_REG_XCC_ID, 2, 1)\n"
"s_lshl1_add_u32 s100, s61, s62\n"
".Ladaln_block_N_SKIP:\n"
"s_add_u32 m0, s54, 0x1000\n"
"s_nop 0\n"
"buffer_load_dwordx4 v2, s[8:11], s51 offen lds\n"
"buffer_load_dwordx4 v3, s[8:11], s51 offen offset:1024 lds\n"
"buffer_load_dwordx4 v4, s[8:11], s51 offen offset:2048 lds\n"
"buffer_load_dwordx4 v5, s[8:11], s51 offen offset:3072 lds\n"
"s_add_u32 s51, s51, s26\n"
"s_mov_b32 s16, s32\n"
"s_and_b32 s17, s33, 0xffff\n"
"s_mov_b32 s18, 0x400000\n"
"s_mov_b32 s19, 0x20000\n"
"s_load_dword s82, s[4:5], 0\n"
"s_mov_b32 s20, s42\n"
"s_and_b32 s21, s43, 0xffff\n"
"s_mov_b32 s22, 0x4b0000\n"
"s_mov_b32 s23, 0x20000\n"
"s_mov_b32 s64, s40\n"
"s_and_b32 s65, s41, 0xffff\n"
"s_mov_b32 s66, 0x500\n"
"s_mov_b32 s67, 0x20000\n"
"s_and_b32 s61, s2, 7\n"
"s_lshr_b32 s62, s2, 5\n"
"s_lshl3_add_u32 s63, s62, s61\n"
"s_bfe_u32 s62, s2, 0x20003\n"
"s_lshl2_add_u32 s68, s63, s62\n"
"s_lshl2_add_u32 s61, s62, s3\n"
"s_mul_i32 s61, s61, 0x4b000\n"
"s_lshl_b32 s63, s63, 7\n"
"s_add_u32 s61, s61, s63\n"
"s_cmp_lt_u32 s68, 0xf0\n"
"s_cselect_b32 s61, s61, 0x40000000\n"
"s_lshl_b32 s69, s3, 13\n"
"s_add_u32 s69, s69, 0x20000\n"
"s_mul_i32 s74, s3, 0x1400\n"
"s_add_u32 s74, s74, 0x20000\n"
"v_lshrrev_b32 v60, 3, v0\n"
"v_and_b32 v61, 7, v0\n"
"v_mul_u32_u24 v100, 0x1e00, v60\n"
"v_lshl_add_u32 v100, v61, 4, v100\n"
"v_lshlrev_b32 v101, 3, v0\n"
"s_mul_i32 s62, s62, 0x140\n"
"buffer_load_dwordx2 v[102:103], v101, s[64:67], s62 offen\n"
"s_mov_b32 s64, s24\n"
"s_and_b32 s65, s25, 0xffff\n"
"s_mov_b32 s66, 0x280000\n"
"s_mov_b32 s67, 0x20000\n"
"s_lshr_b32 s55, s3, 1\n"
"s_lshl_b32 s55, s55, 13\n"
"v_and_b32 v24, 15, v0\n"
"v_lshrrev_b32 v26, 1, v24\n"
"v_xor_b32 v26, v26, v25\n"
"v_lshlrev_b32 v26, 4, v26\n"
"v_lshl_add_u32 v26, v24, 7, v26\n"
"v_add_u32 v10, s55, v26\n"
"v_xor_b32 v11, 64, v10\n"
"v_bfe_u32 v24, v0, 2, 2\n"
"v_and_b32 v26, 1, v25\n"
"v_lshl_or_b32 v26, v26, 2, v24\n"
"v_lshl_add_u32 v24, v25, 3, v24\n"
"v_lshlrev_b32 v24, 8, v24\n"
"v_lshl_or_b32 v24, v26, 5, v24\n"
"v_and_b32 v26, 3, v0\n"
"v_lshl_or_b32 v27, v26, 3, v24\n"
"s_and_b32 s55, s3, 1\n"
"s_lshl_b32 s55, s55, 7\n"
"s_or_b32 s55, s55, 0x10000\n"
"v_xor_b32 v12, s55, v27\n"
"v_xor_b32 v13, 32, v12\n"
"v_xor_b32 v14, 64, v12\n"
"v_xor_b32 v15, 0x60, v12\n"
"v_xor_b32 v16, 0x20000, v27\n"
"v_xor_b32 v17, 0x20020, v27\n"
"v_xor_b32 v18, 0x20040, v27\n"
"v_xor_b32 v19, 0x20060, v27\n"
"s_sub_u32 s59, 15, s49\n"
"s_cmp_lt_u32 s3, 2\n"
"s_cselect_b32 s59, s59, s49\n"
"s_and_b32 s55, s3, 1\n"
"s_lshl1_add_u32 s79, s59, s55\n"
"s_and_b32 s55, s48, 1\n"
"s_add_u32 s55, s55, s49\n"
"s_lshl_b32 s58, s55, 19\n"
"s_lshl_b32 s59, s55, 14\n"
"s_add_u32 m0, s78, 0x18000\n"
"s_nop 0\n"
"buffer_load_dwordx4 v6, s[16:19], s53 offen lds\n"
"buffer_load_dwordx4 v7, s[16:19], s53 offen offset:1024 lds\n"
"buffer_load_dwordx4 v8, s[16:19], s53 offen offset:2048 lds\n"
"buffer_load_dwordx4 v9, s[16:19], s53 offen offset:3072 lds\n"
"s_add_u32 s53, s53, s30\n"
"s_waitcnt lgkmcnt(0)\n"
"s_add_u32 s82, s82, 1\n"
"v_mov_b32 v22, s82\n"
"v_add_u32 v23, s78, v0\n"
"v_lshrrev_b32 v23, 5, v23\n"
"v_min_u32 v23, 1, v23\n"
"v_lshlrev_b32 v23, 30, v23\n"
"v_lshl_add_u32 v23, v0, 2, v23\n"
"s_lshr_b32 s62, s2, 7\n"
"s_lshl1_add_u32 s62, s3, s62\n"
"s_and_b32 s63, s2, 7\n"
"s_lshl3_add_u32 s62, s62, s63\n"
"s_and_b32 s63, s48, 1\n"
"s_lshl1_add_u32 s62, s62, s63\n"
"s_lshl3_add_u32 s62, s62, 0x30000\n"
"s_cmp_lg_u32 s49, 0\n"
"s_cmov_b32 s62, 0x40000000\n"
"v_mov_b32 v60, s100\n"
"v_mov_b32 v61, s82\n"
"v_min_u32 v62, 1, v0\n"
"v_lshlrev_b32 v62, 30, v62\n"
"s_nop 0\n"
"buffer_store_dwordx2 v[60:61], v62, s[4:7], s62 offen sc1\n"
"s_waitcnt vmcnt(6)\n"
"s_barrier\n"
"ds_read_b64_tr_b16 v[40:41], v12\n"
"ds_read_b64_tr_b16 v[42:43], v12 offset:1024\n"
"ds_read_b64_tr_b16 v[44:45], v12 offset:8192\n"
"ds_read_b64_tr_b16 v[46:47], v12 offset:9216\n"
"ds_read_b128 a[64:67], v10\n"
"ds_read_b128 a[68:71], v11\n"
"ds_read_b128 a[72:75], v10 offset:2048\n"
"ds_read_b128 a[76:79], v11 offset:2048\n"
"ds_read_b128 a[80:83], v10 offset:4096\n"
"ds_read_b128 a[84:87], v11 offset:4096\n"
"ds_read_b128 a[88:91], v10 offset:6144\n"
"ds_read_b128 a[92:95], v11 offset:6144\n"
"ds_read_b128 a[96:99], v10 offset:16384\n"
"ds_read_b128 a[100:103], v11 offset:16384\n"
"ds_read_b128 a[104:107], v10 offset:18432\n"
"ds_read_b128 a[108:111], v11 offset:18432\n"
"ds_read_b128 a[112:115], v10 offset:20480\n"
"ds_read_b128 a[116:119], v11 offset:20480\n"
"ds_read_b128 a[120:123], v10 offset:22528\n"
"ds_read_b128 a[124:127], v11 offset:22528\n"
"s_waitcnt lgkmcnt(12)\n"
"v_mfma_f32_16x16x32_bf16 v[128:131], v[40:43], a[64:67], 0\n"
"v_mfma_f32_16x16x32_bf16 v[128:131], v[44:47], a[68:71], v[128:131]\n"
"s_add_u32 m0, s54, 0x8000\n"
"v_mfma_f32_16x16x32_bf16 v[132:135], v[40:43], a[72:75], 0\n"
"buffer_load_dwordx4 v2, s[8:11], s50 offen lds\n"
"v_mfma_f32_16x16x32_bf16 v[132:135], v[44:47], a[76:79], v[132:135]\n"
"ds_read_b64_tr_b16 v[48:49], v13\n"
"s_waitcnt lgkmcnt(9)\n"
"v_mfma_f32_16x16x32_bf16 v[136:139], v[40:43], a[80:83], 0\n"
"ds_read_b64_tr_b16 v[50:51], v13 offset:1024\n"
"v_mfma_f32_16x16x32_bf16 v[136:139], v[44:47], a[84:87], v[136:139]\n"
"ds_read_b64_tr_b16 v[52:53], v13 offset:8192\n"
"v_mfma_f32_16x16x32_bf16 v[140:143], v[40:43], a[88:91], 0\n"
"ds_read_b64_tr_b16 v[54:55], v13 offset:9216\n"
"buffer_load_dwordx4 v3, s[8:11], s50 offen offset:1024 lds\n"
"v_mfma_f32_16x16x32_bf16 v[140:143], v[44:47], a[92:95], v[140:143]\n"
"s_waitcnt lgkmcnt(8)\n"
"v_mfma_f32_16x16x32_bf16 v[144:147], v[40:43], a[96:99], 0\n"
"v_mfma_f32_16x16x32_bf16 v[144:147], v[44:47], a[100:103], v[144:147]\n"
"v_mfma_f32_16x16x32_bf16 v[148:151], v[40:43], a[104:107], 0\n"
"buffer_load_dwordx4 v4, s[8:11], s50 offen offset:2048 lds\n"
"v_mfma_f32_16x16x32_bf16 v[148:151], v[44:47], a[108:111], v[148:151]\n"
"s_waitcnt lgkmcnt(4)\n"
"v_mfma_f32_16x16x32_bf16 v[152:155], v[40:43], a[112:115], 0\n"
"v_mfma_f32_16x16x32_bf16 v[152:155], v[44:47], a[116:119], v[152:155]\n"
"v_mfma_f32_16x16x32_bf16 v[156:159], v[40:43], a[120:123], 0\n"
"buffer_load_dwordx4 v5, s[8:11], s50 offen offset:3072 lds\n"
"v_mfma_f32_16x16x32_bf16 v[156:159], v[44:47], a[124:127], v[156:159]\n"
"s_add_u32 s50, s50, s27\n"
"s_waitcnt lgkmcnt(0)\n"
"v_mfma_f32_16x16x32_bf16 v[160:163], v[48:51], a[64:67], 0\n"
"v_mfma_f32_16x16x32_bf16 v[160:163], v[52:55], a[68:71], v[160:163]\n"
"ds_read_b64_tr_b16 v[40:41], v14\n"
"s_add_u32 m0, s78, 0x14000\n"
"v_mfma_f32_16x16x32_bf16 v[164:167], v[48:51], a[72:75], 0\n"
"buffer_load_dwordx4 v6, s[12:15], s52 offen lds\n"
"v_mfma_f32_16x16x32_bf16 v[164:167], v[52:55], a[76:79], v[164:167]\n"
"ds_read_b64_tr_b16 v[42:43], v14 offset:1024\n"
"v_mfma_f32_16x16x32_bf16 v[168:171], v[48:51], a[80:83], 0\n"
"s_lshl_b32 s75, s3, 9\n"
"s_add_u32 s75, s75, 0x25000\n"
"v_mfma_f32_16x16x32_bf16 v[168:171], v[52:55], a[84:87], v[168:171]\n"
"ds_read_b64_tr_b16 v[44:45], v14 offset:8192\n"
"v_add_u32 v114, s75, v101\n"
"v_mfma_f32_16x16x32_bf16 v[172:175], v[48:51], a[88:91], 0\n"
"buffer_load_dwordx4 v7, s[12:15], s52 offen offset:1024 lds\n"
"v_lshrrev_b32 v113, 4, v0\n"
"v_mfma_f32_16x16x32_bf16 v[172:175], v[52:55], a[92:95], v[172:175]\n"
"ds_read_b64_tr_b16 v[46:47], v14 offset:9216\n"
"v_bfe_u32 v112, v0, 2, 2\n"
"v_mfma_f32_16x16x32_bf16 v[176:179], v[48:51], a[96:99], 0\n"
"v_lshl_add_u32 v112, v113, 3, v112\n"
"v_lshlrev_b32 v112, 7, v112\n"
"v_mfma_f32_16x16x32_bf16 v[176:179], v[52:55], a[100:103], v[176:179]\n"
"v_and_b32 v115, 3, v0\n"
"v_lshl_add_u32 v112, v115, 3, v112\n"
"v_mfma_f32_16x16x32_bf16 v[180:183], v[48:51], a[104:107], 0\n"
"buffer_load_dwordx4 v8, s[12:15], s52 offen offset:2048 lds\n"
"v_lshl_add_u32 v113, v113, 4, s75\n"
"v_mfma_f32_16x16x32_bf16 v[180:183], v[52:55], a[108:111], v[180:183]\n"
"s_lshl_b32 s75, s3, 5\n"
"s_add_u32 s75, s75, 0x20000\n"
"v_mfma_f32_16x16x32_bf16 v[184:187], v[48:51], a[112:115], 0\n"
"v_add_u32 v112, s75, v112\n"
"s_waitcnt vmcnt(12)\n"
"v_mfma_f32_16x16x32_bf16 v[184:187], v[52:55], a[116:119], v[184:187]\n"
"v_lshlrev_b32 v104, 16, v102\n"
"v_and_b32 v105, 0xffff0000, v102\n"
"v_mfma_f32_16x16x32_bf16 v[188:191], v[48:51], a[120:123], 0\n"
"buffer_load_dwordx4 v9, s[12:15], s52 offen offset:3072 lds\n"
"v_lshlrev_b32 v106, 16, v103\n"
"v_mfma_f32_16x16x32_bf16 v[188:191], v[52:55], a[124:127], v[188:191]\n"
"s_add_u32 s52, s52, s31\n"
"s_waitcnt lgkmcnt(0)\n"
"v_mfma_f32_16x16x32_bf16 v[192:195], v[40:43], a[64:67], 0\n"
"v_and_b32 v107, 0xffff0000, v103\n"
"v_mul_f32 v108, 0xbfb8aa3b, v104\n"
"v_mfma_f32_16x16x32_bf16 v[192:195], v[44:47], a[68:71], v[192:195]\n"
"ds_read_b64_tr_b16 v[48:49], v15\n"
"s_add_u32 m0, s54, 0x9000\n"
"v_mfma_f32_16x16x32_bf16 v[196:199], v[40:43], a[72:75], 0\n"
"buffer_load_dwordx4 v2, s[8:11], s51 offen lds\n"
"v_mul_f32 v109, 0xbfb8aa3b, v105\n"
"v_mfma_f32_16x16x32_bf16 v[196:199], v[44:47], a[76:79], v[196:199]\n"
"ds_read_b64_tr_b16 v[50:51], v15 offset:1024\n"
"v_mul_f32 v110, 0xbfb8aa3b, v106\n"
"v_mfma_f32_16x16x32_bf16 v[200:203], v[40:43], a[80:83], 0\n"
"v_mul_f32 v111, 0xbfb8aa3b, v107\n"
"v_mfma_f32_16x16x32_bf16 v[200:203], v[44:47], a[84:87], v[200:203]\n"
"ds_read_b64_tr_b16 v[52:53], v15 offset:8192\n"
"v_mfma_f32_16x16x32_bf16 v[204:207], v[40:43], a[88:91], 0\n"
"buffer_load_dwordx4 v3, s[8:11], s51 offen offset:1024 lds\n"
"v_mfma_f32_16x16x32_bf16 v[204:207], v[44:47], a[92:95], v[204:207]\n"
"ds_read_b64_tr_b16 v[54:55], v15 offset:9216\n"
"v_mfma_f32_16x16x32_bf16 v[208:211], v[40:43], a[96:99], 0\n"
"v_exp_f32 v108, v108\n"
"v_mfma_f32_16x16x32_bf16 v[208:211], v[44:47], a[100:103], v[208:211]\n"
"v_exp_f32 v109, v109\n"
"v_mfma_f32_16x16x32_bf16 v[212:215], v[40:43], a[104:107], 0\n"
"buffer_load_dwordx4 v4, s[8:11], s51 offen offset:2048 lds\n"
"v_mfma_f32_16x16x32_bf16 v[212:215], v[44:47], a[108:111], v[212:215]\n"
"v_exp_f32 v110, v110\n"
"v_mfma_f32_16x16x32_bf16 v[216:219], v[40:43], a[112:115], 0\n"
"v_exp_f32 v111, v111\n"
"v_mfma_f32_16x16x32_bf16 v[216:219], v[44:47], a[116:119], v[216:219]\n"
"v_add_f32 v108, 1.0, v108\n"
"v_add_f32 v109, 1.0, v109\n"
"v_mfma_f32_16x16x32_bf16 v[220:223], v[40:43], a[120:123], 0\n"
"buffer_load_dwordx4 v5, s[8:11], s51 offen offset:3072 lds\n"
"v_add_f32 v110, 1.0, v110\n"
"v_mfma_f32_16x16x32_bf16 v[220:223], v[44:47], a[124:127], v[220:223]\n"
"s_add_u32 s51, s51, s27\n"
"s_waitcnt vmcnt(13) lgkmcnt(0)\n"
"v_mfma_f32_16x16x32_bf16 v[224:227], v[48:51], a[64:67], 0\n"
"s_barrier\n"
"v_add_f32 v111, 1.0, v111\n"
"v_mfma_f32_16x16x32_bf16 v[224:227], v[52:55], a[68:71], v[224:227]\n"
"ds_read_b64_tr_b16 v[40:41], v12 offset:32768\n"
"s_add_u32 m0, s78, 0x1c000\n"
"v_mfma_f32_16x16x32_bf16 v[228:231], v[48:51], a[72:75], 0\n"
"buffer_load_dwordx4 v6, s[16:19], s53 offen lds\n"
"v_mfma_f32_16x16x32_bf16 v[228:231], v[52:55], a[76:79], v[228:231]\n"
"ds_read_b64_tr_b16 v[42:43], v12 offset:33792\n"
"v_mfma_f32_16x16x32_bf16 v[232:235], v[48:51], a[80:83], 0\n"
"v_rcp_f32 v108, v108\n"
"v_mfma_f32_16x16x32_bf16 v[232:235], v[52:55], a[84:87], v[232:235]\n"
"ds_read_b64_tr_b16 v[44:45], v12 offset:40960\n"
"v_mfma_f32_16x16x32_bf16 v[236:239], v[48:51], a[88:91], 0\n"
"buffer_load_dwordx4 v7, s[16:19], s53 offen offset:1024 lds\n"
"v_mfma_f32_16x16x32_bf16 v[236:239], v[52:55], a[92:95], v[236:239]\n"
"ds_read_b64_tr_b16 v[46:47], v12 offset:41984\n"
"v_mfma_f32_16x16x32_bf16 v[240:243], v[48:51], a[96:99], 0\n"
"v_rcp_f32 v109, v109\n"
"v_mfma_f32_16x16x32_bf16 v[240:243], v[52:55], a[100:103], v[240:243]\n"
"v_rcp_f32 v110, v110\n"
"v_mfma_f32_16x16x32_bf16 v[244:247], v[48:51], a[104:107], 0\n"
"buffer_load_dwordx4 v8, s[16:19], s53 offen offset:2048 lds\n"
"v_mfma_f32_16x16x32_bf16 v[244:247], v[52:55], a[108:111], v[244:247]\n"
"v_rcp_f32 v111, v111\n"
"v_mfma_f32_16x16x32_bf16 v[248:251], v[48:51], a[112:115], 0\n"
"v_mul_f32 v104, v104, v108\n"
"v_mul_f32 v105, v105, v109\n"
"v_mfma_f32_16x16x32_bf16 v[248:251], v[52:55], a[116:119], v[248:251]\n"
"v_mul_f32 v106, v106, v110\n"
"v_mul_f32 v107, v107, v111\n"
"v_mfma_f32_16x16x32_bf16 v[252:255], v[48:51], a[120:123], 0\n"
"buffer_load_dwordx4 v9, s[16:19], s53 offen offset:3072 lds\n"
"v_cvt_pk_bf16_f32 v102, v104, v105\n"
"v_mfma_f32_16x16x32_bf16 v[252:255], v[52:55], a[124:127], v[252:255]\n"
"s_add_u32 s53, s53, s31\n"
"s_waitcnt lgkmcnt(0)\n"
"v_mfma_f32_16x16x32_bf16 a[128:131], a[64:67], v[40:43], 0\n"
"s_add_u32 m0, s74, 0\n"
"v_cvt_pk_bf16_f32 v103, v106, v107\n"
"v_mfma_f32_16x16x32_bf16 a[128:131], a[68:71], v[44:47], a[128:131]\n"
"ds_read_b64_tr_b16 v[48:49], v13 offset:32768\n"
"v_mfma_f32_16x16x32_bf16 a[132:135], a[72:75], v[40:43], 0\n"
"buffer_load_dwordx4 v100, s[20:23], s61 offen lds\n"
"s_add_u32 m0, s74, 0x400\n"
"v_mfma_f32_16x16x32_bf16 a[132:135], a[76:79], v[44:47], a[132:135]\n"
"ds_read_b64_tr_b16 v[50:51], v13 offset:33792\n"
"s_add_u32 s61, s61, 0xf000\n"
"v_mfma_f32_16x16x32_bf16 a[136:139], a[80:83], v[40:43], 0\n"
"ds_write_b64 v114, v[102:103]\n"
"v_mfma_f32_16x16x32_bf16 a[136:139], a[84:87], v[44:47], a[136:139]\n"
"ds_read_b64_tr_b16 v[52:53], v13 offset:40960\n"
"v_mfma_f32_16x16x32_bf16 a[140:143], a[88:91], v[40:43], 0\n"
"buffer_load_dwordx4 v100, s[20:23], s61 offen lds\n"
"s_add_u32 m0, s74, 0x800\n"
"v_mfma_f32_16x16x32_bf16 a[140:143], a[92:95], v[44:47], a[140:143]\n"
"ds_read_b64_tr_b16 v[54:55], v13 offset:41984\n"
"s_add_u32 s61, s61, 0xf000\n"
"v_mfma_f32_16x16x32_bf16 a[144:147], a[96:99], v[40:43], 0\n"
"ds_read_b128 a[20:23], v113\n"
"v_mfma_f32_16x16x32_bf16 a[144:147], a[100:103], v[44:47], a[144:147]\n"
"ds_read_b128 a[24:27], v113 offset:64\n"
"v_mfma_f32_16x16x32_bf16 a[148:151], a[104:107], v[40:43], 0\n"
"buffer_load_dwordx4 v100, s[20:23], s61 offen lds\n"
"s_add_u32 m0, s54, 0\n"
"v_mfma_f32_16x16x32_bf16 a[148:151], a[108:111], v[44:47], a[148:151]\n"
"ds_read_b128 a[28:31], v113 offset:128\n"
"s_add_u32 s61, s61, 0xf000\n"
"v_mfma_f32_16x16x32_bf16 a[152:155], a[112:115], v[40:43], 0\n"
"ds_read_b128 a[32:35], v113 offset:192\n"
"v_mfma_f32_16x16x32_bf16 a[152:155], a[116:119], v[44:47], a[152:155]\n"
"ds_read_b128 a[36:39], v113 offset:256\n"
"v_mfma_f32_16x16x32_bf16 a[156:159], a[120:123], v[40:43], 0\n"
"buffer_load_dwordx4 v2, s[8:11], s50 offen lds\n"
"v_mfma_f32_16x16x32_bf16 a[156:159], a[124:127], v[44:47], a[156:159]\n"
"s_mul_i32 s73, s68, 0x667\n"
"s_waitcnt lgkmcnt(5)\n"
"v_mfma_f32_16x16x32_bf16 a[160:163], a[64:67], v[48:51], 0\n"
"s_lshr_b32 s73, s73, 16\n"
"s_mov_b32 s70, 0\n"
"v_mfma_f32_16x16x32_bf16 a[160:163], a[68:71], v[52:55], a[160:163]\n"
"ds_read_b64_tr_b16 v[40:41], v14 offset:32768\n"
"v_mfma_f32_16x16x32_bf16 a[164:167], a[72:75], v[48:51], 0\n"
"buffer_load_dwordx4 v3, s[8:11], s50 offen offset:1024 lds\n"
"v_mfma_f32_16x16x32_bf16 a[164:167], a[76:79], v[52:55], a[164:167]\n"
"ds_read_b64_tr_b16 v[42:43], v14 offset:33792\n"
"v_mfma_f32_16x16x32_bf16 a[168:171], a[80:83], v[48:51], 0\n"
"s_cmp_eq_u32 s73, 1\n"
"s_cmov_b32 s70, 1.0\n"
"v_mfma_f32_16x16x32_bf16 a[168:171], a[84:87], v[52:55], a[168:171]\n"
"ds_read_b64_tr_b16 v[44:45], v14 offset:40960\n"
"v_mfma_f32_16x16x32_bf16 a[172:175], a[88:91], v[48:51], 0\n"
"buffer_load_dwordx4 v4, s[8:11], s50 offen offset:2048 lds\n"
"v_mfma_f32_16x16x32_bf16 a[172:175], a[92:95], v[52:55], a[172:175]\n"
"ds_read_b64_tr_b16 v[46:47], v14 offset:41984\n"
"v_mfma_f32_16x16x32_bf16 a[176:179], a[96:99], v[48:51], 0\n"
"s_cmp_eq_u32 s73, 4\n"
"s_cmov_b32 s70, 1.0\n"
"v_mfma_f32_16x16x32_bf16 a[176:179], a[100:103], v[52:55], a[176:179]\n"
"s_lshl_b32 s73, s68, 6\n"
"s_add_u32 s71, s73, 0x18000\n"
"v_mfma_f32_16x16x32_bf16 a[180:183], a[104:107], v[48:51], 0\n"
"buffer_load_dwordx4 v5, s[8:11], s50 offen offset:3072 lds\n"
"s_add_u32 m0, s74, 0xc00\n"
"v_mfma_f32_16x16x32_bf16 a[180:183], a[108:111], v[52:55], a[180:183]\n"
"s_add_u32 s50, s50, s26\n"
"s_add_u32 s73, s73, 0x20000\n"
"v_mfma_f32_16x16x32_bf16 a[184:187], a[112:115], v[48:51], 0\n"
"s_lshr_b32 s72, s68, 1\n"
"s_lshl_b32 s72, s72, 6\n"
"v_mfma_f32_16x16x32_bf16 a[184:187], a[116:119], v[52:55], a[184:187]\n"
"s_and_b32 s75, s68, 1\n"
"s_lshl3_add_u32 s72, s75, s72\n"
"v_mfma_f32_16x16x32_bf16 a[188:191], a[120:123], v[48:51], 0\n"
"buffer_load_dwordx4 v100, s[20:23], s61 offen lds\n"
"s_add_u32 m0, s74, 0x1000\n"
"v_mfma_f32_16x16x32_bf16 a[188:191], a[124:127], v[52:55], a[188:191]\n"
"s_add_u32 s61, s61, 0xf000\n"
"s_waitcnt lgkmcnt(0)\n"
"v_mfma_f32_16x16x32_bf16 a[192:195], a[64:67], v[40:43], 0\n"
"s_add_u32 s72, s72, 0x10000\n"
"s_lshl_b32 s75, s3, 8\n"
"v_mfma_f32_16x16x32_bf16 a[192:195], a[68:71], v[44:47], a[192:195]\n"
"ds_read_b64_tr_b16 v[48:49], v15 offset:32768\n"
"s_add_u32 s75, s75, s68\n"
"v_mfma_f32_16x16x32_bf16 a[196:199], a[72:75], v[40:43], 0\n"
"buffer_load_dwordx4 v100, s[20:23], s61 offen lds\n"
"s_add_u32 m0, s54, 0x1000\n"
"v_mfma_f32_16x16x32_bf16 a[196:199], a[76:79], v[44:47], a[196:199]\n"
"ds_read_b64_tr_b16 v[50:51], v15 offset:33792\n"
"s_add_u32 s61, s61, 0xf000\n"
"v_mfma_f32_16x16x32_bf16 a[200:203], a[80:83], v[40:43], 0\n"
"s_cmp_ge_u32 s75, 0xf0\n"
"s_cmov_b32 s73, 0x40000000\n"
"v_mfma_f32_16x16x32_bf16 a[200:203], a[84:87], v[44:47], a[200:203]\n"
"ds_read_b64_tr_b16 v[52:53], v15 offset:40960\n"
"s_mov_b32 s20, s28\n"
"v_mfma_f32_16x16x32_bf16 a[204:207], a[88:91], v[40:43], 0\n"
"buffer_load_dwordx4 v2, s[8:11], s51 offen lds\n"
"s_and_b32 s21, s29, 0xffff\n"
"v_mfma_f32_16x16x32_bf16 a[204:207], a[92:95], v[44:47], a[204:207]\n"
"ds_read_b64_tr_b16 v[54:55], v15 offset:41984\n"
"s_mov_b32 s22, 0x500000\n"
"v_mfma_f32_16x16x32_bf16 a[208:211], a[96:99], v[40:43], 0\n"
"s_cmp_ge_u32 s75, 0xf0\n"
"s_cmov_b32 s72, 0x40000000\n"
"v_mfma_f32_16x16x32_bf16 a[208:211], a[100:103], v[44:47], a[208:211]\n"
"s_cmp_ge_u32 s75, 0x50\n"
"s_cmov_b32 s71, 0x40000000\n"
"v_mfma_f32_16x16x32_bf16 a[212:215], a[104:107], v[40:43], 0\n"
"buffer_load_dwordx4 v3, s[8:11], s51 offen offset:1024 lds\n"
"v_and_b32 v114, 15, v0\n"
"v_mfma_f32_16x16x32_bf16 a[212:215], a[108:111], v[44:47], a[212:215]\n"
"v_lshrrev_b32 v113, 1, v114\n"
"v_min_u32 v113, 1, v113\n"
"v_mfma_f32_16x16x32_bf16 a[216:219], a[112:115], v[40:43], 0\n"
"v_lshlrev_b32 v113, 30, v113\n"
"v_lshl_add_u32 v113, v114, 4, v113\n"
"v_mfma_f32_16x16x32_bf16 a[216:219], a[116:119], v[44:47], a[216:219]\n"
"v_and_b32 v114, 48, v0\n"
"v_lshl_add_u32 v113, v114, 1, v113\n"
"v_mfma_f32_16x16x32_bf16 a[220:223], a[120:123], v[40:43], 0\n"
"buffer_load_dwordx4 v4, s[8:11], s51 offen offset:2048 lds\n"
"v_and_b32 v38, 7, v0\n"
"v_mfma_f32_16x16x32_bf16 a[220:223], a[124:127], v[44:47], a[220:223]\n"
"v_lshrrev_b32 v39, 3, v0\n"
"s_waitcnt vmcnt(16) lgkmcnt(0)\n"
"v_mfma_f32_16x16x32_bf16 a[224:227], a[64:67], v[48:51], 0\n"
"s_barrier\n"
"v_mul_u32_u24 v24, 0x500, v38\n"
"v_mfma_f32_16x16x32_bf16 a[224:227], a[68:71], v[52:55], a[224:227]\n"
"ds_read_b64_tr_b16 v[40:41], v12 offset:16384\n"
"ds_read_b64_tr_b16 v[42:43], v12 offset:17408\n"
"v_mfma_f32_16x16x32_bf16 a[228:231], a[72:75], v[48:51], 0\n"
"ds_read_b64_tr_b16 v[44:45], v12 offset:24576\n"
"ds_read_b64_tr_b16 v[46:47], v12 offset:25600\n"
"v_mfma_f32_16x16x32_bf16 a[228:231], a[76:79], v[52:55], a[228:231]\n"
"ds_read_b128 a[64:67], v10 offset:32768\n"
"buffer_load_dwordx4 v5, s[8:11], s51 offen offset:3072 lds\n"
"v_mfma_f32_16x16x32_bf16 a[232:235], a[80:83], v[48:51], 0\n"
"ds_read_b128 a[68:71], v11 offset:32768\n"
"s_add_u32 s51, s51, s26\n"
"v_mfma_f32_16x16x32_bf16 a[232:235], a[84:87], v[52:55], a[232:235]\n"
"ds_read_b128 a[72:75], v10 offset:34816\n"
"s_add_u32 m0, s78, 0x10000\n"
"v_mfma_f32_16x16x32_bf16 a[236:239], a[88:91], v[48:51], 0\n"
"ds_read_b128 a[76:79], v11 offset:34816\n"
"v_lshl_add_u32 v24, v39, 4, v24\n"
"v_mfma_f32_16x16x32_bf16 a[236:239], a[92:95], v[52:55], a[236:239]\n"
"ds_read_b128 a[80:83], v10 offset:36864\n"
"buffer_load_dwordx4 v6, s[12:15], s52 offen lds\n"
"v_mfma_f32_16x16x32_bf16 a[240:243], a[96:99], v[48:51], 0\n"
"ds_read_b128 a[84:87], v11 offset:36864\n"
"v_lshlrev_b32 v28, 5, v39\n"
"v_mfma_f32_16x16x32_bf16 a[240:243], a[100:103], v[52:55], a[240:243]\n"
"ds_read_b128 a[88:91], v10 offset:38912\n"
"v_and_b32 v29, 0x7f, v28\n"
"v_mfma_f32_16x16x32_bf16 a[244:247], a[104:107], v[48:51], 0\n"
"ds_read_b128 a[92:95], v11 offset:38912\n"
"v_lshlrev_b32 v26, 3, v29\n"
"v_mfma_f32_16x16x32_bf16 a[244:247], a[108:111], v[52:55], a[244:247]\n"
"ds_read_b128 a[96:99], v10 offset:49152\n"
"buffer_load_dwordx4 v7, s[12:15], s52 offen offset:1024 lds\n"
"v_mfma_f32_16x16x32_bf16 a[248:251], a[112:115], v[48:51], 0\n"
"ds_read_b128 a[100:103], v11 offset:49152\n"
"v_lshl_add_u32 v26, v38, 4, v26\n"
"v_mfma_f32_16x16x32_bf16 a[248:251], a[116:119], v[52:55], a[248:251]\n"
"ds_read_b128 a[104:107], v10 offset:51200\n"
"v_lshlrev_b32 v30, 3, v38\n"
"v_mfma_f32_16x16x32_bf16 a[252:255], a[120:123], v[48:51], 0\n"
"ds_read_b128 a[108:111], v11 offset:51200\n"
"v_add_u32 v30, 0x26000, v30\n"
"v_mfma_f32_16x16x32_bf16 a[252:255], a[124:127], v[52:55], a[252:255]\n"
"ds_read_b128 a[112:115], v10 offset:53248\n"
"s_waitcnt lgkmcnt(9)\n"
"v_mfma_f32_16x16x32_bf16 v[128:131], v[40:43], a[64:67], v[128:131]\n"
"ds_read_b128 a[116:119], v11 offset:53248\n"
"v_lshrrev_b32 v39, 5, v0\n"
"v_mfma_f32_16x16x32_bf16 v[128:131], v[44:47], a[68:71], v[128:131]\n"
"ds_read_b128 a[120:123], v10 offset:55296\n"
"v_lshl_add_u32 v25, v39, 30, v24\n"
"v_mfma_f32_16x16x32_bf16 v[132:135], v[40:43], a[72:75], v[132:135]\n"
"ds_read_b128 a[124:127], v11 offset:55296\n"
"buffer_load_dwordx4 v8, s[12:15], s52 offen offset:2048 lds\n"
"v_mfma_f32_16x16x32_bf16 v[132:135], v[44:47], a[76:79], v[132:135]\n"
"ds_read_b64_tr_b16 v[48:49], v13 offset:16384\n"
"s_waitcnt lgkmcnt(9)\n"
"v_mfma_f32_16x16x32_bf16 v[136:139], v[40:43], a[80:83], v[136:139]\n"
"ds_read_b64_tr_b16 v[50:51], v13 offset:17408\n"
"v_lshl_add_u32 v27, v39, 30, v26\n"
"v_mfma_f32_16x16x32_bf16 v[136:139], v[44:47], a[84:87], v[136:139]\n"
"ds_read_b64_tr_b16 v[52:53], v13 offset:24576\n"
"s_and_b32 s62, s3, 1\n"
"v_mfma_f32_16x16x32_bf16 v[140:143], v[40:43], a[88:91], v[140:143]\n"
"ds_read_b64_tr_b16 v[54:55], v13 offset:25600\n"
"s_lshl_b32 s62, s62, 13\n"
"v_mfma_f32_16x16x32_bf16 v[140:143], v[44:47], a[92:95], v[140:143]\n"
"s_add_u32 s62, s62, 0x1000\n"
"s_waitcnt lgkmcnt(8)\n"
"v_mfma_f32_16x16x32_bf16 v[144:147], v[40:43], a[96:99], v[144:147]\n"
"v_mad_u32_u24 v26, v39, s62, v26\n"
"s_mul_i32 s62, s2, 0x2800\n"
"v_mfma_f32_16x16x32_bf16 v[144:147], v[44:47], a[100:103], v[144:147]\n"
"s_mul_i32 s63, s3, 0x140\n"
"s_add_u32 s62, s62, s63\n"
"v_mfma_f32_16x16x32_bf16 v[148:151], v[40:43], a[104:107], v[148:151]\n"
"buffer_load_dwordx4 v9, s[12:15], s52 offen offset:3072 lds\n"
"s_mov_b32 s23, 0x20000\n"
"v_mfma_f32_16x16x32_bf16 v[148:151], v[44:47], a[108:111], v[148:151]\n"
"s_add_u32 s52, s52, s30\n"
"s_waitcnt lgkmcnt(4)\n"
"v_mfma_f32_16x16x32_bf16 v[152:155], v[40:43], a[112:115], v[152:155]\n"
"v_mfma_f32_16x16x32_bf16 v[152:155], v[44:47], a[116:119], v[152:155]\n"
"v_mfma_f32_16x16x32_bf16 v[156:159], v[40:43], a[120:123], v[156:159]\n"
"v_mfma_f32_16x16x32_bf16 v[156:159], v[44:47], a[124:127], v[156:159]\n"
"s_waitcnt lgkmcnt(0)\n"
"v_mfma_f32_16x16x32_bf16 v[160:163], v[48:51], a[64:67], v[160:163]\n"
"v_mfma_f32_16x16x32_bf16 v[160:163], v[52:55], a[68:71], v[160:163]\n"
"ds_read_b64_tr_b16 v[40:41], v14 offset:16384\n"
"s_add_u32 m0, s78, 0x18000\n"
"v_mfma_f32_16x16x32_bf16 v[164:167], v[48:51], a[72:75], v[164:167]\n"
"buffer_load_dwordx4 v6, s[16:19], s53 offen lds\n"
"v_mfma_f32_16x16x32_bf16 v[164:167], v[52:55], a[76:79], v[164:167]\n"
"ds_read_b64_tr_b16 v[42:43], v14 offset:17408\n"
"v_mfma_f32_16x16x32_bf16 v[168:171], v[48:51], a[80:83], v[168:171]\n"
"v_mfma_f32_16x16x32_bf16 v[168:171], v[52:55], a[84:87], v[168:171]\n"
"ds_read_b64_tr_b16 v[44:45], v14 offset:24576\n"
"v_mfma_f32_16x16x32_bf16 v[172:175], v[48:51], a[88:91], v[172:175]\n"
"v_mfma_f32_16x16x32_bf16 v[172:175], v[52:55], a[92:95], v[172:175]\n"
"ds_read_b64_tr_b16 v[46:47], v14 offset:25600\n"
"v_mfma_f32_16x16x32_bf16 v[176:179], v[48:51], a[96:99], v[176:179]\n"
"v_mfma_f32_16x16x32_bf16 v[176:179], v[52:55], a[100:103], v[176:179]\n"
"v_mfma_f32_16x16x32_bf16 v[180:183], v[48:51], a[104:107], v[180:183]\n"
"buffer_load_dwordx4 v7, s[16:19], s53 offen offset:1024 lds\n"
"v_mfma_f32_16x16x32_bf16 v[180:183], v[52:55], a[108:111], v[180:183]\n"
"v_mfma_f32_16x16x32_bf16 v[184:187], v[48:51], a[112:115], v[184:187]\n"
"v_mfma_f32_16x16x32_bf16 v[184:187], v[52:55], a[116:119], v[184:187]\n"
"v_mfma_f32_16x16x32_bf16 v[188:191], v[48:51], a[120:123], v[188:191]\n"
"v_mfma_f32_16x16x32_bf16 v[188:191], v[52:55], a[124:127], v[188:191]\n"
"s_waitcnt lgkmcnt(0)\n"
"v_mfma_f32_16x16x32_bf16 v[192:195], v[40:43], a[64:67], v[192:195]\n"
"v_mfma_f32_16x16x32_bf16 v[192:195], v[44:47], a[68:71], v[192:195]\n"
"ds_read_b64_tr_b16 v[48:49], v15 offset:16384\n"
"v_mfma_f32_16x16x32_bf16 v[196:199], v[40:43], a[72:75], v[196:199]\n"
"buffer_load_dwordx4 v8, s[16:19], s53 offen offset:2048 lds\n"
"v_mfma_f32_16x16x32_bf16 v[196:199], v[44:47], a[76:79], v[196:199]\n"
"ds_read_b64_tr_b16 v[50:51], v15 offset:17408\n"
"v_mfma_f32_16x16x32_bf16 v[200:203], v[40:43], a[80:83], v[200:203]\n"
"v_mfma_f32_16x16x32_bf16 v[200:203], v[44:47], a[84:87], v[200:203]\n"
"ds_read_b64_tr_b16 v[52:53], v15 offset:24576\n"
"v_mfma_f32_16x16x32_bf16 v[204:207], v[40:43], a[88:91], v[204:207]\n"
"v_mfma_f32_16x16x32_bf16 v[204:207], v[44:47], a[92:95], v[204:207]\n"
"ds_read_b64_tr_b16 v[54:55], v15 offset:25600\n"
"v_mfma_f32_16x16x32_bf16 v[208:211], v[40:43], a[96:99], v[208:211]\n"
"v_mfma_f32_16x16x32_bf16 v[208:211], v[44:47], a[100:103], v[208:211]\n"
"v_mfma_f32_16x16x32_bf16 v[212:215], v[40:43], a[104:107], v[212:215]\n"
"buffer_load_dwordx4 v9, s[16:19], s53 offen offset:3072 lds\n"
"v_mfma_f32_16x16x32_bf16 v[212:215], v[44:47], a[108:111], v[212:215]\n"
"s_add_u32 s53, s53, s30\n"
"v_mfma_f32_16x16x32_bf16 v[216:219], v[40:43], a[112:115], v[216:219]\n"
"v_mfma_f32_16x16x32_bf16 v[216:219], v[44:47], a[116:119], v[216:219]\n"
"v_mfma_f32_16x16x32_bf16 v[220:223], v[40:43], a[120:123], v[220:223]\n"
"v_mfma_f32_16x16x32_bf16 v[220:223], v[44:47], a[124:127], v[220:223]\n"
"s_waitcnt vmcnt(12) lgkmcnt(0)\n"
"v_mfma_f32_16x16x32_bf16 v[224:227], v[48:51], a[64:67], v[224:227]\n"
"s_barrier\n"
"v_mfma_f32_16x16x32_bf16 v[224:227], v[52:55], a[68:71], v[224:227]\n"
"ds_read_b64_tr_b16 v[40:41], v12 offset:49152\n"
"s_add_u32 m0, s54, 0x8000\n"
"v_mfma_f32_16x16x32_bf16 v[228:231], v[48:51], a[72:75], v[228:231]\n"
"buffer_load_dwordx4 v2, s[8:11], s50 offen lds\n"
"v_mfma_f32_16x16x32_bf16 v[228:231], v[52:55], a[76:79], v[228:231]\n"
"ds_read_b64_tr_b16 v[42:43], v12 offset:50176\n"
"v_mfma_f32_16x16x32_bf16 v[232:235], v[48:51], a[80:83], v[232:235]\n"
"ds_read_b64_tr_b16 a[0:1], v112\n"
"v_mfma_f32_16x16x32_bf16 v[232:235], v[52:55], a[84:87], v[232:235]\n"
"ds_read_b64_tr_b16 v[44:45], v12 offset:57344\n"
"v_mfma_f32_16x16x32_bf16 v[236:239], v[48:51], a[88:91], v[236:239]\n"
"ds_read_b64_tr_b16 a[2:3], v112 offset:512\n"
"v_mfma_f32_16x16x32_bf16 v[236:239], v[52:55], a[92:95], v[236:239]\n"
"ds_read_b64_tr_b16 v[46:47], v12 offset:58368\n"
"v_mfma_f32_16x16x32_bf16 v[240:243], v[48:51], a[96:99], v[240:243]\n"
"ds_read_b64_tr_b16 a[4:5], v112 offset:4096\n"
"v_mfma_f32_16x16x32_bf16 v[240:243], v[52:55], a[100:103], v[240:243]\n"
"ds_read_b64_tr_b16 a[6:7], v112 offset:4608\n"
"v_mfma_f32_16x16x32_bf16 v[244:247], v[48:51], a[104:107], v[244:247]\n"
"buffer_load_dwordx4 v3, s[8:11], s50 offen offset:1024 lds\n"
"v_mfma_f32_16x16x32_bf16 v[244:247], v[52:55], a[108:111], v[244:247]\n"
"ds_read_b64_tr_b16 a[8:9], v112 offset:8192\n"
"v_mfma_f32_16x16x32_bf16 v[248:251], v[48:51], a[112:115], v[248:251]\n"
"ds_read_b64_tr_b16 a[10:11], v112 offset:8704\n"
"v_mfma_f32_16x16x32_bf16 v[248:251], v[52:55], a[116:119], v[248:251]\n"
"ds_read_b64_tr_b16 a[12:13], v112 offset:12288\n"
"v_mfma_f32_16x16x32_bf16 v[252:255], v[48:51], a[120:123], v[252:255]\n"
"buffer_load_dwordx4 v[116:119], v24, s[64:67], s62 offen\n"
"v_mfma_f32_16x16x32_bf16 v[252:255], v[52:55], a[124:127], v[252:255]\n"
"ds_read_b64_tr_b16 a[14:15], v112 offset:12800\n"
"s_waitcnt lgkmcnt(6)\n"
"v_mfma_f32_16x16x32_bf16 a[128:131], a[64:67], v[40:43], a[128:131]\n"
"ds_read_b64_tr_b16 a[16:17], v112 offset:16384\n"
"v_mfma_f32_16x16x32_bf16 a[128:131], a[68:71], v[44:47], a[128:131]\n"
"ds_read_b64_tr_b16 v[48:49], v13 offset:49152\n"
"v_mfma_f32_16x16x32_bf16 a[132:135], a[72:75], v[40:43], a[132:135]\n"
"buffer_load_dwordx4 v4, s[8:11], s50 offen offset:2048 lds\n"
"v_mfma_f32_16x16x32_bf16 a[132:135], a[76:79], v[44:47], a[132:135]\n"
"ds_read_b64_tr_b16 v[50:51], v13 offset:50176\n"
"v_mfma_f32_16x16x32_bf16 a[136:139], a[80:83], v[40:43], a[136:139]\n"
"ds_read_b64_tr_b16 a[18:19], v112 offset:16896\n"
"v_mfma_f32_16x16x32_bf16 a[136:139], a[84:87], v[44:47], a[136:139]\n"
"ds_read_b64_tr_b16 v[52:53], v13 offset:57344\n"
"s_and_b32 s61, s68, 0xfc\n"
"v_mfma_f32_16x16x32_bf16 a[140:143], a[88:91], v[40:43], a[140:143]\n"
"buffer_load_dwordx4 v[120:123], v24, s[64:67], s62 offen offset:128\n"
"s_add_u32 s61, s61, s3\n"
"v_mfma_f32_16x16x32_bf16 a[140:143], a[92:95], v[44:47], a[140:143]\n"
"ds_read_b64_tr_b16 v[54:55], v13 offset:58368\n"
"s_and_b32 s75, s68, 3\n"
"v_mfma_f32_16x16x32_bf16 a[144:147], a[96:99], v[40:43], a[144:147]\n"
"s_lshl2_add_u32 s61, s61, s75\n"
"s_lshl_b32 s61, s61, 7\n"
"v_mfma_f32_16x16x32_bf16 a[144:147], a[100:103], v[44:47], a[144:147]\n"
"s_add_u32 s61, s61, 0x60000\n"
"s_mov_b32 vcc_lo, 0xaaaaaaaa\n"
"v_mfma_f32_16x16x32_bf16 a[148:151], a[104:107], v[40:43], a[148:151]\n"
"buffer_load_dwordx4 v5, s[8:11], s50 offen offset:3072 lds\n"
"s_mov_b32 vcc_hi, 0xaaaaaaaa\n"
"v_mfma_f32_16x16x32_bf16 a[148:151], a[108:111], v[44:47], a[148:151]\n"
"s_add_u32 s50, s50, s27\n"
"v_mov_b32 v89, v22\n"
"v_mfma_f32_16x16x32_bf16 a[152:155], a[112:115], v[40:43], a[152:155]\n"
"v_mov_b32 v91, v22\n"
"s_waitcnt lgkmcnt(2)\n"
"v_mfma_f32_16x16x32_bf16 a[152:155], a[116:119], v[44:47], a[152:155]\n"
"v_mfma_f32_16x16x32_bf16 v[108:111], a[0:3], a[20:23], 0\n"
"v_mfma_f32_16x16x32_bf16 v[108:111], a[4:7], a[24:27], v[108:111]\n"
"v_mfma_f32_16x16x32_bf16 v[108:111], a[8:11], a[28:31], v[108:111]\n"
"v_mfma_f32_16x16x32_bf16 v[108:111], a[12:15], a[32:35], v[108:111]\n"
"v_mfma_f32_16x16x32_bf16 v[108:111], a[16:19], a[36:39], v[108:111]\n"
"v_mfma_f32_16x16x32_bf16 a[156:159], a[120:123], v[40:43], a[156:159]\n"
"buffer_load_dwordx4 v[124:127], v25, s[64:67], s62 offen offset:256\n"
"v_mfma_f32_16x16x32_bf16 a[156:159], a[124:127], v[44:47], a[156:159]\n"
"s_waitcnt lgkmcnt(0)\n"
"v_mfma_f32_16x16x32_bf16 a[160:163], a[64:67], v[48:51], a[160:163]\n"
"v_mfma_f32_16x16x32_bf16 a[160:163], a[68:71], v[52:55], a[160:163]\n"
"ds_read_b64_tr_b16 v[40:41], v14 offset:49152\n"
"s_add_u32 m0, s54, 0x9000\n"
"v_mfma_f32_16x16x32_bf16 a[164:167], a[72:75], v[48:51], a[164:167]\n"
"buffer_load_dwordx4 v2, s[8:11], s51 offen lds\n"
"v_mfma_f32_16x16x32_bf16 a[164:167], a[76:79], v[52:55], a[164:167]\n"
"ds_read_b64_tr_b16 v[42:43], v14 offset:50176\n"
"v_mfma_f32_16x16x32_bf16 a[168:171], a[80:83], v[48:51], a[168:171]\n"
"v_mfma_f32_16x16x32_bf16 a[168:171], a[84:87], v[52:55], a[168:171]\n"
"ds_read_b64_tr_b16 v[44:45], v14 offset:57344\n"
"v_cndmask_b32 v88, v108, v110, vcc\n"
"v_mfma_f32_16x16x32_bf16 a[172:175], a[88:91], v[48:51], a[172:175]\n"
"v_cndmask_b32 v90, v109, v111, vcc\n"
"v_mfma_f32_16x16x32_bf16 a[172:175], a[92:95], v[52:55], a[172:175]\n"
"ds_read_b64_tr_b16 v[46:47], v14 offset:58368\n"
"v_mfma_f32_16x16x32_bf16 a[176:179], a[96:99], v[48:51], a[176:179]\n"
"buffer_store_dwordx4 v[88:91], v113, s[4:7], s61 offen sc1\n"
"v_mfma_f32_16x16x32_bf16 a[176:179], a[100:103], v[52:55], a[176:179]\n"
"s_lshl_b32 s61, s68, 9\n"
"s_add_u32 s61, s61, 0x60000\n"
"v_mfma_f32_16x16x32_bf16 a[180:183], a[104:107], v[48:51], a[180:183]\n"
"buffer_load_dwordx4 v3, s[8:11], s51 offen offset:1024 lds\n"
"v_lshrrev_b32 v112, 4, v0\n"
"v_mfma_f32_16x16x32_bf16 a[180:183], a[108:111], v[52:55], a[180:183]\n"
"v_min_u32 v112, 1, v112\n"
"v_lshlrev_b32 v112, 30, v112\n"
"v_mfma_f32_16x16x32_bf16 a[184:187], a[112:115], v[48:51], a[184:187]\n"
"v_lshl_add_u32 v112, v0, 2, v112\n"
"v_and_b32 v114, 1, v0\n"
"v_mfma_f32_16x16x32_bf16 a[184:187], a[116:119], v[52:55], a[184:187]\n"
"v_lshl_or_b32 v113, v114, 30, v112\n"
"v_and_b32 v114, 2, v0\n"
"v_mfma_f32_16x16x32_bf16 a[188:191], a[120:123], v[48:51], a[188:191]\n"
"v_lshlrev_b32 v114, 1, v114\n"
"v_sub_u32 v114, v113, v114\n"
"v_mfma_f32_16x16x32_bf16 a[188:191], a[124:127], v[52:55], a[188:191]\n"
"s_waitcnt lgkmcnt(0)\n"
"v_mfma_f32_16x16x32_bf16 a[192:195], a[64:67], v[40:43], a[192:195]\n"
"v_mfma_f32_16x16x32_bf16 a[192:195], a[68:71], v[44:47], a[192:195]\n"
"ds_read_b64_tr_b16 v[48:49], v15 offset:49152\n"
"v_mfma_f32_16x16x32_bf16 a[196:199], a[72:75], v[40:43], a[196:199]\n"
"buffer_load_dwordx4 v4, s[8:11], s51 offen offset:2048 lds\n"
"v_mfma_f32_16x16x32_bf16 a[196:199], a[76:79], v[44:47], a[196:199]\n"
"ds_read_b64_tr_b16 v[50:51], v15 offset:50176\n"
"v_mfma_f32_16x16x32_bf16 a[200:203], a[80:83], v[40:43], a[200:203]\n"
"v_mfma_f32_16x16x32_bf16 a[200:203], a[84:87], v[44:47], a[200:203]\n"
"ds_read_b64_tr_b16 v[52:53], v15 offset:57344\n"
"v_mfma_f32_16x16x32_bf16 a[204:207], a[88:91], v[40:43], a[204:207]\n"
"v_mfma_f32_16x16x32_bf16 a[204:207], a[92:95], v[44:47], a[204:207]\n"
"ds_read_b64_tr_b16 v[54:55], v15 offset:58368\n"
"v_mfma_f32_16x16x32_bf16 a[208:211], a[96:99], v[40:43], a[208:211]\n"
"v_mfma_f32_16x16x32_bf16 a[208:211], a[100:103], v[44:47], a[208:211]\n"
"v_mfma_f32_16x16x32_bf16 a[212:215], a[104:107], v[40:43], a[212:215]\n"
"buffer_load_dwordx4 v5, s[8:11], s51 offen offset:3072 lds\n"
"v_mfma_f32_16x16x32_bf16 a[212:215], a[108:111], v[44:47], a[212:215]\n"
"s_add_u32 s51, s51, s27\n"
"v_mfma_f32_16x16x32_bf16 a[216:219], a[112:115], v[40:43], a[216:219]\n"
"v_mfma_f32_16x16x32_bf16 a[216:219], a[116:119], v[44:47], a[216:219]\n"
"v_mfma_f32_16x16x32_bf16 a[220:223], a[120:123], v[40:43], a[220:223]\n"
"v_mfma_f32_16x16x32_bf16 a[220:223], a[124:127], v[44:47], a[220:223]\n"
"s_waitcnt vmcnt(16) lgkmcnt(0)\n"
"v_mfma_f32_16x16x32_bf16 a[224:227], a[64:67], v[48:51], a[224:227]\n"
"s_barrier\n"
"v_mfma_f32_16x16x32_bf16 a[224:227], a[68:71], v[52:55], a[224:227]\n"
"ds_read_b64_tr_b16 v[40:41], v12\n"
"ds_read_b64_tr_b16 v[42:43], v12 offset:1024\n"
"v_mfma_f32_16x16x32_bf16 a[228:231], a[72:75], v[48:51], a[228:231]\n"
"ds_read_b64_tr_b16 v[44:45], v12 offset:8192\n"
"ds_read_b64_tr_b16 v[46:47], v12 offset:9216\n"
"v_mfma_f32_16x16x32_bf16 a[228:231], a[76:79], v[52:55], a[228:231]\n"
"ds_read_b128 a[64:67], v10\n"
"s_add_u32 m0, s78, 0x14000\n"
"v_mfma_f32_16x16x32_bf16 a[232:235], a[80:83], v[48:51], a[232:235]\n"
"ds_read_b128 a[68:71], v11\n"
"buffer_load_dwordx4 v6, s[12:15], s52 offen lds\n"
"v_mfma_f32_16x16x32_bf16 a[232:235], a[84:87], v[52:55], a[232:235]\n"
"ds_read_b128 a[72:75], v10 offset:2048\n"
"v_mfma_f32_16x16x32_bf16 a[236:239], a[88:91], v[48:51], a[236:239]\n"
"ds_read_b128 a[76:79], v11 offset:2048\n"
"v_mfma_f32_16x16x32_bf16 a[236:239], a[92:95], v[52:55], a[236:239]\n"
"ds_read_b128 a[80:83], v10 offset:4096\n"
"v_mfma_f32_16x16x32_bf16 a[240:243], a[96:99], v[48:51], a[240:243]\n"
"ds_read_b128 a[84:87], v11 offset:4096\n"
"v_mfma_f32_16x16x32_bf16 a[240:243], a[100:103], v[52:55], a[240:243]\n"
"ds_read_b128 a[88:91], v10 offset:6144\n"
"v_mfma_f32_16x16x32_bf16 a[244:247], a[104:107], v[48:51], a[244:247]\n"
"ds_read_b128 a[92:95], v11 offset:6144\n"
"buffer_load_dwordx4 v7, s[12:15], s52 offen offset:1024 lds\n"
"v_mfma_f32_16x16x32_bf16 a[244:247], a[108:111], v[52:55], a[244:247]\n"
"ds_read_b128 a[96:99], v10 offset:16384\n"
"v_mfma_f32_16x16x32_bf16 a[248:251], a[112:115], v[48:51], a[248:251]\n"
"ds_read_b128 a[100:103], v11 offset:16384\n"
"v_mfma_f32_16x16x32_bf16 a[248:251], a[116:119], v[52:55], a[248:251]\n"
"ds_read_b128 a[104:107], v10 offset:18432\n"
"v_mfma_f32_16x16x32_bf16 a[252:255], a[120:123], v[48:51], a[252:255]\n"
"ds_read_b128 a[108:111], v11 offset:18432\n"
"v_mfma_f32_16x16x32_bf16 a[252:255], a[124:127], v[52:55], a[252:255]\n"
"ds_read_b128 a[112:115], v10 offset:20480\n"
"s_waitcnt lgkmcnt(9)\n"
"v_mfma_f32_16x16x32_bf16 v[128:131], v[40:43], a[64:67], v[128:131]\n"
"ds_read_b128 a[116:119], v11 offset:20480\n"
"v_mfma_f32_16x16x32_bf16 v[128:131], v[44:47], a[68:71], v[128:131]\n"
"ds_read_b128 a[120:123], v10 offset:22528\n"
"v_mfma_f32_16x16x32_bf16 v[132:135], v[40:43], a[72:75], v[132:135]\n"
"ds_read_b128 a[124:127], v11 offset:22528\n"
"buffer_load_dwordx4 v8, s[12:15], s52 offen offset:2048 lds\n"
"v_mfma_f32_16x16x32_bf16 v[132:135], v[44:47], a[76:79], v[132:135]\n"
"ds_read_b64_tr_b16 v[48:49], v13\n"
"s_waitcnt lgkmcnt(9)\n"
"v_mfma_f32_16x16x32_bf16 v[136:139], v[40:43], a[80:83], v[136:139]\n"
"ds_read_b64_tr_b16 v[50:51], v13 offset:1024\n"
"v_mfma_f32_16x16x32_bf16 v[136:139], v[44:47], a[84:87], v[136:139]\n"
"ds_read_b64_tr_b16 v[52:53], v13 offset:8192\n"
"v_mfma_f32_16x16x32_bf16 v[140:143], v[40:43], a[88:91], v[140:143]\n"
"ds_read_b64_tr_b16 v[54:55], v13 offset:9216\n"
"v_mfma_f32_16x16x32_bf16 v[140:143], v[44:47], a[92:95], v[140:143]\n"
"s_waitcnt lgkmcnt(8)\n"
"v_mfma_f32_16x16x32_bf16 v[144:147], v[40:43], a[96:99], v[144:147]\n"
"v_mfma_f32_16x16x32_bf16 v[144:147], v[44:47], a[100:103], v[144:147]\n"
"v_mfma_f32_16x16x32_bf16 v[148:151], v[40:43], a[104:107], v[148:151]\n"
"buffer_load_dwordx4 v9, s[12:15], s52 offen offset:3072 lds\n"
"v_mfma_f32_16x16x32_bf16 v[148:151], v[44:47], a[108:111], v[148:151]\n"
"s_add_u32 s52, s52, s31\n"
"s_waitcnt lgkmcnt(4)\n"
"v_mfma_f32_16x16x32_bf16 v[152:155], v[40:43], a[112:115], v[152:155]\n"
"v_mfma_f32_16x16x32_bf16 v[152:155], v[44:47], a[116:119], v[152:155]\n"
"v_mfma_f32_16x16x32_bf16 v[156:159], v[40:43], a[120:123], v[156:159]\n"
"v_mfma_f32_16x16x32_bf16 v[156:159], v[44:47], a[124:127], v[156:159]\n"
"s_waitcnt lgkmcnt(0)\n"
"v_mfma_f32_16x16x32_bf16 v[160:163], v[48:51], a[64:67], v[160:163]\n"
"v_mfma_f32_16x16x32_bf16 v[160:163], v[52:55], a[68:71], v[160:163]\n"
"ds_read_b64_tr_b16 v[40:41], v14\n"
"s_add_u32 m0, s78, 0x1c000\n"
"v_mfma_f32_16x16x32_bf16 v[164:167], v[48:51], a[72:75], v[164:167]\n"
"buffer_load_dwordx4 v6, s[16:19], s53 offen lds\n"
"v_mfma_f32_16x16x32_bf16 v[164:167], v[52:55], a[76:79], v[164:167]\n"
"ds_read_b64_tr_b16 v[42:43], v14 offset:1024\n"
"v_mfma_f32_16x16x32_bf16 v[168:171], v[48:51], a[80:83], v[168:171]\n"
"v_mfma_f32_16x16x32_bf16 v[168:171], v[52:55], a[84:87], v[168:171]\n"
"ds_read_b64_tr_b16 v[44:45], v14 offset:8192\n"
"v_mfma_f32_16x16x32_bf16 v[172:175], v[48:51], a[88:91], v[172:175]\n"
"v_mfma_f32_16x16x32_bf16 v[172:175], v[52:55], a[92:95], v[172:175]\n"
"ds_read_b64_tr_b16 v[46:47], v14 offset:9216\n"
"v_mfma_f32_16x16x32_bf16 v[176:179], v[48:51], a[96:99], v[176:179]\n"
"v_mfma_f32_16x16x32_bf16 v[176:179], v[52:55], a[100:103], v[176:179]\n"
"v_mfma_f32_16x16x32_bf16 v[180:183], v[48:51], a[104:107], v[180:183]\n"
"buffer_load_dwordx4 v7, s[16:19], s53 offen offset:1024 lds\n"
"v_mfma_f32_16x16x32_bf16 v[180:183], v[52:55], a[108:111], v[180:183]\n"
"v_mfma_f32_16x16x32_bf16 v[184:187], v[48:51], a[112:115], v[184:187]\n"
"v_mfma_f32_16x16x32_bf16 v[184:187], v[52:55], a[116:119], v[184:187]\n"
"v_mfma_f32_16x16x32_bf16 v[188:191], v[48:51], a[120:123], v[188:191]\n"
"s_waitcnt vmcnt(11)\n"
"v_lshlrev_b32 v56, 16, v116\n"
"v_mfma_f32_16x16x32_bf16 v[188:191], v[52:55], a[124:127], v[188:191]\n"
"v_and_b32 v57, 0xffff0000, v116\n"
"s_waitcnt lgkmcnt(0)\n"
"v_mfma_f32_16x16x32_bf16 v[192:195], v[40:43], a[64:67], v[192:195]\n"
"v_lshlrev_b32 v58, 16, v117\n"
"v_and_b32 v59, 0xffff0000, v117\n"
"v_mfma_f32_16x16x32_bf16 v[192:195], v[44:47], a[68:71], v[192:195]\n"
"ds_read_b64_tr_b16 v[48:49], v15\n"
"v_lshlrev_b32 v60, 16, v118\n"
"v_mfma_f32_16x16x32_bf16 v[196:199], v[40:43], a[72:75], v[196:199]\n"
"buffer_load_dwordx4 v8, s[16:19], s53 offen offset:2048 lds\n"
"v_and_b32 v61, 0xffff0000, v118\n"
"v_mfma_f32_16x16x32_bf16 v[196:199], v[44:47], a[76:79], v[196:199]\n"
"ds_read_b64_tr_b16 v[50:51], v15 offset:1024\n"
"v_lshlrev_b32 v62, 16, v119\n"
"v_mfma_f32_16x16x32_bf16 v[200:203], v[40:43], a[80:83], v[200:203]\n"
"v_and_b32 v63, 0xffff0000, v119\n"
"v_lshlrev_b32 v64, 16, v120\n"
"v_mfma_f32_16x16x32_bf16 v[200:203], v[44:47], a[84:87], v[200:203]\n"
"ds_read_b64_tr_b16 v[52:53], v15 offset:8192\n"
"v_and_b32 v65, 0xffff0000, v120\n"
"v_mfma_f32_16x16x32_bf16 v[204:207], v[40:43], a[88:91], v[204:207]\n"
"v_lshlrev_b32 v66, 16, v121\n"
"v_and_b32 v67, 0xffff0000, v121\n"
"v_mfma_f32_16x16x32_bf16 v[204:207], v[44:47], a[92:95], v[204:207]\n"
"ds_read_b64_tr_b16 v[54:55], v15 offset:9216\n"
"v_lshlrev_b32 v68, 16, v122\n"
"v_mfma_f32_16x16x32_bf16 v[208:211], v[40:43], a[96:99], v[208:211]\n"
"v_and_b32 v69, 0xffff0000, v122\n"
"v_lshlrev_b32 v70, 16, v123\n"
"v_mfma_f32_16x16x32_bf16 v[208:211], v[44:47], a[100:103], v[208:211]\n"
"v_and_b32 v71, 0xffff0000, v123\n"
"v_lshlrev_b32 v72, 16, v124\n"
"v_mfma_f32_16x16x32_bf16 v[212:215], v[40:43], a[104:107], v[212:215]\n"
"buffer_load_dwordx4 v9, s[16:19], s53 offen offset:3072 lds\n"
"v_and_b32 v73, 0xffff0000, v124\n"
"v_mfma_f32_16x16x32_bf16 v[212:215], v[44:47], a[108:111], v[212:215]\n"
"s_add_u32 s53, s53, s31\n"
"v_lshlrev_b32 v74, 16, v125\n"
"v_mfma_f32_16x16x32_bf16 v[216:219], v[40:43], a[112:115], v[216:219]\n"
"v_and_b32 v75, 0xffff0000, v125\n"
"v_lshlrev_b32 v76, 16, v126\n"
"v_mfma_f32_16x16x32_bf16 v[216:219], v[44:47], a[116:119], v[216:219]\n"
"v_and_b32 v77, 0xffff0000, v126\n"
"v_lshlrev_b32 v78, 16, v127\n"
"v_mfma_f32_16x16x32_bf16 v[220:223], v[40:43], a[120:123], v[220:223]\n"
"v_and_b32 v79, 0xffff0000, v127\n"
"v_add_f32 v36, v56, v57\n"
"v_mfma_f32_16x16x32_bf16 v[220:223], v[44:47], a[124:127], v[220:223]\n"
"v_mul_f32 v37, v56, v56\n"
"s_waitcnt lgkmcnt(0)\n"
"v_mfma_f32_16x16x32_bf16 v[224:227], v[48:51], a[64:67], v[224:227]\n"
"s_barrier\n"
"v_fmac_f32 v37, v57, v57\n"
"v_mfma_f32_16x16x32_bf16 v[224:227], v[52:55], a[68:71], v[224:227]\n"
"ds_read_b64_tr_b16 v[40:41], v12 offset:32768\n"
"s_add_u32 m0, s54, 0\n"
"v_mfma_f32_16x16x32_bf16 v[228:231], v[48:51], a[72:75], v[228:231]\n"
"buffer_load_dwordx4 v2, s[8:11], s50 offen lds\n"
"v_add_f32 v36, v36, v58\n"
"v_mfma_f32_16x16x32_bf16 v[228:231], v[52:55], a[76:79], v[228:231]\n"
"ds_read_b64_tr_b16 v[42:43], v12 offset:33792\n"
"v_fmac_f32 v37, v58, v58\n"
"v_mfma_f32_16x16x32_bf16 v[232:235], v[48:51], a[80:83], v[232:235]\n"
"v_add_f32 v36, v36, v59\n"
"v_fmac_f32 v37, v59, v59\n"
"v_mfma_f32_16x16x32_bf16 v[232:235], v[52:55], a[84:87], v[232:235]\n"
"ds_read_b64_tr_b16 v[44:45], v12 offset:40960\n"
"v_add_f32 v36, v36, v60\n"
"v_mfma_f32_16x16x32_bf16 v[236:239], v[48:51], a[88:91], v[236:239]\n"
"v_fmac_f32 v37, v60, v60\n"
"v_add_f32 v36, v36, v61\n"
"v_mfma_f32_16x16x32_bf16 v[236:239], v[52:55], a[92:95], v[236:239]\n"
"ds_read_b64_tr_b16 v[46:47], v12 offset:41984\n"
"v_fmac_f32 v37, v61, v61\n"
"v_mfma_f32_16x16x32_bf16 v[240:243], v[48:51], a[96:99], v[240:243]\n"
"v_add_f32 v36, v36, v62\n"
"v_fmac_f32 v37, v62, v62\n"
"v_mfma_f32_16x16x32_bf16 v[240:243], v[52:55], a[100:103], v[240:243]\n"
"v_add_f32 v36, v36, v63\n"
"v_fmac_f32 v37, v63, v63\n"
"v_mfma_f32_16x16x32_bf16 v[244:247], v[48:51], a[104:107], v[244:247]\n"
"buffer_load_dwordx4 v3, s[8:11], s50 offen offset:1024 lds\n"
"v_add_f32 v36, v36, v64\n"
"v_mfma_f32_16x16x32_bf16 v[244:247], v[52:55], a[108:111], v[244:247]\n"
"v_fmac_f32 v37, v64, v64\n"
"v_add_f32 v36, v36, v65\n"
"v_mfma_f32_16x16x32_bf16 v[248:251], v[48:51], a[112:115], v[248:251]\n"
"v_fmac_f32 v37, v65, v65\n"
"v_add_f32 v36, v36, v66\n"
"v_mfma_f32_16x16x32_bf16 v[248:251], v[52:55], a[116:119], v[248:251]\n"
"v_fmac_f32 v37, v66, v66\n"
"v_add_f32 v36, v36, v67\n"
"v_mfma_f32_16x16x32_bf16 v[252:255], v[48:51], a[120:123], v[252:255]\n"
"v_fmac_f32 v37, v67, v67\n"
"v_add_f32 v36, v36, v68\n"
"v_mfma_f32_16x16x32_bf16 v[252:255], v[52:55], a[124:127], v[252:255]\n"
"v_fmac_f32 v37, v68, v68\n"
"s_waitcnt lgkmcnt(0)\n"
"v_mfma_f32_16x16x32_bf16 a[128:131], a[64:67], v[40:43], a[128:131]\n"
"v_add_f32 v36, v36, v69\n"
"v_fmac_f32 v37, v69, v69\n"
"v_mfma_f32_16x16x32_bf16 a[128:131], a[68:71], v[44:47], a[128:131]\n"
"ds_read_b64_tr_b16 v[48:49], v13 offset:32768\n"
"v_add_f32 v36, v36, v70\n"
"v_mfma_f32_16x16x32_bf16 a[132:135], a[72:75], v[40:43], a[132:135]\n"
"buffer_load_dwordx4 v4, s[8:11], s50 offen offset:2048 lds\n"
"v_fmac_f32 v37, v70, v70\n"
"v_mfma_f32_16x16x32_bf16 a[132:135], a[76:79], v[44:47], a[132:135]\n"
"ds_read_b64_tr_b16 v[50:51], v13 offset:33792\n"
"v_add_f32 v36, v36, v71\n"
"v_mfma_f32_16x16x32_bf16 a[136:139], a[80:83], v[40:43], a[136:139]\n"
"v_fmac_f32 v37, v71, v71\n"
"v_add_f32 v36, v36, v72\n"
"v_mfma_f32_16x16x32_bf16 a[136:139], a[84:87], v[44:47], a[136:139]\n"
"ds_read_b64_tr_b16 v[52:53], v13 offset:40960\n"
"v_fmac_f32 v37, v72, v72\n"
"v_mfma_f32_16x16x32_bf16 a[140:143], a[88:91], v[40:43], a[140:143]\n"
"buffer_load_dwordx2 v[92:93], v101, s[4:7], s61 offen sc1\n"
"v_add_f32 v36, v36, v73\n"
"v_mfma_f32_16x16x32_bf16 a[140:143], a[92:95], v[44:47], a[140:143]\n"
"ds_read_b64_tr_b16 v[54:55], v13 offset:41984\n"
"v_fmac_f32 v37, v73, v73\n"
"v_mfma_f32_16x16x32_bf16 a[144:147], a[96:99], v[40:43], a[144:147]\n"
"v_add_f32 v36, v36, v74\n"
"v_fmac_f32 v37, v74, v74\n"
"v_mfma_f32_16x16x32_bf16 a[144:147], a[100:103], v[44:47], a[144:147]\n"
"v_add_f32 v36, v36, v75\n"
"v_fmac_f32 v37, v75, v75\n"
"v_mfma_f32_16x16x32_bf16 a[148:151], a[104:107], v[40:43], a[148:151]\n"
"buffer_load_dwordx4 v5, s[8:11], s50 offen offset:3072 lds\n"
"v_add_f32 v36, v36, v76\n"
"v_mfma_f32_16x16x32_bf16 a[148:151], a[108:111], v[44:47], a[148:151]\n"
"s_add_u32 s50, s50, s26\n"
"v_fmac_f32 v37, v76, v76\n"
"v_mfma_f32_16x16x32_bf16 a[152:155], a[112:115], v[40:43], a[152:155]\n"
"v_add_f32 v36, v36, v77\n"
"v_fmac_f32 v37, v77, v77\n"
"v_mfma_f32_16x16x32_bf16 a[152:155], a[116:119], v[44:47], a[152:155]\n"
"v_add_f32 v36, v36, v78\n"
"v_fmac_f32 v37, v78, v78\n"
"v_mfma_f32_16x16x32_bf16 a[156:159], a[120:123], v[40:43], a[156:159]\n"
"v_add_f32 v36, v36, v79\n"
"v_fmac_f32 v37, v79, v79\n"
"v_mfma_f32_16x16x32_bf16 a[156:159], a[124:127], v[44:47], a[156:159]\n"
"s_mov_b32 s56, 0x30000\n"
"s_waitcnt lgkmcnt(0)\n"
"v_mfma_f32_16x16x32_bf16 a[160:163], a[64:67], v[48:51], a[160:163]\n"
"v_add_f32_dpp v38, v36, v36 row_ror:8 row_mask:0xf bank_mask:0xf\n"
"v_add_f32_dpp v39, v37, v37 row_ror:8 row_mask:0xf bank_mask:0xf\n"
"v_mfma_f32_16x16x32_bf16 a[160:163], a[68:71], v[52:55], a[160:163]\n"
"ds_read_b64_tr_b16 v[40:41], v14 offset:32768\n"
"s_add_u32 m0, s54, 0x1000\n"
"v_mfma_f32_16x16x32_bf16 a[164:167], a[72:75], v[48:51], a[164:167]\n"
"buffer_load_dwordx4 v2, s[8:11], s51 offen lds\n"
"v_mfma_f32_16x16x32_bf16 a[164:167], a[76:79], v[52:55], a[164:167]\n"
"ds_read_b64_tr_b16 v[42:43], v14 offset:33792\n"
"v_mfma_f32_16x16x32_bf16 a[168:171], a[80:83], v[48:51], a[168:171]\n"
"v_mov_b32 v36, v38\n"
"v_mov_b32 v37, v39\n"
"v_mfma_f32_16x16x32_bf16 a[168:171], a[84:87], v[52:55], a[168:171]\n"
"ds_read_b64_tr_b16 v[44:45], v14 offset:40960\n"
"v_mfma_f32_16x16x32_bf16 a[172:175], a[88:91], v[48:51], a[172:175]\n"
"v_permlane16_swap_b32 v36, v38\n"
"v_permlane16_swap_b32 v37, v39\n"
"v_mfma_f32_16x16x32_bf16 a[172:175], a[92:95], v[52:55], a[172:175]\n"
"ds_read_b64_tr_b16 v[46:47], v14 offset:41984\n"
"v_mfma_f32_16x16x32_bf16 a[176:179], a[96:99], v[48:51], a[176:179]\n"
"v_add_f32 v36, v36, v38\n"
"v_add_f32 v37, v37, v39\n"
"v_mfma_f32_16x16x32_bf16 a[176:179], a[100:103], v[52:55], a[176:179]\n"
"v_mov_b32 v38, v36\n"
"v_mov_b32 v39, v37\n"
"v_mfma_f32_16x16x32_bf16 a[180:183], a[104:107], v[48:51], a[180:183]\n"
"buffer_load_dwordx4 v3, s[8:11], s51 offen offset:1024 lds\n"
"v_mfma_f32_16x16x32_bf16 a[180:183], a[108:111], v[52:55], a[180:183]\n"
"v_permlane32_swap_b32 v36, v38\n"
"v_permlane32_swap_b32 v37, v39\n"
"v_mfma_f32_16x16x32_bf16 a[184:187], a[112:115], v[48:51], a[184:187]\n"
"v_add_f32 v36, v36, v38\n"
"v_add_f32 v37, v37, v39\n"
"v_mfma_f32_16x16x32_bf16 a[184:187], a[116:119], v[52:55], a[184:187]\n"
"s_lshl_b32 s62, s3, 6\n"
"v_add_u32 v38, s62, v30\n"
"v_mfma_f32_16x16x32_bf16 a[188:191], a[120:123], v[48:51], a[188:191]\n"
"ds_write_b64 v38, v[36:37]\n"
"v_mfma_f32_16x16x32_bf16 a[188:191], a[124:127], v[52:55], a[188:191]\n"
"s_waitcnt lgkmcnt(1)\n"
"v_mfma_f32_16x16x32_bf16 a[192:195], a[64:67], v[40:43], a[192:195]\n"
"v_mfma_f32_16x16x32_bf16 a[192:195], a[68:71], v[44:47], a[192:195]\n"
"ds_read_b64_tr_b16 v[48:49], v15 offset:32768\n"
"v_mfma_f32_16x16x32_bf16 a[196:199], a[72:75], v[40:43], a[196:199]\n"
"buffer_load_dwordx4 v4, s[8:11], s51 offen offset:2048 lds\n"
"v_mfma_f32_16x16x32_bf16 a[196:199], a[76:79], v[44:47], a[196:199]\n"
"ds_read_b64_tr_b16 v[50:51], v15 offset:33792\n"
"v_mfma_f32_16x16x32_bf16 a[200:203], a[80:83], v[40:43], a[200:203]\n"
"v_mfma_f32_16x16x32_bf16 a[200:203], a[84:87], v[44:47], a[200:203]\n"
"ds_read_b64_tr_b16 v[52:53], v15 offset:40960\n"
"v_mfma_f32_16x16x32_bf16 a[204:207], a[88:91], v[40:43], a[204:207]\n"
"buffer_load_dwordx4 v[32:35], v1, s[4:7], s56 offen sc1\n"
"v_mfma_f32_16x16x32_bf16 a[204:207], a[92:95], v[44:47], a[204:207]\n"
"ds_read_b64_tr_b16 v[54:55], v15 offset:41984\n"
"v_mfma_f32_16x16x32_bf16 a[208:211], a[96:99], v[40:43], a[208:211]\n"
"v_mfma_f32_16x16x32_bf16 a[208:211], a[100:103], v[44:47], a[208:211]\n"
"v_mfma_f32_16x16x32_bf16 a[212:215], a[104:107], v[40:43], a[212:215]\n"
"buffer_load_dwordx4 v5, s[8:11], s51 offen offset:3072 lds\n"
"v_mfma_f32_16x16x32_bf16 a[212:215], a[108:111], v[44:47], a[212:215]\n"
"s_add_u32 s51, s51, s26\n"
"v_mfma_f32_16x16x32_bf16 a[216:219], a[112:115], v[40:43], a[216:219]\n"
"v_mfma_f32_16x16x32_bf16 a[216:219], a[116:119], v[44:47], a[216:219]\n"
"v_mfma_f32_16x16x32_bf16 a[220:223], a[120:123], v[40:43], a[220:223]\n"
"v_mfma_f32_16x16x32_bf16 a[220:223], a[124:127], v[44:47], a[220:223]\n"
"s_waitcnt vmcnt(14) lgkmcnt(0)\n"
"v_mfma_f32_16x16x32_bf16 a[224:227], a[64:67], v[48:51], a[224:227]\n"
"s_barrier\n"
"v_mfma_f32_16x16x32_bf16 a[224:227], a[68:71], v[52:55], a[224:227]\n"
"ds_read_b64_tr_b16 v[40:41], v12 offset:16384\n"
"ds_read_b64_tr_b16 v[42:43], v12 offset:17408\n"
"v_mfma_f32_16x16x32_bf16 a[228:231], a[72:75], v[48:51], a[228:231]\n"
"ds_read_b64_tr_b16 v[44:45], v12 offset:24576\n"
"ds_read_b64_tr_b16 v[46:47], v12 offset:25600\n"
"v_mfma_f32_16x16x32_bf16 a[228:231], a[76:79], v[52:55], a[228:231]\n"
"ds_read_b128 a[64:67], v10 offset:32768\n"
"s_add_u32 m0, s78, 0x10000\n"
"v_mfma_f32_16x16x32_bf16 a[232:235], a[80:83], v[48:51], a[232:235]\n"
"ds_read_b128 a[68:71], v11 offset:32768\n"
"buffer_load_dwordx4 v6, s[12:15], s52 offen lds\n"
"v_mfma_f32_16x16x32_bf16 a[232:235], a[84:87], v[52:55], a[232:235]\n"
"ds_read_b128 a[72:75], v10 offset:34816\n"
"v_mfma_f32_16x16x32_bf16 a[236:239], a[88:91], v[48:51], a[236:239]\n"
"ds_read_b128 a[76:79], v11 offset:34816\n"
"v_mfma_f32_16x16x32_bf16 a[236:239], a[92:95], v[52:55], a[236:239]\n"
"ds_read_b128 a[80:83], v10 offset:36864\n"
"v_mfma_f32_16x16x32_bf16 a[240:243], a[96:99], v[48:51], a[240:243]\n"
"ds_read_b128 a[84:87], v11 offset:36864\n"
"v_mfma_f32_16x16x32_bf16 a[240:243], a[100:103], v[52:55], a[240:243]\n"
"ds_read_b128 a[88:91], v10 offset:38912\n"
"v_mfma_f32_16x16x32_bf16 a[244:247], a[104:107], v[48:51], a[244:247]\n"
"ds_read_b128 a[92:95], v11 offset:38912\n"
"buffer_load_dwordx4 v7, s[12:15], s52 offen offset:1024 lds\n"
"v_mfma_f32_16x16x32_bf16 a[244:247], a[108:111], v[52:55], a[244:247]\n"
"ds_read_b128 a[96:99], v10 offset:49152\n"
"v_mfma_f32_16x16x32_bf16 a[248:251], a[112:115], v[48:51], a[248:251]\n"
"ds_read_b128 a[100:103], v11 offset:49152\n"
"v_mfma_f32_16x16x32_bf16 a[248:251], a[116:119], v[52:55], a[248:251]\n"
"ds_read_b128 a[104:107], v10 offset:51200\n"
"v_mfma_f32_16x16x32_bf16 a[252:255], a[120:123], v[48:51], a[252:255]\n"
"ds_read_b128 a[108:111], v11 offset:51200\n"
"v_mfma_f32_16x16x32_bf16 a[252:255], a[124:127], v[52:55], a[252:255]\n"
"ds_read_b128 a[112:115], v10 offset:53248\n"
"s_waitcnt lgkmcnt(9)\n"
"v_mfma_f32_16x16x32_bf16 v[128:131], v[40:43], a[64:67], v[128:131]\n"
"ds_read_b128 a[116:119], v11 offset:53248\n"
"v_mfma_f32_16x16x32_bf16 v[128:131], v[44:47], a[68:71], v[128:131]\n"
"ds_read_b128 a[120:123], v10 offset:55296\n"
"v_mfma_f32_16x16x32_bf16 v[132:135], v[40:43], a[72:75], v[132:135]\n"
"ds_read_b128 a[124:127], v11 offset:55296\n"
"buffer_load_dwordx4 v8, s[12:15], s52 offen offset:2048 lds\n"
"v_mfma_f32_16x16x32_bf16 v[132:135], v[44:47], a[76:79], v[132:135]\n"
"ds_read_b64_tr_b16 v[48:49], v13 offset:16384\n"
"s_waitcnt lgkmcnt(9)\n"
"v_mfma_f32_16x16x32_bf16 v[136:139], v[40:43], a[80:83], v[136:139]\n"
"ds_read_b64_tr_b16 v[50:51], v13 offset:17408\n"
"v_mfma_f32_16x16x32_bf16 v[136:139], v[44:47], a[84:87], v[136:139]\n"
"ds_read_b64_tr_b16 v[52:53], v13 offset:24576\n"
"v_mfma_f32_16x16x32_bf16 v[140:143], v[40:43], a[88:91], v[140:143]\n"
"ds_read_b64_tr_b16 v[54:55], v13 offset:25600\n"
"v_mfma_f32_16x16x32_bf16 v[140:143], v[44:47], a[92:95], v[140:143]\n"
"s_waitcnt lgkmcnt(8)\n"
"v_mfma_f32_16x16x32_bf16 v[144:147], v[40:43], a[96:99], v[144:147]\n"
"s_waitcnt vmcnt(9)\n"
"v_cmp_ne_u32 vcc, s82, v93\n"
"s_nop 0\n"
"s_cbranch_vccnz .Ladaln_block_P_par\n"
".Ladaln_block_P_par_R:\n"
"v_mfma_f32_16x16x32_bf16 v[144:147], v[44:47], a[100:103], v[144:147]\n"
"v_mov_b32 v94, v92\n"
"v_mfma_f32_16x16x32_bf16 v[148:151], v[40:43], a[104:107], v[148:151]\n"
"buffer_load_dwordx4 v9, s[12:15], s52 offen offset:3072 lds\n"
"v_mfma_f32_16x16x32_bf16 v[148:151], v[44:47], a[108:111], v[148:151]\n"
"s_add_u32 s52, s52, s30\n"
"s_waitcnt lgkmcnt(4)\n"
"v_mfma_f32_16x16x32_bf16 v[152:155], v[40:43], a[112:115], v[152:155]\n"
"v_permlane16_swap_b32 v92, v94\n"
"v_mfma_f32_16x16x32_bf16 v[152:155], v[44:47], a[116:119], v[152:155]\n"
"v_mfma_f32_16x16x32_bf16 v[156:159], v[40:43], a[120:123], v[156:159]\n"
"v_add_f32 v92, v92, v94\n"
"v_mov_b32 v94, v92\n"
"v_mfma_f32_16x16x32_bf16 v[156:159], v[44:47], a[124:127], v[156:159]\n"
"s_waitcnt lgkmcnt(0)\n"
"v_mfma_f32_16x16x32_bf16 v[160:163], v[48:51], a[64:67], v[160:163]\n"
"v_permlane32_swap_b32 v92, v94\n"
"v_mfma_f32_16x16x32_bf16 v[160:163], v[52:55], a[68:71], v[160:163]\n"
"ds_read_b64_tr_b16 v[40:41], v14 offset:16384\n"
"s_add_u32 m0, s78, 0x18000\n"
"v_mfma_f32_16x16x32_bf16 v[164:167], v[48:51], a[72:75], v[164:167]\n"
"buffer_load_dwordx4 v6, s[16:19], s53 offen lds\n"
"v_mfma_f32_16x16x32_bf16 v[164:167], v[52:55], a[76:79], v[164:167]\n"
"ds_read_b64_tr_b16 v[42:43], v14 offset:17408\n"
"v_mfma_f32_16x16x32_bf16 v[168:171], v[48:51], a[80:83], v[168:171]\n"
"v_add_f32 v92, v92, v94\n"
"v_cvt_pk_bf16_f32 v92, v92, v92\n"
"v_mfma_f32_16x16x32_bf16 v[168:171], v[52:55], a[84:87], v[168:171]\n"
"ds_read_b64_tr_b16 v[44:45], v14 offset:24576\n"
"v_mfma_f32_16x16x32_bf16 v[172:175], v[48:51], a[88:91], v[172:175]\n"
"v_lshlrev_b32 v92, 16, v92\n"
"v_add_f32 v92, s70, v92\n"
"v_mfma_f32_16x16x32_bf16 v[172:175], v[52:55], a[92:95], v[172:175]\n"
"ds_read_b64_tr_b16 v[46:47], v14 offset:25600\n"
"v_mfma_f32_16x16x32_bf16 v[176:179], v[48:51], a[96:99], v[176:179]\n"
"v_cvt_pk_bf16_f32 v94, v92, v92\n"
"v_lshlrev_b32 v94, 16, v94\n"
"v_mfma_f32_16x16x32_bf16 v[176:179], v[52:55], a[100:103], v[176:179]\n"
"v_mfma_f32_16x16x32_bf16 v[180:183], v[48:51], a[104:107], v[180:183]\n"
"buffer_load_dwordx4 v7, s[16:19], s53 offen offset:1024 lds\n"
"v_mov_b32_dpp v92, v94 quad_perm:[1,0,3,2] row_mask:0xf bank_mask:0xf\n"
"v_mfma_f32_16x16x32_bf16 v[180:183], v[52:55], a[108:111], v[180:183]\n"
"v_mfma_f32_16x16x32_bf16 v[184:187], v[48:51], a[112:115], v[184:187]\n"
"v_cvt_pk_bf16_f32 v92, v94, v92\n"
"v_mfma_f32_16x16x32_bf16 v[184:187], v[52:55], a[116:119], v[184:187]\n"
"buffer_store_dwordx2 v[92:93], v113, s[4:7], s71 offen sc1\n"
"v_mfma_f32_16x16x32_bf16 v[188:191], v[48:51], a[120:123], v[188:191]\n"
"buffer_store_dword v94, v112, s[4:7], s73 offen sc1\n"
"v_mfma_f32_16x16x32_bf16 v[188:191], v[52:55], a[124:127], v[188:191]\n"
"buffer_store_dword v92, v114, s[4:7], s72 offen sc1\n"
"s_waitcnt lgkmcnt(0)\n"
"v_mfma_f32_16x16x32_bf16 v[192:195], v[40:43], a[64:67], v[192:195]\n"
"v_mfma_f32_16x16x32_bf16 v[192:195], v[44:47], a[68:71], v[192:195]\n"
"ds_read_b64_tr_b16 v[48:49], v15 offset:16384\n"
"v_mfma_f32_16x16x32_bf16 v[196:199], v[40:43], a[72:75], v[196:199]\n"
"buffer_load_dwordx4 v8, s[16:19], s53 offen offset:2048 lds\n"
"v_mfma_f32_16x16x32_bf16 v[196:199], v[44:47], a[76:79], v[196:199]\n"
"ds_read_b64_tr_b16 v[50:51], v15 offset:17408\n"
"v_mfma_f32_16x16x32_bf16 v[200:203], v[40:43], a[80:83], v[200:203]\n"
"v_mfma_f32_16x16x32_bf16 v[200:203], v[44:47], a[84:87], v[200:203]\n"
"ds_read_b64_tr_b16 v[52:53], v15 offset:24576\n"
"v_mfma_f32_16x16x32_bf16 v[204:207], v[40:43], a[88:91], v[204:207]\n"
"v_mfma_f32_16x16x32_bf16 v[204:207], v[44:47], a[92:95], v[204:207]\n"
"ds_read_b64_tr_b16 v[54:55], v15 offset:25600\n"
"v_mfma_f32_16x16x32_bf16 v[208:211], v[40:43], a[96:99], v[208:211]\n"
"v_mfma_f32_16x16x32_bf16 v[208:211], v[44:47], a[100:103], v[208:211]\n"
"v_mfma_f32_16x16x32_bf16 v[212:215], v[40:43], a[104:107], v[212:215]\n"
"buffer_load_dwordx4 v9, s[16:19], s53 offen offset:3072 lds\n"
"v_mfma_f32_16x16x32_bf16 v[212:215], v[44:47], a[108:111], v[212:215]\n"
"s_add_u32 s53, s53, s30\n"
"v_mfma_f32_16x16x32_bf16 v[216:219], v[40:43], a[112:115], v[216:219]\n"
"v_mfma_f32_16x16x32_bf16 v[216:219], v[44:47], a[116:119], v[216:219]\n"
"v_mfma_f32_16x16x32_bf16 v[220:223], v[40:43], a[120:123], v[220:223]\n"
"v_mfma_f32_16x16x32_bf16 v[220:223], v[44:47], a[124:127], v[220:223]\n"
"s_waitcnt lgkmcnt(0)\n"
"v_mfma_f32_16x16x32_bf16 v[224:227], v[48:51], a[64:67], v[224:227]\n"
"s_barrier\n"
"v_mfma_f32_16x16x32_bf16 v[224:227], v[52:55], a[68:71], v[224:227]\n"
"ds_read_b64_tr_b16 v[40:41], v12 offset:49152\n"
"s_add_u32 m0, s54, 0x8000\n"
"v_mfma_f32_16x16x32_bf16 v[228:231], v[48:51], a[72:75], v[228:231]\n"
"buffer_load_dwordx4 v2, s[8:11], s50 offen lds\n"
"ds_read_b64 v[116:117], v30\n"
"v_mfma_f32_16x16x32_bf16 v[228:231], v[52:55], a[76:79], v[228:231]\n"
"ds_read_b64_tr_b16 v[42:43], v12 offset:50176\n"
"ds_read_b64 v[118:119], v30 offset:64\n"
"v_mfma_f32_16x16x32_bf16 v[232:235], v[48:51], a[80:83], v[232:235]\n"
"ds_read_b64 v[120:121], v30 offset:128\n"
"ds_read_b64 v[122:123], v30 offset:192\n"
"v_mfma_f32_16x16x32_bf16 v[232:235], v[52:55], a[84:87], v[232:235]\n"
"ds_read_b64_tr_b16 v[44:45], v12 offset:57344\n"
"v_mfma_f32_16x16x32_bf16 v[236:239], v[48:51], a[88:91], v[236:239]\n"
"v_mfma_f32_16x16x32_bf16 v[236:239], v[52:55], a[92:95], v[236:239]\n"
"ds_read_b64_tr_b16 v[46:47], v12 offset:58368\n"
"v_mfma_f32_16x16x32_bf16 v[240:243], v[48:51], a[96:99], v[240:243]\n"
"v_mfma_f32_16x16x32_bf16 v[240:243], v[52:55], a[100:103], v[240:243]\n"
"v_mfma_f32_16x16x32_bf16 v[244:247], v[48:51], a[104:107], v[244:247]\n"
"buffer_load_dwordx4 v3, s[8:11], s50 offen offset:1024 lds\n"
"s_waitcnt lgkmcnt(2)\n"
"v_mfma_f32_16x16x32_bf16 v[244:247], v[52:55], a[108:111], v[244:247]\n"
"v_add_f32 v116, v116, v118\n"
"v_add_f32 v117, v117, v119\n"
"v_mfma_f32_16x16x32_bf16 v[248:251], v[48:51], a[112:115], v[248:251]\n"
"v_add_f32 v116, v116, v120\n"
"v_add_f32 v117, v117, v121\n"
"v_mfma_f32_16x16x32_bf16 v[248:251], v[52:55], a[116:119], v[248:251]\n"
"v_add_f32 v116, v116, v122\n"
"v_add_f32 v117, v117, v123\n"
"v_mfma_f32_16x16x32_bf16 v[252:255], v[48:51], a[120:123], v[252:255]\n"
"v_mul_f32 v36, 0xbacccccd, v116\n"
"v_mul_f32 v37, 0x3acccccd, v117\n"
"v_mfma_f32_16x16x32_bf16 v[252:255], v[52:55], a[124:127], v[252:255]\n"
"v_fma_f32 v37, -v36, v36, v37\n"
"s_waitcnt lgkmcnt(0)\n"
"v_mfma_f32_16x16x32_bf16 a[128:131], a[64:67], v[40:43], a[128:131]\n"
"v_max_f32 v37, 0, v37\n"
"v_add_f32 v37, 0x3727c5ac, v37\n"
"v_mfma_f32_16x16x32_bf16 a[128:131], a[68:71], v[44:47], a[128:131]\n"
"ds_read_b64_tr_b16 v[48:49], v13 offset:49152\n"
"v_mfma_f32_16x16x32_bf16 a[132:135], a[72:75], v[40:43], a[132:135]\n"
"buffer_load_dwordx4 v4, s[8:11], s50 offen offset:2048 lds\n"
"v_mfma_f32_16x16x32_bf16 a[132:135], a[76:79], v[44:47], a[132:135]\n"
"ds_read_b64_tr_b16 v[50:51], v13 offset:50176\n"
"v_mfma_f32_16x16x32_bf16 a[136:139], a[80:83], v[40:43], a[136:139]\n"
"v_rsq_f32 v37, v37\n"
"v_mfma_f32_16x16x32_bf16 a[136:139], a[84:87], v[44:47], a[136:139]\n"
"ds_read_b64_tr_b16 v[52:53], v13 offset:57344\n"
"s_mul_i32 s74, s3, 0x280\n"
"v_mfma_f32_16x16x32_bf16 a[140:143], a[88:91], v[40:43], a[140:143]\n"
"v_mul_f32 v37, 0x3e0293ee, v37\n"
"s_add_u32 s74, s74, 0x18000\n"
"v_mfma_f32_16x16x32_bf16 a[140:143], a[92:95], v[44:47], a[140:143]\n"
"ds_read_b64_tr_b16 v[54:55], v13 offset:58368\n"
"v_mul_f32 v36, v36, v37\n"
"v_mfma_f32_16x16x32_bf16 a[144:147], a[96:99], v[40:43], a[144:147]\n"
"v_min_u32 v31, 0x270, v1\n"
"v_add_u32 v39, s69, v31\n"
"v_mfma_f32_16x16x32_bf16 a[144:147], a[100:103], v[44:47], a[144:147]\n"
"v_add_u32 v28, s69, v28\n"
"v_add_u32 v29, s69, v29\n"
"v_mfma_f32_16x16x32_bf16 a[148:151], a[104:107], v[40:43], a[148:151]\n"
"buffer_load_dwordx4 v5, s[8:11], s50 offen offset:3072 lds\n"
"v_fma_f32 v56, v56, v37, v36\n"
"v_mfma_f32_16x16x32_bf16 a[148:151], a[108:111], v[44:47], a[148:151]\n"
"s_add_u32 s50, s50, s27\n"
"v_fma_f32 v57, v57, v37, v36\n"
"v_mfma_f32_16x16x32_bf16 a[152:155], a[112:115], v[40:43], a[152:155]\n"
"v_fma_f32 v58, v58, v37, v36\n"
"v_fma_f32 v59, v59, v37, v36\n"
"v_mfma_f32_16x16x32_bf16 a[152:155], a[116:119], v[44:47], a[152:155]\n"
"v_fma_f32 v60, v60, v37, v36\n"
"v_fma_f32 v61, v61, v37, v36\n"
"v_mfma_f32_16x16x32_bf16 a[156:159], a[120:123], v[40:43], a[156:159]\n"
"v_fma_f32 v62, v62, v37, v36\n"
"v_fma_f32 v63, v63, v37, v36\n"
"v_mfma_f32_16x16x32_bf16 a[156:159], a[124:127], v[44:47], a[156:159]\n"
"v_fma_f32 v64, v64, v37, v36\n"
"s_waitcnt lgkmcnt(0)\n"
"v_mfma_f32_16x16x32_bf16 a[160:163], a[64:67], v[48:51], a[160:163]\n"
"v_fma_f32 v65, v65, v37, v36\n"
"v_fma_f32 v66, v66, v37, v36\n"
"v_mfma_f32_16x16x32_bf16 a[160:163], a[68:71], v[52:55], a[160:163]\n"
"ds_read_b64_tr_b16 v[40:41], v14 offset:49152\n"
"s_add_u32 m0, s54, 0x9000\n"
"v_mfma_f32_16x16x32_bf16 a[164:167], a[72:75], v[48:51], a[164:167]\n"
"buffer_load_dwordx4 v2, s[8:11], s51 offen lds\n"
"v_fma_f32 v67, v67, v37, v36\n"
"v_mfma_f32_16x16x32_bf16 a[164:167], a[76:79], v[52:55], a[164:167]\n"
"ds_read_b64_tr_b16 v[42:43], v14 offset:50176\n"
"v_fma_f32 v68, v68, v37, v36\n"
"v_mfma_f32_16x16x32_bf16 a[168:171], a[80:83], v[48:51], a[168:171]\n"
"v_fma_f32 v69, v69, v37, v36\n"
"v_fma_f32 v70, v70, v37, v36\n"
"v_mfma_f32_16x16x32_bf16 a[168:171], a[84:87], v[52:55], a[168:171]\n"
"ds_read_b64_tr_b16 v[44:45], v14 offset:57344\n"
"v_fma_f32 v71, v71, v37, v36\n"
"v_mfma_f32_16x16x32_bf16 a[172:175], a[88:91], v[48:51], a[172:175]\n"
"v_fma_f32 v72, v72, v37, v36\n"
"v_fma_f32 v73, v73, v37, v36\n"
"v_mfma_f32_16x16x32_bf16 a[172:175], a[92:95], v[52:55], a[172:175]\n"
"ds_read_b64_tr_b16 v[46:47], v14 offset:58368\n"
"v_fma_f32 v74, v74, v37, v36\n"
"v_mfma_f32_16x16x32_bf16 a[176:179], a[96:99], v[48:51], a[176:179]\n"
"v_fma_f32 v75, v75, v37, v36\n"
"v_fma_f32 v76, v76, v37, v36\n"
"v_mfma_f32_16x16x32_bf16 a[176:179], a[100:103], v[52:55], a[176:179]\n"
"v_fma_f32 v77, v77, v37, v36\n"
"v_fma_f32 v78, v78, v37, v36\n"
"v_mfma_f32_16x16x32_bf16 a[180:183], a[104:107], v[48:51], a[180:183]\n"
"buffer_load_dwordx4 v3, s[8:11], s51 offen offset:1024 lds\n"
"v_fma_f32 v79, v79, v37, v36\n"
"v_mfma_f32_16x16x32_bf16 a[180:183], a[108:111], v[52:55], a[180:183]\n"
"s_lshr_b32 s62, s2, 3\n"
"s_and_b32 s63, s62, 3\n"
"v_mfma_f32_16x16x32_bf16 a[184:187], a[112:115], v[48:51], a[184:187]\n"
"s_lshl3_add_u32 s62, s62, s63\n"
"s_lshl_b32 s62, s62, 14\n"
"v_mfma_f32_16x16x32_bf16 a[184:187], a[116:119], v[52:55], a[184:187]\n"
"s_bfe_u32 s63, s2, 0x20001\n"
"s_lshl_b32 s63, s63, 10\n"
"v_mfma_f32_16x16x32_bf16 a[188:191], a[120:123], v[48:51], a[188:191]\n"
"s_add_u32 s62, s62, s63\n"
"s_and_b32 s63, s2, 1\n"
"v_mfma_f32_16x16x32_bf16 a[188:191], a[124:127], v[52:55], a[188:191]\n"
"s_lshl_b32 s63, s63, 7\n"
"s_waitcnt lgkmcnt(0)\n"
"v_mfma_f32_16x16x32_bf16 a[192:195], a[64:67], v[40:43], a[192:195]\n"
"s_add_u32 s62, s62, s63\n"
"s_mul_i32 s63, s3, 0x5000\n"
"v_mfma_f32_16x16x32_bf16 a[192:195], a[68:71], v[44:47], a[192:195]\n"
"ds_read_b64_tr_b16 v[48:49], v15 offset:49152\n"
"s_add_u32 s62, s62, s63\n"
"v_mfma_f32_16x16x32_bf16 a[196:199], a[72:75], v[40:43], a[196:199]\n"
"buffer_load_dwordx4 v4, s[8:11], s51 offen offset:2048 lds\n"
"s_andn2_b32 s63, s62, 0x1fff\n"
"v_mfma_f32_16x16x32_bf16 a[196:199], a[76:79], v[44:47], a[196:199]\n"
"ds_read_b64_tr_b16 v[50:51], v15 offset:50176\n"
"s_add_u32 s62, s62, s63\n"
"v_mfma_f32_16x16x32_bf16 a[200:203], a[80:83], v[40:43], a[200:203]\n"
"s_add_u32 s62, s62, 0x800000\n"
"v_mfma_f32_16x16x32_bf16 a[200:203], a[84:87], v[44:47], a[200:203]\n"
"ds_read_b64_tr_b16 v[52:53], v15 offset:57344\n"
"v_mfma_f32_16x16x32_bf16 a[204:207], a[88:91], v[40:43], a[204:207]\n"
"v_mfma_f32_16x16x32_bf16 a[204:207], a[92:95], v[44:47], a[204:207]\n"
"ds_read_b64_tr_b16 v[54:55], v15 offset:58368\n"
"v_mfma_f32_16x16x32_bf16 a[208:211], a[96:99], v[40:43], a[208:211]\n"
"v_mfma_f32_16x16x32_bf16 a[208:211], a[100:103], v[44:47], a[208:211]\n"
"v_mfma_f32_16x16x32_bf16 a[212:215], a[104:107], v[40:43], a[212:215]\n"
"buffer_load_dwordx4 v5, s[8:11], s51 offen offset:3072 lds\n"
"v_mfma_f32_16x16x32_bf16 a[212:215], a[108:111], v[44:47], a[212:215]\n"
"s_add_u32 s51, s51, s27\n"
"v_mfma_f32_16x16x32_bf16 a[216:219], a[112:115], v[40:43], a[216:219]\n"
"v_mfma_f32_16x16x32_bf16 a[216:219], a[116:119], v[44:47], a[216:219]\n"
"v_mfma_f32_16x16x32_bf16 a[220:223], a[120:123], v[40:43], a[220:223]\n"
"v_mfma_f32_16x16x32_bf16 a[220:223], a[124:127], v[44:47], a[220:223]\n"
"s_waitcnt vmcnt(15) lgkmcnt(0)\n"
"v_mfma_f32_16x16x32_bf16 a[224:227], a[64:67], v[48:51], a[224:227]\n"
"s_barrier\n"
"v_mfma_f32_16x16x32_bf16 a[224:227], a[68:71], v[52:55], a[224:227]\n"
"ds_read_b64_tr_b16 v[40:41], v12\n"
"ds_read_b64_tr_b16 v[42:43], v12 offset:1024\n"
"v_mfma_f32_16x16x32_bf16 a[228:231], a[72:75], v[48:51], a[228:231]\n"
"ds_read_b64_tr_b16 v[44:45], v12 offset:8192\n"
"ds_read_b64_tr_b16 v[46:47], v12 offset:9216\n"
"v_mfma_f32_16x16x32_bf16 a[228:231], a[76:79], v[52:55], a[228:231]\n"
"ds_read_b128 a[64:67], v10\n"
"s_add_u32 m0, s78, 0x14000\n"
"v_mfma_f32_16x16x32_bf16 a[232:235], a[80:83], v[48:51], a[232:235]\n"
"ds_read_b128 a[68:71], v11\n"
"buffer_load_dwordx4 v6, s[12:15], s52 offen lds\n"
"v_mfma_f32_16x16x32_bf16 a[232:235], a[84:87], v[52:55], a[232:235]\n"
"ds_read_b128 a[72:75], v10 offset:2048\n"
"v_mfma_f32_16x16x32_bf16 a[236:239], a[88:91], v[48:51], a[236:239]\n"
"ds_read_b128 a[76:79], v11 offset:2048\n"
"v_mfma_f32_16x16x32_bf16 a[236:239], a[92:95], v[52:55], a[236:239]\n"
"ds_read_b128 a[80:83], v10 offset:4096\n"
"v_mfma_f32_16x16x32_bf16 a[240:243], a[96:99], v[48:51], a[240:243]\n"
"ds_read_b128 a[84:87], v11 offset:4096\n"
"v_mfma_f32_16x16x32_bf16 a[240:243], a[100:103], v[52:55], a[240:243]\n"
"ds_read_b128 a[88:91], v10 offset:6144\n"
"v_mfma_f32_16x16x32_bf16 a[244:247], a[104:107], v[48:51], a[244:247]\n"
"ds_read_b128 a[92:95], v11 offset:6144\n"
"buffer_load_dwordx4 v7, s[12:15], s52 offen offset:1024 lds\n"
"v_mfma_f32_16x16x32_bf16 a[244:247], a[108:111], v[52:55], a[244:247]\n"
"ds_read_b128 a[96:99], v10 offset:16384\n"
"v_mfma_f32_16x16x32_bf16 a[248:251], a[112:115], v[48:51], a[248:251]\n"
"ds_read_b128 a[100:103], v11 offset:16384\n"
"v_mfma_f32_16x16x32_bf16 a[248:251], a[116:119], v[52:55], a[248:251]\n"
"ds_read_b128 a[104:107], v10 offset:18432\n"
"v_mfma_f32_16x16x32_bf16 a[252:255], a[120:123], v[48:51], a[252:255]\n"
"ds_read_b128 a[108:111], v11 offset:18432\n"
"v_mfma_f32_16x16x32_bf16 a[252:255], a[124:127], v[52:55], a[252:255]\n"
"ds_read_b128 a[112:115], v10 offset:20480\n"
"s_waitcnt lgkmcnt(9)\n"
"v_mfma_f32_16x16x32_bf16 v[128:131], v[40:43], a[64:67], v[128:131]\n"
"ds_read_b128 a[116:119], v11 offset:20480\n"
"v_mfma_f32_16x16x32_bf16 v[128:131], v[44:47], a[68:71], v[128:131]\n"
"ds_read_b128 a[120:123], v10 offset:22528\n"
"v_mfma_f32_16x16x32_bf16 v[132:135], v[40:43], a[72:75], v[132:135]\n"
"ds_read_b128 a[124:127], v11 offset:22528\n"
"buffer_load_dwordx4 v8, s[12:15], s52 offen offset:2048 lds\n"
"v_mfma_f32_16x16x32_bf16 v[132:135], v[44:47], a[76:79], v[132:135]\n"
"ds_read_b64_tr_b16 v[48:49], v13\n"
"s_waitcnt lgkmcnt(9)\n"
"v_mfma_f32_16x16x32_bf16 v[136:139], v[40:43], a[80:83], v[136:139]\n"
"ds_read_b64_tr_b16 v[50:51], v13 offset:1024\n"
"v_mfma_f32_16x16x32_bf16 v[136:139], v[44:47], a[84:87], v[136:139]\n"
"ds_read_b64_tr_b16 v[52:53], v13 offset:8192\n"
"v_mfma_f32_16x16x32_bf16 v[140:143], v[40:43], a[88:91], v[140:143]\n"
"ds_read_b64_tr_b16 v[54:55], v13 offset:9216\n"
"v_mfma_f32_16x16x32_bf16 v[140:143], v[44:47], a[92:95], v[140:143]\n"
"s_waitcnt lgkmcnt(8)\n"
"v_mfma_f32_16x16x32_bf16 v[144:147], v[40:43], a[96:99], v[144:147]\n"
"v_mfma_f32_16x16x32_bf16 v[144:147], v[44:47], a[100:103], v[144:147]\n"
"v_mfma_f32_16x16x32_bf16 v[148:151], v[40:43], a[104:107], v[148:151]\n"
"buffer_load_dwordx4 v9, s[12:15], s52 offen offset:3072 lds\n"
"v_mfma_f32_16x16x32_bf16 v[148:151], v[44:47], a[108:111], v[148:151]\n"
"s_add_u32 s52, s52, s31\n"
"s_waitcnt lgkmcnt(4)\n"
"v_mfma_f32_16x16x32_bf16 v[152:155], v[40:43], a[112:115], v[152:155]\n"
"v_mfma_f32_16x16x32_bf16 v[152:155], v[44:47], a[116:119], v[152:155]\n"
"v_mfma_f32_16x16x32_bf16 v[156:159], v[40:43], a[120:123], v[156:159]\n"
"buffer_load_dwordx4 v[80:83], v31, s[4:7], s74 offen sc1\n"
"v_mfma_f32_16x16x32_bf16 v[156:159], v[44:47], a[124:127], v[156:159]\n"
"s_waitcnt lgkmcnt(0)\n"
"v_mfma_f32_16x16x32_bf16 v[160:163], v[48:51], a[64:67], v[160:163]\n"
"v_mfma_f32_16x16x32_bf16 v[160:163], v[52:55], a[68:71], v[160:163]\n"
"ds_read_b64_tr_b16 v[40:41], v14\n"
"s_add_u32 m0, s78, 0x1c000\n"
"v_mfma_f32_16x16x32_bf16 v[164:167], v[48:51], a[72:75], v[164:167]\n"
"buffer_load_dwordx4 v6, s[16:19], s53 offen lds\n"
"v_mfma_f32_16x16x32_bf16 v[164:167], v[52:55], a[76:79], v[164:167]\n"
"ds_read_b64_tr_b16 v[42:43], v14 offset:1024\n"
"v_mfma_f32_16x16x32_bf16 v[168:171], v[48:51], a[80:83], v[168:171]\n"
"v_mfma_f32_16x16x32_bf16 v[168:171], v[52:55], a[84:87], v[168:171]\n"
"ds_read_b64_tr_b16 v[44:45], v14 offset:8192\n"
"v_mfma_f32_16x16x32_bf16 v[172:175], v[48:51], a[88:91], v[172:175]\n"
"buffer_load_dwordx4 v[84:87], v31, s[4:7], s74 offen offset:2560 sc1\n"
"v_mfma_f32_16x16x32_bf16 v[172:175], v[52:55], a[92:95], v[172:175]\n"
"ds_read_b64_tr_b16 v[46:47], v14 offset:9216\n"
"v_mfma_f32_16x16x32_bf16 v[176:179], v[48:51], a[96:99], v[176:179]\n"
"v_mfma_f32_16x16x32_bf16 v[176:179], v[52:55], a[100:103], v[176:179]\n"
"v_mfma_f32_16x16x32_bf16 v[180:183], v[48:51], a[104:107], v[180:183]\n"
"buffer_load_dwordx4 v7, s[16:19], s53 offen offset:1024 lds\n"
"v_mfma_f32_16x16x32_bf16 v[180:183], v[52:55], a[108:111], v[180:183]\n"
"v_mfma_f32_16x16x32_bf16 v[184:187], v[48:51], a[112:115], v[184:187]\n"
"v_mfma_f32_16x16x32_bf16 v[184:187], v[52:55], a[116:119], v[184:187]\n"
"v_mfma_f32_16x16x32_bf16 v[188:191], v[48:51], a[120:123], v[188:191]\n"
"v_mfma_f32_16x16x32_bf16 v[188:191], v[52:55], a[124:127], v[188:191]\n"
"s_waitcnt lgkmcnt(0)\n"
"v_mfma_f32_16x16x32_bf16 v[192:195], v[40:43], a[64:67], v[192:195]\n"
"v_mfma_f32_16x16x32_bf16 v[192:195], v[44:47], a[68:71], v[192:195]\n"
"ds_read_b64_tr_b16 v[48:49], v15\n"
"v_mfma_f32_16x16x32_bf16 v[196:199], v[40:43], a[72:75], v[196:199]\n"
"buffer_load_dwordx4 v8, s[16:19], s53 offen offset:2048 lds\n"
"v_mfma_f32_16x16x32_bf16 v[196:199], v[44:47], a[76:79], v[196:199]\n"
"ds_read_b64_tr_b16 v[50:51], v15 offset:1024\n"
"v_mfma_f32_16x16x32_bf16 v[200:203], v[40:43], a[80:83], v[200:203]\n"
"v_mfma_f32_16x16x32_bf16 v[200:203], v[44:47], a[84:87], v[200:203]\n"
"ds_read_b64_tr_b16 v[52:53], v15 offset:8192\n"
"v_mfma_f32_16x16x32_bf16 v[204:207], v[40:43], a[88:91], v[204:207]\n"
"v_mfma_f32_16x16x32_bf16 v[204:207], v[44:47], a[92:95], v[204:207]\n"
"ds_read_b64_tr_b16 v[54:55], v15 offset:9216\n"
"v_mfma_f32_16x16x32_bf16 v[208:211], v[40:43], a[96:99], v[208:211]\n"
"v_mfma_f32_16x16x32_bf16 v[208:211], v[44:47], a[100:103], v[208:211]\n"
"v_mfma_f32_16x16x32_bf16 v[212:215], v[40:43], a[104:107], v[212:215]\n"
"buffer_load_dwordx4 v9, s[16:19], s53 offen offset:3072 lds\n"
"v_mfma_f32_16x16x32_bf16 v[212:215], v[44:47], a[108:111], v[212:215]\n"
"s_add_u32 s53, s53, s31\n"
"v_mfma_f32_16x16x32_bf16 v[216:219], v[40:43], a[112:115], v[216:219]\n"
"v_mfma_f32_16x16x32_bf16 v[216:219], v[44:47], a[116:119], v[216:219]\n"
"v_mfma_f32_16x16x32_bf16 v[220:223], v[40:43], a[120:123], v[220:223]\n"
"v_mfma_f32_16x16x32_bf16 v[220:223], v[44:47], a[124:127], v[220:223]\n"
"s_waitcnt vmcnt(18) lgkmcnt(0)\n"
"v_mfma_f32_16x16x32_bf16 v[224:227], v[48:51], a[64:67], v[224:227]\n"
"s_barrier\n"
"s_waitcnt vmcnt(3)\n"
"v_mfma_f32_16x16x32_bf16 v[224:227], v[52:55], a[68:71], v[224:227]\n"
"ds_read_b64_tr_b16 v[40:41], v12 offset:32768\n"
"s_add_u32 m0, s54, 0\n"
"v_mfma_f32_16x16x32_bf16 v[228:231], v[48:51], a[72:75], v[228:231]\n"
"buffer_load_dwordx4 v2, s[8:11], s50 offen lds\n"
"v_xor_b32 v81, s82, v81\n"
"v_mfma_f32_16x16x32_bf16 v[228:231], v[52:55], a[76:79], v[228:231]\n"
"ds_read_b64_tr_b16 v[42:43], v12 offset:33792\n"
"v_xor_b32 v83, s82, v83\n"
"v_mfma_f32_16x16x32_bf16 v[232:235], v[48:51], a[80:83], v[232:235]\n"
"v_xor_b32 v85, s82, v85\n"
"v_xor_b32 v87, s82, v87\n"
"v_mfma_f32_16x16x32_bf16 v[232:235], v[52:55], a[84:87], v[232:235]\n"
"ds_read_b64_tr_b16 v[44:45], v12 offset:40960\n"
"v_or3_b32 v38, v81, v83, v85\n"
"v_mfma_f32_16x16x32_bf16 v[236:239], v[48:51], a[88:91], v[236:239]\n"
"v_or_b32 v38, v38, v87\n"
"v_mfma_f32_16x16x32_bf16 v[236:239], v[52:55], a[92:95], v[236:239]\n"
"ds_read_b64_tr_b16 v[46:47], v12 offset:41984\n"
"v_mfma_f32_16x16x32_bf16 v[240:243], v[48:51], a[96:99], v[240:243]\n"
"v_cmp_ne_u32 vcc, 0, v38\n"
"s_nop 0\n"
"s_cbranch_vccnz .Ladaln_block_P_mod\n"
".Ladaln_block_P_mod_R:\n"
"v_mfma_f32_16x16x32_bf16 v[240:243], v[52:55], a[100:103], v[240:243]\n"
"v_and_b32 v81, 0xffff0000, v80\n"
"v_lshlrev_b32 v80, 16, v80\n"
"v_mfma_f32_16x16x32_bf16 v[244:247], v[48:51], a[104:107], v[244:247]\n"
"buffer_load_dwordx4 v3, s[8:11], s50 offen offset:1024 lds\n"
"v_and_b32 v83, 0xffff0000, v82\n"
"v_mfma_f32_16x16x32_bf16 v[244:247], v[52:55], a[108:111], v[244:247]\n"
"v_lshlrev_b32 v82, 16, v82\n"
"v_and_b32 v85, 0xffff0000, v84\n"
"v_mfma_f32_16x16x32_bf16 v[248:251], v[48:51], a[112:115], v[248:251]\n"
"v_lshlrev_b32 v84, 16, v84\n"
"v_and_b32 v87, 0xffff0000, v86\n"
"v_mfma_f32_16x16x32_bf16 v[248:251], v[52:55], a[116:119], v[248:251]\n"
"v_lshlrev_b32 v86, 16, v86\n"
"v_mul_f32 v80, 0x3e0293ee, v80\n"
"v_mfma_f32_16x16x32_bf16 v[252:255], v[48:51], a[120:123], v[252:255]\n"
"v_mul_f32 v81, 0x3e0293ee, v81\n"
"v_mul_f32 v82, 0x3e0293ee, v82\n"
"v_mfma_f32_16x16x32_bf16 v[252:255], v[52:55], a[124:127], v[252:255]\n"
"v_mul_f32 v83, 0x3e0293ee, v83\n"
"s_waitcnt lgkmcnt(0)\n"
"v_mfma_f32_16x16x32_bf16 a[128:131], a[64:67], v[40:43], a[128:131]\n"
"ds_write_b128 v39, v[80:83]\n"
"v_mfma_f32_16x16x32_bf16 a[128:131], a[68:71], v[44:47], a[128:131]\n"
"ds_read_b64_tr_b16 v[48:49], v13 offset:32768\n"
"v_mfma_f32_16x16x32_bf16 a[132:135], a[72:75], v[40:43], a[132:135]\n"
"buffer_load_dwordx4 v4, s[8:11], s50 offen offset:2048 lds\n"
"v_mfma_f32_16x16x32_bf16 a[132:135], a[76:79], v[44:47], a[132:135]\n"
"ds_read_b64_tr_b16 v[50:51], v13 offset:33792\n"
"v_mfma_f32_16x16x32_bf16 a[136:139], a[80:83], v[40:43], a[136:139]\n"
"ds_write_b128 v39, v[84:87] offset:1024\n"
"v_mfma_f32_16x16x32_bf16 a[136:139], a[84:87], v[44:47], a[136:139]\n"
"ds_read_b64_tr_b16 v[52:53], v13 offset:40960\n"
"v_mfma_f32_16x16x32_bf16 a[140:143], a[88:91], v[40:43], a[140:143]\n"
"ds_read_b128 v[88:91], v28\n"
"ds_read_b128 v[92:95], v28 offset:16\n"
"v_mfma_f32_16x16x32_bf16 a[140:143], a[92:95], v[44:47], a[140:143]\n"
"ds_read_b64_tr_b16 v[54:55], v13 offset:41984\n"
"ds_read_b128 v[80:83], v28 offset:1024\n"
"v_mfma_f32_16x16x32_bf16 a[144:147], a[96:99], v[40:43], a[144:147]\n"
"ds_read_b128 v[84:87], v28 offset:1040\n"
"ds_read_b128 v[104:107], v28 offset:256\n"
"v_mfma_f32_16x16x32_bf16 a[144:147], a[100:103], v[44:47], a[144:147]\n"
"ds_read_b128 v[108:111], v28 offset:272\n"
"ds_read_b128 v[96:99], v28 offset:1280\n"
"v_mfma_f32_16x16x32_bf16 a[148:151], a[104:107], v[40:43], a[148:151]\n"
"buffer_load_dwordx4 v5, s[8:11], s50 offen offset:3072 lds\n"
"ds_read_b128 v[100:103], v28 offset:1296\n"
"v_mfma_f32_16x16x32_bf16 a[148:151], a[108:111], v[44:47], a[148:151]\n"
"ds_read_b128 v[120:123], v29 offset:512\n"
"s_add_u32 s50, s50, s26\n"
"v_mfma_f32_16x16x32_bf16 a[152:155], a[112:115], v[40:43], a[152:155]\n"
"ds_read_b128 v[124:127], v29 offset:528\n"
"ds_read_b128 v[112:115], v29 offset:1536\n"
"v_mfma_f32_16x16x32_bf16 a[152:155], a[116:119], v[44:47], a[152:155]\n"
"ds_read_b128 v[116:119], v29 offset:1552\n"
"v_mfma_f32_16x16x32_bf16 a[156:159], a[120:123], v[40:43], a[156:159]\n"
"v_mfma_f32_16x16x32_bf16 a[156:159], a[124:127], v[44:47], a[156:159]\n"
"s_waitcnt lgkmcnt(10)\n"
"v_mfma_f32_16x16x32_bf16 a[160:163], a[64:67], v[48:51], a[160:163]\n"
"s_waitcnt lgkmcnt(8)\n"
"v_fmac_f32 v88, v56, v80\n"
"v_mfma_f32_16x16x32_bf16 a[160:163], a[68:71], v[52:55], a[160:163]\n"
"ds_read_b64_tr_b16 v[40:41], v14 offset:32768\n"
"s_add_u32 m0, s54, 0x1000\n"
"v_mfma_f32_16x16x32_bf16 a[164:167], a[72:75], v[48:51], a[164:167]\n"
"buffer_load_dwordx4 v2, s[8:11], s51 offen lds\n"
"v_fmac_f32 v89, v57, v81\n"
"v_mfma_f32_16x16x32_bf16 a[164:167], a[76:79], v[52:55], a[164:167]\n"
"ds_read_b64_tr_b16 v[42:43], v14 offset:33792\n"
"v_fmac_f32 v90, v58, v82\n"
"v_mfma_f32_16x16x32_bf16 a[168:171], a[80:83], v[48:51], a[168:171]\n"
"v_fmac_f32 v91, v59, v83\n"
"v_fmac_f32 v92, v60, v84\n"
"v_mfma_f32_16x16x32_bf16 a[168:171], a[84:87], v[52:55], a[168:171]\n"
"ds_read_b64_tr_b16 v[44:45], v14 offset:40960\n"
"v_fmac_f32 v93, v61, v85\n"
"v_mfma_f32_16x16x32_bf16 a[172:175], a[88:91], v[48:51], a[172:175]\n"
"v_fmac_f32 v94, v62, v86\n"
"v_fmac_f32 v95, v63, v87\n"
"v_mfma_f32_16x16x32_bf16 a[172:175], a[92:95], v[52:55], a[172:175]\n"
"ds_read_b64_tr_b16 v[46:47], v14 offset:41984\n"
"v_cvt_pk_bf16_f32 v80, v88, v89\n"
"v_mfma_f32_16x16x32_bf16 a[176:179], a[96:99], v[48:51], a[176:179]\n"
"v_cvt_pk_bf16_f32 v81, v90, v91\n"
"v_cvt_pk_bf16_f32 v82, v92, v93\n"
"v_mfma_f32_16x16x32_bf16 a[176:179], a[100:103], v[52:55], a[176:179]\n"
"v_cvt_pk_bf16_f32 v83, v94, v95\n"
"v_mfma_f32_16x16x32_bf16 a[180:183], a[104:107], v[48:51], a[180:183]\n"
"buffer_load_dwordx4 v3, s[8:11], s51 offen offset:1024 lds\n"
"s_waitcnt lgkmcnt(8)\n"
"v_mfma_f32_16x16x32_bf16 a[180:183], a[108:111], v[52:55], a[180:183]\n"
"v_fmac_f32 v104, v64, v96\n"
"v_fmac_f32 v105, v65, v97\n"
"v_mfma_f32_16x16x32_bf16 a[184:187], a[112:115], v[48:51], a[184:187]\n"
"v_fmac_f32 v106, v66, v98\n"
"v_fmac_f32 v107, v67, v99\n"
"v_mfma_f32_16x16x32_bf16 a[184:187], a[116:119], v[52:55], a[184:187]\n"
"v_fmac_f32 v108, v68, v100\n"
"v_fmac_f32 v109, v69, v101\n"
"v_mfma_f32_16x16x32_bf16 a[188:191], a[120:123], v[48:51], a[188:191]\n"
"buffer_store_dwordx4 v[80:83], v26, s[4:7], s62 offen sc1\n"
"s_add_u32 s62, s62, 0x4000\n"
"v_mfma_f32_16x16x32_bf16 a[188:191], a[124:127], v[52:55], a[188:191]\n"
"v_fmac_f32 v110, v70, v102\n"
"s_waitcnt lgkmcnt(0)\n"
"v_mfma_f32_16x16x32_bf16 a[192:195], a[64:67], v[40:43], a[192:195]\n"
"v_fmac_f32 v111, v71, v103\n"
"v_cvt_pk_bf16_f32 v96, v104, v105\n"
"v_mfma_f32_16x16x32_bf16 a[192:195], a[68:71], v[44:47], a[192:195]\n"
"ds_read_b64_tr_b16 v[48:49], v15 offset:32768\n"
"v_cvt_pk_bf16_f32 v97, v106, v107\n"
"v_mfma_f32_16x16x32_bf16 a[196:199], a[72:75], v[40:43], a[196:199]\n"
"buffer_load_dwordx4 v4, s[8:11], s51 offen offset:2048 lds\n"
"v_cvt_pk_bf16_f32 v98, v108, v109\n"
"v_mfma_f32_16x16x32_bf16 a[196:199], a[76:79], v[44:47], a[196:199]\n"
"ds_read_b64_tr_b16 v[50:51], v15 offset:33792\n"
"v_cvt_pk_bf16_f32 v99, v110, v111\n"
"v_mfma_f32_16x16x32_bf16 a[200:203], a[80:83], v[40:43], a[200:203]\n"
"s_nop 0\n"
"v_fmac_f32 v120, v72, v112\n"
"v_mfma_f32_16x16x32_bf16 a[200:203], a[84:87], v[44:47], a[200:203]\n"
"ds_read_b64_tr_b16 v[52:53], v15 offset:40960\n"
"v_fmac_f32 v121, v73, v113\n"
"v_mfma_f32_16x16x32_bf16 a[204:207], a[88:91], v[40:43], a[204:207]\n"
"buffer_store_dwordx4 v[96:99], v26, s[4:7], s62 offen sc1\n"
"s_add_u32 s62, s62, 0x4000\n"
"v_mfma_f32_16x16x32_bf16 a[204:207], a[92:95], v[44:47], a[204:207]\n"
"ds_read_b64_tr_b16 v[54:55], v15 offset:41984\n"
"v_fmac_f32 v122, v74, v114\n"
"v_mfma_f32_16x16x32_bf16 a[208:211], a[96:99], v[40:43], a[208:211]\n"
"v_fmac_f32 v123, v75, v115\n"
"v_fmac_f32 v124, v76, v116\n"
"v_mfma_f32_16x16x32_bf16 a[208:211], a[100:103], v[44:47], a[208:211]\n"
"v_fmac_f32 v125, v77, v117\n"
"v_fmac_f32 v126, v78, v118\n"
"v_mfma_f32_16x16x32_bf16 a[212:215], a[104:107], v[40:43], a[212:215]\n"
"buffer_load_dwordx4 v5, s[8:11], s51 offen offset:3072 lds\n"
"v_fmac_f32 v127, v79, v119\n"
"v_mfma_f32_16x16x32_bf16 a[212:215], a[108:111], v[44:47], a[212:215]\n"
"s_add_u32 s51, s51, s26\n"
"v_cvt_pk_bf16_f32 v112, v120, v121\n"
"v_mfma_f32_16x16x32_bf16 a[216:219], a[112:115], v[40:43], a[216:219]\n"
"v_cvt_pk_bf16_f32 v113, v122, v123\n"
"v_cvt_pk_bf16_f32 v114, v124, v125\n"
"v_mfma_f32_16x16x32_bf16 a[216:219], a[116:119], v[44:47], a[216:219]\n"
"v_cvt_pk_bf16_f32 v115, v126, v127\n"
"v_mfma_f32_16x16x32_bf16 a[220:223], a[120:123], v[40:43], a[220:223]\n"
"buffer_store_dwordx4 v[112:115], v27, s[4:7], s62 offen sc1\n"
"s_add_u32 s62, s62, 0x4000\n"
"v_mfma_f32_16x16x32_bf16 a[220:223], a[124:127], v[44:47], a[220:223]\n"
"s_lshl_b32 s61, s2, 7\n"
"s_waitcnt lgkmcnt(0)\n"
"v_mfma_f32_16x16x32_bf16 a[224:227], a[64:67], v[48:51], a[224:227]\n"
"s_barrier\n"
"s_add_u32 s61, s61, 0xc0000\n"
"v_mfma_f32_16x16x32_bf16 a[224:227], a[68:71], v[52:55], a[224:227]\n"
"ds_read_b64_tr_b16 v[40:41], v12 offset:16384\n"
"ds_read_b64_tr_b16 v[42:43], v12 offset:17408\n"
"v_mfma_f32_16x16x32_bf16 a[228:231], a[72:75], v[48:51], a[228:231]\n"
"ds_read_b64_tr_b16 v[44:45], v12 offset:24576\n"
"ds_read_b64_tr_b16 v[46:47], v12 offset:25600\n"
"v_mfma_f32_16x16x32_bf16 a[228:231], a[76:79], v[52:55], a[228:231]\n"
"ds_read_b128 a[64:67], v10 offset:32768\n"
"s_add_u32 m0, s78, 0x10000\n"
"v_mfma_f32_16x16x32_bf16 a[232:235], a[80:83], v[48:51], a[232:235]\n"
"ds_read_b128 a[68:71], v11 offset:32768\n"
"buffer_load_dwordx4 v6, s[12:15], s52 offen lds\n"
"v_mfma_f32_16x16x32_bf16 a[232:235], a[84:87], v[52:55], a[232:235]\n"
"ds_read_b128 a[72:75], v10 offset:34816\n"
"v_mfma_f32_16x16x32_bf16 a[236:239], a[88:91], v[48:51], a[236:239]\n"
"ds_read_b128 a[76:79], v11 offset:34816\n"
"v_mfma_f32_16x16x32_bf16 a[236:239], a[92:95], v[52:55], a[236:239]\n"
"ds_read_b128 a[80:83], v10 offset:36864\n"
"v_mfma_f32_16x16x32_bf16 a[240:243], a[96:99], v[48:51], a[240:243]\n"
"ds_read_b128 a[84:87], v11 offset:36864\n"
"v_mfma_f32_16x16x32_bf16 a[240:243], a[100:103], v[52:55], a[240:243]\n"
"ds_read_b128 a[88:91], v10 offset:38912\n"
"v_mfma_f32_16x16x32_bf16 a[244:247], a[104:107], v[48:51], a[244:247]\n"
"ds_read_b128 a[92:95], v11 offset:38912\n"
"buffer_load_dwordx4 v7, s[12:15], s52 offen offset:1024 lds\n"
"v_mfma_f32_16x16x32_bf16 a[244:247], a[108:111], v[52:55], a[244:247]\n"
"ds_read_b128 a[96:99], v10 offset:49152\n"
"v_mfma_f32_16x16x32_bf16 a[248:251], a[112:115], v[48:51], a[248:251]\n"
"ds_read_b128 a[100:103], v11 offset:49152\n"
"v_mfma_f32_16x16x32_bf16 a[248:251], a[116:119], v[52:55], a[248:251]\n"
"ds_read_b128 a[104:107], v10 offset:51200\n"
"v_mfma_f32_16x16x32_bf16 a[252:255], a[120:123], v[48:51], a[252:255]\n"
"ds_read_b128 a[108:111], v11 offset:51200\n"
"v_mfma_f32_16x16x32_bf16 a[252:255], a[124:127], v[52:55], a[252:255]\n"
"ds_read_b128 a[112:115], v10 offset:53248\n"
"s_waitcnt lgkmcnt(9)\n"
"v_mfma_f32_16x16x32_bf16 v[128:131], v[40:43], a[64:67], v[128:131]\n"
"ds_read_b128 a[116:119], v11 offset:53248\n"
"v_mfma_f32_16x16x32_bf16 v[128:131], v[44:47], a[68:71], v[128:131]\n"
"ds_read_b128 a[120:123], v10 offset:55296\n"
"v_mfma_f32_16x16x32_bf16 v[132:135], v[40:43], a[72:75], v[132:135]\n"
"ds_read_b128 a[124:127], v11 offset:55296\n"
"buffer_load_dwordx4 v8, s[12:15], s52 offen offset:2048 lds\n"
"v_mfma_f32_16x16x32_bf16 v[132:135], v[44:47], a[76:79], v[132:135]\n"
"ds_read_b64_tr_b16 v[48:49], v13 offset:16384\n"
"s_waitcnt lgkmcnt(9)\n"
"v_mfma_f32_16x16x32_bf16 v[136:139], v[40:43], a[80:83], v[136:139]\n"
"ds_read_b64_tr_b16 v[50:51], v13 offset:17408\n"
"v_mfma_f32_16x16x32_bf16 v[136:139], v[44:47], a[84:87], v[136:139]\n"
"ds_read_b64_tr_b16 v[52:53], v13 offset:24576\n"
"v_mfma_f32_16x16x32_bf16 v[140:143], v[40:43], a[88:91], v[140:143]\n"
"ds_read_b64_tr_b16 v[54:55], v13 offset:25600\n"
"v_mfma_f32_16x16x32_bf16 v[140:143], v[44:47], a[92:95], v[140:143]\n"
"s_waitcnt lgkmcnt(8)\n"
"v_mfma_f32_16x16x32_bf16 v[144:147], v[40:43], a[96:99], v[144:147]\n"
"v_mfma_f32_16x16x32_bf16 v[144:147], v[44:47], a[100:103], v[144:147]\n"
"v_mfma_f32_16x16x32_bf16 v[148:151], v[40:43], a[104:107], v[148:151]\n"
"buffer_load_dwordx4 v9, s[12:15], s52 offen offset:3072 lds\n"
"v_mfma_f32_16x16x32_bf16 v[148:151], v[44:47], a[108:111], v[148:151]\n"
"s_add_u32 s52, s52, s30\n"
"s_waitcnt lgkmcnt(4)\n"
"v_mfma_f32_16x16x32_bf16 v[152:155], v[40:43], a[112:115], v[152:155]\n"
"v_mfma_f32_16x16x32_bf16 v[152:155], v[44:47], a[116:119], v[152:155]\n"
"v_mfma_f32_16x16x32_bf16 v[156:159], v[40:43], a[120:123], v[156:159]\n"
"v_mfma_f32_16x16x32_bf16 v[156:159], v[44:47], a[124:127], v[156:159]\n"
"s_waitcnt lgkmcnt(0)\n"
"v_mfma_f32_16x16x32_bf16 v[160:163], v[48:51], a[64:67], v[160:163]\n"
"v_mfma_f32_16x16x32_bf16 v[160:163], v[52:55], a[68:71], v[160:163]\n"
"ds_read_b64_tr_b16 v[40:41], v14 offset:16384\n"
"s_add_u32 m0, s78, 0x18000\n"
"v_mfma_f32_16x16x32_bf16 v[164:167], v[48:51], a[72:75], v[164:167]\n"
"buffer_load_dwordx4 v6, s[16:19], s53 offen lds\n"
"v_mfma_f32_16x16x32_bf16 v[164:167], v[52:55], a[76:79], v[164:167]\n"
"ds_read_b64_tr_b16 v[42:43], v14 offset:17408\n"
"v_mfma_f32_16x16x32_bf16 v[168:171], v[48:51], a[80:83], v[168:171]\n"
"v_mfma_f32_16x16x32_bf16 v[168:171], v[52:55], a[84:87], v[168:171]\n"
"ds_read_b64_tr_b16 v[44:45], v14 offset:24576\n"
"v_mfma_f32_16x16x32_bf16 v[172:175], v[48:51], a[88:91], v[172:175]\n"
"v_mfma_f32_16x16x32_bf16 v[172:175], v[52:55], a[92:95], v[172:175]\n"
"ds_read_b64_tr_b16 v[46:47], v14 offset:25600\n"
"v_mfma_f32_16x16x32_bf16 v[176:179], v[48:51], a[96:99], v[176:179]\n"
"v_mfma_f32_16x16x32_bf16 v[176:179], v[52:55], a[100:103], v[176:179]\n"
"s_nop 0\n"
"v_xor_b32 v33, s82, v33\n"
"v_mfma_f32_16x16x32_bf16 v[180:183], v[48:51], a[104:107], v[180:183]\n"
"buffer_load_dwordx4 v7, s[16:19], s53 offen offset:1024 lds\n"
"v_xor_b32 v35, s82, v35\n"
"v_mfma_f32_16x16x32_bf16 v[180:183], v[52:55], a[108:111], v[180:183]\n"
"v_or_b32 v33, v33, v35\n"
"v_mfma_f32_16x16x32_bf16 v[184:187], v[48:51], a[112:115], v[184:187]\n"
"v_cmp_ne_u32 vcc, 0, v33\n"
"s_nop 0\n"
"s_cbranch_vccnz .Ladaln_block_P_vote\n"
".Ladaln_block_P_vote_R:\n"
"v_mfma_f32_16x16x32_bf16 v[184:187], v[52:55], a[116:119], v[184:187]\n"
"v_and_b32 v33, 1, v32\n"
"v_cmp_gt_u32 s[56:57], v32, v34\n"
"v_mfma_f32_16x16x32_bf16 v[188:191], v[48:51], a[120:123], v[188:191]\n"
"v_cmp_ne_u32 s[0:1], 0, v33\n"
"s_nop 0\n"
"v_mfma_f32_16x16x32_bf16 v[188:191], v[52:55], a[124:127], v[188:191]\n"
"s_xor_b64 s[56:57], s[56:57], s[0:1]\n"
"s_waitcnt lgkmcnt(0)\n"
"v_mfma_f32_16x16x32_bf16 v[192:195], v[40:43], a[64:67], v[192:195]\n"
"s_bcnt1_i32_b64 s56, s[56:57]\n"
"v_mfma_f32_16x16x32_bf16 v[192:195], v[44:47], a[68:71], v[192:195]\n"
"ds_read_b64_tr_b16 v[48:49], v15 offset:16384\n"
"v_mfma_f32_16x16x32_bf16 v[196:199], v[40:43], a[72:75], v[196:199]\n"
"buffer_load_dwordx4 v8, s[16:19], s53 offen offset:2048 lds\n"
"v_mfma_f32_16x16x32_bf16 v[196:199], v[44:47], a[76:79], v[196:199]\n"
"ds_read_b64_tr_b16 v[50:51], v15 offset:17408\n"
"v_mfma_f32_16x16x32_bf16 v[200:203], v[40:43], a[80:83], v[200:203]\n"
"s_cmp_gt_u32 s56, 32\n"
"s_cselect_b32 s1, -1, 0\n"
"v_mfma_f32_16x16x32_bf16 v[200:203], v[44:47], a[84:87], v[200:203]\n"
"ds_read_b64_tr_b16 v[52:53], v15 offset:24576\n"
"s_and_b32 s56, s0, 0xf0\n"
"v_mfma_f32_16x16x32_bf16 v[204:207], v[40:43], a[88:91], v[204:207]\n"
"s_bcnt1_i32_b32 s56, s56\n"
"v_mfma_f32_16x16x32_bf16 v[204:207], v[44:47], a[92:95], v[204:207]\n"
"ds_read_b64_tr_b16 v[54:55], v15 offset:25600\n"
"v_mfma_f32_16x16x32_bf16 v[208:211], v[40:43], a[96:99], v[208:211]\n"
"s_cmp_lt_u32 s56, 2\n"
"s_cselect_b32 s56, -1, 0\n"
"v_mfma_f32_16x16x32_bf16 v[208:211], v[44:47], a[100:103], v[208:211]\n"
"s_xor_b32 s1, s1, s56\n"
"s_xor_b32 s0, s0, s56\n"
"v_mfma_f32_16x16x32_bf16 v[212:215], v[40:43], a[104:107], v[212:215]\n"
"buffer_load_dwordx4 v9, s[16:19], s53 offen offset:3072 lds\n"
"s_and_b32 s0, s0, 0xff\n"
"v_mfma_f32_16x16x32_bf16 v[212:215], v[44:47], a[108:111], v[212:215]\n"
"s_add_u32 s53, s53, s30\n"
"s_and_b32 s56, s2, 7\n"
"v_mfma_f32_16x16x32_bf16 v[216:219], v[40:43], a[112:115], v[216:219]\n"
"s_lshr_b32 s101, s0, s56\n"
"s_and_b32 s101, s101, 1\n"
"v_mfma_f32_16x16x32_bf16 v[216:219], v[44:47], a[116:119], v[216:219]\n"
"s_lshl_b32 s56, 1, s56\n"
"s_sub_u32 s56, s56, 1\n"
"v_mfma_f32_16x16x32_bf16 v[220:223], v[40:43], a[120:123], v[220:223]\n"
"s_sub_u32 s57, s101, 1\n"
"s_xor_b32 s57, s57, s0\n"
"v_mfma_f32_16x16x32_bf16 v[220:223], v[44:47], a[124:127], v[220:223]\n"
"s_and_b32 s56, s56, s57\n"
"s_waitcnt vmcnt(8) lgkmcnt(0)\n"
"v_mfma_f32_16x16x32_bf16 v[224:227], v[48:51], a[64:67], v[224:227]\n"
"s_barrier\n"
"s_bcnt1_i32_b32 s56, s56\n"
"v_mfma_f32_16x16x32_bf16 v[224:227], v[52:55], a[68:71], v[224:227]\n"
"ds_read_b64_tr_b16 v[40:41], v12 offset:49152\n"
"s_add_u32 m0, s54, 0x8000\n"
"v_mfma_f32_16x16x32_bf16 v[228:231], v[48:51], a[72:75], v[228:231]\n"
"buffer_load_dwordx4 v2, s[8:11], s50 offen lds\n"
"s_lshl1_add_u32 s56, s56, s101\n"
"v_mfma_f32_16x16x32_bf16 v[228:231], v[52:55], a[76:79], v[228:231]\n"
"ds_read_b64_tr_b16 v[42:43], v12 offset:50176\n"
"s_andn2_b32 s100, s2, 7\n"
"v_mfma_f32_16x16x32_bf16 v[232:235], v[48:51], a[80:83], v[232:235]\n"
"s_or_b32 s100, s100, s56\n"
"s_bcnt1_i32_b32 s56, s0\n"
"v_mfma_f32_16x16x32_bf16 v[232:235], v[52:55], a[84:87], v[232:235]\n"
"ds_read_b64_tr_b16 v[44:45], v12 offset:57344\n"
"v_mfma_f32_16x16x32_bf16 v[236:239], v[48:51], a[88:91], v[236:239]\n"
"buffer_store_dword v22, v23, s[4:7], s61 offen sc1\n"
"v_mfma_f32_16x16x32_bf16 v[236:239], v[52:55], a[92:95], v[236:239]\n"
"ds_read_b64_tr_b16 v[46:47], v12 offset:58368\n"
"v_and_b32 v25, 7, v0\n"
"v_mfma_f32_16x16x32_bf16 v[240:243], v[48:51], a[96:99], v[240:243]\n"
"v_lshlrev_b32 v25, 7, v25\n"
"s_lshl_b32 s61, s79, 10\n"
"v_mfma_f32_16x16x32_bf16 v[240:243], v[52:55], a[100:103], v[240:243]\n"
"s_add_u32 s61, s61, 0xc0000\n"
"v_mfma_f32_16x16x32_bf16 v[244:247], v[48:51], a[104:107], v[244:247]\n"
"buffer_load_dwordx4 v3, s[8:11], s50 offen offset:1024 lds\n"
"v_mfma_f32_16x16x32_bf16 v[244:247], v[52:55], a[108:111], v[244:247]\n"
"s_cmp_lg_u32 s56, 4\n"
"s_cmov_b32 s100, s2\n"
"v_mfma_f32_16x16x32_bf16 v[248:251], v[48:51], a[112:115], v[248:251]\n"
"s_xor_b32 s101, s100, s1\n"
"s_and_b32 s101, s101, 1\n"
"v_mfma_f32_16x16x32_bf16 v[248:251], v[52:55], a[116:119], v[248:251]\n"
"s_lshl_b32 s101, s101, 13\n"
"s_bfe_u32 s32, s100, 0x20001\n"
"v_mfma_f32_16x16x32_bf16 v[252:255], v[48:51], a[120:123], v[252:255]\n"
"s_and_b32 s55, s48, 3\n"
"s_lshl2_add_u32 s32, s32, s55\n"
"v_mfma_f32_16x16x32_bf16 v[252:255], v[52:55], a[124:127], v[252:255]\n"
"s_lshl_b32 s32, s32, 20\n"
"s_waitcnt lgkmcnt(0)\n"
"v_mfma_f32_16x16x32_bf16 a[128:131], a[64:67], v[40:43], a[128:131]\n"
"s_add_u32 s32, s32, 0x4000000\n"
"s_lshr_b32 s55, s32, 8\n"
"v_mfma_f32_16x16x32_bf16 a[128:131], a[68:71], v[44:47], a[128:131]\n"
"ds_read_b64_tr_b16 v[48:49], v13 offset:49152\n"
"s_and_b32 s55, s55, 0x2000\n"
"v_mfma_f32_16x16x32_bf16 a[132:135], a[72:75], v[40:43], a[132:135]\n"
"buffer_load_dwordx4 v4, s[8:11], s50 offen offset:2048 lds\n"
"s_xor_b32 s32, s32, s55\n"
"v_mfma_f32_16x16x32_bf16 a[132:135], a[76:79], v[44:47], a[132:135]\n"
"ds_read_b64_tr_b16 v[50:51], v13 offset:50176\n"
"s_xor_b32 s32, s32, s101\n"
"v_mfma_f32_16x16x32_bf16 a[136:139], a[80:83], v[40:43], a[136:139]\n"
"s_lshr_b32 s55, s3, 1\n"
"s_lshl2_add_u32 s55, s49, s55\n"
"v_mfma_f32_16x16x32_bf16 a[136:139], a[84:87], v[44:47], a[136:139]\n"
"ds_read_b64_tr_b16 v[52:53], v13 offset:57344\n"
"s_lshl_b32 s55, s55, 15\n"
"v_mfma_f32_16x16x32_bf16 a[140:143], a[88:91], v[40:43], a[140:143]\n"
"s_add_u32 s32, s32, s55\n"
"s_and_b32 s55, s3, 1\n"
"v_mfma_f32_16x16x32_bf16 a[140:143], a[92:95], v[44:47], a[140:143]\n"
"ds_read_b64_tr_b16 v[54:55], v13 offset:58368\n"
"s_lshl_b32 s33, s55, 14\n"
"v_mfma_f32_16x16x32_bf16 a[144:147], a[96:99], v[40:43], a[144:147]\n"
"s_add_u32 s33, s33, s32\n"
"s_add_u32 s33, s33, 0x1000000\n"
"v_mfma_f32_16x16x32_bf16 a[144:147], a[100:103], v[44:47], a[144:147]\n"
"s_lshl_b32 s55, s55, 11\n"
"s_add_u32 s32, s32, s55\n"
"v_mfma_f32_16x16x32_bf16 a[148:151], a[104:107], v[40:43], a[148:151]\n"
"buffer_load_dwordx4 v5, s[8:11], s50 offen offset:3072 lds\n"
"s_add_u32 m0, s78, 0x20000\n"
"v_mfma_f32_16x16x32_bf16 a[148:151], a[108:111], v[44:47], a[148:151]\n"
"s_add_u32 s50, s50, s27\n"
"s_and_b32 s55, s79, 3\n"
"v_mfma_f32_16x16x32_bf16 a[152:155], a[112:115], v[40:43], a[152:155]\n"
"s_lshl3_add_u32 s60, s79, s55\n"
"s_lshl_b32 s60, s60, 15\n"
"v_mfma_f32_16x16x32_bf16 a[152:155], a[116:119], v[44:47], a[152:155]\n"
"s_add_u32 s60, s60, 0x800000\n"
"v_add_u32 v21, s60, v1\n"
"v_mfma_f32_16x16x32_bf16 a[156:159], a[120:123], v[40:43], a[156:159]\n"
"buffer_load_dwordx4 v6, s[20:23], s58 offen lds\n"
"v_mfma_f32_16x16x32_bf16 a[156:159], a[124:127], v[44:47], a[156:159]\n"
"s_waitcnt lgkmcnt(0)\n"
"v_mfma_f32_16x16x32_bf16 a[160:163], a[64:67], v[48:51], a[160:163]\n"
"v_mfma_f32_16x16x32_bf16 a[160:163], a[68:71], v[52:55], a[160:163]\n"
"ds_read_b64_tr_b16 v[40:41], v14 offset:49152\n"
"v_mfma_f32_16x16x32_bf16 a[164:167], a[72:75], v[48:51], a[164:167]\n"
"buffer_load_dwordx4 v7, s[20:23], s58 offen offset:1024 lds\n"
"v_mfma_f32_16x16x32_bf16 a[164:167], a[76:79], v[52:55], a[164:167]\n"
"ds_read_b64_tr_b16 v[42:43], v14 offset:50176\n"
"v_mfma_f32_16x16x32_bf16 a[168:171], a[80:83], v[48:51], a[168:171]\n"
"v_mfma_f32_16x16x32_bf16 a[168:171], a[84:87], v[52:55], a[168:171]\n"
"ds_read_b64_tr_b16 v[44:45], v14 offset:57344\n"
"v_mfma_f32_16x16x32_bf16 a[172:175], a[88:91], v[48:51], a[172:175]\n"
"buffer_load_dwordx4 v8, s[20:23], s58 offen offset:2048 lds\n"
"v_mfma_f32_16x16x32_bf16 a[172:175], a[92:95], v[52:55], a[172:175]\n"
"ds_read_b64_tr_b16 v[46:47], v14 offset:58368\n"
"v_mfma_f32_16x16x32_bf16 a[176:179], a[96:99], v[48:51], a[176:179]\n"
"v_mfma_f32_16x16x32_bf16 a[176:179], a[100:103], v[52:55], a[176:179]\n"
"v_mfma_f32_16x16x32_bf16 a[180:183], a[104:107], v[48:51], a[180:183]\n"
"buffer_load_dwordx4 v9, s[20:23], s58 offen offset:3072 lds\n"
"s_add_u32 m0, s54, 0x9000\n"
"v_mfma_f32_16x16x32_bf16 a[180:183], a[108:111], v[52:55], a[180:183]\n"
"s_add_u32 s58, s58, 0x80000\n"
"v_mfma_f32_16x16x32_bf16 a[184:187], a[112:115], v[48:51], a[184:187]\n"
"s_cmp_eq_u32 s58, 0x500000\n"
"s_cmov_b32 s58, 0\n"
"v_mfma_f32_16x16x32_bf16 a[184:187], a[116:119], v[52:55], a[184:187]\n"
"v_mfma_f32_16x16x32_bf16 a[188:191], a[120:123], v[48:51], a[188:191]\n"
"buffer_load_dwordx4 v2, s[8:11], s51 offen lds\n"
"v_mfma_f32_16x16x32_bf16 a[188:191], a[124:127], v[52:55], a[188:191]\n"
"s_waitcnt lgkmcnt(0)\n"
"v_mfma_f32_16x16x32_bf16 a[192:195], a[64:67], v[40:43], a[192:195]\n"
"v_mfma_f32_16x16x32_bf16 a[192:195], a[68:71], v[44:47], a[192:195]\n"
"ds_read_b64_tr_b16 v[48:49], v15 offset:49152\n"
"v_mfma_f32_16x16x32_bf16 a[196:199], a[72:75], v[40:43], a[196:199]\n"
"buffer_load_dwordx4 v3, s[8:11], s51 offen offset:1024 lds\n"
"v_mfma_f32_16x16x32_bf16 a[196:199], a[76:79], v[44:47], a[196:199]\n"
"ds_read_b64_tr_b16 v[50:51], v15 offset:50176\n"
"v_mfma_f32_16x16x32_bf16 a[200:203], a[80:83], v[40:43], a[200:203]\n"
"v_mfma_f32_16x16x32_bf16 a[200:203], a[84:87], v[44:47], a[200:203]\n"
"ds_read_b64_tr_b16 v[52:53], v15 offset:57344\n"
"v_mfma_f32_16x16x32_bf16 a[204:207], a[88:91], v[40:43], a[204:207]\n"
"buffer_load_dwordx4 v4, s[8:11], s51 offen offset:2048 lds\n"
"v_mfma_f32_16x16x32_bf16 a[204:207], a[92:95], v[44:47], a[204:207]\n"
"ds_read_b64_tr_b16 v[54:55], v15 offset:58368\n"
"v_mfma_f32_16x16x32_bf16 a[208:211], a[96:99], v[40:43], a[208:211]\n"
"v_mfma_f32_16x16x32_bf16 a[208:211], a[100:103], v[44:47], a[208:211]\n"
"v_mfma_f32_16x16x32_bf16 a[212:215], a[104:107], v[40:43], a[212:215]\n"
"buffer_load_dwordx4 v5, s[8:11], s51 offen offset:3072 lds\n"
"v_mfma_f32_16x16x32_bf16 a[212:215], a[108:111], v[44:47], a[212:215]\n"
"s_add_u32 s51, s51, s27\n"
"v_mfma_f32_16x16x32_bf16 a[216:219], a[112:115], v[40:43], a[216:219]\n"
"v_mfma_f32_16x16x32_bf16 a[216:219], a[116:119], v[44:47], a[216:219]\n"
"v_mfma_f32_16x16x32_bf16 a[220:223], a[120:123], v[40:43], a[220:223]\n"
"v_mfma_f32_16x16x32_bf16 a[220:223], a[124:127], v[44:47], a[220:223]\n"
"s_waitcnt vmcnt(17) lgkmcnt(0)\n"
"v_mfma_f32_16x16x32_bf16 a[224:227], a[64:67], v[48:51], a[224:227]\n"
"s_barrier\n"
"v_mfma_f32_16x16x32_bf16 a[224:227], a[68:71], v[52:55], a[224:227]\n"
"ds_read_b64_tr_b16 v[40:41], v12\n"
"ds_read_b64_tr_b16 v[42:43], v12 offset:1024\n"
"v_mfma_f32_16x16x32_bf16 a[228:231], a[72:75], v[48:51], a[228:231]\n"
"ds_read_b64_tr_b16 v[44:45], v12 offset:8192\n"
"ds_read_b64_tr_b16 v[46:47], v12 offset:9216\n"
"v_mfma_f32_16x16x32_bf16 a[228:231], a[76:79], v[52:55], a[228:231]\n"
"ds_read_b128 a[64:67], v10\n"
"s_add_u32 m0, s78, 0x14000\n"
"v_mfma_f32_16x16x32_bf16 a[232:235], a[80:83], v[48:51], a[232:235]\n"
"ds_read_b128 a[68:71], v11\n"
"buffer_load_dwordx4 v6, s[12:15], s52 offen lds\n"
"v_mfma_f32_16x16x32_bf16 a[232:235], a[84:87], v[52:55], a[232:235]\n"
"ds_read_b128 a[72:75], v10 offset:2048\n"
"v_mfma_f32_16x16x32_bf16 a[236:239], a[88:91], v[48:51], a[236:239]\n"
"ds_read_b128 a[76:79], v11 offset:2048\n"
"v_mfma_f32_16x16x32_bf16 a[236:239], a[92:95], v[52:55], a[236:239]\n"
"ds_read_b128 a[80:83], v10 offset:4096\n"
"v_mfma_f32_16x16x32_bf16 a[240:243], a[96:99], v[48:51], a[240:243]\n"
"ds_read_b128 a[84:87], v11 offset:4096\n"
"v_mfma_f32_16x16x32_bf16 a[240:243], a[100:103], v[52:55], a[240:243]\n"
"ds_read_b128 a[88:91], v10 offset:6144\n"
"v_mfma_f32_16x16x32_bf16 a[244:247], a[104:107], v[48:51], a[244:247]\n"
"ds_read_b128 a[92:95], v11 offset:6144\n"
"buffer_load_dwordx4 v7, s[12:15], s52 offen offset:1024 lds\n"
"v_mfma_f32_16x16x32_bf16 a[244:247], a[108:111], v[52:55], a[244:247]\n"
"ds_read_b128 a[96:99], v10 offset:16384\n"
"v_mfma_f32_16x16x32_bf16 a[248:251], a[112:115], v[48:51], a[248:251]\n"
"ds_read_b128 a[100:103], v11 offset:16384\n"
"v_mfma_f32_16x16x32_bf16 a[248:251], a[116:119], v[52:55], a[248:251]\n"
"ds_read_b128 a[104:107], v10 offset:18432\n"
"v_mfma_f32_16x16x32_bf16 a[252:255], a[120:123], v[48:51], a[252:255]\n"
"ds_read_b128 a[108:111], v11 offset:18432\n"
"v_mfma_f32_16x16x32_bf16 a[252:255], a[124:127], v[52:55], a[252:255]\n"
"ds_read_b128 a[112:115], v10 offset:20480\n"
"s_waitcnt lgkmcnt(9)\n"
"v_mfma_f32_16x16x32_bf16 v[128:131], v[40:43], a[64:67], v[128:131]\n"
"ds_read_b128 a[116:119], v11 offset:20480\n"
"v_mfma_f32_16x16x32_bf16 v[128:131], v[44:47], a[68:71], v[128:131]\n"
"ds_read_b128 a[120:123], v10 offset:22528\n"
"v_mfma_f32_16x16x32_bf16 v[132:135], v[40:43], a[72:75], v[132:135]\n"
"ds_read_b128 a[124:127], v11 offset:22528\n"
"buffer_load_dwordx4 v8, s[12:15], s52 offen offset:2048 lds\n"
"v_mfma_f32_16x16x32_bf16 v[132:135], v[44:47], a[76:79], v[132:135]\n"
"ds_read_b64_tr_b16 v[48:49], v13\n"
"s_waitcnt lgkmcnt(9)\n"
"v_mfma_f32_16x16x32_bf16 v[136:139], v[40:43], a[80:83], v[136:139]\n"
"ds_read_b64_tr_b16 v[50:51], v13 offset:1024\n"
"v_mfma_f32_16x16x32_bf16 v[136:139], v[44:47], a[84:87], v[136:139]\n"
"ds_read_b64_tr_b16 v[52:53], v13 offset:8192\n"
"v_mfma_f32_16x16x32_bf16 v[140:143], v[40:43], a[88:91], v[140:143]\n"
"ds_read_b64_tr_b16 v[54:55], v13 offset:9216\n"
"v_mfma_f32_16x16x32_bf16 v[140:143], v[44:47], a[92:95], v[140:143]\n"
"buffer_load_dword v20, v25, s[4:7], s61 offen sc1\n"
"s_waitcnt lgkmcnt(8)\n"
"v_mfma_f32_16x16x32_bf16 v[144:147], v[40:43], a[96:99], v[144:147]\n"
"v_mfma_f32_16x16x32_bf16 v[144:147], v[44:47], a[100:103], v[144:147]\n"
"v_mfma_f32_16x16x32_bf16 v[148:151], v[40:43], a[104:107], v[148:151]\n"
"buffer_load_dwordx4 v9, s[12:15], s52 offen offset:3072 lds\n"
"s_add_u32 m0, s78, 0x24000\n"
"v_mfma_f32_16x16x32_bf16 v[148:151], v[44:47], a[108:111], v[148:151]\n"
"s_add_u32 s52, s52, s31\n"
"s_waitcnt lgkmcnt(4)\n"
"v_mfma_f32_16x16x32_bf16 v[152:155], v[40:43], a[112:115], v[152:155]\n"
"v_mfma_f32_16x16x32_bf16 v[152:155], v[44:47], a[116:119], v[152:155]\n"
"v_mfma_f32_16x16x32_bf16 v[156:159], v[40:43], a[120:123], v[156:159]\n"
"buffer_load_dwordx4 v6, s[20:23], s58 offen lds\n"
"v_mfma_f32_16x16x32_bf16 v[156:159], v[44:47], a[124:127], v[156:159]\n"
"s_waitcnt lgkmcnt(0)\n"
"v_mfma_f32_16x16x32_bf16 v[160:163], v[48:51], a[64:67], v[160:163]\n"
"v_mfma_f32_16x16x32_bf16 v[160:163], v[52:55], a[68:71], v[160:163]\n"
"ds_read_b64_tr_b16 v[40:41], v14\n"
"v_mfma_f32_16x16x32_bf16 v[164:167], v[48:51], a[72:75], v[164:167]\n"
"buffer_load_dwordx4 v7, s[20:23], s58 offen offset:1024 lds\n"
"v_mfma_f32_16x16x32_bf16 v[164:167], v[52:55], a[76:79], v[164:167]\n"
"ds_read_b64_tr_b16 v[42:43], v14 offset:1024\n"
"v_mfma_f32_16x16x32_bf16 v[168:171], v[48:51], a[80:83], v[168:171]\n"
"v_mfma_f32_16x16x32_bf16 v[168:171], v[52:55], a[84:87], v[168:171]\n"
"ds_read_b64_tr_b16 v[44:45], v14 offset:8192\n"
"v_mfma_f32_16x16x32_bf16 v[172:175], v[48:51], a[88:91], v[172:175]\n"
"buffer_load_dwordx4 v8, s[20:23], s58 offen offset:2048 lds\n"
"v_mfma_f32_16x16x32_bf16 v[172:175], v[52:55], a[92:95], v[172:175]\n"
"ds_read_b64_tr_b16 v[46:47], v14 offset:9216\n"
"v_mfma_f32_16x16x32_bf16 v[176:179], v[48:51], a[96:99], v[176:179]\n"
"v_mfma_f32_16x16x32_bf16 v[176:179], v[52:55], a[100:103], v[176:179]\n"
"v_mfma_f32_16x16x32_bf16 v[180:183], v[48:51], a[104:107], v[180:183]\n"
"buffer_load_dwordx4 v9, s[20:23], s58 offen offset:3072 lds\n"
"s_add_u32 m0, s78, 0x1c000\n"
"v_mfma_f32_16x16x32_bf16 v[180:183], v[52:55], a[108:111], v[180:183]\n"
"s_add_u32 s58, s58, 0x80000\n"
"v_mfma_f32_16x16x32_bf16 v[184:187], v[48:51], a[112:115], v[184:187]\n"
"s_cmp_eq_u32 s58, 0x500000\n"
"s_cmov_b32 s58, 0\n"
"v_mfma_f32_16x16x32_bf16 v[184:187], v[52:55], a[116:119], v[184:187]\n"
"v_mfma_f32_16x16x32_bf16 v[188:191], v[48:51], a[120:123], v[188:191]\n"
"buffer_load_dwordx4 v6, s[16:19], s53 offen lds\n"
"v_mfma_f32_16x16x32_bf16 v[188:191], v[52:55], a[124:127], v[188:191]\n"
"s_waitcnt lgkmcnt(0)\n"
"v_mfma_f32_16x16x32_bf16 v[192:195], v[40:43], a[64:67], v[192:195]\n"
"v_mfma_f32_16x16x32_bf16 v[192:195], v[44:47], a[68:71], v[192:195]\n"
"ds_read_b64_tr_b16 v[48:49], v15\n"
"v_mfma_f32_16x16x32_bf16 v[196:199], v[40:43], a[72:75], v[196:199]\n"
"buffer_load_dwordx4 v7, s[16:19], s53 offen offset:1024 lds\n"
"v_mfma_f32_16x16x32_bf16 v[196:199], v[44:47], a[76:79], v[196:199]\n"
"ds_read_b64_tr_b16 v[50:51], v15 offset:1024\n"
"v_mfma_f32_16x16x32_bf16 v[200:203], v[40:43], a[80:83], v[200:203]\n"
"v_mfma_f32_16x16x32_bf16 v[200:203], v[44:47], a[84:87], v[200:203]\n"
"ds_read_b64_tr_b16 v[52:53], v15 offset:8192\n"
"v_mfma_f32_16x16x32_bf16 v[204:207], v[40:43], a[88:91], v[204:207]\n"
"buffer_load_dwordx4 v8, s[16:19], s53 offen offset:2048 lds\n"
"v_mfma_f32_16x16x32_bf16 v[204:207], v[44:47], a[92:95], v[204:207]\n"
"ds_read_b64_tr_b16 v[54:55], v15 offset:9216\n"
"v_mfma_f32_16x16x32_bf16 v[208:211], v[40:43], a[96:99], v[208:211]\n"
"v_mfma_f32_16x16x32_bf16 v[208:211], v[44:47], a[100:103], v[208:211]\n"
"v_mfma_f32_16x16x32_bf16 v[212:215], v[40:43], a[104:107], v[212:215]\n"
"buffer_load_dwordx4 v9, s[16:19], s53 offen offset:3072 lds\n"
"v_mfma_f32_16x16x32_bf16 v[212:215], v[44:47], a[108:111], v[212:215]\n"
"s_add_u32 s53, s53, s31\n"
"v_mfma_f32_16x16x32_bf16 v[216:219], v[40:43], a[112:115], v[216:219]\n"
"v_mfma_f32_16x16x32_bf16 v[216:219], v[44:47], a[116:119], v[216:219]\n"
"v_mfma_f32_16x16x32_bf16 v[220:223], v[40:43], a[120:123], v[220:223]\n"
"v_mfma_f32_16x16x32_bf16 v[220:223], v[44:47], a[124:127], v[220:223]\n"
"s_waitcnt vmcnt(26) lgkmcnt(0)\n"
"v_mfma_f32_16x16x32_bf16 v[224:227], v[48:51], a[64:67], v[224:227]\n"
"s_barrier\n"
"v_mfma_f32_16x16x32_bf16 v[224:227], v[52:55], a[68:71], v[224:227]\n"
"ds_read_b64_tr_b16 v[40:41], v12 offset:32768\n"
"s_add_u32 m0, s78, 0\n"
"v_mfma_f32_16x16x32_bf16 v[228:231], v[48:51], a[72:75], v[228:231]\n"
"buffer_load_dwordx4 v6, s[20:23], s58 offen lds\n"
"v_mfma_f32_16x16x32_bf16 v[228:231], v[52:55], a[76:79], v[228:231]\n"
"ds_read_b64_tr_b16 v[42:43], v12 offset:33792\n"
"v_mfma_f32_16x16x32_bf16 v[232:235], v[48:51], a[80:83], v[232:235]\n"
"v_mfma_f32_16x16x32_bf16 v[232:235], v[52:55], a[84:87], v[232:235]\n"
"ds_read_b64_tr_b16 v[44:45], v12 offset:40960\n"
"v_mfma_f32_16x16x32_bf16 v[236:239], v[48:51], a[88:91], v[236:239]\n"
"buffer_load_dwordx4 v7, s[20:23], s58 offen offset:1024 lds\n"
"v_mfma_f32_16x16x32_bf16 v[236:239], v[52:55], a[92:95], v[236:239]\n"
"ds_read_b64_tr_b16 v[46:47], v12 offset:41984\n"
"v_mfma_f32_16x16x32_bf16 v[240:243], v[48:51], a[96:99], v[240:243]\n"
"v_mfma_f32_16x16x32_bf16 v[240:243], v[52:55], a[100:103], v[240:243]\n"
"v_mfma_f32_16x16x32_bf16 v[244:247], v[48:51], a[104:107], v[244:247]\n"
"buffer_load_dwordx4 v8, s[20:23], s58 offen offset:2048 lds\n"
"v_mfma_f32_16x16x32_bf16 v[244:247], v[52:55], a[108:111], v[244:247]\n"
"v_mfma_f32_16x16x32_bf16 v[248:251], v[48:51], a[112:115], v[248:251]\n"
"s_waitcnt vmcnt(12)\n"
"v_cmp_ne_u32 vcc, s82, v20\n"
"s_nop 0\n"
"s_cbranch_vccnz .Ladaln_block_P_h\n"
".Ladaln_block_P_h_R:\n"
"v_mfma_f32_16x16x32_bf16 v[248:251], v[52:55], a[116:119], v[248:251]\n"
"v_mfma_f32_16x16x32_bf16 v[252:255], v[48:51], a[120:123], v[252:255]\n"
"buffer_load_dwordx4 v9, s[20:23], s58 offen offset:3072 lds\n"
"v_mfma_f32_16x16x32_bf16 v[252:255], v[52:55], a[124:127], v[252:255]\n"
"s_add_u32 s58, s58, 0x80000\n"
"s_waitcnt lgkmcnt(0)\n"
"v_mfma_f32_16x16x32_bf16 a[128:131], a[64:67], v[40:43], a[128:131]\n"
"s_cmp_eq_u32 s58, 0x500000\n"
"s_cmov_b32 s58, 0\n"
"v_mfma_f32_16x16x32_bf16 a[128:131], a[68:71], v[44:47], a[128:131]\n"
"ds_read_b64_tr_b16 v[48:49], v13 offset:32768\n"
"v_mfma_f32_16x16x32_bf16 a[132:135], a[72:75], v[40:43], a[132:135]\n"
"v_mfma_f32_16x16x32_bf16 a[132:135], a[76:79], v[44:47], a[132:135]\n"
"ds_read_b64_tr_b16 v[50:51], v13 offset:33792\n"
"v_mfma_f32_16x16x32_bf16 a[136:139], a[80:83], v[40:43], a[136:139]\n"
"v_mfma_f32_16x16x32_bf16 a[136:139], a[84:87], v[44:47], a[136:139]\n"
"ds_read_b64_tr_b16 v[52:53], v13 offset:40960\n"
"v_mfma_f32_16x16x32_bf16 a[140:143], a[88:91], v[40:43], a[140:143]\n"
"v_mfma_f32_16x16x32_bf16 a[140:143], a[92:95], v[44:47], a[140:143]\n"
"ds_read_b64_tr_b16 v[54:55], v13 offset:41984\n"
"s_add_u32 m0, s78, 0x4000\n"
"v_mfma_f32_16x16x32_bf16 a[144:147], a[96:99], v[40:43], a[144:147]\n"
"buffer_load_dwordx4 v6, s[20:23], s58 offen lds\n"
"v_mfma_f32_16x16x32_bf16 a[144:147], a[100:103], v[44:47], a[144:147]\n"
"v_mfma_f32_16x16x32_bf16 a[148:151], a[104:107], v[40:43], a[148:151]\n"
"v_mfma_f32_16x16x32_bf16 a[148:151], a[108:111], v[44:47], a[148:151]\n"
"v_mfma_f32_16x16x32_bf16 a[152:155], a[112:115], v[40:43], a[152:155]\n"
"buffer_load_dwordx4 v7, s[20:23], s58 offen offset:1024 lds\n"
"v_mfma_f32_16x16x32_bf16 a[152:155], a[116:119], v[44:47], a[152:155]\n"
"v_mfma_f32_16x16x32_bf16 a[156:159], a[120:123], v[40:43], a[156:159]\n"
"v_mfma_f32_16x16x32_bf16 a[156:159], a[124:127], v[44:47], a[156:159]\n"
"s_waitcnt lgkmcnt(0)\n"
"v_mfma_f32_16x16x32_bf16 a[160:163], a[64:67], v[48:51], a[160:163]\n"
"buffer_load_dwordx4 v8, s[20:23], s58 offen offset:2048 lds\n"
"v_mfma_f32_16x16x32_bf16 a[160:163], a[68:71], v[52:55], a[160:163]\n"
"ds_read_b64_tr_b16 v[40:41], v14 offset:32768\n"
"v_mfma_f32_16x16x32_bf16 a[164:167], a[72:75], v[48:51], a[164:167]\n"
"v_mfma_f32_16x16x32_bf16 a[164:167], a[76:79], v[52:55], a[164:167]\n"
"ds_read_b64_tr_b16 v[42:43], v14 offset:33792\n"
"v_mfma_f32_16x16x32_bf16 a[168:171], a[80:83], v[48:51], a[168:171]\n"
"buffer_load_dwordx4 v9, s[20:23], s58 offen offset:3072 lds\n"
"v_mfma_f32_16x16x32_bf16 a[168:171], a[84:87], v[52:55], a[168:171]\n"
"ds_read_b64_tr_b16 v[44:45], v14 offset:40960\n"
"s_add_u32 s58, s58, 0x80000\n"
"v_mfma_f32_16x16x32_bf16 a[172:175], a[88:91], v[48:51], a[172:175]\n"
"s_cmp_eq_u32 s58, 0x500000\n"
"s_cmov_b32 s58, 0\n"
"v_mfma_f32_16x16x32_bf16 a[172:175], a[92:95], v[52:55], a[172:175]\n"
"ds_read_b64_tr_b16 v[46:47], v14 offset:41984\n"
"v_mfma_f32_16x16x32_bf16 a[176:179], a[96:99], v[48:51], a[176:179]\n"
"buffer_load_dwordx4 a[0:3], v21, s[4:7], s59 offen sc1\n"
"v_mfma_f32_16x16x32_bf16 a[176:179], a[100:103], v[52:55], a[176:179]\n"
"v_mfma_f32_16x16x32_bf16 a[180:183], a[104:107], v[48:51], a[180:183]\n"
"v_mfma_f32_16x16x32_bf16 a[180:183], a[108:111], v[52:55], a[180:183]\n"
"v_mfma_f32_16x16x32_bf16 a[184:187], a[112:115], v[48:51], a[184:187]\n"
"v_mfma_f32_16x16x32_bf16 a[184:187], a[116:119], v[52:55], a[184:187]\n"
"s_add_u32 m0, s78, 0x10000\n"
"v_mfma_f32_16x16x32_bf16 a[188:191], a[120:123], v[48:51], a[188:191]\n"
"buffer_load_dwordx4 v6, s[20:23], s58 offen lds\n"
"v_mfma_f32_16x16x32_bf16 a[188:191], a[124:127], v[52:55], a[188:191]\n"
"s_waitcnt lgkmcnt(0)\n"
"v_mfma_f32_16x16x32_bf16 a[192:195], a[64:67], v[40:43], a[192:195]\n"
"v_mfma_f32_16x16x32_bf16 a[192:195], a[68:71], v[44:47], a[192:195]\n"
"ds_read_b64_tr_b16 v[48:49], v15 offset:32768\n"
"v_mfma_f32_16x16x32_bf16 a[196:199], a[72:75], v[40:43], a[196:199]\n"
"buffer_load_dwordx4 v7, s[20:23], s58 offen offset:1024 lds\n"
"v_mfma_f32_16x16x32_bf16 a[196:199], a[76:79], v[44:47], a[196:199]\n"
"ds_read_b64_tr_b16 v[50:51], v15 offset:33792\n"
"v_mfma_f32_16x16x32_bf16 a[200:203], a[80:83], v[40:43], a[200:203]\n"
"v_mfma_f32_16x16x32_bf16 a[200:203], a[84:87], v[44:47], a[200:203]\n"
"ds_read_b64_tr_b16 v[52:53], v15 offset:40960\n"
"v_mfma_f32_16x16x32_bf16 a[204:207], a[88:91], v[40:43], a[204:207]\n"
"buffer_load_dwordx4 v8, s[20:23], s58 offen offset:2048 lds\n"
"v_mfma_f32_16x16x32_bf16 a[204:207], a[92:95], v[44:47], a[204:207]\n"
"ds_read_b64_tr_b16 v[54:55], v15 offset:41984\n"
"v_mfma_f32_16x16x32_bf16 a[208:211], a[96:99], v[40:43], a[208:211]\n"
"v_mfma_f32_16x16x32_bf16 a[208:211], a[100:103], v[44:47], a[208:211]\n"
"v_mfma_f32_16x16x32_bf16 a[212:215], a[104:107], v[40:43], a[212:215]\n"
"buffer_load_dwordx4 v9, s[20:23], s58 offen offset:3072 lds\n"
"v_mfma_f32_16x16x32_bf16 a[212:215], a[108:111], v[44:47], a[212:215]\n"
"s_add_u32 s58, s58, 0x80000\n"
"v_mfma_f32_16x16x32_bf16 a[216:219], a[112:115], v[40:43], a[216:219]\n"
"s_cmp_eq_u32 s58, 0x500000\n"
"s_cmov_b32 s58, 0\n"
"v_mfma_f32_16x16x32_bf16 a[216:219], a[116:119], v[44:47], a[216:219]\n"
"v_mfma_f32_16x16x32_bf16 a[220:223], a[120:123], v[40:43], a[220:223]\n"
"buffer_load_dwordx4 a[4:7], v21, s[4:7], s59 offen offset:1024 sc1\n"
"v_mfma_f32_16x16x32_bf16 a[220:223], a[124:127], v[44:47], a[220:223]\n"
"s_waitcnt vmcnt(22) lgkmcnt(0)\n"
"v_mfma_f32_16x16x32_bf16 a[224:227], a[64:67], v[48:51], a[224:227]\n"
"s_barrier\n"
"v_mfma_f32_16x16x32_bf16 a[224:227], a[68:71], v[52:55], a[224:227]\n"
"ds_read_b64_tr_b16 v[40:41], v12 offset:16384\n"
"ds_read_b64_tr_b16 v[42:43], v12 offset:17408\n"
"v_mfma_f32_16x16x32_bf16 a[228:231], a[72:75], v[48:51], a[228:231]\n"
"ds_read_b64_tr_b16 v[44:45], v12 offset:24576\n"
"ds_read_b64_tr_b16 v[46:47], v12 offset:25600\n"
"v_mfma_f32_16x16x32_bf16 a[228:231], a[76:79], v[52:55], a[228:231]\n"
"ds_read_b128 a[64:67], v10 offset:32768\n"
"s_add_u32 m0, s78, 0x18000\n"
"v_mfma_f32_16x16x32_bf16 a[232:235], a[80:83], v[48:51], a[232:235]\n"
"ds_read_b128 a[68:71], v11 offset:32768\n"
"buffer_load_dwordx4 v6, s[20:23], s58 offen lds\n"
"v_mfma_f32_16x16x32_bf16 a[232:235], a[84:87], v[52:55], a[232:235]\n"
"ds_read_b128 a[72:75], v10 offset:34816\n"
"v_mfma_f32_16x16x32_bf16 a[236:239], a[88:91], v[48:51], a[236:239]\n"
"ds_read_b128 a[76:79], v11 offset:34816\n"
"v_mfma_f32_16x16x32_bf16 a[236:239], a[92:95], v[52:55], a[236:239]\n"
"ds_read_b128 a[80:83], v10 offset:36864\n"
"v_mfma_f32_16x16x32_bf16 a[240:243], a[96:99], v[48:51], a[240:243]\n"
"ds_read_b128 a[84:87], v11 offset:36864\n"
"buffer_load_dwordx4 v7, s[20:23], s58 offen offset:1024 lds\n"
"v_mfma_f32_16x16x32_bf16 a[240:243], a[100:103], v[52:55], a[240:243]\n"
"ds_read_b128 a[88:91], v10 offset:38912\n"
"v_mfma_f32_16x16x32_bf16 a[244:247], a[104:107], v[48:51], a[244:247]\n"
"ds_read_b128 a[92:95], v11 offset:38912\n"
"v_mfma_f32_16x16x32_bf16 a[244:247], a[108:111], v[52:55], a[244:247]\n"
"ds_read_b128 a[96:99], v10 offset:49152\n"
"v_mfma_f32_16x16x32_bf16 a[248:251], a[112:115], v[48:51], a[248:251]\n"
"ds_read_b128 a[100:103], v11 offset:49152\n"
"buffer_load_dwordx4 v8, s[20:23], s58 offen offset:2048 lds\n"
"v_mfma_f32_16x16x32_bf16 a[248:251], a[116:119], v[52:55], a[248:251]\n"
"ds_read_b128 a[104:107], v10 offset:51200\n"
"v_mfma_f32_16x16x32_bf16 a[252:255], a[120:123], v[48:51], a[252:255]\n"
"ds_read_b128 a[108:111], v11 offset:51200\n"
"v_mfma_f32_16x16x32_bf16 a[252:255], a[124:127], v[52:55], a[252:255]\n"
"ds_read_b128 a[112:115], v10 offset:53248\n"
"s_waitcnt lgkmcnt(9)\n"
"v_mfma_f32_16x16x32_bf16 v[128:131], v[40:43], a[64:67], v[128:131]\n"
"ds_read_b128 a[116:119], v11 offset:53248\n"
"buffer_load_dwordx4 v9, s[20:23], s58 offen offset:3072 lds\n"
"v_mfma_f32_16x16x32_bf16 v[128:131], v[44:47], a[68:71], v[128:131]\n"
"ds_read_b128 a[120:123], v10 offset:55296\n"
"s_add_u32 s58, s58, 0x80000\n"
"v_mfma_f32_16x16x32_bf16 v[132:135], v[40:43], a[72:75], v[132:135]\n"
"ds_read_b128 a[124:127], v11 offset:55296\n"
"v_mfma_f32_16x16x32_bf16 v[132:135], v[44:47], a[76:79], v[132:135]\n"
"ds_read_b64_tr_b16 v[48:49], v13 offset:16384\n"
"s_waitcnt lgkmcnt(9)\n"
"v_mfma_f32_16x16x32_bf16 v[136:139], v[40:43], a[80:83], v[136:139]\n"
"ds_read_b64_tr_b16 v[50:51], v13 offset:17408\n"
"v_mfma_f32_16x16x32_bf16 v[136:139], v[44:47], a[84:87], v[136:139]\n"
"ds_read_b64_tr_b16 v[52:53], v13 offset:24576\n"
"v_mfma_f32_16x16x32_bf16 v[140:143], v[40:43], a[88:91], v[140:143]\n"
"ds_read_b64_tr_b16 v[54:55], v13 offset:25600\n"
"v_mfma_f32_16x16x32_bf16 v[140:143], v[44:47], a[92:95], v[140:143]\n"
"buffer_load_dwordx4 a[8:11], v21, s[4:7], s59 offen offset:2048 sc1\n"
"s_waitcnt lgkmcnt(8)\n"
"v_mfma_f32_16x16x32_bf16 v[144:147], v[40:43], a[96:99], v[144:147]\n"
"s_cmp_eq_u32 s58, 0x500000\n"
"s_cmov_b32 s58, 0\n"
"v_mfma_f32_16x16x32_bf16 v[144:147], v[44:47], a[100:103], v[144:147]\n"
"v_mfma_f32_16x16x32_bf16 v[148:151], v[40:43], a[104:107], v[148:151]\n"
"v_mfma_f32_16x16x32_bf16 v[148:151], v[44:47], a[108:111], v[148:151]\n"
"buffer_load_dwordx4 a[12:15], v21, s[4:7], s59 offen offset:3072 sc1\n"
"s_waitcnt lgkmcnt(4)\n"
"v_mfma_f32_16x16x32_bf16 v[152:155], v[40:43], a[112:115], v[152:155]\n"
"s_add_u32 s59, s59, 0x1000\n"
"v_mfma_f32_16x16x32_bf16 v[152:155], v[44:47], a[116:119], v[152:155]\n"
"v_mfma_f32_16x16x32_bf16 v[156:159], v[40:43], a[120:123], v[156:159]\n"
"v_mfma_f32_16x16x32_bf16 v[156:159], v[44:47], a[124:127], v[156:159]\n"
"buffer_load_dwordx4 a[16:19], v21, s[4:7], s59 offen sc1\n"
"s_waitcnt lgkmcnt(0)\n"
"v_mfma_f32_16x16x32_bf16 v[160:163], v[48:51], a[64:67], v[160:163]\n"
"v_mfma_f32_16x16x32_bf16 v[160:163], v[52:55], a[68:71], v[160:163]\n"
"ds_read_b64_tr_b16 v[40:41], v14 offset:16384\n"
"v_mfma_f32_16x16x32_bf16 v[164:167], v[48:51], a[72:75], v[164:167]\n"
"v_mfma_f32_16x16x32_bf16 v[164:167], v[52:55], a[76:79], v[164:167]\n"
"ds_read_b64_tr_b16 v[42:43], v14 offset:17408\n"
"v_mfma_f32_16x16x32_bf16 v[168:171], v[48:51], a[80:83], v[168:171]\n"
"buffer_load_dwordx4 a[20:23], v21, s[4:7], s59 offen offset:1024 sc1\n"
"v_mfma_f32_16x16x32_bf16 v[168:171], v[52:55], a[84:87], v[168:171]\n"
"ds_read_b64_tr_b16 v[44:45], v14 offset:24576\n"
"v_mfma_f32_16x16x32_bf16 v[172:175], v[48:51], a[88:91], v[172:175]\n"
"v_mfma_f32_16x16x32_bf16 v[172:175], v[52:55], a[92:95], v[172:175]\n"
"ds_read_b64_tr_b16 v[46:47], v14 offset:25600\n"
"v_cvt_pk_bf16_f32 v56, v128, v129\n"
"v_mfma_f32_16x16x32_bf16 v[176:179], v[48:51], a[96:99], v[176:179]\n"
"buffer_load_dwordx4 a[24:27], v21, s[4:7], s59 offen offset:2048 sc1\n"
"v_cvt_pk_bf16_f32 v57, v130, v131\n"
"v_mfma_f32_16x16x32_bf16 v[176:179], v[52:55], a[100:103], v[176:179]\n"
"v_cvt_pk_bf16_f32 v58, v160, v161\n"
"v_cvt_pk_bf16_f32 v59, v162, v163\n"
"v_mfma_f32_16x16x32_bf16 v[180:183], v[48:51], a[104:107], v[180:183]\n"
"v_cvt_pk_bf16_f32 v60, v132, v133\n"
"v_cvt_pk_bf16_f32 v61, v134, v135\n"
"v_mfma_f32_16x16x32_bf16 v[180:183], v[52:55], a[108:111], v[180:183]\n"
"v_cvt_pk_bf16_f32 v62, v164, v165\n"
"v_cvt_pk_bf16_f32 v63, v166, v167\n"
"v_mfma_f32_16x16x32_bf16 v[184:187], v[48:51], a[112:115], v[184:187]\n"
"buffer_load_dwordx4 a[28:31], v21, s[4:7], s59 offen offset:3072 sc1\n"
"v_cvt_pk_bf16_f32 v64, v136, v137\n"
"v_mfma_f32_16x16x32_bf16 v[184:187], v[52:55], a[116:119], v[184:187]\n"
"s_add_u32 s59, s59, 0x3000\n"
"v_cvt_pk_bf16_f32 v65, v138, v139\n"
"v_mfma_f32_16x16x32_bf16 v[188:191], v[48:51], a[120:123], v[188:191]\n"
"s_cmp_eq_u32 s59, 0x28000\n"
"s_cmov_b32 s59, 0\n"
"v_mfma_f32_16x16x32_bf16 v[188:191], v[52:55], a[124:127], v[188:191]\n"
"v_cvt_pk_bf16_f32 v66, v168, v169\n"
"s_waitcnt lgkmcnt(0)\n"
"v_mfma_f32_16x16x32_bf16 v[192:195], v[40:43], a[64:67], v[192:195]\n"
"buffer_load_dwordx4 a[32:35], v21, s[4:7], s59 offen sc1\n"
"v_cvt_pk_bf16_f32 v67, v170, v171\n"
"v_mfma_f32_16x16x32_bf16 v[192:195], v[44:47], a[68:71], v[192:195]\n"
"ds_read_b64_tr_b16 v[48:49], v15 offset:16384\n"
"v_cvt_pk_bf16_f32 v68, v140, v141\n"
"v_mfma_f32_16x16x32_bf16 v[196:199], v[40:43], a[72:75], v[196:199]\n"
"v_cvt_pk_bf16_f32 v69, v142, v143\n"
"v_cvt_pk_bf16_f32 v70, v172, v173\n"
"v_mfma_f32_16x16x32_bf16 v[196:199], v[44:47], a[76:79], v[196:199]\n"
"ds_read_b64_tr_b16 v[50:51], v15 offset:17408\n"
"v_cvt_pk_bf16_f32 v71, v174, v175\n"
"v_mfma_f32_16x16x32_bf16 v[200:203], v[40:43], a[80:83], v[200:203]\n"
"buffer_load_dwordx4 a[36:39], v21, s[4:7], s59 offen offset:1024 sc1\n"
"v_cvt_pk_bf16_f32 v88, v144, v145\n"
"v_mfma_f32_16x16x32_bf16 v[200:203], v[44:47], a[84:87], v[200:203]\n"
"ds_read_b64_tr_b16 v[52:53], v15 offset:24576\n"
"v_cvt_pk_bf16_f32 v89, v146, v147\n"
"v_mfma_f32_16x16x32_bf16 v[204:207], v[40:43], a[88:91], v[204:207]\n"
"v_cvt_pk_bf16_f32 v90, v176, v177\n"
"v_cvt_pk_bf16_f32 v91, v178, v179\n"
"v_mfma_f32_16x16x32_bf16 v[204:207], v[44:47], a[92:95], v[204:207]\n"
"ds_read_b64_tr_b16 v[54:55], v15 offset:25600\n"
"v_cvt_pk_bf16_f32 v92, v148, v149\n"
"v_mfma_f32_16x16x32_bf16 v[208:211], v[40:43], a[96:99], v[208:211]\n"
"buffer_load_dwordx4 a[40:43], v21, s[4:7], s59 offen offset:2048 sc1\n"
"v_cvt_pk_bf16_f32 v93, v150, v151\n"
"v_mfma_f32_16x16x32_bf16 v[208:211], v[44:47], a[100:103], v[208:211]\n"
"v_cvt_pk_bf16_f32 v94, v180, v181\n"
"v_cvt_pk_bf16_f32 v95, v182, v183\n"
"v_mfma_f32_16x16x32_bf16 v[212:215], v[40:43], a[104:107], v[212:215]\n"
"v_cvt_pk_bf16_f32 v96, v152, v153\n"
"v_cvt_pk_bf16_f32 v97, v154, v155\n"
"v_mfma_f32_16x16x32_bf16 v[212:215], v[44:47], a[108:111], v[212:215]\n"
"v_cvt_pk_bf16_f32 v98, v184, v185\n"
"v_cvt_pk_bf16_f32 v99, v186, v187\n"
"v_mfma_f32_16x16x32_bf16 v[216:219], v[40:43], a[112:115], v[216:219]\n"
"buffer_load_dwordx4 a[44:47], v21, s[4:7], s59 offen offset:3072 sc1\n"
"v_cvt_pk_bf16_f32 v100, v156, v157\n"
"v_mfma_f32_16x16x32_bf16 v[216:219], v[44:47], a[116:119], v[216:219]\n"
"s_add_u32 s59, s59, 0x1000\n"
"v_cvt_pk_bf16_f32 v101, v158, v159\n"
"v_mfma_f32_16x16x32_bf16 v[220:223], v[40:43], a[120:123], v[220:223]\n"
"v_cvt_pk_bf16_f32 v102, v188, v189\n"
"v_cvt_pk_bf16_f32 v103, v190, v191\n"
"v_mfma_f32_16x16x32_bf16 v[220:223], v[44:47], a[124:127], v[220:223]\n"
"s_waitcnt vmcnt(28) lgkmcnt(0)\n"
"v_mfma_f32_16x16x32_bf16 v[224:227], v[48:51], a[64:67], v[224:227]\n"
"s_barrier\n"
"buffer_load_dwordx4 a[48:51], v21, s[4:7], s59 offen sc1\n"
"v_mfma_f32_16x16x32_bf16 v[224:227], v[52:55], a[68:71], v[224:227]\n"
"ds_read_b64_tr_b16 v[40:41], v12 offset:49152\n"
"v_mfma_f32_16x16x32_bf16 v[228:231], v[48:51], a[72:75], v[228:231]\n"
"v_mfma_f32_16x16x32_bf16 v[228:231], v[52:55], a[76:79], v[228:231]\n"
"ds_read_b64_tr_b16 v[42:43], v12 offset:50176\n"
"v_mfma_f32_16x16x32_bf16 v[232:235], v[48:51], a[80:83], v[232:235]\n"
"buffer_load_dwordx4 a[52:55], v21, s[4:7], s59 offen offset:1024 sc1\n"
"v_mfma_f32_16x16x32_bf16 v[232:235], v[52:55], a[84:87], v[232:235]\n"
"ds_read_b64_tr_b16 v[44:45], v12 offset:57344\n"
"v_mfma_f32_16x16x32_bf16 v[236:239], v[48:51], a[88:91], v[236:239]\n"
"v_mfma_f32_16x16x32_bf16 v[236:239], v[52:55], a[92:95], v[236:239]\n"
"ds_read_b64_tr_b16 v[46:47], v12 offset:58368\n"
"v_cvt_pk_bf16_f32 v72, v192, v193\n"
"v_mfma_f32_16x16x32_bf16 v[240:243], v[48:51], a[96:99], v[240:243]\n"
"buffer_load_dwordx4 a[56:59], v21, s[4:7], s59 offen offset:2048 sc1\n"
"v_cvt_pk_bf16_f32 v73, v194, v195\n"
"v_mfma_f32_16x16x32_bf16 v[240:243], v[52:55], a[100:103], v[240:243]\n"
"v_cvt_pk_bf16_f32 v74, v224, v225\n"
"v_cvt_pk_bf16_f32 v75, v226, v227\n"
"v_mfma_f32_16x16x32_bf16 v[244:247], v[48:51], a[104:107], v[244:247]\n"
"v_cvt_pk_bf16_f32 v76, v196, v197\n"
"v_cvt_pk_bf16_f32 v77, v198, v199\n"
"v_mfma_f32_16x16x32_bf16 v[244:247], v[52:55], a[108:111], v[244:247]\n"
"v_cvt_pk_bf16_f32 v78, v228, v229\n"
"v_cvt_pk_bf16_f32 v79, v230, v231\n"
"v_mfma_f32_16x16x32_bf16 v[248:251], v[48:51], a[112:115], v[248:251]\n"
"v_cvt_pk_bf16_f32 v80, v200, v201\n"
"v_cvt_pk_bf16_f32 v81, v202, v203\n"
"v_mfma_f32_16x16x32_bf16 v[248:251], v[52:55], a[116:119], v[248:251]\n"
"v_cvt_pk_bf16_f32 v82, v232, v233\n"
"v_cvt_pk_bf16_f32 v83, v234, v235\n"
"v_mfma_f32_16x16x32_bf16 v[252:255], v[48:51], a[120:123], v[252:255]\n"
"s_add_u32 s60, s32, 0\n"
"v_cvt_pk_bf16_f32 v84, v204, v205\n"
"v_mfma_f32_16x16x32_bf16 v[252:255], v[52:55], a[124:127], v[252:255]\n"
"buffer_store_dwordx4 v[56:59], v1, s[4:7], s60 offen sc1\n"
"s_waitcnt lgkmcnt(0)\n"
"v_mfma_f32_16x16x32_bf16 a[128:131], a[64:67], v[40:43], a[128:131]\n"
"v_cvt_pk_bf16_f32 v85, v206, v207\n"
"v_cvt_pk_bf16_f32 v86, v236, v237\n"
"v_mfma_f32_16x16x32_bf16 a[128:131], a[68:71], v[44:47], a[128:131]\n"
"ds_read_b64_tr_b16 v[48:49], v13 offset:49152\n"
"v_cvt_pk_bf16_f32 v87, v238, v239\n"
"v_mfma_f32_16x16x32_bf16 a[132:135], a[72:75], v[40:43], a[132:135]\n"
"v_cvt_pk_bf16_f32 v104, v208, v209\n"
"v_cvt_pk_bf16_f32 v105, v210, v211\n"
"v_mfma_f32_16x16x32_bf16 a[132:135], a[76:79], v[44:47], a[132:135]\n"
"ds_read_b64_tr_b16 v[50:51], v13 offset:50176\n"
"v_cvt_pk_bf16_f32 v106, v240, v241\n"
"v_mfma_f32_16x16x32_bf16 a[136:139], a[80:83], v[40:43], a[136:139]\n"
"buffer_load_dwordx4 a[60:63], v21, s[4:7], s59 offen offset:3072 sc1\n"
"v_cvt_pk_bf16_f32 v107, v242, v243\n"
"v_mfma_f32_16x16x32_bf16 a[136:139], a[84:87], v[44:47], a[136:139]\n"
"ds_read_b64_tr_b16 v[52:53], v13 offset:57344\n"
"s_add_u32 s59, s59, 0x3000\n"
"v_mfma_f32_16x16x32_bf16 a[140:143], a[88:91], v[40:43], a[140:143]\n"
"s_cmp_eq_u32 s59, 0x28000\n"
"s_cmov_b32 s59, 0\n"
"v_mfma_f32_16x16x32_bf16 a[140:143], a[92:95], v[44:47], a[140:143]\n"
"ds_read_b64_tr_b16 v[54:55], v13 offset:58368\n"
"v_cvt_pk_bf16_f32 v108, v212, v213\n"
"v_mfma_f32_16x16x32_bf16 a[144:147], a[96:99], v[40:43], a[144:147]\n"
"v_cvt_pk_bf16_f32 v109, v214, v215\n"
"v_cvt_pk_bf16_f32 v110, v244, v245\n"
"v_mfma_f32_16x16x32_bf16 a[144:147], a[100:103], v[44:47], a[144:147]\n"
"v_cvt_pk_bf16_f32 v111, v246, v247\n"
"v_cvt_pk_bf16_f32 v112, v216, v217\n"
"v_mfma_f32_16x16x32_bf16 a[148:151], a[104:107], v[40:43], a[148:151]\n"
"v_cvt_pk_bf16_f32 v113, v218, v219\n"
"v_cvt_pk_bf16_f32 v114, v248, v249\n"
"v_mfma_f32_16x16x32_bf16 a[148:151], a[108:111], v[44:47], a[148:151]\n"
"v_cvt_pk_bf16_f32 v115, v250, v251\n"
"v_cvt_pk_bf16_f32 v116, v220, v221\n"
"v_mfma_f32_16x16x32_bf16 a[152:155], a[112:115], v[40:43], a[152:155]\n"
"v_cvt_pk_bf16_f32 v117, v222, v223\n"
"v_cvt_pk_bf16_f32 v118, v252, v253\n"
"v_mfma_f32_16x16x32_bf16 a[152:155], a[116:119], v[44:47], a[152:155]\n"
"v_cvt_pk_bf16_f32 v119, v254, v255\n"
"v_mfma_f32_16x16x32_bf16 a[156:159], a[120:123], v[40:43], a[156:159]\n"
"v_mfma_f32_16x16x32_bf16 a[156:159], a[124:127], v[44:47], a[156:159]\n"
"s_waitcnt lgkmcnt(0)\n"
"v_mfma_f32_16x16x32_bf16 a[160:163], a[64:67], v[48:51], a[160:163]\n"
"v_mfma_f32_16x16x32_bf16 a[160:163], a[68:71], v[52:55], a[160:163]\n"
"ds_read_b64_tr_b16 v[40:41], v14 offset:49152\n"
"s_add_u32 s60, s32, 0x1000\n"
"v_mfma_f32_16x16x32_bf16 a[164:167], a[72:75], v[48:51], a[164:167]\n"
"buffer_store_dwordx4 v[60:63], v1, s[4:7], s60 offen sc1\n"
"v_mfma_f32_16x16x32_bf16 a[164:167], a[76:79], v[52:55], a[164:167]\n"
"ds_read_b64_tr_b16 v[42:43], v14 offset:50176\n"
"v_mfma_f32_16x16x32_bf16 a[168:171], a[80:83], v[48:51], a[168:171]\n"
"v_mfma_f32_16x16x32_bf16 a[168:171], a[84:87], v[52:55], a[168:171]\n"
"ds_read_b64_tr_b16 v[44:45], v14 offset:57344\n"
"v_mfma_f32_16x16x32_bf16 a[172:175], a[88:91], v[48:51], a[172:175]\n"
"v_mfma_f32_16x16x32_bf16 a[172:175], a[92:95], v[52:55], a[172:175]\n"
"ds_read_b64_tr_b16 v[46:47], v14 offset:58368\n"
"v_mfma_f32_16x16x32_bf16 a[176:179], a[96:99], v[48:51], a[176:179]\n"
"v_mfma_f32_16x16x32_bf16 a[176:179], a[100:103], v[52:55], a[176:179]\n"
"v_mfma_f32_16x16x32_bf16 a[180:183], a[104:107], v[48:51], a[180:183]\n"
"v_mfma_f32_16x16x32_bf16 a[180:183], a[108:111], v[52:55], a[180:183]\n"
"v_mfma_f32_16x16x32_bf16 a[184:187], a[112:115], v[48:51], a[184:187]\n"
"v_mfma_f32_16x16x32_bf16 a[184:187], a[116:119], v[52:55], a[184:187]\n"
"v_mfma_f32_16x16x32_bf16 a[188:191], a[120:123], v[48:51], a[188:191]\n"
"v_mfma_f32_16x16x32_bf16 a[188:191], a[124:127], v[52:55], a[188:191]\n"
"s_waitcnt lgkmcnt(0)\n"
"v_mfma_f32_16x16x32_bf16 a[192:195], a[64:67], v[40:43], a[192:195]\n"
"v_accvgpr_read_b32 v120, a128\n"
"v_accvgpr_read_b32 v121, a129\n"
"v_mfma_f32_16x16x32_bf16 a[192:195], a[68:71], v[44:47], a[192:195]\n"
"ds_read_b64_tr_b16 v[48:49], v15 offset:49152\n"
"v_accvgpr_read_b32 v122, a130\n"
"v_mfma_f32_16x16x32_bf16 a[196:199], a[72:75], v[40:43], a[196:199]\n"
"v_accvgpr_read_b32 v123, a131\n"
"v_accvgpr_read_b32 v124, a132\n"
"v_mfma_f32_16x16x32_bf16 a[196:199], a[76:79], v[44:47], a[196:199]\n"
"ds_read_b64_tr_b16 v[50:51], v15 offset:50176\n"
"v_accvgpr_read_b32 v125, a133\n"
"v_mfma_f32_16x16x32_bf16 a[200:203], a[80:83], v[40:43], a[200:203]\n"
"s_add_u32 s60, s32, 0x4000\n"
"v_accvgpr_read_b32 v126, a134\n"
"v_mfma_f32_16x16x32_bf16 a[200:203], a[84:87], v[44:47], a[200:203]\n"
"ds_read_b64_tr_b16 v[52:53], v15 offset:57344\n"
"v_accvgpr_read_b32 v127, a135\n"
"v_mfma_f32_16x16x32_bf16 a[204:207], a[88:91], v[40:43], a[204:207]\n"
"buffer_store_dwordx4 v[64:67], v1, s[4:7], s60 offen sc1\n"
"v_cvt_pk_bf16_f32 v56, v120, v121\n"
"v_mfma_f32_16x16x32_bf16 a[204:207], a[92:95], v[44:47], a[204:207]\n"
"ds_read_b64_tr_b16 v[54:55], v15 offset:58368\n"
"v_cvt_pk_bf16_f32 v57, v122, v123\n"
"v_mfma_f32_16x16x32_bf16 a[208:211], a[96:99], v[40:43], a[208:211]\n"
"v_cvt_pk_bf16_f32 v58, v124, v125\n"
"v_cvt_pk_bf16_f32 v59, v126, v127\n"
"v_mfma_f32_16x16x32_bf16 a[208:211], a[100:103], v[44:47], a[208:211]\n"
"v_mfma_f32_16x16x32_bf16 a[212:215], a[104:107], v[40:43], a[212:215]\n"
"v_mfma_f32_16x16x32_bf16 a[212:215], a[108:111], v[44:47], a[212:215]\n"
"v_mfma_f32_16x16x32_bf16 a[216:219], a[112:115], v[40:43], a[216:219]\n"
"v_mfma_f32_16x16x32_bf16 a[216:219], a[116:119], v[44:47], a[216:219]\n"
"v_mfma_f32_16x16x32_bf16 a[220:223], a[120:123], v[40:43], a[220:223]\n"
"v_mfma_f32_16x16x32_bf16 a[220:223], a[124:127], v[44:47], a[220:223]\n"
"s_waitcnt vmcnt(17) lgkmcnt(0)\n"
"v_mfma_f32_16x16x32_bf16 a[224:227], a[64:67], v[48:51], a[224:227]\n"
"s_barrier\n"
"v_mfma_f32_16x16x32_bf16 a[224:227], a[68:71], v[52:55], a[224:227]\n"
"ds_read_b64_tr_b16 v[24:25], v16\n"
"v_mfma_f32_16x16x32_bf16 a[228:231], a[72:75], v[48:51], a[228:231]\n"
"ds_read_b64_tr_b16 v[26:27], v16 offset:1024\n"
"v_xor_b32 v12, 0x80, v16\n"
"v_mfma_f32_16x16x32_bf16 a[228:231], a[76:79], v[52:55], a[228:231]\n"
"ds_read_b64_tr_b16 v[28:29], v16 offset:8192\n"
"v_xor_b32 v13, 0x80, v17\n"
"v_mfma_f32_16x16x32_bf16 a[232:235], a[80:83], v[48:51], a[232:235]\n"
"ds_read_b64_tr_b16 v[30:31], v16 offset:9216\n"
"v_xor_b32 v14, 0x80, v18\n"
"v_mfma_f32_16x16x32_bf16 a[232:235], a[84:87], v[52:55], a[232:235]\n"
"ds_read_b64_tr_b16 v[32:33], v17\n"
"v_xor_b32 v15, 0x80, v19\n"
"v_mfma_f32_16x16x32_bf16 a[236:239], a[88:91], v[48:51], a[236:239]\n"
"ds_read_b64_tr_b16 v[34:35], v17 offset:1024\n"
"v_accvgpr_read_b32 v120, a136\n"
"v_mfma_f32_16x16x32_bf16 a[236:239], a[92:95], v[52:55], a[236:239]\n"
"ds_read_b64_tr_b16 v[36:37], v17 offset:8192\n"
"s_add_u32 s60, s32, 0x5000\n"
"v_mfma_f32_16x16x32_bf16 a[240:243], a[96:99], v[48:51], a[240:243]\n"
"ds_read_b64_tr_b16 v[38:39], v17 offset:9216\n"
"v_accvgpr_read_b32 v121, a137\n"
"v_mfma_f32_16x16x32_bf16 a[240:243], a[100:103], v[52:55], a[240:243]\n"
"ds_read_b64_tr_b16 v[40:41], v18\n"
"v_accvgpr_read_b32 v122, a138\n"
"v_mfma_f32_16x16x32_bf16 a[244:247], a[104:107], v[48:51], a[244:247]\n"
"ds_read_b64_tr_b16 v[42:43], v18 offset:1024\n"
"v_accvgpr_read_b32 v123, a139\n"
"v_mfma_f32_16x16x32_bf16 a[244:247], a[108:111], v[52:55], a[244:247]\n"
"ds_read_b64_tr_b16 v[44:45], v18 offset:8192\n"
"v_accvgpr_read_b32 v124, a140\n"
"v_mfma_f32_16x16x32_bf16 a[248:251], a[112:115], v[48:51], a[248:251]\n"
"ds_read_b64_tr_b16 v[46:47], v18 offset:9216\n"
"v_accvgpr_read_b32 v125, a141\n"
"v_mfma_f32_16x16x32_bf16 a[248:251], a[116:119], v[52:55], a[248:251]\n"
"buffer_store_dwordx4 v[68:71], v1, s[4:7], s60 offen sc1\n"
"v_accvgpr_read_b32 v126, a142\n"
"v_mfma_f32_16x16x32_bf16 a[252:255], a[120:123], v[48:51], a[252:255]\n"
"v_accvgpr_read_b32 v127, a143\n"
"v_cvt_pk_bf16_f32 v60, v120, v121\n"
"v_mfma_f32_16x16x32_bf16 a[252:255], a[124:127], v[52:55], a[252:255]\n"
"v_cvt_pk_bf16_f32 v61, v122, v123\n"
"s_waitcnt vmcnt(12) lgkmcnt(4)\n"
"v_mfma_f32_16x16x32_bf16 v[128:131], v[24:27], a[0:3], 0\n"
"v_cvt_pk_bf16_f32 v62, v124, v125\n"
"v_cvt_pk_bf16_f32 v63, v126, v127\n"
"v_mfma_f32_16x16x32_bf16 v[128:131], v[28:31], a[16:19], v[128:131]\n"
"ds_read_b64_tr_b16 v[48:49], v19\n"
"v_mfma_f32_16x16x32_bf16 v[132:135], v[24:27], a[4:7], 0\n"
"buffer_load_dwordx4 a[64:67], v21, s[4:7], s59 offen sc1\n"
"v_mfma_f32_16x16x32_bf16 v[132:135], v[28:31], a[20:23], v[132:135]\n"
"ds_read_b64_tr_b16 v[50:51], v19 offset:1024\n"
"v_mfma_f32_16x16x32_bf16 v[136:139], v[24:27], a[8:11], 0\n"
"v_mfma_f32_16x16x32_bf16 v[136:139], v[28:31], a[24:27], v[136:139]\n"
"ds_read_b64_tr_b16 v[52:53], v19 offset:8192\n"
"v_mfma_f32_16x16x32_bf16 v[140:143], v[24:27], a[12:15], 0\n"
"buffer_load_dwordx4 a[68:71], v21, s[4:7], s59 offen offset:1024 sc1\n"
"v_accvgpr_read_b32 v120, a160\n"
"v_mfma_f32_16x16x32_bf16 v[140:143], v[28:31], a[28:31], v[140:143]\n"
"ds_read_b64_tr_b16 v[54:55], v19 offset:9216\n"
"v_accvgpr_read_b32 v121, a161\n"
"v_mfma_f32_16x16x32_bf16 v[144:147], v[32:35], a[0:3], 0\n"
"v_accvgpr_read_b32 v122, a162\n"
"v_accvgpr_read_b32 v123, a163\n"
"v_mfma_f32_16x16x32_bf16 v[144:147], v[36:39], a[16:19], v[144:147]\n"
"ds_read_b64_tr_b16 v[24:25], v12\n"
"v_accvgpr_read_b32 v124, a164\n"
"v_mfma_f32_16x16x32_bf16 v[148:151], v[32:35], a[4:7], 0\n"
"s_add_u32 s60, s32, 0\n"
"v_accvgpr_read_b32 v125, a165\n"
"v_mfma_f32_16x16x32_bf16 v[148:151], v[36:39], a[20:23], v[148:151]\n"
"ds_read_b64_tr_b16 v[26:27], v12 offset:1024\n"
"v_accvgpr_read_b32 v126, a166\n"
"v_mfma_f32_16x16x32_bf16 v[152:155], v[32:35], a[8:11], 0\n"
"buffer_store_dwordx4 v[72:75], v1, s[4:7], s60 offen offset:1024 sc1\n"
"v_accvgpr_read_b32 v127, a167\n"
"v_mfma_f32_16x16x32_bf16 v[152:155], v[36:39], a[24:27], v[152:155]\n"
"ds_read_b64_tr_b16 v[28:29], v12 offset:8192\n"
"v_cvt_pk_bf16_f32 v64, v120, v121\n"
"v_mfma_f32_16x16x32_bf16 v[156:159], v[32:35], a[12:15], 0\n"
"v_cvt_pk_bf16_f32 v65, v122, v123\n"
"v_cvt_pk_bf16_f32 v66, v124, v125\n"
"v_mfma_f32_16x16x32_bf16 v[156:159], v[36:39], a[28:31], v[156:159]\n"
"ds_read_b64_tr_b16 v[30:31], v12 offset:9216\n"
"s_waitcnt lgkmcnt(4)\n"
"v_mfma_f32_16x16x32_bf16 v[160:163], v[40:43], a[0:3], 0\n"
"buffer_load_dwordx4 a[72:75], v21, s[4:7], s59 offen offset:2048 sc1\n"
"v_cvt_pk_bf16_f32 v67, v126, v127\n"
"v_mfma_f32_16x16x32_bf16 v[160:163], v[44:47], a[16:19], v[160:163]\n"
"ds_read_b64_tr_b16 v[32:33], v13\n"
"v_mfma_f32_16x16x32_bf16 v[164:167], v[40:43], a[4:7], 0\n"
"v_mfma_f32_16x16x32_bf16 v[164:167], v[44:47], a[20:23], v[164:167]\n"
"ds_read_b64_tr_b16 v[34:35], v13 offset:1024\n"
"v_mfma_f32_16x16x32_bf16 v[168:171], v[40:43], a[8:11], 0\n"
"buffer_load_dwordx4 a[76:79], v21, s[4:7], s59 offen offset:3072 sc1\n"
"s_mov_b32 s50, 0xf0000\n"
"v_mfma_f32_16x16x32_bf16 v[168:171], v[44:47], a[24:27], v[168:171]\n"
"ds_read_b64_tr_b16 v[36:37], v13 offset:8192\n"
"s_add_u32 s59, s59, 0x1000\n"
"v_mfma_f32_16x16x32_bf16 v[172:175], v[40:43], a[12:15], 0\n"
"s_mov_b32 s51, 0xfff00ff\n"
"s_mov_b32 s52, 0x1f0001\n"
"v_mfma_f32_16x16x32_bf16 v[172:175], v[44:47], a[28:31], v[172:175]\n"
"ds_read_b64_tr_b16 v[38:39], v13 offset:9216\n"
"s_mov_b32 s53, 0x1fff01ff\n"
"v_mfma_f32_16x16x32_bf16 v[176:179], v[48:51], a[0:3], 0\n"
"buffer_load_dwordx4 a[80:83], v21, s[4:7], s59 offen sc1\n"
"s_mov_b32 s54, 0x3f0003\n"
"v_mfma_f32_16x16x32_bf16 v[176:179], v[52:55], a[16:19], v[176:179]\n"
"ds_read_b64_tr_b16 v[40:41], v14\n"
"s_mov_b32 s55, 0x3fff03ff\n"
"v_mfma_f32_16x16x32_bf16 v[180:183], v[48:51], a[4:7], 0\n"
"v_accvgpr_read_b32 v120, a168\n"
"v_accvgpr_read_b32 v121, a169\n"
"v_mfma_f32_16x16x32_bf16 v[180:183], v[52:55], a[20:23], v[180:183]\n"
"ds_read_b64_tr_b16 v[42:43], v14 offset:1024\n"
"v_accvgpr_read_b32 v122, a170\n"
"v_mfma_f32_16x16x32_bf16 v[184:187], v[48:51], a[8:11], 0\n"
"v_accvgpr_read_b32 v123, a171\n"
"v_accvgpr_read_b32 v124, a172\n"
"v_mfma_f32_16x16x32_bf16 v[184:187], v[52:55], a[24:27], v[184:187]\n"
"ds_read_b64_tr_b16 v[44:45], v14 offset:8192\n"
"s_add_u32 s60, s32, 0x1000\n"
"v_mfma_f32_16x16x32_bf16 v[188:191], v[48:51], a[12:15], 0\n"
"buffer_store_dwordx4 v[76:79], v1, s[4:7], s60 offen offset:1024 sc1\n"
"v_accvgpr_read_b32 v125, a173\n"
"v_mfma_f32_16x16x32_bf16 v[188:191], v[52:55], a[28:31], v[188:191]\n"
"ds_read_b64_tr_b16 v[46:47], v14 offset:9216\n"
"s_waitcnt lgkmcnt(4)\n"
"v_mfma_f32_16x16x32_bf16 v[192:195], v[24:27], a[0:3], 0\n"
"v_accvgpr_read_b32 v126, a174\n"
"v_accvgpr_read_b32 v127, a175\n"
"v_mfma_f32_16x16x32_bf16 v[192:195], v[28:31], a[16:19], v[192:195]\n"
"ds_read_b64_tr_b16 v[48:49], v15\n"
"v_cvt_pk_bf16_f32 v68, v120, v121\n"
"v_mfma_f32_16x16x32_bf16 v[196:199], v[24:27], a[4:7], 0\n"
"buffer_load_dwordx4 a[84:87], v21, s[4:7], s59 offen offset:1024 sc1\n"
"v_cvt_pk_bf16_f32 v69, v122, v123\n"
"v_mfma_f32_16x16x32_bf16 v[196:199], v[28:31], a[20:23], v[196:199]\n"
"ds_read_b64_tr_b16 v[50:51], v15 offset:1024\n"
"v_cvt_pk_bf16_f32 v70, v124, v125\n"
"v_mfma_f32_16x16x32_bf16 v[200:203], v[24:27], a[8:11], 0\n"
"v_cvt_pk_bf16_f32 v71, v126, v127\n"
"s_mov_b32 s56, 0x7f0007\n"
"v_mfma_f32_16x16x32_bf16 v[200:203], v[28:31], a[24:27], v[200:203]\n"
"ds_read_b64_tr_b16 v[52:53], v15 offset:8192\n"
"s_mov_b32 s57, 0x7fff07ff\n"
"v_mfma_f32_16x16x32_bf16 v[204:207], v[24:27], a[12:15], 0\n"
"buffer_load_dwordx4 a[88:91], v21, s[4:7], s59 offen offset:2048 sc1\n"
"v_mfma_f32_16x16x32_bf16 v[204:207], v[28:31], a[28:31], v[204:207]\n"
"ds_read_b64_tr_b16 v[54:55], v15 offset:9216\n"
"v_mfma_f32_16x16x32_bf16 v[208:211], v[32:35], a[0:3], 0\n"
"v_mfma_f32_16x16x32_bf16 v[208:211], v[36:39], a[16:19], v[208:211]\n"
"ds_read_b64_tr_b16 v[24:25], v16 offset:16384\n"
"v_mfma_f32_16x16x32_bf16 v[212:215], v[32:35], a[4:7], 0\n"
"buffer_load_dwordx4 a[92:95], v21, s[4:7], s59 offen offset:3072 sc1\n"
"v_mfma_f32_16x16x32_bf16 v[212:215], v[36:39], a[20:23], v[212:215]\n"
"ds_read_b64_tr_b16 v[26:27], v16 offset:17408\n"
"s_add_u32 s59, s59, 0x3000\n"
"v_mfma_f32_16x16x32_bf16 v[216:219], v[32:35], a[8:11], 0\n"
"s_cmp_eq_u32 s59, 0x28000\n"
"s_cmov_b32 s59, 0\n"
"v_mfma_f32_16x16x32_bf16 v[216:219], v[36:39], a[24:27], v[216:219]\n"
"ds_read_b64_tr_b16 v[28:29], v16 offset:24576\n"
"v_accvgpr_read_b32 v120, a192\n"
"v_mfma_f32_16x16x32_bf16 v[220:223], v[32:35], a[12:15], 0\n"
"buffer_load_dwordx4 a[96:99], v21, s[4:7], s59 offen sc1\n"
"v_accvgpr_read_b32 v121, a193\n"
"v_mfma_f32_16x16x32_bf16 v[220:223], v[36:39], a[28:31], v[220:223]\n"
"ds_read_b64_tr_b16 v[30:31], v16 offset:25600\n"
"s_waitcnt lgkmcnt(4)\n"
"v_mfma_f32_16x16x32_bf16 v[224:227], v[40:43], a[0:3], 0\n"
"v_xor_b32 v16, 0x20000, v16\n"
"s_add_u32 s60, s32, 0x4000\n"
"v_mfma_f32_16x16x32_bf16 v[224:227], v[44:47], a[16:19], v[224:227]\n"
"ds_read_b64_tr_b16 v[32:33], v17 offset:16384\n"
"v_accvgpr_read_b32 v122, a194\n"
"v_mfma_f32_16x16x32_bf16 v[228:231], v[40:43], a[4:7], 0\n"
"buffer_store_dwordx4 v[80:83], v1, s[4:7], s60 offen offset:1024 sc1\n"
"v_accvgpr_read_b32 v123, a195\n"
"v_mfma_f32_16x16x32_bf16 v[228:231], v[44:47], a[20:23], v[228:231]\n"
"ds_read_b64_tr_b16 v[34:35], v17 offset:17408\n"
"v_accvgpr_read_b32 v124, a196\n"
"v_mfma_f32_16x16x32_bf16 v[232:235], v[40:43], a[8:11], 0\n"
"v_accvgpr_read_b32 v125, a197\n"
"v_accvgpr_read_b32 v126, a198\n"
"v_mfma_f32_16x16x32_bf16 v[232:235], v[44:47], a[24:27], v[232:235]\n"
"ds_read_b64_tr_b16 v[36:37], v17 offset:24576\n"
"v_accvgpr_read_b32 v127, a199\n"
"v_mfma_f32_16x16x32_bf16 v[236:239], v[40:43], a[12:15], 0\n"
"buffer_load_dwordx4 a[100:103], v21, s[4:7], s59 offen offset:1024 sc1\n"
"v_cvt_pk_bf16_f32 v72, v120, v121\n"
"v_mfma_f32_16x16x32_bf16 v[236:239], v[44:47], a[28:31], v[236:239]\n"
"ds_read_b64_tr_b16 v[38:39], v17 offset:25600\n"
"v_cvt_pk_bf16_f32 v73, v122, v123\n"
"v_mfma_f32_16x16x32_bf16 v[240:243], v[48:51], a[0:3], 0\n"
"v_xor_b32 v17, 0x20000, v17\n"
"v_cvt_pk_bf16_f32 v74, v124, v125\n"
"v_mfma_f32_16x16x32_bf16 v[240:243], v[52:55], a[16:19], v[240:243]\n"
"ds_read_b64_tr_b16 v[40:41], v18 offset:16384\n"
"v_cvt_pk_bf16_f32 v75, v126, v127\n"
"v_mfma_f32_16x16x32_bf16 v[244:247], v[48:51], a[4:7], 0\n"
"buffer_load_dwordx4 a[104:107], v21, s[4:7], s59 offen offset:2048 sc1\n"
"v_mfma_f32_16x16x32_bf16 v[244:247], v[52:55], a[20:23], v[244:247]\n"
"ds_read_b64_tr_b16 v[42:43], v18 offset:17408\n"
"v_mfma_f32_16x16x32_bf16 v[248:251], v[48:51], a[8:11], 0\n"
"s_bfe_u32 s61, s100, 0x20001\n"
"s_and_b32 s74, s48, 3\n"
"v_mfma_f32_16x16x32_bf16 v[248:251], v[52:55], a[24:27], v[248:251]\n"
"ds_read_b64_tr_b16 v[44:45], v18 offset:24576\n"
"s_lshl2_add_u32 s61, s61, s74\n"
"v_mfma_f32_16x16x32_bf16 v[252:255], v[48:51], a[12:15], 0\n"
"buffer_load_dwordx4 a[108:111], v21, s[4:7], s59 offen offset:3072 sc1\n"
"v_accvgpr_read_b32 v120, a200\n"
"v_mfma_f32_16x16x32_bf16 v[252:255], v[52:55], a[28:31], v[252:255]\n"
"ds_read_b64_tr_b16 v[46:47], v18 offset:25600\n"
"s_waitcnt vmcnt(18) lgkmcnt(4)\n"
"v_mfma_f32_16x16x32_bf16 v[128:131], v[24:27], a[32:35], v[128:131]\n"
"v_xor_b32 v18, 0x20000, v18\n"
"s_add_u32 s59, s59, 0x1000\n"
"v_mfma_f32_16x16x32_bf16 v[128:131], v[28:31], a[48:51], v[128:131]\n"
"ds_read_b64_tr_b16 v[48:49], v19 offset:16384\n"
"v_accvgpr_read_b32 v121, a201\n"
"v_mfma_f32_16x16x32_bf16 v[132:135], v[24:27], a[36:39], v[132:135]\n"
"v_accvgpr_read_b32 v122, a202\n"
"v_accvgpr_read_b32 v123, a203\n"
"v_mfma_f32_16x16x32_bf16 v[132:135], v[28:31], a[52:55], v[132:135]\n"
"ds_read_b64_tr_b16 v[50:51], v19 offset:17408\n"
"s_add_u32 s60, s32, 0x5000\n"
"v_mfma_f32_16x16x32_bf16 v[136:139], v[24:27], a[40:43], v[136:139]\n"
"buffer_store_dwordx4 v[84:87], v1, s[4:7], s60 offen offset:1024 sc1\n"
"v_accvgpr_read_b32 v124, a204\n"
"v_mfma_f32_16x16x32_bf16 v[136:139], v[28:31], a[56:59], v[136:139]\n"
"ds_read_b64_tr_b16 v[52:53], v19 offset:24576\n"
"v_accvgpr_read_b32 v125, a205\n"
"v_mfma_f32_16x16x32_bf16 v[140:143], v[24:27], a[44:47], v[140:143]\n"
"v_accvgpr_read_b32 v126, a206\n"
"v_accvgpr_read_b32 v127, a207\n"
"v_mfma_f32_16x16x32_bf16 v[140:143], v[28:31], a[60:63], v[140:143]\n"
"ds_read_b64_tr_b16 v[54:55], v19 offset:25600\n"
"v_cvt_pk_bf16_f32 v76, v120, v121\n"
"v_mfma_f32_16x16x32_bf16 v[144:147], v[32:35], a[32:35], v[144:147]\n"
"buffer_load_dwordx4 a[112:115], v21, s[4:7], s59 offen sc1\n"
"v_xor_b32 v19, 0x20000, v19\n"
"v_mfma_f32_16x16x32_bf16 v[144:147], v[36:39], a[48:51], v[144:147]\n"
"ds_read_b64_tr_b16 v[24:25], v12 offset:16384\n"
"v_cvt_pk_bf16_f32 v77, v122, v123\n"
"v_mfma_f32_16x16x32_bf16 v[148:151], v[32:35], a[36:39], v[148:151]\n"
"v_cvt_pk_bf16_f32 v78, v124, v125\n"
"v_cvt_pk_bf16_f32 v79, v126, v127\n"
"v_mfma_f32_16x16x32_bf16 v[148:151], v[36:39], a[52:55], v[148:151]\n"
"ds_read_b64_tr_b16 v[26:27], v12 offset:17408\n"
"s_lshl_b32 s61, s61, 20\n"
"v_mfma_f32_16x16x32_bf16 v[152:155], v[32:35], a[40:43], v[152:155]\n"
"buffer_load_dwordx4 a[116:119], v21, s[4:7], s59 offen offset:1024 sc1\n"
"s_add_u32 s61, s61, 0x4000000\n"
"v_mfma_f32_16x16x32_bf16 v[152:155], v[36:39], a[56:59], v[152:155]\n"
"ds_read_b64_tr_b16 v[28:29], v12 offset:24576\n"
"s_lshr_b32 s74, s61, 8\n"
"v_mfma_f32_16x16x32_bf16 v[156:159], v[32:35], a[44:47], v[156:159]\n"
"s_and_b32 s74, s74, 0x2000\n"
"s_xor_b32 s61, s61, s74\n"
"v_mfma_f32_16x16x32_bf16 v[156:159], v[36:39], a[60:63], v[156:159]\n"
"ds_read_b64_tr_b16 v[30:31], v12 offset:25600\n"
"s_waitcnt lgkmcnt(4)\n"
"v_mfma_f32_16x16x32_bf16 v[160:163], v[40:43], a[32:35], v[160:163]\n"
"buffer_load_dwordx4 a[120:123], v21, s[4:7], s59 offen offset:2048 sc1\n"
"v_xor_b32 v12, 0x20000, v12\n"
"v_mfma_f32_16x16x32_bf16 v[160:163], v[44:47], a[48:51], v[160:163]\n"
"ds_read_b64_tr_b16 v[32:33], v13 offset:16384\n"
"s_xor_b32 s61, s61, s101\n"
"v_mfma_f32_16x16x32_bf16 v[164:167], v[40:43], a[36:39], v[164:167]\n"
"s_and_b32 s74, s78, 0x2000\n"
"s_add_u32 s61, s61, s74\n"
"v_mfma_f32_16x16x32_bf16 v[164:167], v[44:47], a[52:55], v[164:167]\n"
"ds_read_b64_tr_b16 v[34:35], v13 offset:17408\n"
"s_add_u32 s61, s61, s78\n"
"v_mfma_f32_16x16x32_bf16 v[168:171], v[40:43], a[40:43], v[168:171]\n"
"buffer_load_dwordx4 a[124:127], v21, s[4:7], s59 offen offset:3072 sc1\n"
"s_add_u32 s62, s78, 0x18000\n"
"v_mfma_f32_16x16x32_bf16 v[168:171], v[44:47], a[56:59], v[168:171]\n"
"ds_read_b64_tr_b16 v[36:37], v13 offset:24576\n"
"s_add_u32 s59, s59, 0x3000\n"
"v_mfma_f32_16x16x32_bf16 v[172:175], v[40:43], a[44:47], v[172:175]\n"
"s_add_u32 s60, s33, 0\n"
"s_add_u32 s63, s78, 0x8000\n"
"v_mfma_f32_16x16x32_bf16 v[172:175], v[44:47], a[60:63], v[172:175]\n"
"ds_read_b64_tr_b16 v[38:39], v13 offset:25600\n"
"s_lshl_b32 s74, s49, 17\n"
"v_mfma_f32_16x16x32_bf16 v[176:179], v[48:51], a[32:35], v[176:179]\n"
"buffer_store_dwordx4 v[56:59], v1, s[4:7], s60 offen sc1\n"
"v_xor_b32 v13, 0x20000, v13\n"
"v_mfma_f32_16x16x32_bf16 v[176:179], v[52:55], a[48:51], v[176:179]\n"
"ds_read_b64_tr_b16 v[40:41], v14 offset:16384\n"
"s_sub_u32 s64, 0xe8000, s74\n"
"v_mfma_f32_16x16x32_bf16 v[180:183], v[48:51], a[36:39], v[180:183]\n"
"s_cmp_eq_u32 s59, 0x28000\n"
"s_cmov_b32 s59, 0\n"
"v_mfma_f32_16x16x32_bf16 v[180:183], v[52:55], a[52:55], v[180:183]\n"
"ds_read_b64_tr_b16 v[42:43], v14 offset:17408\n"
"s_sub_u32 s65, s74, 0xe0000\n"
"v_mfma_f32_16x16x32_bf16 v[184:187], v[48:51], a[40:43], v[184:187]\n"
"buffer_load_dwordx4 a[0:3], v21, s[4:7], s59 offen sc1\n"
"s_and_b32 s74, s3, 1\n"
"v_mfma_f32_16x16x32_bf16 v[184:187], v[52:55], a[56:59], v[184:187]\n"
"ds_read_b64_tr_b16 v[44:45], v14 offset:24576\n"
"s_lshl_b32 s74, s74, 14\n"
"v_mfma_f32_16x16x32_bf16 v[188:191], v[48:51], a[44:47], v[188:191]\n"
"s_or_b32 s68, s74, 0x8000\n"
"s_mov_b32 s66, 0x8000\n"
"v_mfma_f32_16x16x32_bf16 v[188:191], v[52:55], a[60:63], v[188:191]\n"
"ds_read_b64_tr_b16 v[46:47], v14 offset:25600\n"
"s_waitcnt lgkmcnt(4)\n"
"v_mfma_f32_16x16x32_bf16 v[192:195], v[24:27], a[32:35], v[192:195]\n"
"buffer_load_dwordx4 a[4:7], v21, s[4:7], s59 offen offset:1024 sc1\n"
"v_xor_b32 v14, 0x20000, v14\n"
"v_mfma_f32_16x16x32_bf16 v[192:195], v[28:31], a[48:51], v[192:195]\n"
"ds_read_b64_tr_b16 v[48:49], v15 offset:16384\n"
"v_accvgpr_read_b32 v120, a224\n"
"v_mfma_f32_16x16x32_bf16 v[196:199], v[24:27], a[36:39], v[196:199]\n"
"v_accvgpr_read_b32 v121, a225\n"
"v_accvgpr_read_b32 v122, a226\n"
"v_mfma_f32_16x16x32_bf16 v[196:199], v[28:31], a[52:55], v[196:199]\n"
"ds_read_b64_tr_b16 v[50:51], v15 offset:17408\n"
"v_accvgpr_read_b32 v123, a227\n"
"v_mfma_f32_16x16x32_bf16 v[200:203], v[24:27], a[40:43], v[200:203]\n"
"buffer_load_dwordx4 a[8:11], v21, s[4:7], s59 offen offset:2048 sc1\n"
"v_accvgpr_read_b32 v124, a228\n"
"v_mfma_f32_16x16x32_bf16 v[200:203], v[28:31], a[56:59], v[200:203]\n"
"ds_read_b64_tr_b16 v[52:53], v15 offset:24576\n"
"v_accvgpr_read_b32 v125, a229\n"
"v_mfma_f32_16x16x32_bf16 v[204:207], v[24:27], a[44:47], v[204:207]\n"
"v_accvgpr_read_b32 v126, a230\n"
"v_accvgpr_read_b32 v127, a231\n"
"v_mfma_f32_16x16x32_bf16 v[204:207], v[28:31], a[60:63], v[204:207]\n"
"ds_read_b64_tr_b16 v[54:55], v15 offset:25600\n"
"v_cvt_pk_bf16_f32 v80, v120, v121\n"
"v_mfma_f32_16x16x32_bf16 v[208:211], v[32:35], a[32:35], v[208:211]\n"
"v_xor_b32 v15, 0x20000, v15\n"
"v_cvt_pk_bf16_f32 v81, v122, v123\n"
"v_mfma_f32_16x16x32_bf16 v[208:211], v[36:39], a[48:51], v[208:211]\n"
"ds_read_b64_tr_b16 v[24:25], v16\n"
"s_add_u32 s60, s33, 0\n"
"v_mfma_f32_16x16x32_bf16 v[212:215], v[32:35], a[36:39], v[212:215]\n"
"buffer_store_dwordx4 v[60:63], v1, s[4:7], s60 offen offset:1024 sc1\n"
"v_cvt_pk_bf16_f32 v82, v124, v125\n"
"v_mfma_f32_16x16x32_bf16 v[212:215], v[36:39], a[52:55], v[212:215]\n"
"ds_read_b64_tr_b16 v[26:27], v16 offset:1024\n"
"v_cvt_pk_bf16_f32 v83, v126, v127\n"
"v_mfma_f32_16x16x32_bf16 v[216:219], v[32:35], a[40:43], v[216:219]\n"
"v_accvgpr_read_b32 v120, a232\n"
"v_accvgpr_read_b32 v121, a233\n"
"v_mfma_f32_16x16x32_bf16 v[216:219], v[36:39], a[56:59], v[216:219]\n"
"ds_read_b64_tr_b16 v[28:29], v16 offset:8192\n"
"v_accvgpr_read_b32 v122, a234\n"
"v_mfma_f32_16x16x32_bf16 v[220:223], v[32:35], a[44:47], v[220:223]\n"
"buffer_load_dwordx4 a[12:15], v21, s[4:7], s59 offen offset:3072 sc1\n"
"v_accvgpr_read_b32 v123, a235\n"
"v_mfma_f32_16x16x32_bf16 v[220:223], v[36:39], a[60:63], v[220:223]\n"
"ds_read_b64_tr_b16 v[30:31], v16 offset:9216\n"
"s_waitcnt lgkmcnt(4)\n"
"v_mfma_f32_16x16x32_bf16 v[224:227], v[40:43], a[32:35], v[224:227]\n"
"s_add_u32 s59, s59, 0x1000\n"
"v_accvgpr_read_b32 v124, a236\n"
"v_mfma_f32_16x16x32_bf16 v[224:227], v[44:47], a[48:51], v[224:227]\n"
"ds_read_b64_tr_b16 v[32:33], v17\n"
"v_accvgpr_read_b32 v125, a237\n"
"v_mfma_f32_16x16x32_bf16 v[228:231], v[40:43], a[36:39], v[228:231]\n"
"buffer_load_dwordx4 a[16:19], v21, s[4:7], s59 offen sc1\n"
"v_accvgpr_read_b32 v126, a238\n"
"v_mfma_f32_16x16x32_bf16 v[228:231], v[44:47], a[52:55], v[228:231]\n"
"ds_read_b64_tr_b16 v[34:35], v17 offset:1024\n"
"v_accvgpr_read_b32 v127, a239\n"
"v_mfma_f32_16x16x32_bf16 v[232:235], v[40:43], a[40:43], v[232:235]\n"
"v_cvt_pk_bf16_f32 v84, v120, v121\n"
"v_cvt_pk_bf16_f32 v85, v122, v123\n"
"v_mfma_f32_16x16x32_bf16 v[232:235], v[44:47], a[56:59], v[232:235]\n"
"ds_read_b64_tr_b16 v[36:37], v17 offset:8192\n"
"v_cvt_pk_bf16_f32 v86, v124, v125\n"
"v_mfma_f32_16x16x32_bf16 v[236:239], v[40:43], a[44:47], v[236:239]\n"
"buffer_load_dwordx4 a[20:23], v21, s[4:7], s59 offen offset:1024 sc1\n"
"v_cvt_pk_bf16_f32 v87, v126, v127\n"
"v_mfma_f32_16x16x32_bf16 v[236:239], v[44:47], a[60:63], v[236:239]\n"
"ds_read_b64_tr_b16 v[38:39], v17 offset:9216\n"
"s_mov_b32 s67, 0x8000\n"
"v_mfma_f32_16x16x32_bf16 v[240:243], v[48:51], a[32:35], v[240:243]\n"
"s_lshl_b32 s74, s49, 1\n"
"s_sub_u32 s70, 14, s74\n"
"v_mfma_f32_16x16x32_bf16 v[240:243], v[52:55], a[48:51], v[240:243]\n"
"ds_read_b64_tr_b16 v[40:41], v18\n"
"v_mfma_f32_16x16x32_bf16 v[244:247], v[48:51], a[36:39], v[244:247]\n"
"buffer_load_dwordx4 a[24:27], v21, s[4:7], s59 offen offset:2048 sc1\n"
"v_mfma_f32_16x16x32_bf16 v[244:247], v[52:55], a[52:55], v[244:247]\n"
"ds_read_b64_tr_b16 v[42:43], v18 offset:1024\n"
"v_mfma_f32_16x16x32_bf16 v[248:251], v[48:51], a[40:43], v[248:251]\n"
"s_add_u32 s60, s33, 0\n"
"v_mfma_f32_16x16x32_bf16 v[248:251], v[52:55], a[56:59], v[248:251]\n"
"ds_read_b64_tr_b16 v[44:45], v18 offset:8192\n"
"v_mfma_f32_16x16x32_bf16 v[252:255], v[48:51], a[44:47], v[252:255]\n"
"buffer_store_dwordx4 v[64:67], v1, s[4:7], s60 offen offset:2048 sc1\n"
"v_mfma_f32_16x16x32_bf16 v[252:255], v[52:55], a[60:63], v[252:255]\n"
"ds_read_b64_tr_b16 v[46:47], v18 offset:9216\n"
"s_waitcnt vmcnt(20) lgkmcnt(4)\n"
"v_mfma_f32_16x16x32_bf16 v[128:131], v[24:27], a[64:67], v[128:131]\n"
"s_cmp_eq_u32 s3, 2\n"
"s_addc_u32 s70, s70, 0\n"
"v_mfma_f32_16x16x32_bf16 v[128:131], v[28:31], a[80:83], v[128:131]\n"
"ds_read_b64_tr_b16 v[48:49], v19\n"
"s_mov_b32 s71, 0\n"
"v_mfma_f32_16x16x32_bf16 v[132:135], v[24:27], a[68:71], v[132:135]\n"
"buffer_load_dwordx4 a[28:31], v21, s[4:7], s59 offen offset:3072 sc1\n"
"s_and_b32 s46, s100, 0xfe\n"
"v_mfma_f32_16x16x32_bf16 v[132:135], v[28:31], a[84:87], v[132:135]\n"
"ds_read_b64_tr_b16 v[50:51], v19 offset:1024\n"
"s_add_u32 s59, s59, 0x3000\n"
"v_mfma_f32_16x16x32_bf16 v[136:139], v[24:27], a[72:75], v[136:139]\n"
"s_cmp_eq_u32 s59, 0x28000\n"
"s_cmov_b32 s59, 0\n"
"v_mfma_f32_16x16x32_bf16 v[136:139], v[28:31], a[88:91], v[136:139]\n"
"ds_read_b64_tr_b16 v[52:53], v19 offset:8192\n"
"s_add_u32 s46, s46, s3\n"
"v_mfma_f32_16x16x32_bf16 v[140:143], v[24:27], a[76:79], v[140:143]\n"
"s_lshl_b32 s46, s46, 15\n"
"s_add_u32 s46, s46, 0x3800000\n"
"v_mfma_f32_16x16x32_bf16 v[140:143], v[28:31], a[92:95], v[140:143]\n"
"ds_read_b64_tr_b16 v[54:55], v19 offset:9216\n"
"s_lshr_b32 s74, s46, 8\n"
"v_mfma_f32_16x16x32_bf16 v[144:147], v[32:35], a[64:67], v[144:147]\n"
"s_and_b32 s74, s74, 0x2000\n"
"s_xor_b32 s46, s46, s74\n"
"v_mfma_f32_16x16x32_bf16 v[144:147], v[36:39], a[80:83], v[144:147]\n"
"ds_read_b64_tr_b16 v[24:25], v12\n"
"s_xor_b32 s46, s46, s101\n"
"v_mfma_f32_16x16x32_bf16 v[148:151], v[32:35], a[68:71], v[148:151]\n"
"s_cmp_ge_u32 s3, 2\n"
"s_cmov_b32 s46, 0x40000000\n"
"v_mfma_f32_16x16x32_bf16 v[148:151], v[36:39], a[84:87], v[148:151]\n"
"ds_read_b64_tr_b16 v[26:27], v12 offset:1024\n"
"s_add_u32 s47, s78, 0x20000\n"
"v_mfma_f32_16x16x32_bf16 v[152:155], v[32:35], a[72:75], v[152:155]\n"
"s_lshl_b32 s72, s79, 19\n"
"s_and_b32 s74, s48, 15\n"
"v_mfma_f32_16x16x32_bf16 v[152:155], v[36:39], a[88:91], v[152:155]\n"
"ds_read_b64_tr_b16 v[28:29], v12 offset:8192\n"
"s_lshl_b32 s74, s74, 15\n"
"v_mfma_f32_16x16x32_bf16 v[156:159], v[32:35], a[76:79], v[156:159]\n"
"s_add_u32 s74, s74, 0x6000000\n"
"s_add_u32 s72, s72, s74\n"
"v_mfma_f32_16x16x32_bf16 v[156:159], v[36:39], a[92:95], v[156:159]\n"
"ds_read_b64_tr_b16 v[30:31], v12 offset:9216\n"
"s_waitcnt lgkmcnt(4)\n"
"v_mfma_f32_16x16x32_bf16 v[160:163], v[40:43], a[64:67], v[160:163]\n"
"s_add_u32 s60, s33, 0\n"
"s_lshl_b32 s73, s49, 1\n"
"v_mfma_f32_16x16x32_bf16 v[160:163], v[44:47], a[80:83], v[160:163]\n"
"ds_read_b64_tr_b16 v[32:33], v13\n"
"s_add_u32 s73, s73, s3\n"
"v_mfma_f32_16x16x32_bf16 v[164:167], v[40:43], a[68:71], v[164:167]\n"
"buffer_store_dwordx4 v[68:71], v1, s[4:7], s60 offen offset:3072 sc1\n"
"s_sub_u32 s73, 33, s73\n"
"v_mfma_f32_16x16x32_bf16 v[164:167], v[44:47], a[84:87], v[164:167]\n"
"ds_read_b64_tr_b16 v[34:35], v13 offset:1024\n"
"s_lshl_b32 s73, s73, 19\n"
"v_mfma_f32_16x16x32_bf16 v[168:171], v[40:43], a[72:75], v[168:171]\n"
"s_add_u32 s73, s73, s74\n"
"s_add_u32 s73, s73, 0x400\n"
"v_mfma_f32_16x16x32_bf16 v[168:171], v[44:47], a[88:91], v[168:171]\n"
"ds_read_b64_tr_b16 v[36:37], v13 offset:8192\n"
"v_mfma_f32_16x16x32_bf16 v[172:175], v[40:43], a[76:79], v[172:175]\n"
"buffer_load_dwordx4 a[32:35], v21, s[4:7], s59 offen sc1\n"
"s_bfe_u32 s40, s100, 0x10002\n"
"v_mfma_f32_16x16x32_bf16 v[172:175], v[44:47], a[92:95], v[172:175]\n"
"ds_read_b64_tr_b16 v[38:39], v13 offset:9216\n"
"s_lshr_b32 s27, s100, 5\n"
"v_mfma_f32_16x16x32_bf16 v[176:179], v[48:51], a[64:67], v[176:179]\n"
"s_cmp_lt_u32 s3, 2\n"
"s_cselect_b32 s73, s72, s73\n"
"v_mfma_f32_16x16x32_bf16 v[176:179], v[52:55], a[80:83], v[176:179]\n"
"ds_read_b64_tr_b16 v[40:41], v14\n"
"s_lshr_b32 s71, s48, 4\n"
"v_mfma_f32_16x16x32_bf16 v[180:183], v[48:51], a[68:71], v[180:183]\n"
"buffer_load_dwordx4 a[36:39], v21, s[4:7], s59 offen offset:1024 sc1\n"
"s_xor_b32 s71, s71, s100\n"
"v_mfma_f32_16x16x32_bf16 v[180:183], v[52:55], a[84:87], v[180:183]\n"
"ds_read_b64_tr_b16 v[42:43], v14 offset:1024\n"
"s_and_b32 s71, s71, 1\n"
"v_mfma_f32_16x16x32_bf16 v[184:187], v[48:51], a[72:75], v[184:187]\n"
"s_lshl_b32 s71, s71, 13\n"
"s_xor_b32 s71, s71, s101\n"
"v_mfma_f32_16x16x32_bf16 v[184:187], v[52:55], a[88:91], v[184:187]\n"
"ds_read_b64_tr_b16 v[44:45], v14 offset:8192\n"
"s_lshr_b32 s74, s72, 8\n"
"v_mfma_f32_16x16x32_bf16 v[188:191], v[48:51], a[76:79], v[188:191]\n"
"buffer_load_dwordx4 a[40:43], v21, s[4:7], s59 offen offset:2048 sc1\n"
"s_and_b32 s74, s74, 0x2000\n"
"v_mfma_f32_16x16x32_bf16 v[188:191], v[52:55], a[92:95], v[188:191]\n"
"ds_read_b64_tr_b16 v[46:47], v14 offset:9216\n"
"s_waitcnt lgkmcnt(4)\n"
"v_mfma_f32_16x16x32_bf16 v[192:195], v[24:27], a[64:67], v[192:195]\n"
"s_xor_b32 s72, s72, s74\n"
"s_xor_b32 s72, s72, s71\n"
"v_mfma_f32_16x16x32_bf16 v[192:195], v[28:31], a[80:83], v[192:195]\n"
"ds_read_b64_tr_b16 v[48:49], v15\n"
"s_lshr_b32 s74, s73, 8\n"
"v_mfma_f32_16x16x32_bf16 v[196:199], v[24:27], a[68:71], v[196:199]\n"
"s_add_u32 s60, s33, 0x1000\n"
"s_and_b32 s74, s74, 0x2000\n"
"v_mfma_f32_16x16x32_bf16 v[196:199], v[28:31], a[84:87], v[196:199]\n"
"ds_read_b64_tr_b16 v[50:51], v15 offset:1024\n"
"s_xor_b32 s73, s73, s74\n"
"v_mfma_f32_16x16x32_bf16 v[200:203], v[24:27], a[72:75], v[200:203]\n"
"buffer_store_dwordx4 v[72:75], v1, s[4:7], s60 offen sc1\n"
"s_xor_b32 s73, s73, s71\n"
"v_mfma_f32_16x16x32_bf16 v[200:203], v[28:31], a[88:91], v[200:203]\n"
"ds_read_b64_tr_b16 v[52:53], v15 offset:8192\n"
"s_mov_b32 s71, 0\n"
"v_mfma_f32_16x16x32_bf16 v[204:207], v[24:27], a[76:79], v[204:207]\n"
"s_lshl3_add_u32 s27, s40, s27\n"
"s_and_b32 s40, s100, 1\n"
"v_mfma_f32_16x16x32_bf16 v[204:207], v[28:31], a[92:95], v[204:207]\n"
"ds_read_b64_tr_b16 v[54:55], v15 offset:9216\n"
"s_lshl1_add_u32 s27, s27, s40\n"
"v_mfma_f32_16x16x32_bf16 v[208:211], v[32:35], a[64:67], v[208:211]\n"
"buffer_load_dwordx4 a[44:47], v21, s[4:7], s59 offen offset:3072 sc1\n"
"s_bfe_u32 s40, s100, 0x10001\n"
"v_mfma_f32_16x16x32_bf16 v[208:211], v[36:39], a[80:83], v[208:211]\n"
"ds_read_b64_tr_b16 v[24:25], v16 offset:16384\n"
"s_add_u32 s59, s59, 0x1000\n"
"v_mfma_f32_16x16x32_bf16 v[212:215], v[32:35], a[68:71], v[212:215]\n"
"s_bfe_u32 s28, s100, 0x20003\n"
"s_lshl2_add_u32 s28, s40, s28\n"
"v_mfma_f32_16x16x32_bf16 v[212:215], v[36:39], a[84:87], v[212:215]\n"
"ds_read_b64_tr_b16 v[26:27], v16 offset:17408\n"
"s_lshl2_add_u32 s9, s28, s3\n"
"v_mfma_f32_16x16x32_bf16 v[216:219], v[32:35], a[72:75], v[216:219]\n"
"buffer_load_dwordx4 a[48:51], v21, s[4:7], s59 offen sc1\n"
"s_lshl3_add_u32 s9, s9, s3\n"
"v_mfma_f32_16x16x32_bf16 v[216:219], v[36:39], a[88:91], v[216:219]\n"
"ds_read_b64_tr_b16 v[28:29], v16 offset:24576\n"
"s_lshl_b32 s9, s9, 15\n"
"v_mfma_f32_16x16x32_bf16 v[220:223], v[32:35], a[76:79], v[220:223]\n"
"s_add_u32 s9, s9, 0x180d000\n"
"s_lshr_b32 s27, s9, 8\n"
"v_mfma_f32_16x16x32_bf16 v[220:223], v[36:39], a[92:95], v[220:223]\n"
"ds_read_b64_tr_b16 v[30:31], v16 offset:25600\n"
"s_waitcnt lgkmcnt(4)\n"
"v_mfma_f32_16x16x32_bf16 v[224:227], v[40:43], a[64:67], v[224:227]\n"
"buffer_load_dwordx4 a[52:55], v21, s[4:7], s59 offen offset:1024 sc1\n"
"v_xor_b32 v16, 0x10000, v16\n"
"v_mfma_f32_16x16x32_bf16 v[224:227], v[44:47], a[80:83], v[224:227]\n"
"ds_read_b64_tr_b16 v[32:33], v17 offset:16384\n"
"s_and_b32 s27, s27, 0x2000\n"
"v_mfma_f32_16x16x32_bf16 v[228:231], v[40:43], a[68:71], v[228:231]\n"
"s_xor_b32 s9, s9, s27\n"
"s_xor_b32 s9, s9, s101\n"
"v_mfma_f32_16x16x32_bf16 v[228:231], v[44:47], a[84:87], v[228:231]\n"
"ds_read_b64_tr_b16 v[34:35], v17 offset:17408\n"
"s_lshl_b32 s8, s49, 1\n"
"v_mfma_f32_16x16x32_bf16 v[232:235], v[40:43], a[72:75], v[232:235]\n"
"s_max_u32 s8, s8, 1\n"
"s_mul_i32 s10, s3, 0x1400\n"
"v_mfma_f32_16x16x32_bf16 v[232:235], v[44:47], a[88:91], v[232:235]\n"
"ds_read_b64_tr_b16 v[36:37], v17 offset:24576\n"
"s_add_u32 s60, s33, 0x1000\n"
"v_mfma_f32_16x16x32_bf16 v[236:239], v[40:43], a[76:79], v[236:239]\n"
"buffer_store_dwordx4 v[76:79], v1, s[4:7], s60 offen offset:1024 sc1\n"
"s_add_u32 s10, s10, 0xe8000\n"
"v_mfma_f32_16x16x32_bf16 v[236:239], v[44:47], a[92:95], v[236:239]\n"
"ds_read_b64_tr_b16 v[38:39], v17 offset:25600\n"
"v_mfma_f32_16x16x32_bf16 v[240:243], v[48:51], a[64:67], v[240:243]\n"
"v_xor_b32 v17, 0x10000, v17\n"
"v_mfma_f32_16x16x32_bf16 v[240:243], v[52:55], a[80:83], v[240:243]\n"
"ds_read_b64_tr_b16 v[40:41], v18 offset:16384\n"
"v_mfma_f32_16x16x32_bf16 v[244:247], v[48:51], a[68:71], v[244:247]\n"
"buffer_load_dwordx4 a[56:59], v21, s[4:7], s59 offen offset:2048 sc1\n"
"v_mfma_f32_16x16x32_bf16 v[244:247], v[52:55], a[84:87], v[244:247]\n"
"ds_read_b64_tr_b16 v[42:43], v18 offset:17408\n"
"v_mfma_f32_16x16x32_bf16 v[248:251], v[48:51], a[72:75], v[248:251]\n"
"s_add_u32 s0, s4, 0x3000\n"
"s_addc_u32 s1, s5, 0\n"
"v_mfma_f32_16x16x32_bf16 v[248:251], v[52:55], a[88:91], v[248:251]\n"
"ds_read_b64_tr_b16 v[44:45], v18 offset:24576\n"
"v_mfma_f32_16x16x32_bf16 v[252:255], v[48:51], a[76:79], v[252:255]\n"
"buffer_load_dwordx4 a[60:63], v21, s[4:7], s59 offen offset:3072 sc1\n"
"v_mfma_f32_16x16x32_bf16 v[252:255], v[52:55], a[92:95], v[252:255]\n"
"ds_read_b64_tr_b16 v[46:47], v18 offset:25600\n"
"s_waitcnt vmcnt(22) lgkmcnt(4)\n"
"v_mfma_f32_16x16x32_bf16 v[128:131], v[24:27], a[96:99], v[128:131]\n"
"v_xor_b32 v18, 0x10000, v18\n"
"s_add_u32 s59, s59, 0x3000\n"
"v_mfma_f32_16x16x32_bf16 v[128:131], v[28:31], a[112:115], v[128:131]\n"
"ds_read_b64_tr_b16 v[48:49], v19 offset:16384\n"
"v_mfma_f32_16x16x32_bf16 v[132:135], v[24:27], a[100:103], v[132:135]\n"
"s_cmp_eq_u32 s59, 0x28000\n"
"s_cmov_b32 s59, 0\n"
"v_mfma_f32_16x16x32_bf16 v[132:135], v[28:31], a[116:119], v[132:135]\n"
"ds_read_b64_tr_b16 v[50:51], v19 offset:17408\n"
"v_mfma_f32_16x16x32_bf16 v[136:139], v[24:27], a[104:107], v[136:139]\n"
"v_mfma_f32_16x16x32_bf16 v[136:139], v[28:31], a[120:123], v[136:139]\n"
"ds_read_b64_tr_b16 v[52:53], v19 offset:24576\n"
"v_mfma_f32_16x16x32_bf16 v[140:143], v[24:27], a[108:111], v[140:143]\n"
"v_mfma_f32_16x16x32_bf16 v[140:143], v[28:31], a[124:127], v[140:143]\n"
"ds_read_b64_tr_b16 v[54:55], v19 offset:25600\n"
"s_add_u32 m0, s78, 0x8000\n"
"v_mfma_f32_16x16x32_bf16 v[144:147], v[32:35], a[96:99], v[144:147]\n"
"buffer_load_dwordx4 v6, s[20:23], s58 offen lds\n"
"v_xor_b32 v19, 0x10000, v19\n"
"v_mfma_f32_16x16x32_bf16 v[144:147], v[36:39], a[112:115], v[144:147]\n"
"ds_read_b64_tr_b16 v[24:25], v12 offset:16384\n"
"s_add_u32 s60, s33, 0x1000\n"
"v_mfma_f32_16x16x32_bf16 v[148:151], v[32:35], a[100:103], v[148:151]\n"
"s_mov_b32 s12, s76\n"
"s_and_b32 s13, s77, 0xffff\n"
"v_mfma_f32_16x16x32_bf16 v[148:151], v[36:39], a[116:119], v[148:151]\n"
"ds_read_b64_tr_b16 v[26:27], v12 offset:17408\n"
"s_mov_b32 s14, .Ladaln_block_C_END-.Ladaln_block_C_PC\n"
"v_mfma_f32_16x16x32_bf16 v[152:155], v[32:35], a[104:107], v[152:155]\n"
"buffer_load_dwordx4 v7, s[20:23], s58 offen offset:1024 lds\n"
"s_mov_b32 s15, 0x20000\n"
"v_mfma_f32_16x16x32_bf16 v[152:155], v[36:39], a[120:123], v[152:155]\n"
"ds_read_b64_tr_b16 v[28:29], v12 offset:24576\n"
"s_lshr_b32 s83, s2, 3\n"
"v_mfma_f32_16x16x32_bf16 v[156:159], v[32:35], a[108:111], v[156:159]\n"
"s_lshl2_add_u32 s83, s83, s3\n"
"s_lshl_b32 s83, s83, 10\n"
"v_mfma_f32_16x16x32_bf16 v[156:159], v[36:39], a[124:127], v[156:159]\n"
"ds_read_b64_tr_b16 v[30:31], v12 offset:25600\n"
"s_waitcnt lgkmcnt(4)\n"
"v_mfma_f32_16x16x32_bf16 v[160:163], v[40:43], a[96:99], v[160:163]\n"
"buffer_load_dwordx4 v8, s[20:23], s58 offen offset:2048 lds\n"
"v_xor_b32 v12, 0x10000, v12\n"
"v_mfma_f32_16x16x32_bf16 v[160:163], v[44:47], a[112:115], v[160:163]\n"
"ds_read_b64_tr_b16 v[32:33], v13 offset:16384\n"
"v_lshrrev_b32 v3, 3, v0\n"
"v_mfma_f32_16x16x32_bf16 v[164:167], v[40:43], a[100:103], v[164:167]\n"
"v_min_u32 v3, 1, v3\n"
"v_lshlrev_b32 v3, 30, v3\n"
"v_mfma_f32_16x16x32_bf16 v[164:167], v[44:47], a[116:119], v[164:167]\n"
"ds_read_b64_tr_b16 v[34:35], v13 offset:17408\n"
"v_lshl_add_u32 v2, v0, 7, v3\n"
"v_mfma_f32_16x16x32_bf16 v[168:171], v[40:43], a[104:107], v[168:171]\n"
"buffer_load_dwordx4 v9, s[20:23], s58 offen offset:3072 lds\n"
"v_mfma_f32_16x16x32_bf16 v[168:171], v[44:47], a[120:123], v[168:171]\n"
"ds_read_b64_tr_b16 v[36:37], v13 offset:24576\n"
"s_add_u32 s58, s58, 0x80000\n"
"v_mfma_f32_16x16x32_bf16 v[172:175], v[40:43], a[108:111], v[172:175]\n"
"s_cmp_eq_u32 s58, 0x500000\n"
"s_cmov_b32 s58, 0\n"
"v_mfma_f32_16x16x32_bf16 v[172:175], v[44:47], a[124:127], v[172:175]\n"
"ds_read_b64_tr_b16 v[38:39], v13 offset:25600\n"
"v_mfma_f32_16x16x32_bf16 v[176:179], v[48:51], a[96:99], v[176:179]\n"
"buffer_store_dwordx4 v[80:83], v1, s[4:7], s60 offen offset:2048 sc1\n"
"v_xor_b32 v13, 0x10000, v13\n"
"v_mfma_f32_16x16x32_bf16 v[176:179], v[52:55], a[112:115], v[176:179]\n"
"ds_read_b64_tr_b16 v[40:41], v14 offset:16384\n"
"v_mfma_f32_16x16x32_bf16 v[180:183], v[48:51], a[100:103], v[180:183]\n"
"v_mfma_f32_16x16x32_bf16 v[180:183], v[52:55], a[116:119], v[180:183]\n"
"ds_read_b64_tr_b16 v[42:43], v14 offset:17408\n"
"s_add_u32 s60, s33, 0x1000\n"
"v_mfma_f32_16x16x32_bf16 v[184:187], v[48:51], a[104:107], v[184:187]\n"
"buffer_store_dwordx4 v[84:87], v1, s[4:7], s60 offen offset:3072 sc1\n"
"v_mfma_f32_16x16x32_bf16 v[184:187], v[52:55], a[120:123], v[184:187]\n"
"ds_read_b64_tr_b16 v[44:45], v14 offset:24576\n"
"v_mfma_f32_16x16x32_bf16 v[188:191], v[48:51], a[108:111], v[188:191]\n"
"v_mfma_f32_16x16x32_bf16 v[188:191], v[52:55], a[124:127], v[188:191]\n"
"ds_read_b64_tr_b16 v[46:47], v14 offset:25600\n"
"s_waitcnt lgkmcnt(4)\n"
"v_mfma_f32_16x16x32_bf16 v[192:195], v[24:27], a[96:99], v[192:195]\n"
"buffer_load_dwordx4 a[64:67], v21, s[4:7], s59 offen sc1\n"
"v_xor_b32 v14, 0x10000, v14\n"
"v_mfma_f32_16x16x32_bf16 v[192:195], v[28:31], a[112:115], v[192:195]\n"
"ds_read_b64_tr_b16 v[48:49], v15 offset:16384\n"
"v_mfma_f32_16x16x32_bf16 v[196:199], v[24:27], a[100:103], v[196:199]\n"
"v_mfma_f32_16x16x32_bf16 v[196:199], v[28:31], a[116:119], v[196:199]\n"
"ds_read_b64_tr_b16 v[50:51], v15 offset:17408\n"
"v_mfma_f32_16x16x32_bf16 v[200:203], v[24:27], a[104:107], v[200:203]\n"
"buffer_load_dwordx4 a[68:71], v21, s[4:7], s59 offen offset:1024 sc1\n"
"v_mfma_f32_16x16x32_bf16 v[200:203], v[28:31], a[120:123], v[200:203]\n"
"ds_read_b64_tr_b16 v[52:53], v15 offset:24576\n"
"v_mfma_f32_16x16x32_bf16 v[204:207], v[24:27], a[108:111], v[204:207]\n"
"v_mfma_f32_16x16x32_bf16 v[204:207], v[28:31], a[124:127], v[204:207]\n"
"ds_read_b64_tr_b16 v[54:55], v15 offset:25600\n"
"v_mfma_f32_16x16x32_bf16 v[208:211], v[32:35], a[96:99], v[208:211]\n"
"buffer_load_dwordx4 a[72:75], v21, s[4:7], s59 offen offset:2048 sc1\n"
"v_xor_b32 v15, 0x10000, v15\n"
"v_mfma_f32_16x16x32_bf16 v[208:211], v[36:39], a[112:115], v[208:211]\n"
"ds_read_b64_tr_b16 v[24:25], v16\n"
"v_mfma_f32_16x16x32_bf16 v[212:215], v[32:35], a[100:103], v[212:215]\n"
"v_mfma_f32_16x16x32_bf16 v[212:215], v[36:39], a[116:119], v[212:215]\n"
"ds_read_b64_tr_b16 v[26:27], v16 offset:1024\n"
"v_mfma_f32_16x16x32_bf16 v[216:219], v[32:35], a[104:107], v[216:219]\n"
"buffer_load_dwordx4 a[76:79], v21, s[4:7], s59 offen offset:3072 sc1\n"
"v_mfma_f32_16x16x32_bf16 v[216:219], v[36:39], a[120:123], v[216:219]\n"
"ds_read_b64_tr_b16 v[28:29], v16 offset:8192\n"
"s_add_u32 s59, s59, 0x1000\n"
"v_mfma_f32_16x16x32_bf16 v[220:223], v[32:35], a[108:111], v[220:223]\n"
"s_add_u32 s60, s32, 0x10000\n"
"v_mfma_f32_16x16x32_bf16 v[220:223], v[36:39], a[124:127], v[220:223]\n"
"ds_read_b64_tr_b16 v[30:31], v16 offset:9216\n"
"s_waitcnt lgkmcnt(4)\n"
"v_mfma_f32_16x16x32_bf16 v[224:227], v[40:43], a[96:99], v[224:227]\n"
"buffer_store_dwordx4 v[88:91], v1, s[4:7], s60 offen sc1\n"
"v_mfma_f32_16x16x32_bf16 v[224:227], v[44:47], a[112:115], v[224:227]\n"
"ds_read_b64_tr_b16 v[32:33], v17\n"
"v_mfma_f32_16x16x32_bf16 v[228:231], v[40:43], a[100:103], v[228:231]\n"
"v_mfma_f32_16x16x32_bf16 v[228:231], v[44:47], a[116:119], v[228:231]\n"
"ds_read_b64_tr_b16 v[34:35], v17 offset:1024\n"
"v_mfma_f32_16x16x32_bf16 v[232:235], v[40:43], a[104:107], v[232:235]\n"
"buffer_load_dwordx4 a[80:83], v21, s[4:7], s59 offen sc1\n"
"v_mfma_f32_16x16x32_bf16 v[232:235], v[44:47], a[120:123], v[232:235]\n"
"ds_read_b64_tr_b16 v[36:37], v17 offset:8192\n"
"v_mfma_f32_16x16x32_bf16 v[236:239], v[40:43], a[108:111], v[236:239]\n"
"v_mfma_f32_16x16x32_bf16 v[236:239], v[44:47], a[124:127], v[236:239]\n"
"ds_read_b64_tr_b16 v[38:39], v17 offset:9216\n"
"v_mfma_f32_16x16x32_bf16 v[240:243], v[48:51], a[96:99], v[240:243]\n"
"buffer_load_dwordx4 a[84:87], v21, s[4:7], s59 offen offset:1024 sc1\n"
"v_mfma_f32_16x16x32_bf16 v[240:243], v[52:55], a[112:115], v[240:243]\n"
"ds_read_b64_tr_b16 v[40:41], v18\n"
"v_mfma_f32_16x16x32_bf16 v[244:247], v[48:51], a[100:103], v[244:247]\n"
"buffer_load_dword v3, v2, s[12:15], s83 offen\n"
"v_mfma_f32_16x16x32_bf16 v[244:247], v[52:55], a[116:119], v[244:247]\n"
"ds_read_b64_tr_b16 v[42:43], v18 offset:1024\n"
"v_mfma_f32_16x16x32_bf16 v[248:251], v[48:51], a[104:107], v[248:251]\n"
"buffer_load_dwordx4 a[88:91], v21, s[4:7], s59 offen offset:2048 sc1\n"
"v_mfma_f32_16x16x32_bf16 v[248:251], v[52:55], a[120:123], v[248:251]\n"
"ds_read_b64_tr_b16 v[44:45], v18 offset:8192\n"
"v_mfma_f32_16x16x32_bf16 v[252:255], v[48:51], a[108:111], v[252:255]\n"
"v_mfma_f32_16x16x32_bf16 v[252:255], v[52:55], a[124:127], v[252:255]\n"
"ds_read_b64_tr_b16 v[46:47], v18 offset:9216\n"
"s_waitcnt vmcnt(26) lgkmcnt(4)\n"
"v_mfma_f32_16x16x32_bf16 v[128:131], v[24:27], a[0:3], v[128:131]\n"
"v_mfma_f32_16x16x32_bf16 v[128:131], v[28:31], a[16:19], v[128:131]\n"
"ds_read_b64_tr_b16 v[48:49], v19\n"
"s_add_u32 s60, s32, 0x11000\n"
"v_mfma_f32_16x16x32_bf16 v[132:135], v[24:27], a[4:7], v[132:135]\n"
"buffer_store_dwordx4 v[92:95], v1, s[4:7], s60 offen sc1\n"
"v_mfma_f32_16x16x32_bf16 v[132:135], v[28:31], a[20:23], v[132:135]\n"
"ds_read_b64_tr_b16 v[50:51], v19 offset:1024\n"
"v_mfma_f32_16x16x32_bf16 v[136:139], v[24:27], a[8:11], v[136:139]\n"
"v_mfma_f32_16x16x32_bf16 v[136:139], v[28:31], a[24:27], v[136:139]\n"
"ds_read_b64_tr_b16 v[52:53], v19 offset:8192\n"
"v_mfma_f32_16x16x32_bf16 v[140:143], v[24:27], a[12:15], v[140:143]\n"
"buffer_load_dwordx4 a[92:95], v21, s[4:7], s59 offen offset:3072 sc1\n"
"v_mfma_f32_16x16x32_bf16 v[140:143], v[28:31], a[28:31], v[140:143]\n"
"ds_read_b64_tr_b16 v[54:55], v19 offset:9216\n"
"s_add_u32 s59, s59, 0x3000\n"
"v_mfma_f32_16x16x32_bf16 v[144:147], v[32:35], a[0:3], v[144:147]\n"
"s_cmp_eq_u32 s59, 0x28000\n"
"s_cmov_b32 s59, 0\n"
"v_mfma_f32_16x16x32_bf16 v[144:147], v[36:39], a[16:19], v[144:147]\n"
"ds_read_b64_tr_b16 v[24:25], v12\n"
"v_mfma_f32_16x16x32_bf16 v[148:151], v[32:35], a[4:7], v[148:151]\n"
"v_mfma_f32_16x16x32_bf16 v[148:151], v[36:39], a[20:23], v[148:151]\n"
"ds_read_b64_tr_b16 v[26:27], v12 offset:1024\n"
"v_mfma_f32_16x16x32_bf16 v[152:155], v[32:35], a[8:11], v[152:155]\n"
"v_mfma_f32_16x16x32_bf16 v[152:155], v[36:39], a[24:27], v[152:155]\n"
"ds_read_b64_tr_b16 v[28:29], v12 offset:8192\n"
"v_mfma_f32_16x16x32_bf16 v[156:159], v[32:35], a[12:15], v[156:159]\n"
"v_mfma_f32_16x16x32_bf16 v[156:159], v[36:39], a[28:31], v[156:159]\n"
"ds_read_b64_tr_b16 v[30:31], v12 offset:9216\n"
"s_waitcnt lgkmcnt(4)\n"
"v_mfma_f32_16x16x32_bf16 v[160:163], v[40:43], a[0:3], v[160:163]\n"
"v_accvgpr_read_b32 v120, a144\n"
"v_accvgpr_read_b32 v121, a145\n"
"v_mfma_f32_16x16x32_bf16 v[160:163], v[44:47], a[16:19], v[160:163]\n"
"ds_read_b64_tr_b16 v[32:33], v13\n"
"v_accvgpr_read_b32 v122, a146\n"
"v_mfma_f32_16x16x32_bf16 v[164:167], v[40:43], a[4:7], v[164:167]\n"
"v_accvgpr_read_b32 v123, a147\n"
"v_accvgpr_read_b32 v124, a148\n"
"v_mfma_f32_16x16x32_bf16 v[164:167], v[44:47], a[20:23], v[164:167]\n"
"ds_read_b64_tr_b16 v[34:35], v13 offset:1024\n"
"v_accvgpr_read_b32 v125, a149\n"
"v_mfma_f32_16x16x32_bf16 v[168:171], v[40:43], a[8:11], v[168:171]\n"
"buffer_load_dwordx4 a[96:99], v21, s[4:7], s59 offen sc1\n"
"s_add_u32 s60, s32, 0x14000\n"
"v_mfma_f32_16x16x32_bf16 v[168:171], v[44:47], a[24:27], v[168:171]\n"
"ds_read_b64_tr_b16 v[36:37], v13 offset:8192\n"
"v_accvgpr_read_b32 v126, a150\n"
"v_mfma_f32_16x16x32_bf16 v[172:175], v[40:43], a[12:15], v[172:175]\n"
"v_accvgpr_read_b32 v127, a151\n"
"v_cvt_pk_bf16_f32 v56, v120, v121\n"
"v_mfma_f32_16x16x32_bf16 v[172:175], v[44:47], a[28:31], v[172:175]\n"
"ds_read_b64_tr_b16 v[38:39], v13 offset:9216\n"
"s_add_u32 m0, s78, 0xc000\n"
"v_mfma_f32_16x16x32_bf16 v[176:179], v[48:51], a[0:3], v[176:179]\n"
"buffer_load_dwordx4 v6, s[20:23], s58 offen lds\n"
"v_cvt_pk_bf16_f32 v57, v122, v123\n"
"v_mfma_f32_16x16x32_bf16 v[176:179], v[52:55], a[16:19], v[176:179]\n"
"ds_read_b64_tr_b16 v[40:41], v14\n"
"v_cvt_pk_bf16_f32 v58, v124, v125\n"
"v_mfma_f32_16x16x32_bf16 v[180:183], v[48:51], a[4:7], v[180:183]\n"
"v_cvt_pk_bf16_f32 v59, v126, v127\n"
"v_mfma_f32_16x16x32_bf16 v[180:183], v[52:55], a[20:23], v[180:183]\n"
"ds_read_b64_tr_b16 v[42:43], v14 offset:1024\n"
"v_mfma_f32_16x16x32_bf16 v[184:187], v[48:51], a[8:11], v[184:187]\n"
"buffer_load_dwordx4 v7, s[20:23], s58 offen offset:1024 lds\n"
"v_mfma_f32_16x16x32_bf16 v[184:187], v[52:55], a[24:27], v[184:187]\n"
"ds_read_b64_tr_b16 v[44:45], v14 offset:8192\n"
"v_mfma_f32_16x16x32_bf16 v[188:191], v[48:51], a[12:15], v[188:191]\n"
"v_mfma_f32_16x16x32_bf16 v[188:191], v[52:55], a[28:31], v[188:191]\n"
"ds_read_b64_tr_b16 v[46:47], v14 offset:9216\n"
"s_waitcnt lgkmcnt(4)\n"
"v_mfma_f32_16x16x32_bf16 v[192:195], v[24:27], a[0:3], v[192:195]\n"
"buffer_load_dwordx4 v8, s[20:23], s58 offen offset:2048 lds\n"
"v_mfma_f32_16x16x32_bf16 v[192:195], v[28:31], a[16:19], v[192:195]\n"
"ds_read_b64_tr_b16 v[48:49], v15\n"
"v_mfma_f32_16x16x32_bf16 v[196:199], v[24:27], a[4:7], v[196:199]\n"
"v_accvgpr_read_b32 v120, a152\n"
"v_accvgpr_read_b32 v121, a153\n"
"v_mfma_f32_16x16x32_bf16 v[196:199], v[28:31], a[20:23], v[196:199]\n"
"ds_read_b64_tr_b16 v[50:51], v15 offset:1024\n"
"v_accvgpr_read_b32 v122, a154\n"
"v_mfma_f32_16x16x32_bf16 v[200:203], v[24:27], a[8:11], v[200:203]\n"
"buffer_load_dwordx4 v9, s[20:23], s58 offen offset:3072 lds\n"
"v_accvgpr_read_b32 v123, a155\n"
"v_mfma_f32_16x16x32_bf16 v[200:203], v[28:31], a[24:27], v[200:203]\n"
"ds_read_b64_tr_b16 v[52:53], v15 offset:8192\n"
"s_add_u32 s58, s58, 0x80000\n"
"v_mfma_f32_16x16x32_bf16 v[204:207], v[24:27], a[12:15], v[204:207]\n"
"s_cmp_eq_u32 s58, 0x500000\n"
"s_cmov_b32 s58, 0\n"
"v_mfma_f32_16x16x32_bf16 v[204:207], v[28:31], a[28:31], v[204:207]\n"
"ds_read_b64_tr_b16 v[54:55], v15 offset:9216\n"
"v_accvgpr_read_b32 v124, a156\n"
"v_mfma_f32_16x16x32_bf16 v[208:211], v[32:35], a[0:3], v[208:211]\n"
"buffer_store_dwordx4 v[96:99], v1, s[4:7], s60 offen sc1\n"
"s_add_u32 s60, s32, 0x15000\n"
"v_mfma_f32_16x16x32_bf16 v[208:211], v[36:39], a[16:19], v[208:211]\n"
"ds_read_b64_tr_b16 v[24:25], v16 offset:32768\n"
"v_accvgpr_read_b32 v125, a157\n"
"v_mfma_f32_16x16x32_bf16 v[212:215], v[32:35], a[4:7], v[212:215]\n"
"v_accvgpr_read_b32 v126, a158\n"
"v_accvgpr_read_b32 v127, a159\n"
"v_mfma_f32_16x16x32_bf16 v[212:215], v[36:39], a[20:23], v[212:215]\n"
"ds_read_b64_tr_b16 v[26:27], v16 offset:33792\n"
"v_cvt_pk_bf16_f32 v60, v120, v121\n"
"v_mfma_f32_16x16x32_bf16 v[216:219], v[32:35], a[8:11], v[216:219]\n"
"s_bfe_u32 s75, s100, 0x20001\n"
"s_and_b32 s11, s48, 3\n"
"v_mfma_f32_16x16x32_bf16 v[216:219], v[36:39], a[24:27], v[216:219]\n"
"ds_read_b64_tr_b16 v[28:29], v16 offset:40960\n"
"s_lshl2_add_u32 s75, s75, s11\n"
"v_mfma_f32_16x16x32_bf16 v[220:223], v[32:35], a[12:15], v[220:223]\n"
"s_lshl_b32 s75, s75, 10\n"
"s_and_b32 s11, s75, 0x2000\n"
"v_mfma_f32_16x16x32_bf16 v[220:223], v[36:39], a[28:31], v[220:223]\n"
"ds_read_b64_tr_b16 v[30:31], v16 offset:41984\n"
"s_waitcnt lgkmcnt(4)\n"
"v_mfma_f32_16x16x32_bf16 v[224:227], v[40:43], a[0:3], v[224:227]\n"
"v_xor_b32 v16, 0x10000, v16\n"
"s_add_u32 s75, s75, s11\n"
"v_mfma_f32_16x16x32_bf16 v[224:227], v[44:47], a[16:19], v[224:227]\n"
"ds_read_b64_tr_b16 v[32:33], v17 offset:32768\n"
"s_add_u32 s75, s75, 0xf0000\n"
"v_mfma_f32_16x16x32_bf16 v[228:231], v[40:43], a[4:7], v[228:231]\n"
"s_xor_b32 s75, s75, s101\n"
"s_lshl_b32 s11, s49, 7\n"
"v_mfma_f32_16x16x32_bf16 v[228:231], v[44:47], a[20:23], v[228:231]\n"
"ds_read_b64_tr_b16 v[34:35], v17 offset:33792\n"
"s_add_u32 s69, s75, s11\n"
"v_mfma_f32_16x16x32_bf16 v[232:235], v[40:43], a[8:11], v[232:235]\n"
"buffer_store_dwordx4 v[100:103], v1, s[4:7], s60 offen sc1\n"
"v_cvt_pk_bf16_f32 v61, v122, v123\n"
"v_mfma_f32_16x16x32_bf16 v[232:235], v[44:47], a[24:27], v[232:235]\n"
"ds_read_b64_tr_b16 v[36:37], v17 offset:40960\n"
"v_cvt_pk_bf16_f32 v62, v124, v125\n"
"v_mfma_f32_16x16x32_bf16 v[236:239], v[40:43], a[12:15], v[236:239]\n"
"v_cvt_pk_bf16_f32 v63, v126, v127\n"
"v_mfma_f32_16x16x32_bf16 v[236:239], v[44:47], a[28:31], v[236:239]\n"
"ds_read_b64_tr_b16 v[38:39], v17 offset:41984\n"
"v_mfma_f32_16x16x32_bf16 v[240:243], v[48:51], a[0:3], v[240:243]\n"
"buffer_load_dwordx4 a[100:103], v21, s[4:7], s59 offen offset:1024 sc1\n"
"v_xor_b32 v17, 0x10000, v17\n"
"v_mfma_f32_16x16x32_bf16 v[240:243], v[52:55], a[16:19], v[240:243]\n"
"ds_read_b64_tr_b16 v[40:41], v18 offset:32768\n"
"v_mfma_f32_16x16x32_bf16 v[244:247], v[48:51], a[4:7], v[244:247]\n"
"s_add_u32 s60, s32, 0x10000\n"
"v_mfma_f32_16x16x32_bf16 v[244:247], v[52:55], a[20:23], v[244:247]\n"
"ds_read_b64_tr_b16 v[42:43], v18 offset:33792\n"
"v_mfma_f32_16x16x32_bf16 v[248:251], v[48:51], a[8:11], v[248:251]\n"
"buffer_store_dwordx4 v[104:107], v1, s[4:7], s60 offen offset:1024 sc1\n"
"v_mfma_f32_16x16x32_bf16 v[248:251], v[52:55], a[24:27], v[248:251]\n"
"ds_read_b64_tr_b16 v[44:45], v18 offset:40960\n"
"v_mfma_f32_16x16x32_bf16 v[252:255], v[48:51], a[12:15], v[252:255]\n"
"v_accvgpr_read_b32 v120, a176\n"
"v_accvgpr_read_b32 v121, a177\n"
"v_mfma_f32_16x16x32_bf16 v[252:255], v[52:55], a[28:31], v[252:255]\n"
"ds_read_b64_tr_b16 v[46:47], v18 offset:41984\n"
"s_waitcnt vmcnt(26) lgkmcnt(4)\n"
"v_mfma_f32_16x16x32_bf16 v[128:131], v[24:27], a[32:35], v[128:131]\n"
"buffer_load_dwordx4 a[104:107], v21, s[4:7], s59 offen offset:2048 sc1\n"
"v_xor_b32 v18, 0x10000, v18\n"
"v_mfma_f32_16x16x32_bf16 v[128:131], v[28:31], a[48:51], v[128:131]\n"
"ds_read_b64_tr_b16 v[48:49], v19 offset:32768\n"
"v_accvgpr_read_b32 v122, a178\n"
"v_mfma_f32_16x16x32_bf16 v[132:135], v[24:27], a[36:39], v[132:135]\n"
"v_accvgpr_read_b32 v123, a179\n"
"v_accvgpr_read_b32 v124, a180\n"
"v_mfma_f32_16x16x32_bf16 v[132:135], v[28:31], a[52:55], v[132:135]\n"
"ds_read_b64_tr_b16 v[50:51], v19 offset:33792\n"
"v_accvgpr_read_b32 v125, a181\n"
"v_mfma_f32_16x16x32_bf16 v[136:139], v[24:27], a[40:43], v[136:139]\n"
"buffer_load_dwordx4 a[108:111], v21, s[4:7], s59 offen offset:3072 sc1\n"
"v_accvgpr_read_b32 v126, a182\n"
"v_mfma_f32_16x16x32_bf16 v[136:139], v[28:31], a[56:59], v[136:139]\n"
"ds_read_b64_tr_b16 v[52:53], v19 offset:40960\n"
"s_add_u32 s59, s59, 0x1000\n"
"v_mfma_f32_16x16x32_bf16 v[140:143], v[24:27], a[44:47], v[140:143]\n"
"v_accvgpr_read_b32 v127, a183\n"
"v_cvt_pk_bf16_f32 v64, v120, v121\n"
"v_mfma_f32_16x16x32_bf16 v[140:143], v[28:31], a[60:63], v[140:143]\n"
"ds_read_b64_tr_b16 v[54:55], v19 offset:41984\n"
"v_cvt_pk_bf16_f32 v65, v122, v123\n"
"v_mfma_f32_16x16x32_bf16 v[144:147], v[32:35], a[32:35], v[144:147]\n"
"buffer_load_dwordx4 a[112:115], v21, s[4:7], s59 offen sc1\n"
"v_xor_b32 v19, 0x10000, v19\n"
"v_mfma_f32_16x16x32_bf16 v[144:147], v[36:39], a[48:51], v[144:147]\n"
"ds_read_b64_tr_b16 v[24:25], v12 offset:32768\n"
"v_cvt_pk_bf16_f32 v66, v124, v125\n"
"v_mfma_f32_16x16x32_bf16 v[148:151], v[32:35], a[36:39], v[148:151]\n"
"v_cvt_pk_bf16_f32 v67, v126, v127\n"
"v_accvgpr_read_b32 v120, a184\n"
"v_mfma_f32_16x16x32_bf16 v[148:151], v[36:39], a[52:55], v[148:151]\n"
"ds_read_b64_tr_b16 v[26:27], v12 offset:33792\n"
"v_accvgpr_read_b32 v121, a185\n"
"v_mfma_f32_16x16x32_bf16 v[152:155], v[32:35], a[40:43], v[152:155]\n"
"v_accvgpr_read_b32 v122, a186\n"
"v_accvgpr_read_b32 v123, a187\n"
"v_mfma_f32_16x16x32_bf16 v[152:155], v[36:39], a[56:59], v[152:155]\n"
"ds_read_b64_tr_b16 v[28:29], v12 offset:40960\n"
"s_add_u32 s60, s32, 0x11000\n"
"v_mfma_f32_16x16x32_bf16 v[156:159], v[32:35], a[44:47], v[156:159]\n"
"buffer_store_dwordx4 v[108:111], v1, s[4:7], s60 offen offset:1024 sc1\n"
"v_accvgpr_read_b32 v124, a188\n"
"v_mfma_f32_16x16x32_bf16 v[156:159], v[36:39], a[60:63], v[156:159]\n"
"ds_read_b64_tr_b16 v[30:31], v12 offset:41984\n"
"s_waitcnt lgkmcnt(4)\n"
"v_mfma_f32_16x16x32_bf16 v[160:163], v[40:43], a[32:35], v[160:163]\n"
"v_xor_b32 v12, 0x10000, v12\n"
"v_accvgpr_read_b32 v125, a189\n"
"v_mfma_f32_16x16x32_bf16 v[160:163], v[44:47], a[48:51], v[160:163]\n"
"ds_read_b64_tr_b16 v[32:33], v13 offset:32768\n"
"v_accvgpr_read_b32 v126, a190\n"
"v_mfma_f32_16x16x32_bf16 v[164:167], v[40:43], a[36:39], v[164:167]\n"
"buffer_load_dwordx4 a[116:119], v21, s[4:7], s59 offen offset:1024 sc1\n"
"v_accvgpr_read_b32 v127, a191\n"
"v_mfma_f32_16x16x32_bf16 v[164:167], v[44:47], a[52:55], v[164:167]\n"
"ds_read_b64_tr_b16 v[34:35], v13 offset:33792\n"
"v_cvt_pk_bf16_f32 v68, v120, v121\n"
"v_mfma_f32_16x16x32_bf16 v[168:171], v[40:43], a[40:43], v[168:171]\n"
"v_cvt_pk_bf16_f32 v69, v122, v123\n"
"v_cvt_pk_bf16_f32 v70, v124, v125\n"
"v_mfma_f32_16x16x32_bf16 v[168:171], v[44:47], a[56:59], v[168:171]\n"
"ds_read_b64_tr_b16 v[36:37], v13 offset:40960\n"
"v_cvt_pk_bf16_f32 v71, v126, v127\n"
"v_mfma_f32_16x16x32_bf16 v[172:175], v[40:43], a[44:47], v[172:175]\n"
"buffer_load_dwordx4 a[120:123], v21, s[4:7], s59 offen offset:2048 sc1\n"
"v_mfma_f32_16x16x32_bf16 v[172:175], v[44:47], a[60:63], v[172:175]\n"
"ds_read_b64_tr_b16 v[38:39], v13 offset:41984\n"
"v_mfma_f32_16x16x32_bf16 v[176:179], v[48:51], a[32:35], v[176:179]\n"
"v_xor_b32 v13, 0x10000, v13\n"
"v_mfma_f32_16x16x32_bf16 v[176:179], v[52:55], a[48:51], v[176:179]\n"
"ds_read_b64_tr_b16 v[40:41], v14 offset:32768\n"
"v_mfma_f32_16x16x32_bf16 v[180:183], v[48:51], a[36:39], v[180:183]\n"
"buffer_load_dwordx4 a[124:127], v21, s[4:7], s59 offen offset:3072 sc1\n"
"v_mfma_f32_16x16x32_bf16 v[180:183], v[52:55], a[52:55], v[180:183]\n"
"ds_read_b64_tr_b16 v[42:43], v14 offset:33792\n"
"s_add_u32 s59, s59, 0x3000\n"
"v_mfma_f32_16x16x32_bf16 v[184:187], v[48:51], a[40:43], v[184:187]\n"
"s_cmp_eq_u32 s59, 0x28000\n"
"s_cmov_b32 s59, 0\n"
"v_mfma_f32_16x16x32_bf16 v[184:187], v[52:55], a[56:59], v[184:187]\n"
"ds_read_b64_tr_b16 v[44:45], v14 offset:40960\n"
"s_add_u32 m0, s78, 0x14000\n"
"v_mfma_f32_16x16x32_bf16 v[188:191], v[48:51], a[44:47], v[188:191]\n"
"buffer_load_dwordx4 v6, s[20:23], s58 offen lds\n"
"v_accvgpr_read_b32 v120, a208\n"
"v_mfma_f32_16x16x32_bf16 v[188:191], v[52:55], a[60:63], v[188:191]\n"
"ds_read_b64_tr_b16 v[46:47], v14 offset:41984\n"
"s_waitcnt lgkmcnt(4)\n"
"v_mfma_f32_16x16x32_bf16 v[192:195], v[24:27], a[32:35], v[192:195]\n"
"v_xor_b32 v14, 0x10000, v14\n"
"s_add_u32 s60, s32, 0x14000\n"
"v_mfma_f32_16x16x32_bf16 v[192:195], v[28:31], a[48:51], v[192:195]\n"
"ds_read_b64_tr_b16 v[48:49], v15 offset:32768\n"
"v_accvgpr_read_b32 v121, a209\n"
"v_mfma_f32_16x16x32_bf16 v[196:199], v[24:27], a[36:39], v[196:199]\n"
"buffer_load_dwordx4 v7, s[20:23], s58 offen offset:1024 lds\n"
"v_accvgpr_read_b32 v122, a210\n"
"v_mfma_f32_16x16x32_bf16 v[196:199], v[28:31], a[52:55], v[196:199]\n"
"ds_read_b64_tr_b16 v[50:51], v15 offset:33792\n"
"v_accvgpr_read_b32 v123, a211\n"
"v_mfma_f32_16x16x32_bf16 v[200:203], v[24:27], a[40:43], v[200:203]\n"
"v_accvgpr_read_b32 v124, a212\n"
"v_accvgpr_read_b32 v125, a213\n"
"v_mfma_f32_16x16x32_bf16 v[200:203], v[28:31], a[56:59], v[200:203]\n"
"ds_read_b64_tr_b16 v[52:53], v15 offset:40960\n"
"v_accvgpr_read_b32 v126, a214\n"
"v_mfma_f32_16x16x32_bf16 v[204:207], v[24:27], a[44:47], v[204:207]\n"
"buffer_load_dwordx4 v8, s[20:23], s58 offen offset:2048 lds\n"
"v_accvgpr_read_b32 v127, a215\n"
"v_mfma_f32_16x16x32_bf16 v[204:207], v[28:31], a[60:63], v[204:207]\n"
"ds_read_b64_tr_b16 v[54:55], v15 offset:41984\n"
"s_waitcnt vmcnt(30)\n"
"v_mfma_f32_16x16x32_bf16 v[208:211], v[32:35], a[32:35], v[208:211]\n"
"v_xor_b32 v15, 0x10000, v15\n"
"s_barrier\n"
"v_mfma_f32_16x16x32_bf16 v[208:211], v[36:39], a[48:51], v[208:211]\n"
"ds_read_b64_tr_b16 v[24:25], v16 offset:32768\n"
"v_cvt_pk_bf16_f32 v72, v120, v121\n"
"v_mfma_f32_16x16x32_bf16 v[212:215], v[32:35], a[36:39], v[212:215]\n"
"buffer_load_dwordx4 v9, s[20:23], s58 offen offset:3072 lds\n"
"v_cvt_pk_bf16_f32 v73, v122, v123\n"
"v_mfma_f32_16x16x32_bf16 v[212:215], v[36:39], a[52:55], v[212:215]\n"
"ds_read_b64_tr_b16 v[26:27], v16 offset:33792\n"
"s_add_u32 s58, s58, 0x80000\n"
"v_mfma_f32_16x16x32_bf16 v[216:219], v[32:35], a[40:43], v[216:219]\n"
"s_cmp_eq_u32 s58, 0x500000\n"
"s_cmov_b32 s58, 0\n"
"v_mfma_f32_16x16x32_bf16 v[216:219], v[36:39], a[56:59], v[216:219]\n"
"ds_read_b64_tr_b16 v[28:29], v16 offset:40960\n"
"v_cvt_pk_bf16_f32 v74, v124, v125\n"
"v_mfma_f32_16x16x32_bf16 v[220:223], v[32:35], a[44:47], v[220:223]\n"
"buffer_store_dword v22, v23, s[4:7], s69 offen sc1\n"
"s_add_u32 s69, s69, 0xfffd8000\n"
"v_mfma_f32_16x16x32_bf16 v[220:223], v[36:39], a[60:63], v[220:223]\n"
"ds_read_b64_tr_b16 v[30:31], v16 offset:41984\n"
"s_waitcnt lgkmcnt(4)\n"
"v_mfma_f32_16x16x32_bf16 v[224:227], v[40:43], a[32:35], v[224:227]\n"
"buffer_store_dwordx4 v[112:115], v1, s[4:7], s60 offen offset:1024 sc1\n"
"v_mov_b32 v20, 0\n"
"v_mfma_f32_16x16x32_bf16 v[224:227], v[44:47], a[48:51], v[224:227]\n"
"ds_read_b64_tr_b16 v[32:33], v17 offset:32768\n"
"v_cvt_pk_bf16_f32 v75, v126, v127\n"
"v_mfma_f32_16x16x32_bf16 v[228:231], v[40:43], a[36:39], v[228:231]\n"
"v_accvgpr_read_b32 v120, a216\n"
"v_accvgpr_read_b32 v121, a217\n"
"v_mfma_f32_16x16x32_bf16 v[228:231], v[44:47], a[52:55], v[228:231]\n"
"ds_read_b64_tr_b16 v[34:35], v17 offset:33792\n"
"s_add_u32 s60, s32, 0x15000\n"
"v_mfma_f32_16x16x32_bf16 v[232:235], v[40:43], a[40:43], v[232:235]\n"
"buffer_store_dwordx4 v[116:119], v1, s[4:7], s60 offen offset:1024 sc1\n"
"v_accvgpr_read_b32 v122, a218\n"
"v_mfma_f32_16x16x32_bf16 v[232:235], v[44:47], a[56:59], v[232:235]\n"
"ds_read_b64_tr_b16 v[36:37], v17 offset:40960\n"
"v_accvgpr_read_b32 v123, a219\n"
"v_mfma_f32_16x16x32_bf16 v[236:239], v[40:43], a[44:47], v[236:239]\n"
"v_accvgpr_read_b32 v124, a220\n"
"v_accvgpr_read_b32 v125, a221\n"
"v_mfma_f32_16x16x32_bf16 v[236:239], v[44:47], a[60:63], v[236:239]\n"
"ds_read_b64_tr_b16 v[38:39], v17 offset:41984\n"
"v_accvgpr_read_b32 v126, a222\n"
"v_mfma_f32_16x16x32_bf16 v[240:243], v[48:51], a[32:35], v[240:243]\n"
"buffer_load_dwordx4 a[0:3], v21, s[4:7], s59 offen sc1\n"
"v_accvgpr_read_b32 v127, a223\n"
"v_mfma_f32_16x16x32_bf16 v[240:243], v[52:55], a[48:51], v[240:243]\n"
"ds_read_b64_tr_b16 v[40:41], v18 offset:32768\n"
"v_cvt_pk_bf16_f32 v76, v120, v121\n"
"v_mfma_f32_16x16x32_bf16 v[244:247], v[48:51], a[36:39], v[244:247]\n"
"v_cvt_pk_bf16_f32 v77, v122, v123\n"
"v_cvt_pk_bf16_f32 v78, v124, v125\n"
"v_mfma_f32_16x16x32_bf16 v[244:247], v[52:55], a[52:55], v[244:247]\n"
"ds_read_b64_tr_b16 v[42:43], v18 offset:33792\n"
"v_cvt_pk_bf16_f32 v79, v126, v127\n"
"v_mfma_f32_16x16x32_bf16 v[248:251], v[48:51], a[40:43], v[248:251]\n"
"buffer_load_dwordx4 a[4:7], v21, s[4:7], s59 offen offset:1024 sc1\n"
"v_mfma_f32_16x16x32_bf16 v[248:251], v[52:55], a[56:59], v[248:251]\n"
"ds_read_b64_tr_b16 v[44:45], v18 offset:40960\n"
"v_mfma_f32_16x16x32_bf16 v[252:255], v[48:51], a[44:47], v[252:255]\n"
"v_mfma_f32_16x16x32_bf16 v[252:255], v[52:55], a[60:63], v[252:255]\n"
"ds_read_b64_tr_b16 v[46:47], v18 offset:41984\n"
"s_waitcnt vmcnt(25) lgkmcnt(4)\n"
"v_mfma_f32_16x16x32_bf16 v[128:131], v[24:27], a[64:67], v[128:131]\n"
"buffer_load_dwordx4 a[8:11], v21, s[4:7], s59 offen offset:2048 sc1\n"
"v_mfma_f32_16x16x32_bf16 v[128:131], v[28:31], a[80:83], v[128:131]\n"
"ds_read_b64_tr_b16 v[48:49], v19 offset:32768\n"
"v_mfma_f32_16x16x32_bf16 v[132:135], v[24:27], a[68:71], v[132:135]\n"
"v_mfma_f32_16x16x32_bf16 v[132:135], v[28:31], a[84:87], v[132:135]\n"
"ds_read_b64_tr_b16 v[50:51], v19 offset:33792\n"
"v_mfma_f32_16x16x32_bf16 v[136:139], v[24:27], a[72:75], v[136:139]\n"
"buffer_load_dwordx4 a[12:15], v21, s[4:7], s59 offen offset:3072 sc1\n"
"v_mfma_f32_16x16x32_bf16 v[136:139], v[28:31], a[88:91], v[136:139]\n"
"ds_read_b64_tr_b16 v[52:53], v19 offset:40960\n"
"s_add_u32 s59, s59, 0x1000\n"
"v_mfma_f32_16x16x32_bf16 v[140:143], v[24:27], a[76:79], v[140:143]\n"
"s_add_u32 s60, s33, 0x10000\n"
"v_mfma_f32_16x16x32_bf16 v[140:143], v[28:31], a[92:95], v[140:143]\n"
"ds_read_b64_tr_b16 v[54:55], v19 offset:41984\n"
"v_mfma_f32_16x16x32_bf16 v[144:147], v[32:35], a[64:67], v[144:147]\n"
"buffer_store_dwordx4 v[56:59], v1, s[4:7], s60 offen sc1\n"
"v_mfma_f32_16x16x32_bf16 v[144:147], v[36:39], a[80:83], v[144:147]\n"
"ds_read_b64_tr_b16 v[24:25], v12 offset:32768\n"
"v_mfma_f32_16x16x32_bf16 v[148:151], v[32:35], a[68:71], v[148:151]\n"
"v_mfma_f32_16x16x32_bf16 v[148:151], v[36:39], a[84:87], v[148:151]\n"
"ds_read_b64_tr_b16 v[26:27], v12 offset:33792\n"
"v_mfma_f32_16x16x32_bf16 v[152:155], v[32:35], a[72:75], v[152:155]\n"
"buffer_load_dwordx4 a[16:19], v21, s[4:7], s59 offen sc1\n"
"v_mfma_f32_16x16x32_bf16 v[152:155], v[36:39], a[88:91], v[152:155]\n"
"ds_read_b64_tr_b16 v[28:29], v12 offset:40960\n"
"v_mfma_f32_16x16x32_bf16 v[156:159], v[32:35], a[76:79], v[156:159]\n"
"v_mfma_f32_16x16x32_bf16 v[156:159], v[36:39], a[92:95], v[156:159]\n"
"ds_read_b64_tr_b16 v[30:31], v12 offset:41984\n"
"s_waitcnt lgkmcnt(4)\n"
"v_mfma_f32_16x16x32_bf16 v[160:163], v[40:43], a[64:67], v[160:163]\n"
"buffer_load_dwordx4 a[20:23], v21, s[4:7], s59 offen offset:1024 sc1\n"
"v_mfma_f32_16x16x32_bf16 v[160:163], v[44:47], a[80:83], v[160:163]\n"
"ds_read_b64_tr_b16 v[32:33], v13 offset:32768\n"
"v_mfma_f32_16x16x32_bf16 v[164:167], v[40:43], a[68:71], v[164:167]\n"
"v_mfma_f32_16x16x32_bf16 v[164:167], v[44:47], a[84:87], v[164:167]\n"
"ds_read_b64_tr_b16 v[34:35], v13 offset:33792\n"
"v_mfma_f32_16x16x32_bf16 v[168:171], v[40:43], a[72:75], v[168:171]\n"
"buffer_load_dwordx4 a[24:27], v21, s[4:7], s59 offen offset:2048 sc1\n"
"v_mfma_f32_16x16x32_bf16 v[168:171], v[44:47], a[88:91], v[168:171]\n"
"ds_read_b64_tr_b16 v[36:37], v13 offset:40960\n"
"v_mfma_f32_16x16x32_bf16 v[172:175], v[40:43], a[76:79], v[172:175]\n"
"v_mfma_f32_16x16x32_bf16 v[172:175], v[44:47], a[92:95], v[172:175]\n"
"ds_read_b64_tr_b16 v[38:39], v13 offset:41984\n"
"v_mfma_f32_16x16x32_bf16 v[176:179], v[48:51], a[64:67], v[176:179]\n"
"v_mfma_f32_16x16x32_bf16 v[176:179], v[52:55], a[80:83], v[176:179]\n"
"ds_read_b64_tr_b16 v[40:41], v14 offset:32768\n"
"s_add_u32 s60, s33, 0x10000\n"
"v_mfma_f32_16x16x32_bf16 v[180:183], v[48:51], a[68:71], v[180:183]\n"
"buffer_store_dwordx4 v[60:63], v1, s[4:7], s60 offen offset:1024 sc1\n"
"v_mfma_f32_16x16x32_bf16 v[180:183], v[52:55], a[84:87], v[180:183]\n"
"ds_read_b64_tr_b16 v[42:43], v14 offset:33792\n"
"v_mfma_f32_16x16x32_bf16 v[184:187], v[48:51], a[72:75], v[184:187]\n"
"v_mfma_f32_16x16x32_bf16 v[184:187], v[52:55], a[88:91], v[184:187]\n"
"ds_read_b64_tr_b16 v[44:45], v14 offset:40960\n"
"v_mfma_f32_16x16x32_bf16 v[188:191], v[48:51], a[76:79], v[188:191]\n"
"buffer_load_dwordx4 a[28:31], v21, s[4:7], s59 offen offset:3072 sc1\n"
"v_accvgpr_read_b32 v120, a240\n"
"v_mfma_f32_16x16x32_bf16 v[188:191], v[52:55], a[92:95], v[188:191]\n"
"ds_read_b64_tr_b16 v[46:47], v14 offset:41984\n"
"s_waitcnt lgkmcnt(4)\n"
"v_mfma_f32_16x16x32_bf16 v[192:195], v[24:27], a[64:67], v[192:195]\n"
"s_add_u32 s59, s59, 0x3000\n"
"v_accvgpr_read_b32 v121, a241\n"
"v_mfma_f32_16x16x32_bf16 v[192:195], v[28:31], a[80:83], v[192:195]\n"
"ds_read_b64_tr_b16 v[48:49], v15 offset:32768\n"
"v_accvgpr_read_b32 v122, a242\n"
"v_mfma_f32_16x16x32_bf16 v[196:199], v[24:27], a[68:71], v[196:199]\n"
"s_cmp_eq_u32 s59, 0x28000\n"
"s_cmov_b32 s59, 0\n"
"v_mfma_f32_16x16x32_bf16 v[196:199], v[28:31], a[84:87], v[196:199]\n"
"ds_read_b64_tr_b16 v[50:51], v15 offset:33792\n"
"s_add_u32 m0, s78, 0x1c000\n"
"v_mfma_f32_16x16x32_bf16 v[200:203], v[24:27], a[72:75], v[200:203]\n"
"buffer_load_dwordx4 v6, s[20:23], s58 offen lds\n"
"v_accvgpr_read_b32 v123, a243\n"
"v_mfma_f32_16x16x32_bf16 v[200:203], v[28:31], a[88:91], v[200:203]\n"
"ds_read_b64_tr_b16 v[52:53], v15 offset:40960\n"
"v_accvgpr_read_b32 v124, a244\n"
"v_mfma_f32_16x16x32_bf16 v[204:207], v[24:27], a[76:79], v[204:207]\n"
"v_accvgpr_read_b32 v125, a245\n"
"v_accvgpr_read_b32 v126, a246\n"
"v_mfma_f32_16x16x32_bf16 v[204:207], v[28:31], a[92:95], v[204:207]\n"
"ds_read_b64_tr_b16 v[54:55], v15 offset:41984\n"
"s_waitcnt vmcnt(29)\n"
"v_mfma_f32_16x16x32_bf16 v[208:211], v[32:35], a[64:67], v[208:211]\n"
"s_barrier\n"
"buffer_load_dwordx4 v7, s[20:23], s58 offen offset:1024 lds\n"
"v_mfma_f32_16x16x32_bf16 v[208:211], v[36:39], a[80:83], v[208:211]\n"
"ds_read_b64_tr_b16 v[24:25], v16 offset:49152\n"
"v_accvgpr_read_b32 v127, a247\n"
"v_mfma_f32_16x16x32_bf16 v[212:215], v[32:35], a[68:71], v[212:215]\n"
"v_cvt_pk_bf16_f32 v80, v120, v121\n"
"v_cvt_pk_bf16_f32 v81, v122, v123\n"
"v_mfma_f32_16x16x32_bf16 v[212:215], v[36:39], a[84:87], v[212:215]\n"
"ds_read_b64_tr_b16 v[26:27], v16 offset:50176\n"
"v_cvt_pk_bf16_f32 v82, v124, v125\n"
"v_mfma_f32_16x16x32_bf16 v[216:219], v[32:35], a[72:75], v[216:219]\n"
"buffer_load_dwordx4 v8, s[20:23], s58 offen offset:2048 lds\n"
"s_add_u32 s60, s33, 0x10000\n"
"v_mfma_f32_16x16x32_bf16 v[216:219], v[36:39], a[88:91], v[216:219]\n"
"ds_read_b64_tr_b16 v[28:29], v16 offset:57344\n"
"v_cvt_pk_bf16_f32 v83, v126, v127\n"
"v_mfma_f32_16x16x32_bf16 v[220:223], v[32:35], a[76:79], v[220:223]\n"
"v_mfma_f32_16x16x32_bf16 v[220:223], v[36:39], a[92:95], v[220:223]\n"
"ds_read_b64_tr_b16 v[30:31], v16 offset:58368\n"
"s_waitcnt lgkmcnt(4)\n"
"v_mfma_f32_16x16x32_bf16 v[224:227], v[40:43], a[64:67], v[224:227]\n"
"buffer_load_dwordx4 v9, s[20:23], s58 offen offset:3072 lds\n"
"v_xor_b32 v16, 0x10000, v16\n"
"v_mfma_f32_16x16x32_bf16 v[224:227], v[44:47], a[80:83], v[224:227]\n"
"ds_read_b64_tr_b16 v[32:33], v17 offset:49152\n"
"s_add_u32 s58, s58, 0x80000\n"
"v_mfma_f32_16x16x32_bf16 v[228:231], v[40:43], a[68:71], v[228:231]\n"
"s_cmp_eq_u32 s58, 0x500000\n"
"s_cmov_b32 s58, 0\n"
"v_mfma_f32_16x16x32_bf16 v[228:231], v[44:47], a[84:87], v[228:231]\n"
"ds_read_b64_tr_b16 v[34:35], v17 offset:50176\n"
"v_mfma_f32_16x16x32_bf16 v[232:235], v[40:43], a[72:75], v[232:235]\n"
"buffer_load_dword v11, v20, s[4:7], s75 offen sc1\n"
"v_mfma_f32_16x16x32_bf16 v[232:235], v[44:47], a[88:91], v[232:235]\n"
"ds_read_b64_tr_b16 v[36:37], v17 offset:57344\n"
"v_mfma_f32_16x16x32_bf16 v[236:239], v[40:43], a[76:79], v[236:239]\n"
"buffer_store_dwordx4 v[64:67], v1, s[4:7], s60 offen offset:2048 sc1\n"
"v_mfma_f32_16x16x32_bf16 v[236:239], v[44:47], a[92:95], v[236:239]\n"
"ds_read_b64_tr_b16 v[38:39], v17 offset:58368\n"
"v_mfma_f32_16x16x32_bf16 v[240:243], v[48:51], a[64:67], v[240:243]\n"
"v_xor_b32 v17, 0x10000, v17\n"
"v_accvgpr_read_b32 v120, a248\n"
"v_mfma_f32_16x16x32_bf16 v[240:243], v[52:55], a[80:83], v[240:243]\n"
"ds_read_b64_tr_b16 v[40:41], v18 offset:49152\n"
"v_accvgpr_read_b32 v121, a249\n"
"v_mfma_f32_16x16x32_bf16 v[244:247], v[48:51], a[68:71], v[244:247]\n"
"buffer_load_dwordx4 a[32:35], v21, s[4:7], s59 offen sc1\n"
"v_accvgpr_read_b32 v122, a250\n"
"v_mfma_f32_16x16x32_bf16 v[244:247], v[52:55], a[84:87], v[244:247]\n"
"ds_read_b64_tr_b16 v[42:43], v18 offset:50176\n"
"v_accvgpr_read_b32 v123, a251\n"
"v_mfma_f32_16x16x32_bf16 v[248:251], v[48:51], a[72:75], v[248:251]\n"
"v_accvgpr_read_b32 v124, a252\n"
"v_accvgpr_read_b32 v125, a253\n"
"v_mfma_f32_16x16x32_bf16 v[248:251], v[52:55], a[88:91], v[248:251]\n"
"ds_read_b64_tr_b16 v[44:45], v18 offset:57344\n"
"v_accvgpr_read_b32 v126, a254\n"
"v_mfma_f32_16x16x32_bf16 v[252:255], v[48:51], a[76:79], v[252:255]\n"
"buffer_load_dwordx4 a[36:39], v21, s[4:7], s59 offen offset:1024 sc1\n"
"v_accvgpr_read_b32 v127, a255\n"
"v_mfma_f32_16x16x32_bf16 v[252:255], v[52:55], a[92:95], v[252:255]\n"
"ds_read_b64_tr_b16 v[46:47], v18 offset:58368\n"
"s_waitcnt vmcnt(25) lgkmcnt(4)\n"
"v_mfma_f32_16x16x32_bf16 v[128:131], v[24:27], a[96:99], v[128:131]\n"
"v_xor_b32 v18, 0x10000, v18\n"
"s_add_u32 s60, s33, 0x10000\n"
"v_mfma_f32_16x16x32_bf16 v[128:131], v[28:31], a[112:115], v[128:131]\n"
"ds_read_b64_tr_b16 v[48:49], v19 offset:49152\n"
"v_cvt_pk_bf16_f32 v84, v120, v121\n"
"v_mfma_f32_16x16x32_bf16 v[132:135], v[24:27], a[100:103], v[132:135]\n"
"buffer_store_dwordx4 v[68:71], v1, s[4:7], s60 offen offset:3072 sc1\n"
"v_cvt_pk_bf16_f32 v85, v122, v123\n"
"v_mfma_f32_16x16x32_bf16 v[132:135], v[28:31], a[116:119], v[132:135]\n"
"ds_read_b64_tr_b16 v[50:51], v19 offset:50176\n"
"v_cvt_pk_bf16_f32 v86, v124, v125\n"
"v_mfma_f32_16x16x32_bf16 v[136:139], v[24:27], a[104:107], v[136:139]\n"
"v_cvt_pk_bf16_f32 v87, v126, v127\n"
"v_accvgpr_write_b32 a64, 0\n"
"v_mfma_f32_16x16x32_bf16 v[136:139], v[28:31], a[120:123], v[136:139]\n"
"ds_read_b64_tr_b16 v[52:53], v19 offset:57344\n"
"v_accvgpr_write_b32 a136, 0\n"
"v_mfma_f32_16x16x32_bf16 v[140:143], v[24:27], a[108:111], v[140:143]\n"
"buffer_load_dwordx4 a[40:43], v21, s[4:7], s59 offen offset:2048 sc1\n"
"v_accvgpr_write_b32 a137, 0\n"
"v_mfma_f32_16x16x32_bf16 v[140:143], v[28:31], a[124:127], v[140:143]\n"
"ds_read_b64_tr_b16 v[54:55], v19 offset:58368\n"
"v_accvgpr_write_b32 a138, 0\n"
"v_mfma_f32_16x16x32_bf16 v[144:147], v[32:35], a[96:99], v[144:147]\n"
"v_xor_b32 v19, 0x10000, v19\n"
"v_accvgpr_write_b32 a139, 0\n"
"v_mfma_f32_16x16x32_bf16 v[144:147], v[36:39], a[112:115], v[144:147]\n"
"ds_read_b64_tr_b16 v[24:25], v12 offset:49152\n"
"v_accvgpr_write_b32 a140, 0\n"
"v_mfma_f32_16x16x32_bf16 v[148:151], v[32:35], a[100:103], v[148:151]\n"
"buffer_load_dwordx4 a[44:47], v21, s[4:7], s59 offen offset:3072 sc1\n"
"v_accvgpr_write_b32 a141, 0\n"
"v_mfma_f32_16x16x32_bf16 v[148:151], v[36:39], a[116:119], v[148:151]\n"
"ds_read_b64_tr_b16 v[26:27], v12 offset:50176\n"
"s_add_u32 s59, s59, 0x1000\n"
"v_mfma_f32_16x16x32_bf16 v[152:155], v[32:35], a[104:107], v[152:155]\n"
"s_add_u32 s16, s4, 0x1000000\n"
"s_addc_u32 s17, s5, 0\n"
"v_mfma_f32_16x16x32_bf16 v[152:155], v[36:39], a[120:123], v[152:155]\n"
"ds_read_b64_tr_b16 v[28:29], v12 offset:57344\n"
"s_and_b32 s17, s17, 0xffff\n"
"v_mfma_f32_16x16x32_bf16 v[156:159], v[32:35], a[108:111], v[156:159]\n"
"buffer_load_dwordx4 a[48:51], v21, s[4:7], s59 offen sc1\n"
"s_mov_b32 s18, 0x6800000\n"
"v_mfma_f32_16x16x32_bf16 v[156:159], v[36:39], a[124:127], v[156:159]\n"
"ds_read_b64_tr_b16 v[30:31], v12 offset:58368\n"
"s_waitcnt lgkmcnt(4)\n"
"v_mfma_f32_16x16x32_bf16 v[160:163], v[40:43], a[96:99], v[160:163]\n"
"v_xor_b32 v12, 0x10000, v12\n"
"s_mov_b32 s19, 0x20000\n"
"v_mfma_f32_16x16x32_bf16 v[160:163], v[44:47], a[112:115], v[160:163]\n"
"ds_read_b64_tr_b16 v[32:33], v13 offset:49152\n"
"v_add_u32 v9, 0x8000, v1\n"
"v_mfma_f32_16x16x32_bf16 v[164:167], v[40:43], a[100:103], v[164:167]\n"
"s_add_u32 s60, s33, 0x11000\n"
"v_accvgpr_write_b32 a142, 0\n"
"v_mfma_f32_16x16x32_bf16 v[164:167], v[44:47], a[116:119], v[164:167]\n"
"ds_read_b64_tr_b16 v[34:35], v13 offset:50176\n"
"v_accvgpr_write_b32 a143, 0\n"
"v_mfma_f32_16x16x32_bf16 v[168:171], v[40:43], a[104:107], v[168:171]\n"
"buffer_store_dwordx4 v[72:75], v1, s[4:7], s60 offen sc1\n"
"v_accvgpr_write_b32 a65, 0\n"
"v_mfma_f32_16x16x32_bf16 v[168:171], v[44:47], a[120:123], v[168:171]\n"
"ds_read_b64_tr_b16 v[36:37], v13 offset:57344\n"
"v_accvgpr_write_b32 a66, 0\n"
"v_mfma_f32_16x16x32_bf16 v[172:175], v[40:43], a[108:111], v[172:175]\n"
"v_accvgpr_write_b32 a67, 0\n"
"v_accvgpr_write_b32 a68, 0\n"
"v_mfma_f32_16x16x32_bf16 v[172:175], v[44:47], a[124:127], v[172:175]\n"
"ds_read_b64_tr_b16 v[38:39], v13 offset:58368\n"
"v_accvgpr_write_b32 a69, 0\n"
"v_mfma_f32_16x16x32_bf16 v[176:179], v[48:51], a[96:99], v[176:179]\n"
"buffer_load_dwordx4 a[52:55], v21, s[4:7], s59 offen offset:1024 sc1\n"
"v_xor_b32 v13, 0x10000, v13\n"
"v_mfma_f32_16x16x32_bf16 v[176:179], v[52:55], a[112:115], v[176:179]\n"
"ds_read_b64_tr_b16 v[40:41], v14 offset:49152\n"
"v_accvgpr_write_b32 a70, 0\n"
"v_mfma_f32_16x16x32_bf16 v[180:183], v[48:51], a[100:103], v[180:183]\n"
"v_accvgpr_write_b32 a71, 0\n"
"v_accvgpr_write_b32 a72, 0\n"
"v_mfma_f32_16x16x32_bf16 v[180:183], v[52:55], a[116:119], v[180:183]\n"
"ds_read_b64_tr_b16 v[42:43], v14 offset:50176\n"
"v_accvgpr_write_b32 a73, 0\n"
"v_mfma_f32_16x16x32_bf16 v[184:187], v[48:51], a[104:107], v[184:187]\n"
"buffer_load_dwordx4 a[56:59], v21, s[4:7], s59 offen offset:2048 sc1\n"
"v_accvgpr_write_b32 a74, 0\n"
"v_mfma_f32_16x16x32_bf16 v[184:187], v[52:55], a[120:123], v[184:187]\n"
"ds_read_b64_tr_b16 v[44:45], v14 offset:57344\n"
"v_accvgpr_write_b32 a75, 0\n"
"v_mfma_f32_16x16x32_bf16 v[188:191], v[48:51], a[108:111], v[188:191]\n"
"v_accvgpr_write_b32 a76, 0\n"
"v_accvgpr_write_b32 a77, 0\n"
"v_mfma_f32_16x16x32_bf16 v[188:191], v[52:55], a[124:127], v[188:191]\n"
"ds_read_b64_tr_b16 v[46:47], v14 offset:58368\n"
"s_waitcnt lgkmcnt(4)\n"
"v_mfma_f32_16x16x32_bf16 v[192:195], v[24:27], a[96:99], v[192:195]\n"
"buffer_load_dwordx4 a[60:63], v21, s[4:7], s59 offen offset:3072 sc1\n"
"v_xor_b32 v14, 0x10000, v14\n"
"v_mfma_f32_16x16x32_bf16 v[192:195], v[28:31], a[112:115], v[192:195]\n"
"ds_read_b64_tr_b16 v[48:49], v15 offset:49152\n"
"s_add_u32 s59, s59, 0x3000\n"
"v_mfma_f32_16x16x32_bf16 v[196:199], v[24:27], a[100:103], v[196:199]\n"
"s_cmp_eq_u32 s59, 0x28000\n"
"s_cmov_b32 s59, 0\n"
"v_mfma_f32_16x16x32_bf16 v[196:199], v[28:31], a[116:119], v[196:199]\n"
"ds_read_b64_tr_b16 v[50:51], v15 offset:50176\n"
"v_accvgpr_write_b32 a78, 0\n"
"v_mfma_f32_16x16x32_bf16 v[200:203], v[24:27], a[104:107], v[200:203]\n"
"s_waitcnt vmcnt(11)\n"
"v_cmp_ne_u32 vcc, s82, v11\n"
"s_nop 0\n"
"s_cbranch_vccnz .Ladaln_block_P_kva\n"
".Ladaln_block_P_kva_R:\n"
"v_mfma_f32_16x16x32_bf16 v[200:203], v[28:31], a[120:123], v[200:203]\n"
"ds_read_b64_tr_b16 v[52:53], v15 offset:57344\n"
"s_add_u32 s75, s75, 0xfffd8000\n"
"v_mfma_f32_16x16x32_bf16 v[204:207], v[24:27], a[108:111], v[204:207]\n"
"s_add_u32 s60, s33, 0x11000\n"
"v_min_u32 v20, 39, v0\n"
"v_mfma_f32_16x16x32_bf16 v[204:207], v[28:31], a[124:127], v[204:207]\n"
"ds_read_b64_tr_b16 v[54:55], v15 offset:58368\n"
"s_nop 0\n"
"v_mfma_f32_16x16x32_bf16 v[208:211], v[32:35], a[96:99], v[208:211]\n"
"v_xor_b32 v15, 0x10000, v15\n"
"s_barrier\n"
"v_mfma_f32_16x16x32_bf16 v[208:211], v[36:39], a[112:115], v[208:211]\n"
"ds_read_b64_tr_b16 v[24:25], v16 offset:16384\n"
"v_lshlrev_b32 v20, 7, v20\n"
"v_mfma_f32_16x16x32_bf16 v[212:215], v[32:35], a[100:103], v[212:215]\n"
"buffer_store_dwordx4 v[76:79], v1, s[4:7], s60 offen offset:1024 sc1\n"
"v_add_u32 v2, 0x10000, v1\n"
"v_mfma_f32_16x16x32_bf16 v[212:215], v[36:39], a[116:119], v[212:215]\n"
"ds_read_b64_tr_b16 v[26:27], v16 offset:17408\n"
"v_mov_b32 v3, v1\n"
"v_mfma_f32_16x16x32_bf16 v[216:219], v[32:35], a[104:107], v[216:219]\n"
"v_mov_b32 v8, 0xff800000\n"
"v_and_b32 v4, 3, v0\n"
"v_mfma_f32_16x16x32_bf16 v[216:219], v[36:39], a[120:123], v[216:219]\n"
"ds_read_b64_tr_b16 v[28:29], v16 offset:24576\n"
"v_min_u32 v4, 1, v4\n"
"v_mfma_f32_16x16x32_bf16 v[220:223], v[32:35], a[108:111], v[220:223]\n"
"v_subrev_u32 v4, 1, v4\n"
"v_and_b32 v4, 0x3f803f80, v4\n"
"v_mfma_f32_16x16x32_bf16 v[220:223], v[36:39], a[124:127], v[220:223]\n"
"ds_read_b64_tr_b16 v[30:31], v16 offset:25600\n"
"s_waitcnt lgkmcnt(4)\n"
"v_mfma_f32_16x16x32_bf16 v[224:227], v[40:43], a[96:99], v[224:227]\n"
"v_mov_b32 v5, v4\n"
"v_mov_b32 v6, v4\n"
"v_mfma_f32_16x16x32_bf16 v[224:227], v[44:47], a[112:115], v[224:227]\n"
"ds_read_b64_tr_b16 v[32:33], v17 offset:16384\n"
"v_mov_b32 v7, v4\n"
"v_mfma_f32_16x16x32_bf16 v[228:231], v[40:43], a[100:103], v[228:231]\n"
"s_lshl_b32 s74, s3, 10\n"
"s_add_u32 s74, s74, 0x4000\n"
"v_mfma_f32_16x16x32_bf16 v[228:231], v[44:47], a[116:119], v[228:231]\n"
"ds_read_b64_tr_b16 v[34:35], v17 offset:17408\n"
"s_add_u32 m0, s78, 0x10000\n"
"v_mfma_f32_16x16x32_bf16 v[232:235], v[40:43], a[104:107], v[232:235]\n"
"buffer_load_dwordx4 v1, s[4:7], s61 offen sc1 lds\n"
"v_add_u32 v11, s74, v1\n"
"v_mfma_f32_16x16x32_bf16 v[232:235], v[44:47], a[120:123], v[232:235]\n"
"ds_read_b64_tr_b16 v[36:37], v17 offset:24576\n"
"v_accvgpr_write_b32 a79, 0\n"
"v_mfma_f32_16x16x32_bf16 v[236:239], v[40:43], a[108:111], v[236:239]\n"
"v_accvgpr_write_b32 a80, 0\n"
"v_accvgpr_write_b32 a81, 0\n"
"v_mfma_f32_16x16x32_bf16 v[236:239], v[44:47], a[124:127], v[236:239]\n"
"ds_read_b64_tr_b16 v[38:39], v17 offset:25600\n"
"v_accvgpr_write_b32 a82, 0\n"
"v_mfma_f32_16x16x32_bf16 v[240:243], v[48:51], a[96:99], v[240:243]\n"
"buffer_load_dwordx4 v1, s[4:7], s61 offen offset:1024 sc1 lds\n"
"s_add_u32 s60, s33, 0x11000\n"
"v_mfma_f32_16x16x32_bf16 v[240:243], v[52:55], a[112:115], v[240:243]\n"
"ds_read_b64_tr_b16 v[40:41], v18 offset:16384\n"
"v_accvgpr_write_b32 a83, 0\n"
"v_mfma_f32_16x16x32_bf16 v[244:247], v[48:51], a[100:103], v[244:247]\n"
"v_accvgpr_write_b32 a84, 0\n"
"v_accvgpr_write_b32 a85, 0\n"
"v_mfma_f32_16x16x32_bf16 v[244:247], v[52:55], a[116:119], v[244:247]\n"
"ds_read_b64_tr_b16 v[42:43], v18 offset:17408\n"
"v_accvgpr_write_b32 a86, 0\n"
"v_mfma_f32_16x16x32_bf16 v[248:251], v[48:51], a[104:107], v[248:251]\n"
"buffer_load_dwordx4 v1, s[4:7], s61 offen offset:2048 sc1 lds\n"
"v_accvgpr_write_b32 a87, 0\n"
"v_mfma_f32_16x16x32_bf16 v[248:251], v[52:55], a[120:123], v[248:251]\n"
"ds_read_b64_tr_b16 v[44:45], v18 offset:24576\n"
"v_accvgpr_write_b32 a88, 0\n"
"v_mfma_f32_16x16x32_bf16 v[252:255], v[48:51], a[108:111], v[252:255]\n"
"v_accvgpr_write_b32 a89, 0\n"
"v_accvgpr_write_b32 a90, 0\n"
"v_mfma_f32_16x16x32_bf16 v[252:255], v[52:55], a[124:127], v[252:255]\n"
"ds_read_b64_tr_b16 v[46:47], v18 offset:25600\n"
"s_waitcnt lgkmcnt(4)\n"
"v_mfma_f32_16x16x32_bf16 v[128:131], v[24:27], a[0:3], v[128:131]\n"
"buffer_load_dwordx4 v1, s[4:7], s61 offen offset:3072 sc1 lds\n"
"s_add_u32 m0, s78, 0\n"
"v_mfma_f32_16x16x32_bf16 v[128:131], v[28:31], a[16:19], v[128:131]\n"
"ds_read_b64_tr_b16 v[48:49], v19 offset:16384\n"
"v_accvgpr_write_b32 a91, 0\n"
"v_mfma_f32_16x16x32_bf16 v[132:135], v[24:27], a[4:7], v[132:135]\n"
"v_accvgpr_write_b32 a92, 0\n"
"v_accvgpr_write_b32 a93, 0\n"
"v_mfma_f32_16x16x32_bf16 v[132:135], v[28:31], a[20:23], v[132:135]\n"
"ds_read_b64_tr_b16 v[50:51], v19 offset:17408\n"
"v_accvgpr_write_b32 a94, 0\n"
"v_mfma_f32_16x16x32_bf16 v[136:139], v[24:27], a[8:11], v[136:139]\n"
"buffer_load_dwordx4 v1, s[16:19], s61 offen sc1 lds\n"
"v_accvgpr_write_b32 a95, 0\n"
"v_mfma_f32_16x16x32_bf16 v[136:139], v[28:31], a[24:27], v[136:139]\n"
"ds_read_b64_tr_b16 v[52:53], v19 offset:24576\n"
"v_accvgpr_write_b32 a96, 0\n"
"v_mfma_f32_16x16x32_bf16 v[140:143], v[24:27], a[12:15], v[140:143]\n"
"v_accvgpr_write_b32 a97, 0\n"
"v_accvgpr_write_b32 a98, 0\n"
"v_mfma_f32_16x16x32_bf16 v[140:143], v[28:31], a[28:31], v[140:143]\n"
"ds_read_b64_tr_b16 v[54:55], v19 offset:25600\n"
"v_accvgpr_write_b32 a99, 0\n"
"v_mfma_f32_16x16x32_bf16 v[144:147], v[32:35], a[0:3], v[144:147]\n"
"buffer_load_dwordx4 v1, s[16:19], s61 offen offset:1024 sc1 lds\n"
"v_accvgpr_write_b32 a100, 0\n"
"v_mfma_f32_16x16x32_bf16 v[144:147], v[36:39], a[16:19], v[144:147]\n"
"ds_read_b64_tr_b16 v[24:25], v12 offset:16384\n"
"v_accvgpr_write_b32 a101, 0\n"
"v_mfma_f32_16x16x32_bf16 v[148:151], v[32:35], a[4:7], v[148:151]\n"
"v_accvgpr_write_b32 a102, 0\n"
"v_accvgpr_write_b32 a103, 0\n"
"v_mfma_f32_16x16x32_bf16 v[148:151], v[36:39], a[20:23], v[148:151]\n"
"ds_read_b64_tr_b16 v[26:27], v12 offset:17408\n"
"v_accvgpr_write_b32 a104, 0\n"
"v_mfma_f32_16x16x32_bf16 v[152:155], v[32:35], a[8:11], v[152:155]\n"
"buffer_load_dwordx4 v1, s[16:19], s61 offen offset:2048 sc1 lds\n"
"v_accvgpr_write_b32 a105, 0\n"
"v_mfma_f32_16x16x32_bf16 v[152:155], v[36:39], a[24:27], v[152:155]\n"
"ds_read_b64_tr_b16 v[28:29], v12 offset:24576\n"
"v_accvgpr_write_b32 a106, 0\n"
"v_mfma_f32_16x16x32_bf16 v[156:159], v[32:35], a[12:15], v[156:159]\n"
"v_accvgpr_write_b32 a107, 0\n"
"v_accvgpr_write_b32 a108, 0\n"
"v_mfma_f32_16x16x32_bf16 v[156:159], v[36:39], a[28:31], v[156:159]\n"
"ds_read_b64_tr_b16 v[30:31], v12 offset:25600\n"
"s_waitcnt lgkmcnt(4)\n"
"v_mfma_f32_16x16x32_bf16 v[160:163], v[40:43], a[0:3], v[160:163]\n"
"v_accvgpr_write_b32 a109, 0\n"
"v_accvgpr_write_b32 a110, 0\n"
"v_mfma_f32_16x16x32_bf16 v[160:163], v[44:47], a[16:19], v[160:163]\n"
"ds_read_b64_tr_b16 v[32:33], v13 offset:16384\n"
"v_accvgpr_write_b32 a111, 0\n"
"v_mfma_f32_16x16x32_bf16 v[164:167], v[40:43], a[4:7], v[164:167]\n"
"buffer_load_dwordx4 v1, s[16:19], s61 offen offset:3072 sc1 lds\n"
"v_accvgpr_write_b32 a112, 0\n"
"v_mfma_f32_16x16x32_bf16 v[164:167], v[44:47], a[20:23], v[164:167]\n"
"ds_read_b64_tr_b16 v[34:35], v13 offset:17408\n"
"v_accvgpr_write_b32 a113, 0\n"
"v_mfma_f32_16x16x32_bf16 v[168:171], v[40:43], a[8:11], v[168:171]\n"
"v_accvgpr_write_b32 a114, 0\n"
"v_accvgpr_write_b32 a115, 0\n"
"v_mfma_f32_16x16x32_bf16 v[168:171], v[44:47], a[24:27], v[168:171]\n"
"ds_read_b64_tr_b16 v[36:37], v13 offset:24576\n"
"v_accvgpr_write_b32 a116, 0\n"
"v_mfma_f32_16x16x32_bf16 v[172:175], v[40:43], a[12:15], v[172:175]\n"
"buffer_store_dwordx4 v[80:83], v1, s[4:7], s60 offen offset:2048 sc1\n"
"s_add_u32 s60, s33, 0x11000\n"
"v_mfma_f32_16x16x32_bf16 v[172:175], v[44:47], a[28:31], v[172:175]\n"
"ds_read_b64_tr_b16 v[38:39], v13 offset:25600\n"
"v_accvgpr_write_b32 a117, 0\n"
"v_mfma_f32_16x16x32_bf16 v[176:179], v[48:51], a[0:3], v[176:179]\n"
"v_accvgpr_write_b32 a118, 0\n"
"v_accvgpr_write_b32 a119, 0\n"
"v_mfma_f32_16x16x32_bf16 v[176:179], v[52:55], a[16:19], v[176:179]\n"
"ds_read_b64_tr_b16 v[40:41], v14 offset:16384\n"
"s_add_u32 m0, s78, 0x18000\n"
"v_mfma_f32_16x16x32_bf16 v[180:183], v[48:51], a[4:7], v[180:183]\n"
"buffer_load_dwordx4 v9, s[4:7], s61 offen sc1 lds\n"
"v_accvgpr_write_b32 a120, 0\n"
"v_mfma_f32_16x16x32_bf16 v[180:183], v[52:55], a[20:23], v[180:183]\n"
"ds_read_b64_tr_b16 v[42:43], v14 offset:17408\n"
"v_accvgpr_write_b32 a121, 0\n"
"v_mfma_f32_16x16x32_bf16 v[184:187], v[48:51], a[8:11], v[184:187]\n"
"v_accvgpr_write_b32 a122, 0\n"
"v_accvgpr_write_b32 a123, 0\n"
"v_mfma_f32_16x16x32_bf16 v[184:187], v[52:55], a[24:27], v[184:187]\n"
"ds_read_b64_tr_b16 v[44:45], v14 offset:24576\n"
"v_accvgpr_write_b32 a124, 0\n"
"v_mfma_f32_16x16x32_bf16 v[188:191], v[48:51], a[12:15], v[188:191]\n"
"v_accvgpr_write_b32 a125, 0\n"
"v_accvgpr_write_b32 a126, 0\n"
"v_mfma_f32_16x16x32_bf16 v[188:191], v[52:55], a[28:31], v[188:191]\n"
"ds_read_b64_tr_b16 v[46:47], v14 offset:25600\n"
"s_waitcnt lgkmcnt(4)\n"
"v_mfma_f32_16x16x32_bf16 v[192:195], v[24:27], a[0:3], v[192:195]\n"
"buffer_load_dwordx4 v9, s[4:7], s61 offen offset:1024 sc1 lds\n"
"v_accvgpr_write_b32 a127, 0\n"
"v_mfma_f32_16x16x32_bf16 v[192:195], v[28:31], a[16:19], v[192:195]\n"
"ds_read_b64_tr_b16 v[48:49], v15 offset:16384\n"
"v_mfma_f32_16x16x32_bf16 v[196:199], v[24:27], a[4:7], v[196:199]\n"
"v_mfma_f32_16x16x32_bf16 v[196:199], v[28:31], a[20:23], v[196:199]\n"
"ds_read_b64_tr_b16 v[50:51], v15 offset:17408\n"
"v_mfma_f32_16x16x32_bf16 v[200:203], v[24:27], a[8:11], v[200:203]\n"
"v_mfma_f32_16x16x32_bf16 v[200:203], v[28:31], a[24:27], v[200:203]\n"
"ds_read_b64_tr_b16 v[52:53], v15 offset:24576\n"
"v_mfma_f32_16x16x32_bf16 v[204:207], v[24:27], a[12:15], v[204:207]\n"
"buffer_load_dwordx4 v9, s[4:7], s61 offen offset:2048 sc1 lds\n"
"v_mfma_f32_16x16x32_bf16 v[204:207], v[28:31], a[28:31], v[204:207]\n"
"ds_read_b64_tr_b16 v[54:55], v15 offset:25600\n"
"s_nop 0\n"
"v_mfma_f32_16x16x32_bf16 v[208:211], v[32:35], a[0:3], v[208:211]\n"
"s_barrier\n"
"v_mfma_f32_16x16x32_bf16 v[208:211], v[36:39], a[16:19], v[208:211]\n"
"ds_read_b64_tr_b16 v[24:25], v16 offset:49152\n"
"v_mfma_f32_16x16x32_bf16 v[212:215], v[32:35], a[4:7], v[212:215]\n"
"v_mfma_f32_16x16x32_bf16 v[212:215], v[36:39], a[20:23], v[212:215]\n"
"ds_read_b64_tr_b16 v[26:27], v16 offset:50176\n"
"v_mfma_f32_16x16x32_bf16 v[216:219], v[32:35], a[8:11], v[216:219]\n"
"buffer_load_dwordx4 v9, s[4:7], s61 offen offset:3072 sc1 lds\n"
"v_mfma_f32_16x16x32_bf16 v[216:219], v[36:39], a[24:27], v[216:219]\n"
"ds_read_b64_tr_b16 v[28:29], v16 offset:57344\n"
"v_mfma_f32_16x16x32_bf16 v[220:223], v[32:35], a[12:15], v[220:223]\n"
"v_mfma_f32_16x16x32_bf16 v[220:223], v[36:39], a[28:31], v[220:223]\n"
"ds_read_b64_tr_b16 v[30:31], v16 offset:58368\n"
"s_waitcnt lgkmcnt(4)\n"
"v_mfma_f32_16x16x32_bf16 v[224:227], v[40:43], a[0:3], v[224:227]\n"
"buffer_store_dwordx4 v[84:87], v1, s[4:7], s60 offen offset:3072 sc1\n"
"v_mfma_f32_16x16x32_bf16 v[224:227], v[44:47], a[16:19], v[224:227]\n"
"ds_read_b64_tr_b16 v[32:33], v17 offset:49152\n"
"v_mfma_f32_16x16x32_bf16 v[228:231], v[40:43], a[4:7], v[228:231]\n"
"v_mfma_f32_16x16x32_bf16 v[228:231], v[44:47], a[20:23], v[228:231]\n"
"ds_read_b64_tr_b16 v[34:35], v17 offset:50176\n"
"s_add_u32 m0, s78, 0x8000\n"
"v_mfma_f32_16x16x32_bf16 v[232:235], v[40:43], a[8:11], v[232:235]\n"
"buffer_load_dwordx4 v9, s[16:19], s61 offen sc1 lds\n"
"v_mfma_f32_16x16x32_bf16 v[232:235], v[44:47], a[24:27], v[232:235]\n"
"ds_read_b64_tr_b16 v[36:37], v17 offset:57344\n"
"v_mfma_f32_16x16x32_bf16 v[236:239], v[40:43], a[12:15], v[236:239]\n"
"v_mfma_f32_16x16x32_bf16 v[236:239], v[44:47], a[28:31], v[236:239]\n"
"ds_read_b64_tr_b16 v[38:39], v17 offset:58368\n"
"v_mfma_f32_16x16x32_bf16 v[240:243], v[48:51], a[0:3], v[240:243]\n"
"v_mfma_f32_16x16x32_bf16 v[240:243], v[52:55], a[16:19], v[240:243]\n"
"ds_read_b64_tr_b16 v[40:41], v18 offset:49152\n"
"v_mfma_f32_16x16x32_bf16 v[244:247], v[48:51], a[4:7], v[244:247]\n"
"buffer_load_dwordx4 v9, s[16:19], s61 offen offset:1024 sc1 lds\n"
"v_mfma_f32_16x16x32_bf16 v[244:247], v[52:55], a[20:23], v[244:247]\n"
"ds_read_b64_tr_b16 v[42:43], v18 offset:50176\n"
"v_mfma_f32_16x16x32_bf16 v[248:251], v[48:51], a[8:11], v[248:251]\n"
"v_mfma_f32_16x16x32_bf16 v[248:251], v[52:55], a[24:27], v[248:251]\n"
"ds_read_b64_tr_b16 v[44:45], v18 offset:57344\n"
"v_mfma_f32_16x16x32_bf16 v[252:255], v[48:51], a[12:15], v[252:255]\n"
"v_mfma_f32_16x16x32_bf16 v[252:255], v[52:55], a[28:31], v[252:255]\n"
"ds_read_b64_tr_b16 v[46:47], v18 offset:58368\n"
"s_waitcnt vmcnt(17) lgkmcnt(4)\n"
"v_mfma_f32_16x16x32_bf16 v[128:131], v[24:27], a[32:35], v[128:131]\n"
"buffer_load_dwordx4 v9, s[16:19], s61 offen offset:2048 sc1 lds\n"
"v_mfma_f32_16x16x32_bf16 v[128:131], v[28:31], a[48:51], v[128:131]\n"
"ds_read_b64_tr_b16 v[48:49], v19 offset:49152\n"
"v_mfma_f32_16x16x32_bf16 v[132:135], v[24:27], a[36:39], v[132:135]\n"
"v_mfma_f32_16x16x32_bf16 v[132:135], v[28:31], a[52:55], v[132:135]\n"
"ds_read_b64_tr_b16 v[50:51], v19 offset:50176\n"
"v_mfma_f32_16x16x32_bf16 v[136:139], v[24:27], a[40:43], v[136:139]\n"
"v_mfma_f32_16x16x32_bf16 v[136:139], v[28:31], a[56:59], v[136:139]\n"
"ds_read_b64_tr_b16 v[52:53], v19 offset:57344\n"
"v_mfma_f32_16x16x32_bf16 v[140:143], v[24:27], a[44:47], v[140:143]\n"
"buffer_load_dwordx4 v9, s[16:19], s61 offen offset:3072 sc1 lds\n"
"v_mfma_f32_16x16x32_bf16 v[140:143], v[28:31], a[60:63], v[140:143]\n"
"ds_read_b64_tr_b16 v[54:55], v19 offset:58368\n"
"v_mfma_f32_16x16x32_bf16 v[144:147], v[32:35], a[32:35], v[144:147]\n"
"v_mfma_f32_16x16x32_bf16 v[144:147], v[36:39], a[48:51], v[144:147]\n"
"ds_read_b64_tr_b16 v[24:25], v12 offset:49152\n"
"v_mfma_f32_16x16x32_bf16 v[148:151], v[32:35], a[36:39], v[148:151]\n"
"v_mfma_f32_16x16x32_bf16 v[148:151], v[36:39], a[52:55], v[148:151]\n"
"ds_read_b64_tr_b16 v[26:27], v12 offset:50176\n"
"v_mfma_f32_16x16x32_bf16 v[152:155], v[32:35], a[40:43], v[152:155]\n"
"v_mfma_f32_16x16x32_bf16 v[152:155], v[36:39], a[56:59], v[152:155]\n"
"ds_read_b64_tr_b16 v[28:29], v12 offset:57344\n"
"v_mfma_f32_16x16x32_bf16 v[156:159], v[32:35], a[44:47], v[156:159]\n"
"v_cvt_pk_bf16_f32 v56, v128, v129\n"
"v_cvt_pk_bf16_f32 v57, v130, v131\n"
"v_mfma_f32_16x16x32_bf16 v[156:159], v[36:39], a[60:63], v[156:159]\n"
"ds_read_b64_tr_b16 v[30:31], v12 offset:58368\n"
"s_waitcnt lgkmcnt(4)\n"
"v_mfma_f32_16x16x32_bf16 v[160:163], v[40:43], a[32:35], v[160:163]\n"
"v_cvt_pk_bf16_f32 v58, v144, v145\n"
"v_cvt_pk_bf16_f32 v59, v146, v147\n"
"v_mfma_f32_16x16x32_bf16 v[160:163], v[44:47], a[48:51], v[160:163]\n"
"ds_read_b64_tr_b16 v[32:33], v13 offset:49152\n"
"v_cvt_pk_bf16_f32 v72, v132, v133\n"
"v_mfma_f32_16x16x32_bf16 v[164:167], v[40:43], a[36:39], v[164:167]\n"
"v_cvt_pk_bf16_f32 v73, v134, v135\n"
"v_cvt_pk_bf16_f32 v74, v148, v149\n"
"v_mfma_f32_16x16x32_bf16 v[164:167], v[44:47], a[52:55], v[164:167]\n"
"ds_read_b64_tr_b16 v[34:35], v13 offset:50176\n"
"v_cvt_pk_bf16_f32 v75, v150, v151\n"
"v_mfma_f32_16x16x32_bf16 v[168:171], v[40:43], a[40:43], v[168:171]\n"
"v_cvt_pk_bf16_f32 v88, v136, v137\n"
"v_cvt_pk_bf16_f32 v89, v138, v139\n"
"v_mfma_f32_16x16x32_bf16 v[168:171], v[44:47], a[56:59], v[168:171]\n"
"ds_read_b64_tr_b16 v[36:37], v13 offset:57344\n"
"v_cvt_pk_bf16_f32 v90, v152, v153\n"
"v_mfma_f32_16x16x32_bf16 v[172:175], v[40:43], a[44:47], v[172:175]\n"
"v_cvt_pk_bf16_f32 v91, v154, v155\n"
"v_cvt_pk_bf16_f32 v104, v140, v141\n"
"v_mfma_f32_16x16x32_bf16 v[172:175], v[44:47], a[60:63], v[172:175]\n"
"ds_read_b64_tr_b16 v[38:39], v13 offset:58368\n"
"s_waitcnt vmcnt(14)\n"
"v_mfma_f32_16x16x32_bf16 v[176:179], v[48:51], a[32:35], v[176:179]\n"
"s_barrier\n"
"v_cvt_pk_bf16_f32 v105, v142, v143\n"
"v_mfma_f32_16x16x32_bf16 v[176:179], v[52:55], a[48:51], v[176:179]\n"
"ds_read_b64_tr_b16 v[40:41], v14 offset:49152\n"
"v_cvt_pk_bf16_f32 v106, v156, v157\n"
"v_mfma_f32_16x16x32_bf16 v[180:183], v[48:51], a[36:39], v[180:183]\n"
"buffer_store_dwordx4 v[56:59], v1, s[4:7], s46 offen sc1\n"
"v_cvt_pk_bf16_f32 v107, v158, v159\n"
"v_mfma_f32_16x16x32_bf16 v[180:183], v[52:55], a[52:55], v[180:183]\n"
"ds_read_b64_tr_b16 v[42:43], v14 offset:50176\n"
"ds_read_b128 a[144:147], v2\n"
"v_mfma_f32_16x16x32_bf16 v[184:187], v[48:51], a[40:43], v[184:187]\n"
"ds_read_b128 a[148:151], v2 offset:1024\n"
"ds_read_b128 a[152:155], v2 offset:2048\n"
"v_mfma_f32_16x16x32_bf16 v[184:187], v[52:55], a[56:59], v[184:187]\n"
"ds_read_b64_tr_b16 v[44:45], v14 offset:57344\n"
"buffer_store_dwordx4 v[72:75], v1, s[4:7], s46 offen offset:1024 sc1\n"
"v_mfma_f32_16x16x32_bf16 v[188:191], v[48:51], a[44:47], v[188:191]\n"
"v_cvt_pk_bf16_f32 v60, v160, v161\n"
"v_cvt_pk_bf16_f32 v61, v162, v163\n"
"v_mfma_f32_16x16x32_bf16 v[188:191], v[52:55], a[60:63], v[188:191]\n"
"ds_read_b64_tr_b16 v[46:47], v14 offset:58368\n"
"s_waitcnt lgkmcnt(7)\n"
"v_mfma_f32_16x16x32_bf16 v[192:195], v[24:27], a[32:35], v[192:195]\n"
"v_cvt_pk_bf16_f32 v62, v176, v177\n"
"v_cvt_pk_bf16_f32 v63, v178, v179\n"
"v_mfma_f32_16x16x32_bf16 v[192:195], v[28:31], a[48:51], v[192:195]\n"
"ds_read_b64_tr_b16 v[48:49], v15 offset:49152\n"
"v_cvt_pk_bf16_f32 v76, v164, v165\n"
"v_mfma_f32_16x16x32_bf16 v[196:199], v[24:27], a[36:39], v[196:199]\n"
"v_cvt_pk_bf16_f32 v77, v166, v167\n"
"v_cvt_pk_bf16_f32 v78, v180, v181\n"
"v_mfma_f32_16x16x32_bf16 v[196:199], v[28:31], a[52:55], v[196:199]\n"
"ds_read_b64_tr_b16 v[50:51], v15 offset:50176\n"
"v_cvt_pk_bf16_f32 v79, v182, v183\n"
"v_mfma_f32_16x16x32_bf16 v[200:203], v[24:27], a[40:43], v[200:203]\n"
"v_cvt_pk_bf16_f32 v92, v168, v169\n"
"v_cvt_pk_bf16_f32 v93, v170, v171\n"
"v_mfma_f32_16x16x32_bf16 v[200:203], v[28:31], a[56:59], v[200:203]\n"
"ds_read_b64_tr_b16 v[52:53], v15 offset:57344\n"
"v_cvt_pk_bf16_f32 v94, v184, v185\n"
"v_mfma_f32_16x16x32_bf16 v[204:207], v[24:27], a[44:47], v[204:207]\n"
"v_cvt_pk_bf16_f32 v95, v186, v187\n"
"v_cvt_pk_bf16_f32 v108, v172, v173\n"
"v_mfma_f32_16x16x32_bf16 v[204:207], v[28:31], a[60:63], v[204:207]\n"
"ds_read_b64_tr_b16 v[54:55], v15 offset:58368\n"
"v_cvt_pk_bf16_f32 v109, v174, v175\n"
"v_mfma_f32_16x16x32_bf16 v[208:211], v[32:35], a[32:35], v[208:211]\n"
"v_cvt_pk_bf16_f32 v110, v188, v189\n"
"v_cvt_pk_bf16_f32 v111, v190, v191\n"
"v_mfma_f32_16x16x32_bf16 v[208:211], v[36:39], a[48:51], v[208:211]\n"
"buffer_store_dwordx4 v[88:91], v1, s[4:7], s46 offen offset:2048 sc1\n"
"ds_read_b128 a[156:159], v2 offset:3072\n"
"v_mfma_f32_16x16x32_bf16 v[212:215], v[32:35], a[36:39], v[212:215]\n"
"ds_read_b128 a[160:163], v2 offset:4096\n"
"ds_read_b128 a[164:167], v2 offset:5120\n"
"v_mfma_f32_16x16x32_bf16 v[212:215], v[36:39], a[52:55], v[212:215]\n"
"ds_read_b128 a[168:171], v2 offset:6144\n"
"ds_read_b128 a[172:175], v2 offset:7168\n"
"v_mfma_f32_16x16x32_bf16 v[216:219], v[32:35], a[40:43], v[216:219]\n"
"buffer_store_dwordx4 v[104:107], v1, s[4:7], s46 offen offset:3072 sc1\n"
"s_add_u32 s46, s46, 0x1000\n"
"v_mfma_f32_16x16x32_bf16 v[216:219], v[36:39], a[56:59], v[216:219]\n"
"ds_read_b128 a[176:179], v2 offset:8192\n"
"ds_read_b128 a[180:183], v2 offset:9216\n"
"v_mfma_f32_16x16x32_bf16 v[220:223], v[32:35], a[44:47], v[220:223]\n"
"v_cvt_pk_bf16_f32 v64, v192, v193\n"
"v_cvt_pk_bf16_f32 v65, v194, v195\n"
"v_mfma_f32_16x16x32_bf16 v[220:223], v[36:39], a[60:63], v[220:223]\n"
"s_waitcnt lgkmcnt(7)\n"
"s_waitcnt vmcnt(8)\n"
"v_mfma_f32_16x16x32_bf16 v[224:227], v[40:43], a[32:35], v[224:227]\n"
"s_barrier\n"
"v_cvt_pk_bf16_f32 v66, v208, v209\n"
"v_mfma_f32_16x16x32_bf16 v[224:227], v[44:47], a[48:51], v[224:227]\n"
"buffer_store_dword v22, v23, s[4:7], s69 offen sc1\n"
"v_cvt_pk_bf16_f32 v67, v210, v211\n"
"v_mfma_f32_16x16x32_bf16 v[228:231], v[40:43], a[36:39], v[228:231]\n"
"v_cvt_pk_bf16_f32 v80, v196, v197\n"
"v_cvt_pk_bf16_f32 v81, v198, v199\n"
"v_mfma_f32_16x16x32_bf16 v[228:231], v[44:47], a[52:55], v[228:231]\n"
"v_cvt_pk_bf16_f32 v82, v212, v213\n"
"v_cvt_pk_bf16_f32 v83, v214, v215\n"
"v_mfma_f32_16x16x32_bf16 v[232:235], v[40:43], a[40:43], v[232:235]\n"
"v_cvt_pk_bf16_f32 v96, v200, v201\n"
"v_cvt_pk_bf16_f32 v97, v202, v203\n"
"v_mfma_f32_16x16x32_bf16 v[232:235], v[44:47], a[56:59], v[232:235]\n"
"v_cvt_pk_bf16_f32 v98, v216, v217\n"
"v_cvt_pk_bf16_f32 v99, v218, v219\n"
"v_mfma_f32_16x16x32_bf16 v[236:239], v[40:43], a[44:47], v[236:239]\n"
"v_cvt_pk_bf16_f32 v112, v204, v205\n"
"v_cvt_pk_bf16_f32 v113, v206, v207\n"
"v_mfma_f32_16x16x32_bf16 v[236:239], v[44:47], a[60:63], v[236:239]\n"
"v_cvt_pk_bf16_f32 v114, v220, v221\n"
"v_cvt_pk_bf16_f32 v115, v222, v223\n"
"v_mfma_f32_16x16x32_bf16 v[240:243], v[48:51], a[32:35], v[240:243]\n"
"ds_read_b128 a[184:187], v2 offset:10240\n"
"ds_read_b128 a[188:191], v2 offset:11264\n"
"v_mfma_f32_16x16x32_bf16 v[240:243], v[52:55], a[48:51], v[240:243]\n"
"ds_read_b128 a[192:195], v2 offset:12288\n"
"ds_read_b128 a[196:199], v2 offset:13312\n"
"v_mfma_f32_16x16x32_bf16 v[244:247], v[48:51], a[36:39], v[244:247]\n"
"ds_read_b128 a[200:203], v2 offset:14336\n"
"ds_read_b128 a[204:207], v2 offset:15360\n"
"v_mfma_f32_16x16x32_bf16 v[244:247], v[52:55], a[52:55], v[244:247]\n"
"v_mfma_f32_16x16x32_bf16 v[248:251], v[48:51], a[40:43], v[248:251]\n"
"v_mfma_f32_16x16x32_bf16 v[248:251], v[52:55], a[56:59], v[248:251]\n"
"v_mfma_f32_16x16x32_bf16 v[252:255], v[48:51], a[44:47], v[252:255]\n"
"v_mfma_f32_16x16x32_bf16 v[252:255], v[52:55], a[60:63], v[252:255]\n"
"s_lshl2_add_u32 s74, s49, s3\n"
"s_cmp_eq_u32 s74, 2\n"
"s_cbranch_scc1 .Ladaln_block_A_FIRSTD\n"
"s_nop 1\n"
"s_waitcnt lgkmcnt(0)\n"
"v_cvt_pk_bf16_f32 v68, v224, v225\n"
"v_cvt_pk_bf16_f32 v69, v226, v227\n"
"v_cvt_pk_bf16_f32 v70, v240, v241\n"
"v_cvt_pk_bf16_f32 v71, v242, v243\n"
"v_cvt_pk_bf16_f32 v84, v228, v229\n"
"v_cvt_pk_bf16_f32 v85, v230, v231\n"
"v_cvt_pk_bf16_f32 v86, v244, v245\n"
"v_cvt_pk_bf16_f32 v87, v246, v247\n"
"v_mfma_f32_16x16x32_bf16 v[128:131], a[144:147], v[56:59], 0\n"
"v_cvt_pk_bf16_f32 v100, v232, v233\n"
"v_mfma_f32_16x16x32_bf16 v[128:131], a[148:151], v[60:63], v[128:131]\n"
"v_cvt_pk_bf16_f32 v101, v234, v235\n"
"v_mfma_f32_16x16x32_bf16 v[128:131], a[152:155], v[64:67], v[128:131]\n"
"v_cvt_pk_bf16_f32 v102, v248, v249\n"
"v_mfma_f32_16x16x32_bf16 v[128:131], a[156:159], v[68:71], v[128:131]\n"
"v_cvt_pk_bf16_f32 v103, v250, v251\n"
"v_mfma_f32_16x16x32_bf16 v[132:135], a[144:147], v[72:75], 0\n"
"v_cvt_pk_bf16_f32 v116, v236, v237\n"
"v_mfma_f32_16x16x32_bf16 v[132:135], a[148:151], v[76:79], v[132:135]\n"
"v_cvt_pk_bf16_f32 v117, v238, v239\n"
"v_mfma_f32_16x16x32_bf16 v[132:135], a[152:155], v[80:83], v[132:135]\n"
"v_cvt_pk_bf16_f32 v118, v252, v253\n"
"v_mfma_f32_16x16x32_bf16 v[132:135], a[156:159], v[84:87], v[132:135]\n"
"v_cvt_pk_bf16_f32 v119, v254, v255\n"
"v_mfma_f32_16x16x32_bf16 v[136:139], a[144:147], v[88:91], 0\n"
"v_mfma_f32_16x16x32_bf16 v[136:139], a[148:151], v[92:95], v[136:139]\n"
"v_mfma_f32_16x16x32_bf16 v[136:139], a[152:155], v[96:99], v[136:139]\n"
"v_mfma_f32_16x16x32_bf16 v[136:139], a[156:159], v[100:103], v[136:139]\n"
"v_mfma_f32_16x16x32_bf16 v[140:143], a[144:147], v[104:107], 0\n"
"v_max3_f32 v120, v128, v129, v130\n"
"v_mfma_f32_16x16x32_bf16 v[140:143], a[148:151], v[108:111], v[140:143]\n"
"v_max_f32 v124, v120, v131\n"
"v_mfma_f32_16x16x32_bf16 v[140:143], a[152:155], v[112:115], v[140:143]\n"
"v_max_f32 v120, v120, v131\n"
"v_mfma_f32_16x16x32_bf16 v[140:143], a[156:159], v[116:119], v[140:143]\n"
"v_max3_f32 v121, v132, v133, v134\n"
"v_max_f32 v125, v121, v135\n"
"v_max_f32 v121, v121, v135\n"
"v_max3_f32 v122, v136, v137, v138\n"
"v_max_f32 v126, v122, v139\n"
"v_max_f32 v122, v122, v139\n"
"s_nop 1\n"
"v_max3_f32 v123, v140, v141, v142\n"
"v_max_f32 v127, v123, v143\n"
"v_max_f32 v123, v123, v143\n"
"v_permlane16_swap_b32 v120, v124\n"
"v_permlane16_swap_b32 v121, v125\n"
"v_permlane16_swap_b32 v122, v126\n"
"v_permlane16_swap_b32 v123, v127\n"
"v_max_f32 v120, v120, v124\n"
"v_max_f32 v121, v121, v125\n"
"v_max_f32 v122, v122, v126\n"
"v_max_f32 v123, v123, v127\n"
"v_max_f32 v124, v120, v124\n"
"v_max_f32 v125, v121, v125\n"
"v_max_f32 v126, v122, v126\n"
"v_max_f32 v127, v123, v127\n"
"v_permlane32_swap_b32 v120, v124\n"
"v_permlane32_swap_b32 v121, v125\n"
"v_permlane32_swap_b32 v122, v126\n"
"v_permlane32_swap_b32 v123, v127\n"
"v_max_f32 v120, v120, v124\n"
"v_max_f32 v121, v121, v125\n"
"v_max_f32 v122, v122, v126\n"
"v_max_f32 v123, v123, v127\n"
"v_xor_b32 v224, 0x80000000, v120\n"
"v_xor_b32 v225, 0x80000000, v120\n"
"v_xor_b32 v226, 0x80000000, v120\n"
"v_xor_b32 v227, 0x80000000, v120\n"
"v_xor_b32 v228, 0x80000000, v121\n"
"v_xor_b32 v229, 0x80000000, v121\n"
"v_xor_b32 v230, 0x80000000, v121\n"
"v_xor_b32 v231, 0x80000000, v121\n"
"v_mfma_f32_16x16x32_bf16 v[128:131], a[144:147], v[56:59], v[224:227]\n"
"ds_write_b128 v11, v[120:123]\n"
"v_xor_b32 v11, 0xc00, v11\n"
"v_mfma_f32_16x16x32_bf16 v[128:131], a[148:151], v[60:63], v[128:131]\n"
"v_xor_b32 v232, 0x80000000, v122\n"
"v_xor_b32 v233, 0x80000000, v122\n"
"v_xor_b32 v234, 0x80000000, v122\n"
"v_mfma_f32_16x16x32_bf16 v[128:131], a[152:155], v[64:67], v[128:131]\n"
"v_xor_b32 v235, 0x80000000, v122\n"
"v_xor_b32 v236, 0x80000000, v123\n"
"v_xor_b32 v237, 0x80000000, v123\n"
"v_mfma_f32_16x16x32_bf16 v[128:131], a[156:159], v[68:71], v[128:131]\n"
"v_xor_b32 v238, 0x80000000, v123\n"
"v_xor_b32 v239, 0x80000000, v123\n"
"s_add_u32 s61, s61, 0x8000\n"
"v_mfma_f32_16x16x32_bf16 v[132:135], a[144:147], v[72:75], v[228:231]\n"
"s_cmp_eq_u32 s49, 0\n"
"s_cselect_b32 s74, s64, 0x8000\n"
"s_add_u32 s61, s61, s74\n"
"v_mfma_f32_16x16x32_bf16 v[132:135], a[148:151], v[76:79], v[132:135]\n"
"s_mov_b32 s60, s61\n"
"s_xor_b32 s62, s62, 0x8000\n"
"s_xor_b32 s63, s63, 0x8000\n"
"v_mfma_f32_16x16x32_bf16 v[132:135], a[152:155], v[80:83], v[132:135]\n"
"v_and_b32 v19, 7, v0\n"
"v_lshlrev_b32 v19, 7, v19\n"
"v_mfma_f32_16x16x32_bf16 v[132:135], a[156:159], v[84:87], v[132:135]\n"
"s_waitcnt lgkmcnt(0)\n"
"v_mfma_f32_16x16x32_bf16 v[144:147], a[160:163], v[56:59], v[224:227]\n"
"s_barrier\n"
"v_exp_f32 v128, v128\n"
"v_mfma_f32_16x16x32_bf16 v[144:147], a[164:167], v[60:63], v[144:147]\n"
"ds_read_b128 v[40:43], v11\n"
"v_exp_f32 v129, v129\n"
"v_mfma_f32_16x16x32_bf16 v[144:147], a[168:171], v[64:67], v[144:147]\n"
"ds_read_b128 a[208:211], v3\n"
"v_cvt_pk_bf16_f32 v192, v128, v129\n"
"v_mfma_f32_16x16x32_bf16 v[144:147], a[172:175], v[68:71], v[144:147]\n"
"v_exp_f32 v130, v130\n"
"v_mfma_f32_16x16x32_bf16 v[148:151], a[160:163], v[72:75], v[228:231]\n"
"ds_read_b128 a[212:215], v3 offset:1024\n"
"v_exp_f32 v131, v131\n"
"v_mfma_f32_16x16x32_bf16 v[148:151], a[164:167], v[76:79], v[148:151]\n"
"v_cvt_pk_bf16_f32 v193, v130, v131\n"
"v_exp_f32 v132, v132\n"
"v_mfma_f32_16x16x32_bf16 v[148:151], a[168:171], v[80:83], v[148:151]\n"
"ds_read_b128 a[216:219], v3 offset:2048\n"
"v_exp_f32 v133, v133\n"
"v_mfma_f32_16x16x32_bf16 v[148:151], a[172:175], v[84:87], v[148:151]\n"
"v_cvt_pk_bf16_f32 v200, v132, v133\n"
"v_exp_f32 v134, v134\n"
"v_mfma_f32_16x16x32_bf16 v[160:163], a[176:179], v[56:59], v[224:227]\n"
"ds_read_b128 a[220:223], v3 offset:3072\n"
"v_exp_f32 v135, v135\n"
"v_mfma_f32_16x16x32_bf16 v[160:163], a[180:183], v[60:63], v[160:163]\n"
"v_cvt_pk_bf16_f32 v201, v134, v135\n"
"v_exp_f32 v144, v144\n"
"v_mfma_f32_16x16x32_bf16 v[160:163], a[184:187], v[64:67], v[160:163]\n"
"ds_read_b128 a[224:227], v3 offset:4096\n"
"v_exp_f32 v145, v145\n"
"v_mfma_f32_16x16x32_bf16 v[160:163], a[188:191], v[68:71], v[160:163]\n"
"v_cvt_pk_bf16_f32 v194, v144, v145\n"
"v_exp_f32 v146, v146\n"
"v_mfma_f32_16x16x32_bf16 v[164:167], a[176:179], v[72:75], v[228:231]\n"
"ds_read_b128 a[228:231], v3 offset:5120\n"
"v_exp_f32 v147, v147\n"
"v_mfma_f32_16x16x32_bf16 v[164:167], a[180:183], v[76:79], v[164:167]\n"
"v_cvt_pk_bf16_f32 v195, v146, v147\n"
"v_exp_f32 v148, v148\n"
"v_mfma_f32_16x16x32_bf16 v[164:167], a[184:187], v[80:83], v[164:167]\n"
"ds_read_b128 a[232:235], v3 offset:6144\n"
"v_exp_f32 v149, v149\n"
"v_mfma_f32_16x16x32_bf16 v[164:167], a[188:191], v[84:87], v[164:167]\n"
"v_cvt_pk_bf16_f32 v202, v148, v149\n"
"v_exp_f32 v150, v150\n"
"v_mfma_f32_16x16x32_bf16 v[176:179], a[192:195], v[56:59], v[224:227]\n"
"ds_read_b128 a[236:239], v3 offset:7168\n"
"v_exp_f32 v151, v151\n"
"v_mfma_f32_16x16x32_bf16 v[176:179], a[196:199], v[60:63], v[176:179]\n"
"v_cvt_pk_bf16_f32 v203, v150, v151\n"
"v_exp_f32 v160, v160\n"
"v_mfma_f32_16x16x32_bf16 v[176:179], a[200:203], v[64:67], v[176:179]\n"
"ds_read_b128 a[240:243], v3 offset:8192\n"
"v_exp_f32 v161, v161\n"
"v_mfma_f32_16x16x32_bf16 v[176:179], a[204:207], v[68:71], v[176:179]\n"
"v_cvt_pk_bf16_f32 v196, v160, v161\n"
"v_exp_f32 v162, v162\n"
"v_mfma_f32_16x16x32_bf16 v[180:183], a[192:195], v[72:75], v[228:231]\n"
"ds_read_b128 a[244:247], v3 offset:9216\n"
"v_exp_f32 v163, v163\n"
"v_mfma_f32_16x16x32_bf16 v[180:183], a[196:199], v[76:79], v[180:183]\n"
"v_cvt_pk_bf16_f32 v197, v162, v163\n"
"v_exp_f32 v164, v164\n"
"v_mfma_f32_16x16x32_bf16 v[180:183], a[200:203], v[80:83], v[180:183]\n"
"ds_read_b128 a[248:251], v3 offset:10240\n"
"v_exp_f32 v165, v165\n"
"v_mfma_f32_16x16x32_bf16 v[180:183], a[204:207], v[84:87], v[180:183]\n"
"v_cvt_pk_bf16_f32 v204, v164, v165\n"
"v_exp_f32 v166, v166\n"
"v_mfma_f32_16x16x32_bf16 v[136:139], a[144:147], v[88:91], v[232:235]\n"
"ds_read_b128 a[252:255], v3 offset:11264\n"
"v_exp_f32 v167, v167\n"
"v_mfma_f32_16x16x32_bf16 v[136:139], a[148:151], v[92:95], v[136:139]\n"
"v_cvt_pk_bf16_f32 v205, v166, v167\n"
"v_exp_f32 v176, v176\n"
"v_mfma_f32_16x16x32_bf16 v[136:139], a[152:155], v[96:99], v[136:139]\n"
"ds_read_b128 v[24:27], v3 offset:12288\n"
"v_exp_f32 v177, v177\n"
"v_mfma_f32_16x16x32_bf16 v[136:139], a[156:159], v[100:103], v[136:139]\n"
"v_cvt_pk_bf16_f32 v198, v176, v177\n"
"v_exp_f32 v178, v178\n"
"v_mfma_f32_16x16x32_bf16 v[140:143], a[144:147], v[104:107], v[236:239]\n"
"ds_read_b128 v[28:31], v3 offset:13312\n"
"v_exp_f32 v179, v179\n"
"v_mfma_f32_16x16x32_bf16 v[140:143], a[148:151], v[108:111], v[140:143]\n"
"v_cvt_pk_bf16_f32 v199, v178, v179\n"
"v_exp_f32 v180, v180\n"
"v_mfma_f32_16x16x32_bf16 v[140:143], a[152:155], v[112:115], v[140:143]\n"
"ds_read_b128 v[32:35], v3 offset:14336\n"
"v_exp_f32 v181, v181\n"
"v_mfma_f32_16x16x32_bf16 v[140:143], a[156:159], v[116:119], v[140:143]\n"
"v_cvt_pk_bf16_f32 v206, v180, v181\n"
"v_exp_f32 v182, v182\n"
"v_mfma_f32_16x16x32_bf16 v[152:155], a[160:163], v[88:91], v[232:235]\n"
"ds_read_b128 v[36:39], v3 offset:15360\n"
"buffer_load_dword v18, v19, s[4:7], s75 offen sc1\n"
"v_xor_b32 v2, s66, v2\n"
"v_mfma_f32_16x16x32_bf16 v[152:155], a[164:167], v[92:95], v[152:155]\n"
"buffer_store_dwordx4 v[60:63], v1, s[4:7], s46 offen sc1\n"
"v_exp_f32 v183, v183\n"
"v_mfma_f32_16x16x32_bf16 v[152:155], a[168:171], v[96:99], v[152:155]\n"
"buffer_store_dwordx4 v[76:79], v1, s[4:7], s46 offen offset:1024 sc1\n"
"buffer_store_dwordx4 v[92:95], v1, s[4:7], s46 offen offset:2048 sc1\n"
"v_cvt_pk_bf16_f32 v207, v182, v183\n"
"v_mfma_f32_16x16x32_bf16 v[152:155], a[172:175], v[100:103], v[152:155]\n"
"buffer_store_dwordx4 v[108:111], v1, s[4:7], s46 offen offset:3072 sc1\n"
"s_add_u32 s46, s46, 0x3000\n"
"buffer_store_dwordx4 v[64:67], v1, s[4:7], s46 offen sc1\n"
"v_mfma_f32_16x16x32_bf16 v[156:159], a[160:163], v[104:107], v[236:239]\n"
"s_max_u32 s26, s49, 3\n"
"s_sub_u32 s26, s26, 3\n"
"v_mfma_f32_16x16x32_bf16 v[156:159], a[164:167], v[108:111], v[156:159]\n"
"s_and_b32 s27, s100, 1\n"
"s_waitcnt lgkmcnt(0)\n"
"v_mfma_f32_16x16x32_bf16 v[156:159], a[168:171], v[112:115], v[156:159]\n"
"s_barrier\n"
"s_mul_i32 s27, s27, 5\n"
"v_mfma_f32_16x16x32_bf16 v[156:159], a[172:175], v[116:119], v[156:159]\n"
"buffer_store_dwordx4 v[80:83], v1, s[4:7], s46 offen offset:1024 sc1\n"
"s_add_u32 s26, s26, s27\n"
"v_mfma_f32_16x16x32_bf16 v[168:171], a[176:179], v[88:91], v[232:235]\n"
"ds_read_b128 a[144:147], v2\n"
"s_bfe_u32 s27, s100, 0x20001\n"
"v_mfma_f32_16x16x32_bf16 v[168:171], a[180:183], v[92:95], v[168:171]\n"
"s_lshl2_add_u32 s26, s26, s27\n"
"s_lshr_b32 s27, s100, 6\n"
"v_mfma_f32_16x16x32_bf16 v[168:171], a[184:187], v[96:99], v[168:171]\n"
"ds_read_b128 a[148:151], v2 offset:1024\n"
"s_lshl2_add_u32 s26, s26, s27\n"
"v_mfma_f32_16x16x32_bf16 v[168:171], a[188:191], v[100:103], v[168:171]\n"
"s_lshl2_add_u32 s26, s26, s3\n"
"v_lshrrev_b32 v16, 2, v0\n"
"v_mfma_f32_16x16x32_bf16 v[172:175], a[176:179], v[104:107], v[236:239]\n"
"ds_read_b128 a[152:155], v2 offset:2048\n"
"v_lshlrev_b32 v16, 7, v16\n"
"v_mfma_f32_16x16x32_bf16 v[172:175], a[180:183], v[108:111], v[172:175]\n"
"v_and_b32 v52, 3, v0\n"
"v_lshl_add_u32 v16, v52, 3, v16\n"
"v_mfma_f32_16x16x32_bf16 v[172:175], a[184:187], v[112:115], v[172:175]\n"
"ds_read_b128 a[156:159], v2 offset:3072\n"
"s_add_u32 s27, s78, 0x14000\n"
"v_mfma_f32_16x16x32_bf16 v[172:175], a[188:191], v[116:119], v[172:175]\n"
"buffer_store_dwordx4 v[96:99], v1, s[4:7], s46 offen offset:2048 sc1\n"
"v_add_u32 v16, s27, v16\n"
"v_mfma_f32_16x16x32_bf16 v[184:187], a[192:195], v[88:91], v[232:235]\n"
"ds_read_b128 a[160:163], v2 offset:4096\n"
"s_waitcnt vmcnt(7)\n"
"v_cmp_ne_u32 vcc, s82, v18\n"
"s_nop 0\n"
"s_cbranch_vccnz .Ladaln_block_P_kvb\n"
".Ladaln_block_P_kvb_R:\n"
"v_mfma_f32_16x16x32_bf16 v[184:187], a[196:199], v[92:95], v[184:187]\n"
"s_mov_b32 m0, s62\n"
"s_mov_b32 s20, s34\n"
"v_mfma_f32_16x16x32_bf16 v[184:187], a[200:203], v[96:99], v[184:187]\n"
"ds_read_b128 a[164:167], v2 offset:5120\n"
"v_exp_f32 v136, v136\n"
"v_mfma_f32_16x16x32_bf16 v[184:187], a[204:207], v[100:103], v[184:187]\n"
"buffer_load_dwordx4 v1, s[4:7], s60 offen sc1 lds\n"
"s_and_b32 s21, s35, 0xffff\n"
"v_mfma_f32_16x16x32_bf16 v[188:191], a[192:195], v[104:107], v[236:239]\n"
"ds_read_b128 a[168:171], v2 offset:6144\n"
"v_exp_f32 v137, v137\n"
"v_mfma_f32_16x16x32_bf16 v[188:191], a[196:199], v[108:111], v[188:191]\n"
"v_cvt_pk_bf16_f32 v208, v136, v137\n"
"s_mov_b32 s22, 0x500000\n"
"v_mfma_f32_16x16x32_bf16 v[188:191], a[200:203], v[112:115], v[188:191]\n"
"ds_read_b128 a[172:175], v2 offset:7168\n"
"v_exp_f32 v138, v138\n"
"v_mfma_f32_16x16x32_bf16 v[188:191], a[204:207], v[116:119], v[188:191]\n"
"buffer_store_dwordx4 v[112:115], v1, s[4:7], s46 offen offset:3072 sc1\n"
"s_add_u32 s46, s46, 0x1000\n"
"v_mfma_f32_16x16x32_bf16 a[0:3], a[208:211], v[192:195], 0\n"
"ds_read_b128 a[176:179], v2 offset:8192\n"
"v_exp_f32 v139, v139\n"
"v_mfma_f32_16x16x32_bf16 a[0:3], a[212:215], v[196:199], a[0:3]\n"
"buffer_load_dwordx4 v1, s[4:7], s60 offen offset:1024 sc1 lds\n"
"v_cvt_pk_bf16_f32 v209, v138, v139\n"
"v_mfma_f32_16x16x32_bf16 a[32:35], a[208:211], v[200:203], 0\n"
"ds_read_b128 a[180:183], v2 offset:9216\n"
"v_exp_f32 v140, v140\n"
"v_mfma_f32_16x16x32_bf16 a[32:35], a[212:215], v[204:207], a[32:35]\n"
"v_lshrrev_b32 v53, 3, v0\n"
"v_mul_u32_u24 v12, 0x500, v53\n"
"v_mfma_f32_16x16x32_bf16 a[4:7], a[216:219], v[192:195], 0\n"
"ds_read_b128 a[184:187], v2 offset:10240\n"
"v_exp_f32 v141, v141\n"
"v_mfma_f32_16x16x32_bf16 a[4:7], a[220:223], v[196:199], a[4:7]\n"
"buffer_store_dwordx4 v[68:71], v1, s[4:7], s46 offen sc1\n"
"v_cvt_pk_bf16_f32 v216, v140, v141\n"
"v_mfma_f32_16x16x32_bf16 a[36:39], a[216:219], v[200:203], 0\n"
"ds_read_b128 a[188:191], v2 offset:11264\n"
"v_exp_f32 v142, v142\n"
"v_mfma_f32_16x16x32_bf16 a[36:39], a[220:223], v[204:207], a[36:39]\n"
"buffer_load_dwordx4 v1, s[4:7], s60 offen offset:2048 sc1 lds\n"
"v_and_b32 v53, 7, v0\n"
"v_mfma_f32_16x16x32_bf16 a[8:11], a[224:227], v[192:195], 0\n"
"ds_read_b128 a[192:195], v2 offset:12288\n"
"v_exp_f32 v143, v143\n"
"v_mfma_f32_16x16x32_bf16 a[8:11], a[228:231], v[196:199], a[8:11]\n"
"v_cvt_pk_bf16_f32 v217, v142, v143\n"
"v_lshl_add_u32 v12, v53, 4, v12\n"
"v_mfma_f32_16x16x32_bf16 a[40:43], a[224:227], v[200:203], 0\n"
"ds_read_b128 a[196:199], v2 offset:13312\n"
"v_exp_f32 v152, v152\n"
"v_mfma_f32_16x16x32_bf16 a[40:43], a[228:231], v[204:207], a[40:43]\n"
"v_add_u32 v13, 0x2400, v12\n"
"v_add_u32 v14, 0x4800, v12\n"
"v_mfma_f32_16x16x32_bf16 a[12:15], a[232:235], v[192:195], 0\n"
"ds_read_b128 a[200:203], v2 offset:14336\n"
"v_exp_f32 v153, v153\n"
"v_mfma_f32_16x16x32_bf16 a[12:15], a[236:239], v[196:199], a[12:15]\n"
"buffer_load_dwordx4 v1, s[4:7], s60 offen offset:3072 sc1 lds\n"
"buffer_store_dwordx4 v[84:87], v1, s[4:7], s46 offen offset:1024 sc1\n"
"v_cvt_pk_bf16_f32 v210, v152, v153\n"
"v_mfma_f32_16x16x32_bf16 a[44:47], a[232:235], v[200:203], 0\n"
"ds_read_b128 a[204:207], v2 offset:15360\n"
"v_exp_f32 v154, v154\n"
"v_mfma_f32_16x16x32_bf16 a[44:47], a[236:239], v[204:207], a[44:47]\n"
"s_mov_b32 m0, s63\n"
"v_add_u32 v15, 0x6c00, v12\n"
"v_mfma_f32_16x16x32_bf16 a[16:19], a[240:243], v[192:195], 0\n"
"buffer_load_dwordx4 v1, s[16:19], s60 offen sc1 lds\n"
"v_exp_f32 v155, v155\n"
"v_mfma_f32_16x16x32_bf16 a[16:19], a[244:247], v[196:199], a[16:19]\n"
"v_cvt_pk_bf16_f32 v211, v154, v155\n"
"s_mul_i32 s27, s26, 0x1999a\n"
"v_mfma_f32_16x16x32_bf16 a[48:51], a[240:243], v[200:203], 0\n"
"v_exp_f32 v156, v156\n"
"v_mfma_f32_16x16x32_bf16 a[48:51], a[244:247], v[204:207], a[48:51]\n"
"buffer_store_dwordx4 v[100:103], v1, s[4:7], s46 offen offset:2048 sc1\n"
"s_lshr_b32 s27, s27, 20\n"
"v_mfma_f32_16x16x32_bf16 a[20:23], a[248:251], v[192:195], 0\n"
"v_exp_f32 v157, v157\n"
"v_mfma_f32_16x16x32_bf16 a[20:23], a[252:255], v[196:199], a[20:23]\n"
"buffer_load_dwordx4 v1, s[16:19], s60 offen offset:1024 sc1 lds\n"
"v_cvt_pk_bf16_f32 v218, v156, v157\n"
"v_mfma_f32_16x16x32_bf16 a[52:55], a[248:251], v[200:203], 0\n"
"v_exp_f32 v158, v158\n"
"v_mfma_f32_16x16x32_bf16 a[52:55], a[252:255], v[204:207], a[52:55]\n"
"s_mul_i32 s29, s27, 10\n"
"s_sub_u32 s31, s26, s29\n"
"v_mfma_f32_16x16x32_bf16 a[24:27], v[24:27], v[192:195], 0\n"
"v_exp_f32 v159, v159\n"
"v_mfma_f32_16x16x32_bf16 a[24:27], v[28:31], v[196:199], a[24:27]\n"
"v_cvt_pk_bf16_f32 v219, v158, v159\n"
"s_mul_i32 s29, s27, 0xa000\n"
"v_mfma_f32_16x16x32_bf16 a[56:59], v[24:27], v[200:203], 0\n"
"v_exp_f32 v168, v168\n"
"v_mfma_f32_16x16x32_bf16 a[56:59], v[28:31], v[204:207], a[56:59]\n"
"buffer_load_dwordx4 v1, s[16:19], s60 offen offset:2048 sc1 lds\n"
"buffer_store_dwordx4 v[116:119], v1, s[4:7], s46 offen offset:3072 sc1\n"
"s_add_u32 s46, s46, 0x3000\n"
"v_mfma_f32_16x16x32_bf16 a[28:31], v[32:35], v[192:195], 0\n"
"v_exp_f32 v169, v169\n"
"v_mfma_f32_16x16x32_bf16 a[28:31], v[36:39], v[196:199], a[28:31]\n"
"v_cvt_pk_bf16_f32 v212, v168, v169\n"
"s_lshl_b32 s28, s31, 7\n"
"v_mfma_f32_16x16x32_bf16 a[60:63], v[32:35], v[200:203], 0\n"
"v_exp_f32 v170, v170\n"
"v_mfma_f32_16x16x32_bf16 a[60:63], v[36:39], v[204:207], a[60:63]\n"
"s_add_u32 s29, s29, s28\n"
"s_and_b32 s28, s27, 32\n"
"v_mfma_f32_16x16x32_bf16 a[128:131], v[4:7], v[192:195], 0\n"
"v_exp_f32 v171, v171\n"
"v_mfma_f32_16x16x32_bf16 a[128:131], v[4:7], v[196:199], a[128:131]\n"
"buffer_load_dwordx4 v1, s[16:19], s60 offen offset:3072 sc1 lds\n"
"v_cvt_pk_bf16_f32 v213, v170, v171\n"
"v_mfma_f32_16x16x32_bf16 a[132:135], v[4:7], v[200:203], 0\n"
"s_mul_i32 s28, s28, 0xa000\n"
"s_add_u32 s29, s29, s28\n"
"v_mfma_f32_16x16x32_bf16 a[132:135], v[4:7], v[204:207], a[132:135]\n"
"s_waitcnt lgkmcnt(0)\n"
".Ladaln_block_A_FIRST_DONE:\n"
"s_sub_u32 s75, s3, 2\n"
"s_cmp_lt_u32 s3, 2\n"
"s_cselect_b32 s75, 2, s75\n"
"s_cmp_eq_u32 s49, 0\n"
"s_cbranch_scc1 .Ladaln_block_A_P0\n"
"s_lshl_b32 s69, s49, 1\n"
"s_sub_u32 s69, s69, 1\n"
"s_mov_b32 s76, -1\n"
"s_cmp_lt_u32 s49, 3\n"
"s_cbranch_scc1 .Ladaln_block_A_FULL1\n"
"s_sub_u32 s76, s69, 5\n"
"s_min_u32 s69, s76, 6\n"
"s_sub_u32 s76, s76, s69\n"
"s_cmp_eq_u32 s69, 0\n"
"s_cbranch_scc1 .Ladaln_block_A_PREP\n"
".p2alignl 6, 0xbf800000\n"
"s_nop 0\n"
"s_nop 0\n"
"s_nop 0\n"
".Ladaln_block_A_FULL1:\n"
"v_mfma_f32_16x16x32_bf16 v[128:131], a[144:147], v[56:59], v[224:227]\n"
"v_exp_f32 v172, v172\n"
"v_mfma_f32_16x16x32_bf16 v[128:131], a[148:151], v[60:63], v[128:131]\n"
"s_add_u32 s60, s60, 0x8000\n"
"s_xor_b32 s62, s62, 0x8000\n"
"v_mfma_f32_16x16x32_bf16 v[128:131], a[152:155], v[64:67], v[128:131]\n"
"v_exp_f32 v173, v173\n"
"v_mfma_f32_16x16x32_bf16 v[128:131], a[156:159], v[68:71], v[128:131]\n"
"v_cvt_pk_bf16_f32 v220, v172, v173\n"
"s_xor_b32 s63, s63, 0x8000\n"
"v_mfma_f32_16x16x32_bf16 v[132:135], a[144:147], v[72:75], v[228:231]\n"
"v_exp_f32 v174, v174\n"
"v_mfma_f32_16x16x32_bf16 v[132:135], a[148:151], v[76:79], v[132:135]\n"
"s_mov_b32 s61, s60\n"
"v_mfma_f32_16x16x32_bf16 v[132:135], a[152:155], v[80:83], v[132:135]\n"
"v_exp_f32 v175, v175\n"
"v_mfma_f32_16x16x32_bf16 v[132:135], a[156:159], v[84:87], v[132:135]\n"
"v_cvt_pk_bf16_f32 v221, v174, v175\n"
"v_mfma_f32_16x16x32_bf16 v[144:147], a[160:163], v[56:59], v[224:227]\n"
"v_exp_f32 v184, v184\n"
"v_mfma_f32_16x16x32_bf16 v[144:147], a[164:167], v[60:63], v[144:147]\n"
"s_sub_u32 s69, s69, 1\n"
"s_cmp_lg_u32 s69, 0\n"
"v_mfma_f32_16x16x32_bf16 v[144:147], a[168:171], v[64:67], v[144:147]\n"
"v_exp_f32 v185, v185\n"
"v_mfma_f32_16x16x32_bf16 v[144:147], a[172:175], v[68:71], v[144:147]\n"
"v_cvt_pk_bf16_f32 v214, v184, v185\n"
"v_mfma_f32_16x16x32_bf16 v[148:151], a[160:163], v[72:75], v[228:231]\n"
"v_exp_f32 v186, v186\n"
"v_mfma_f32_16x16x32_bf16 v[148:151], a[164:167], v[76:79], v[148:151]\n"
"v_mfma_f32_16x16x32_bf16 v[148:151], a[168:171], v[80:83], v[148:151]\n"
"v_exp_f32 v187, v187\n"
"v_mfma_f32_16x16x32_bf16 v[148:151], a[172:175], v[84:87], v[148:151]\n"
"v_cvt_pk_bf16_f32 v215, v186, v187\n"
"v_mfma_f32_16x16x32_bf16 v[160:163], a[176:179], v[56:59], v[224:227]\n"
"v_exp_f32 v188, v188\n"
"v_mfma_f32_16x16x32_bf16 v[160:163], a[180:183], v[60:63], v[160:163]\n"
"v_mfma_f32_16x16x32_bf16 v[160:163], a[184:187], v[64:67], v[160:163]\n"
"v_exp_f32 v189, v189\n"
"v_mfma_f32_16x16x32_bf16 v[160:163], a[188:191], v[68:71], v[160:163]\n"
"v_cvt_pk_bf16_f32 v222, v188, v189\n"
"v_mfma_f32_16x16x32_bf16 v[164:167], a[176:179], v[72:75], v[228:231]\n"
"v_exp_f32 v190, v190\n"
"v_mfma_f32_16x16x32_bf16 v[164:167], a[180:183], v[76:79], v[164:167]\n"
"v_mfma_f32_16x16x32_bf16 v[164:167], a[184:187], v[80:83], v[164:167]\n"
"v_exp_f32 v191, v191\n"
"v_mfma_f32_16x16x32_bf16 v[164:167], a[188:191], v[84:87], v[164:167]\n"
"v_cvt_pk_bf16_f32 v223, v190, v191\n"
"v_mfma_f32_16x16x32_bf16 v[176:179], a[192:195], v[56:59], v[224:227]\n"
"v_exp_f32 v128, v128\n"
"v_mfma_f32_16x16x32_bf16 v[176:179], a[196:199], v[60:63], v[176:179]\n"
"v_mfma_f32_16x16x32_bf16 v[176:179], a[200:203], v[64:67], v[176:179]\n"
"v_exp_f32 v129, v129\n"
"v_mfma_f32_16x16x32_bf16 v[176:179], a[204:207], v[68:71], v[176:179]\n"
"v_xor_b32 v3, 0x8000, v3\n"
"v_cvt_pk_bf16_f32 v192, v128, v129\n"
"v_mfma_f32_16x16x32_bf16 v[180:183], a[192:195], v[72:75], v[228:231]\n"
"v_exp_f32 v130, v130\n"
"v_mfma_f32_16x16x32_bf16 v[180:183], a[196:199], v[76:79], v[180:183]\n"
"v_mfma_f32_16x16x32_bf16 v[180:183], a[200:203], v[80:83], v[180:183]\n"
"v_exp_f32 v131, v131\n"
"v_mfma_f32_16x16x32_bf16 v[180:183], a[204:207], v[84:87], v[180:183]\n"
"v_cvt_pk_bf16_f32 v193, v130, v131\n"
"v_mfma_f32_16x16x32_bf16 a[64:67], a[208:211], v[208:211], a[64:67]\n"
"v_exp_f32 v132, v132\n"
"v_mfma_f32_16x16x32_bf16 a[64:67], a[212:215], v[212:215], a[64:67]\n"
"v_mfma_f32_16x16x32_bf16 a[96:99], a[208:211], v[216:219], a[96:99]\n"
"s_waitcnt vmcnt(8)\n"
"v_mfma_f32_16x16x32_bf16 a[96:99], a[212:215], v[220:223], a[96:99]\n"
"s_barrier\n"
"v_mfma_f32_16x16x32_bf16 a[68:71], a[216:219], v[208:211], a[68:71]\n"
"v_exp_f32 v133, v133\n"
"v_mfma_f32_16x16x32_bf16 a[68:71], a[220:223], v[212:215], a[68:71]\n"
"ds_read_b128 a[208:211], v3\n"
"v_cvt_pk_bf16_f32 v200, v132, v133\n"
"v_mfma_f32_16x16x32_bf16 a[100:103], a[216:219], v[216:219], a[100:103]\n"
"v_exp_f32 v134, v134\n"
"v_mfma_f32_16x16x32_bf16 a[100:103], a[220:223], v[220:223], a[100:103]\n"
"ds_read_b128 a[212:215], v3 offset:1024\n"
"s_mov_b32 m0, s62\n"
"v_mfma_f32_16x16x32_bf16 a[72:75], a[224:227], v[208:211], a[72:75]\n"
"v_exp_f32 v135, v135\n"
"v_mfma_f32_16x16x32_bf16 a[72:75], a[228:231], v[212:215], a[72:75]\n"
"ds_read_b128 a[216:219], v3 offset:2048\n"
"v_cvt_pk_bf16_f32 v201, v134, v135\n"
"v_mfma_f32_16x16x32_bf16 a[104:107], a[224:227], v[216:219], a[104:107]\n"
"v_exp_f32 v144, v144\n"
"v_mfma_f32_16x16x32_bf16 a[104:107], a[228:231], v[220:223], a[104:107]\n"
"ds_read_b128 a[220:223], v3 offset:3072\n"
"buffer_load_dwordx4 v1, s[4:7], s60 offen sc1 lds\n"
"v_mfma_f32_16x16x32_bf16 a[76:79], a[232:235], v[208:211], a[76:79]\n"
"v_exp_f32 v145, v145\n"
"v_mfma_f32_16x16x32_bf16 a[76:79], a[236:239], v[212:215], a[76:79]\n"
"ds_read_b128 a[224:227], v3 offset:4096\n"
"v_cvt_pk_bf16_f32 v194, v144, v145\n"
"v_mfma_f32_16x16x32_bf16 a[108:111], a[232:235], v[216:219], a[108:111]\n"
"v_exp_f32 v146, v146\n"
"v_mfma_f32_16x16x32_bf16 a[108:111], a[236:239], v[220:223], a[108:111]\n"
"ds_read_b128 a[228:231], v3 offset:5120\n"
"v_mfma_f32_16x16x32_bf16 a[80:83], a[240:243], v[208:211], a[80:83]\n"
"v_exp_f32 v147, v147\n"
"v_mfma_f32_16x16x32_bf16 a[80:83], a[244:247], v[212:215], a[80:83]\n"
"ds_read_b128 a[232:235], v3 offset:6144\n"
"v_cvt_pk_bf16_f32 v195, v146, v147\n"
"v_mfma_f32_16x16x32_bf16 a[112:115], a[240:243], v[216:219], a[112:115]\n"
"v_exp_f32 v148, v148\n"
"v_mfma_f32_16x16x32_bf16 a[112:115], a[244:247], v[220:223], a[112:115]\n"
"ds_read_b128 a[236:239], v3 offset:7168\n"
"v_mfma_f32_16x16x32_bf16 a[84:87], a[248:251], v[208:211], a[84:87]\n"
"v_exp_f32 v149, v149\n"
"v_mfma_f32_16x16x32_bf16 a[84:87], a[252:255], v[212:215], a[84:87]\n"
"ds_read_b128 a[240:243], v3 offset:8192\n"
"v_cvt_pk_bf16_f32 v202, v148, v149\n"
"v_mfma_f32_16x16x32_bf16 a[116:119], a[248:251], v[216:219], a[116:119]\n"
"v_exp_f32 v150, v150\n"
"v_mfma_f32_16x16x32_bf16 a[116:119], a[252:255], v[220:223], a[116:119]\n"
"ds_read_b128 a[244:247], v3 offset:9216\n"
"buffer_load_dwordx4 v1, s[4:7], s60 offen offset:1024 sc1 lds\n"
"v_mfma_f32_16x16x32_bf16 a[88:91], v[24:27], v[208:211], a[88:91]\n"
"v_exp_f32 v151, v151\n"
"v_mfma_f32_16x16x32_bf16 a[88:91], v[28:31], v[212:215], a[88:91]\n"
"ds_read_b128 a[248:251], v3 offset:10240\n"
"v_cvt_pk_bf16_f32 v203, v150, v151\n"
"v_mfma_f32_16x16x32_bf16 a[120:123], v[24:27], v[216:219], a[120:123]\n"
"v_exp_f32 v160, v160\n"
"v_mfma_f32_16x16x32_bf16 a[120:123], v[28:31], v[220:223], a[120:123]\n"
"ds_read_b128 a[252:255], v3 offset:11264\n"
"v_mfma_f32_16x16x32_bf16 a[92:95], v[32:35], v[208:211], a[92:95]\n"
"v_exp_f32 v161, v161\n"
"v_mfma_f32_16x16x32_bf16 a[92:95], v[36:39], v[212:215], a[92:95]\n"
"ds_read_b128 v[24:27], v3 offset:12288\n"
"v_cvt_pk_bf16_f32 v196, v160, v161\n"
"v_mfma_f32_16x16x32_bf16 a[124:127], v[32:35], v[216:219], a[124:127]\n"
"v_exp_f32 v162, v162\n"
"v_mfma_f32_16x16x32_bf16 a[124:127], v[36:39], v[220:223], a[124:127]\n"
"ds_read_b128 v[28:31], v3 offset:13312\n"
"v_mfma_f32_16x16x32_bf16 a[136:139], v[4:7], v[208:211], a[136:139]\n"
"v_exp_f32 v163, v163\n"
"v_mfma_f32_16x16x32_bf16 a[136:139], v[4:7], v[212:215], a[136:139]\n"
"ds_read_b128 v[32:35], v3 offset:14336\n"
"v_cvt_pk_bf16_f32 v197, v162, v163\n"
"v_mfma_f32_16x16x32_bf16 a[140:143], v[4:7], v[216:219], a[140:143]\n"
"v_exp_f32 v164, v164\n"
"v_mfma_f32_16x16x32_bf16 a[140:143], v[4:7], v[220:223], a[140:143]\n"
"ds_read_b128 v[36:39], v3 offset:15360\n"
"v_mfma_f32_16x16x32_bf16 v[136:139], a[144:147], v[88:91], v[232:235]\n"
"v_exp_f32 v165, v165\n"
"v_mfma_f32_16x16x32_bf16 v[136:139], a[148:151], v[92:95], v[136:139]\n"
"v_xor_b32 v2, 0x8000, v2\n"
"v_cvt_pk_bf16_f32 v204, v164, v165\n"
"v_mfma_f32_16x16x32_bf16 v[136:139], a[152:155], v[96:99], v[136:139]\n"
"v_exp_f32 v166, v166\n"
"v_mfma_f32_16x16x32_bf16 v[136:139], a[156:159], v[100:103], v[136:139]\n"
"buffer_load_dwordx4 v1, s[4:7], s60 offen offset:2048 sc1 lds\n"
"v_mfma_f32_16x16x32_bf16 v[140:143], a[144:147], v[104:107], v[236:239]\n"
"v_exp_f32 v167, v167\n"
"v_mfma_f32_16x16x32_bf16 v[140:143], a[148:151], v[108:111], v[140:143]\n"
"v_cvt_pk_bf16_f32 v205, v166, v167\n"
"v_mfma_f32_16x16x32_bf16 v[140:143], a[152:155], v[112:115], v[140:143]\n"
"s_waitcnt vmcnt(7) lgkmcnt(0)\n"
"v_mfma_f32_16x16x32_bf16 v[140:143], a[156:159], v[116:119], v[140:143]\n"
"s_barrier\n"
"v_mfma_f32_16x16x32_bf16 v[152:155], a[160:163], v[88:91], v[232:235]\n"
"v_exp_f32 v176, v176\n"
"v_mfma_f32_16x16x32_bf16 v[152:155], a[164:167], v[92:95], v[152:155]\n"
"ds_read_b128 a[144:147], v2\n"
"v_mfma_f32_16x16x32_bf16 v[152:155], a[168:171], v[96:99], v[152:155]\n"
"v_exp_f32 v177, v177\n"
"v_mfma_f32_16x16x32_bf16 v[152:155], a[172:175], v[100:103], v[152:155]\n"
"ds_read_b128 a[148:151], v2 offset:1024\n"
"v_cvt_pk_bf16_f32 v198, v176, v177\n"
"v_mfma_f32_16x16x32_bf16 v[156:159], a[160:163], v[104:107], v[236:239]\n"
"v_exp_f32 v178, v178\n"
"v_mfma_f32_16x16x32_bf16 v[156:159], a[164:167], v[108:111], v[156:159]\n"
"ds_read_b128 a[152:155], v2 offset:2048\n"
"v_mfma_f32_16x16x32_bf16 v[156:159], a[168:171], v[112:115], v[156:159]\n"
"v_exp_f32 v179, v179\n"
"v_mfma_f32_16x16x32_bf16 v[156:159], a[172:175], v[116:119], v[156:159]\n"
"ds_read_b128 a[156:159], v2 offset:3072\n"
"v_cvt_pk_bf16_f32 v199, v178, v179\n"
"v_mfma_f32_16x16x32_bf16 v[168:171], a[176:179], v[88:91], v[232:235]\n"
"v_exp_f32 v180, v180\n"
"v_mfma_f32_16x16x32_bf16 v[168:171], a[180:183], v[92:95], v[168:171]\n"
"ds_read_b128 a[160:163], v2 offset:4096\n"
"v_mfma_f32_16x16x32_bf16 v[168:171], a[184:187], v[96:99], v[168:171]\n"
"v_exp_f32 v181, v181\n"
"v_mfma_f32_16x16x32_bf16 v[168:171], a[188:191], v[100:103], v[168:171]\n"
"ds_read_b128 a[164:167], v2 offset:5120\n"
"v_cvt_pk_bf16_f32 v206, v180, v181\n"
"v_mfma_f32_16x16x32_bf16 v[172:175], a[176:179], v[104:107], v[236:239]\n"
"v_exp_f32 v182, v182\n"
"v_mfma_f32_16x16x32_bf16 v[172:175], a[180:183], v[108:111], v[172:175]\n"
"ds_read_b128 a[168:171], v2 offset:6144\n"
"buffer_load_dwordx4 v1, s[4:7], s60 offen offset:3072 sc1 lds\n"
"v_mfma_f32_16x16x32_bf16 v[172:175], a[184:187], v[112:115], v[172:175]\n"
"v_exp_f32 v183, v183\n"
"v_mfma_f32_16x16x32_bf16 v[172:175], a[188:191], v[116:119], v[172:175]\n"
"ds_read_b128 a[172:175], v2 offset:7168\n"
"v_cvt_pk_bf16_f32 v207, v182, v183\n"
"v_mfma_f32_16x16x32_bf16 v[184:187], a[192:195], v[88:91], v[232:235]\n"
"v_mfma_f32_16x16x32_bf16 v[184:187], a[196:199], v[92:95], v[184:187]\n"
"ds_read_b128 a[176:179], v2 offset:8192\n"
"v_mfma_f32_16x16x32_bf16 v[184:187], a[200:203], v[96:99], v[184:187]\n"
"v_exp_f32 v136, v136\n"
"v_mfma_f32_16x16x32_bf16 v[184:187], a[204:207], v[100:103], v[184:187]\n"
"ds_read_b128 a[180:183], v2 offset:9216\n"
"v_mfma_f32_16x16x32_bf16 v[188:191], a[192:195], v[104:107], v[236:239]\n"
"v_exp_f32 v137, v137\n"
"v_mfma_f32_16x16x32_bf16 v[188:191], a[196:199], v[108:111], v[188:191]\n"
"ds_read_b128 a[184:187], v2 offset:10240\n"
"v_cvt_pk_bf16_f32 v208, v136, v137\n"
"v_mfma_f32_16x16x32_bf16 v[188:191], a[200:203], v[112:115], v[188:191]\n"
"v_exp_f32 v138, v138\n"
"v_mfma_f32_16x16x32_bf16 v[188:191], a[204:207], v[116:119], v[188:191]\n"
"ds_read_b128 a[188:191], v2 offset:11264\n"
"v_mfma_f32_16x16x32_bf16 a[0:3], a[208:211], v[192:195], a[0:3]\n"
"v_exp_f32 v139, v139\n"
"v_mfma_f32_16x16x32_bf16 a[0:3], a[212:215], v[196:199], a[0:3]\n"
"ds_read_b128 a[192:195], v2 offset:12288\n"
"v_cvt_pk_bf16_f32 v209, v138, v139\n"
"v_mfma_f32_16x16x32_bf16 a[32:35], a[208:211], v[200:203], a[32:35]\n"
"v_exp_f32 v140, v140\n"
"v_mfma_f32_16x16x32_bf16 a[32:35], a[212:215], v[204:207], a[32:35]\n"
"ds_read_b128 a[196:199], v2 offset:13312\n"
"s_mov_b32 m0, s63\n"
"v_mfma_f32_16x16x32_bf16 a[4:7], a[216:219], v[192:195], a[4:7]\n"
"v_exp_f32 v141, v141\n"
"v_mfma_f32_16x16x32_bf16 a[4:7], a[220:223], v[196:199], a[4:7]\n"
"ds_read_b128 a[200:203], v2 offset:14336\n"
"v_cvt_pk_bf16_f32 v216, v140, v141\n"
"v_mfma_f32_16x16x32_bf16 a[36:39], a[216:219], v[200:203], a[36:39]\n"
"v_exp_f32 v142, v142\n"
"v_mfma_f32_16x16x32_bf16 a[36:39], a[220:223], v[204:207], a[36:39]\n"
"ds_read_b128 a[204:207], v2 offset:15360\n"
"buffer_load_dwordx4 v1, s[16:19], s60 offen sc1 lds\n"
"v_mfma_f32_16x16x32_bf16 a[8:11], a[224:227], v[192:195], a[8:11]\n"
"v_exp_f32 v143, v143\n"
"v_mfma_f32_16x16x32_bf16 a[8:11], a[228:231], v[196:199], a[8:11]\n"
"buffer_load_dwordx4 v1, s[16:19], s60 offen offset:1024 sc1 lds\n"
"v_cvt_pk_bf16_f32 v217, v142, v143\n"
"v_mfma_f32_16x16x32_bf16 a[40:43], a[224:227], v[200:203], a[40:43]\n"
"v_exp_f32 v152, v152\n"
"v_mfma_f32_16x16x32_bf16 a[40:43], a[228:231], v[204:207], a[40:43]\n"
"v_mfma_f32_16x16x32_bf16 a[12:15], a[232:235], v[192:195], a[12:15]\n"
"v_exp_f32 v153, v153\n"
"v_mfma_f32_16x16x32_bf16 a[12:15], a[236:239], v[196:199], a[12:15]\n"
"v_cvt_pk_bf16_f32 v210, v152, v153\n"
"v_mfma_f32_16x16x32_bf16 a[44:47], a[232:235], v[200:203], a[44:47]\n"
"v_exp_f32 v154, v154\n"
"v_mfma_f32_16x16x32_bf16 a[44:47], a[236:239], v[204:207], a[44:47]\n"
"v_mfma_f32_16x16x32_bf16 a[16:19], a[240:243], v[192:195], a[16:19]\n"
"v_exp_f32 v155, v155\n"
"v_mfma_f32_16x16x32_bf16 a[16:19], a[244:247], v[196:199], a[16:19]\n"
"buffer_load_dwordx4 v1, s[16:19], s60 offen offset:2048 sc1 lds\n"
"v_cvt_pk_bf16_f32 v211, v154, v155\n"
"v_mfma_f32_16x16x32_bf16 a[48:51], a[240:243], v[200:203], a[48:51]\n"
"v_exp_f32 v156, v156\n"
"v_mfma_f32_16x16x32_bf16 a[48:51], a[244:247], v[204:207], a[48:51]\n"
"v_mfma_f32_16x16x32_bf16 a[20:23], a[248:251], v[192:195], a[20:23]\n"
"v_exp_f32 v157, v157\n"
"v_mfma_f32_16x16x32_bf16 a[20:23], a[252:255], v[196:199], a[20:23]\n"
"v_cvt_pk_bf16_f32 v218, v156, v157\n"
"v_mfma_f32_16x16x32_bf16 a[52:55], a[248:251], v[200:203], a[52:55]\n"
"v_exp_f32 v158, v158\n"
"v_mfma_f32_16x16x32_bf16 a[52:55], a[252:255], v[204:207], a[52:55]\n"
"v_mfma_f32_16x16x32_bf16 a[24:27], v[24:27], v[192:195], a[24:27]\n"
"v_exp_f32 v159, v159\n"
"v_mfma_f32_16x16x32_bf16 a[24:27], v[28:31], v[196:199], a[24:27]\n"
"buffer_load_dwordx4 v1, s[16:19], s60 offen offset:3072 sc1 lds\n"
"v_cvt_pk_bf16_f32 v219, v158, v159\n"
"v_mfma_f32_16x16x32_bf16 a[56:59], v[24:27], v[200:203], a[56:59]\n"
"v_exp_f32 v168, v168\n"
"v_mfma_f32_16x16x32_bf16 a[56:59], v[28:31], v[204:207], a[56:59]\n"
"v_mfma_f32_16x16x32_bf16 a[28:31], v[32:35], v[192:195], a[28:31]\n"
"v_exp_f32 v169, v169\n"
"v_mfma_f32_16x16x32_bf16 a[28:31], v[36:39], v[196:199], a[28:31]\n"
"v_cvt_pk_bf16_f32 v212, v168, v169\n"
"v_mfma_f32_16x16x32_bf16 a[60:63], v[32:35], v[200:203], a[60:63]\n"
"v_exp_f32 v170, v170\n"
"v_mfma_f32_16x16x32_bf16 a[60:63], v[36:39], v[204:207], a[60:63]\n"
"v_mfma_f32_16x16x32_bf16 a[128:131], v[4:7], v[192:195], a[128:131]\n"
"v_exp_f32 v171, v171\n"
"v_mfma_f32_16x16x32_bf16 a[128:131], v[4:7], v[196:199], a[128:131]\n"
"v_cvt_pk_bf16_f32 v213, v170, v171\n"
"s_waitcnt lgkmcnt(0)\n"
"v_mfma_f32_16x16x32_bf16 a[132:135], v[4:7], v[200:203], a[132:135]\n"
"v_mfma_f32_16x16x32_bf16 a[132:135], v[4:7], v[204:207], a[132:135]\n"
"s_cbranch_scc1 .Ladaln_block_A_FULL1\n"
"s_cmp_eq_u32 s76, -1\n"
"s_cbranch_scc1 .Ladaln_block_A_MODE2\n"
".Ladaln_block_A_PREP:\n"
"v_mfma_f32_16x16x32_bf16 v[128:131], a[144:147], v[56:59], v[224:227]\n"
"v_exp_f32 v172, v172\n"
"v_mfma_f32_16x16x32_bf16 v[128:131], a[148:151], v[60:63], v[128:131]\n"
"s_add_u32 s60, s60, 0x8000\n"
"s_xor_b32 s62, s62, 0x8000\n"
"v_mfma_f32_16x16x32_bf16 v[128:131], a[152:155], v[64:67], v[128:131]\n"
"v_exp_f32 v173, v173\n"
"v_mfma_f32_16x16x32_bf16 v[128:131], a[156:159], v[68:71], v[128:131]\n"
"v_cvt_pk_bf16_f32 v220, v172, v173\n"
"s_xor_b32 s63, s63, 0x8000\n"
"v_mfma_f32_16x16x32_bf16 v[132:135], a[144:147], v[72:75], v[228:231]\n"
"v_exp_f32 v174, v174\n"
"v_mfma_f32_16x16x32_bf16 v[132:135], a[148:151], v[76:79], v[132:135]\n"
"s_mov_b32 s61, s60\n"
"s_add_u32 m0, s78, 0x14000\n"
"v_mfma_f32_16x16x32_bf16 v[132:135], a[152:155], v[80:83], v[132:135]\n"
"v_exp_f32 v175, v175\n"
"v_mfma_f32_16x16x32_bf16 v[132:135], a[156:159], v[84:87], v[132:135]\n"
"buffer_load_dwordx4 v12, s[20:23], s29 offen lds\n"
"v_cvt_pk_bf16_f32 v221, v174, v175\n"
"v_mfma_f32_16x16x32_bf16 v[144:147], a[160:163], v[56:59], v[224:227]\n"
"v_exp_f32 v184, v184\n"
"v_mfma_f32_16x16x32_bf16 v[144:147], a[164:167], v[60:63], v[144:147]\n"
"buffer_load_dwordx4 v13, s[20:23], s29 offen offset:1024 lds\n"
"v_lshl_add_u32 v54, s31, 2, v0\n"
"v_mfma_f32_16x16x32_bf16 v[144:147], a[168:171], v[64:67], v[144:147]\n"
"v_exp_f32 v185, v185\n"
"v_mfma_f32_16x16x32_bf16 v[144:147], a[172:175], v[68:71], v[144:147]\n"
"buffer_load_dwordx4 v14, s[20:23], s29 offen offset:2048 lds\n"
"v_cvt_pk_bf16_f32 v214, v184, v185\n"
"v_mfma_f32_16x16x32_bf16 v[148:151], a[160:163], v[72:75], v[228:231]\n"
"v_exp_f32 v186, v186\n"
"v_mfma_f32_16x16x32_bf16 v[148:151], a[164:167], v[76:79], v[148:151]\n"
"v_mul_u32_u24 v52, 0xcccd, v54\n"
"v_lshrrev_b32 v52, 18, v52\n"
"v_mfma_f32_16x16x32_bf16 v[148:151], a[168:171], v[80:83], v[148:151]\n"
"v_exp_f32 v187, v187\n"
"v_mfma_f32_16x16x32_bf16 v[148:151], a[172:175], v[84:87], v[148:151]\n"
"buffer_load_dwordx4 v15, s[20:23], s29 offen offset:3072 lds\n"
"v_cvt_pk_bf16_f32 v215, v186, v187\n"
"v_mfma_f32_16x16x32_bf16 v[160:163], a[176:179], v[56:59], v[224:227]\n"
"v_exp_f32 v188, v188\n"
"v_mfma_f32_16x16x32_bf16 v[160:163], a[180:183], v[60:63], v[160:163]\n"
"v_mul_u32_u24 v53, 5, v52\n"
"v_sub_u32 v54, v54, v53\n"
"v_mfma_f32_16x16x32_bf16 v[160:163], a[184:187], v[64:67], v[160:163]\n"
"v_exp_f32 v189, v189\n"
"v_mfma_f32_16x16x32_bf16 v[160:163], a[188:191], v[68:71], v[160:163]\n"
"v_cvt_pk_bf16_f32 v222, v188, v189\n"
"v_and_b32 v53, 1, v52\n"
"v_mfma_f32_16x16x32_bf16 v[164:167], a[176:179], v[72:75], v[228:231]\n"
"v_exp_f32 v190, v190\n"
"v_mfma_f32_16x16x32_bf16 v[164:167], a[180:183], v[76:79], v[164:167]\n"
"v_sub_u32 v54, v54, v53\n"
"v_min_u32 v19, 4, v54\n"
"v_mfma_f32_16x16x32_bf16 v[164:167], a[184:187], v[80:83], v[164:167]\n"
"v_exp_f32 v191, v191\n"
"v_mfma_f32_16x16x32_bf16 v[164:167], a[188:191], v[84:87], v[164:167]\n"
"v_cvt_pk_bf16_f32 v223, v190, v191\n"
"s_and_b32 s28, s27, 15\n"
"v_mfma_f32_16x16x32_bf16 v[176:179], a[192:195], v[56:59], v[224:227]\n"
"v_exp_f32 v128, v128\n"
"v_mfma_f32_16x16x32_bf16 v[176:179], a[196:199], v[60:63], v[176:179]\n"
"s_mul_i32 s28, s28, 0x1400\n"
"v_lshl_add_u32 v54, v19, 10, s28\n"
"v_mfma_f32_16x16x32_bf16 v[176:179], a[200:203], v[64:67], v[176:179]\n"
"v_exp_f32 v129, v129\n"
"v_mfma_f32_16x16x32_bf16 v[176:179], a[204:207], v[68:71], v[176:179]\n"
"v_xor_b32 v3, 0x8000, v3\n"
"v_cvt_pk_bf16_f32 v192, v128, v129\n"
"v_mfma_f32_16x16x32_bf16 v[180:183], a[192:195], v[72:75], v[228:231]\n"
"v_exp_f32 v130, v130\n"
"v_mfma_f32_16x16x32_bf16 v[180:183], a[196:199], v[76:79], v[180:183]\n"
"v_and_b32 v53, 0xffffe000, v54\n"
"v_add_u32 v54, v54, v53\n"
"v_mfma_f32_16x16x32_bf16 v[180:183], a[200:203], v[80:83], v[180:183]\n"
"v_exp_f32 v131, v131\n"
"v_mfma_f32_16x16x32_bf16 v[180:183], a[204:207], v[84:87], v[180:183]\n"
"v_cvt_pk_bf16_f32 v193, v130, v131\n"
"s_bfe_u32 s28, s27, 0x10004\n"
"v_mfma_f32_16x16x32_bf16 a[64:67], a[208:211], v[208:211], a[64:67]\n"
"v_exp_f32 v132, v132\n"
"v_mfma_f32_16x16x32_bf16 a[64:67], a[212:215], v[212:215], a[64:67]\n"
"s_mul_i32 s28, s28, 0x48000\n"
"s_add_u32 s28, s28, 0x1800000\n"
"v_mfma_f32_16x16x32_bf16 a[96:99], a[208:211], v[216:219], a[96:99]\n"
"v_lshl_add_u32 v53, v52, 20, s28\n"
"s_waitcnt vmcnt(12)\n"
"v_mfma_f32_16x16x32_bf16 a[96:99], a[212:215], v[220:223], a[96:99]\n"
"s_barrier\n"
"v_add_u32 v54, v54, v53\n"
"v_mfma_f32_16x16x32_bf16 a[68:71], a[216:219], v[208:211], a[68:71]\n"
"v_exp_f32 v133, v133\n"
"v_mfma_f32_16x16x32_bf16 a[68:71], a[220:223], v[212:215], a[68:71]\n"
"ds_read_b128 a[208:211], v3\n"
"v_cvt_pk_bf16_f32 v200, v132, v133\n"
"v_mfma_f32_16x16x32_bf16 a[100:103], a[216:219], v[216:219], a[100:103]\n"
"v_exp_f32 v134, v134\n"
"v_mfma_f32_16x16x32_bf16 a[100:103], a[220:223], v[220:223], a[100:103]\n"
"ds_read_b128 a[212:215], v3 offset:1024\n"
"s_mov_b32 m0, s62\n"
"v_mfma_f32_16x16x32_bf16 a[72:75], a[224:227], v[208:211], a[72:75]\n"
"v_exp_f32 v135, v135\n"
"v_mfma_f32_16x16x32_bf16 a[72:75], a[228:231], v[212:215], a[72:75]\n"
"ds_read_b128 a[216:219], v3 offset:2048\n"
"v_cvt_pk_bf16_f32 v201, v134, v135\n"
"v_mfma_f32_16x16x32_bf16 a[104:107], a[224:227], v[216:219], a[104:107]\n"
"v_exp_f32 v144, v144\n"
"v_mfma_f32_16x16x32_bf16 a[104:107], a[228:231], v[220:223], a[104:107]\n"
"ds_read_b128 a[220:223], v3 offset:3072\n"
"buffer_load_dwordx4 v1, s[4:7], s60 offen sc1 lds\n"
"v_mfma_f32_16x16x32_bf16 a[76:79], a[232:235], v[208:211], a[76:79]\n"
"v_exp_f32 v145, v145\n"
"v_mfma_f32_16x16x32_bf16 a[76:79], a[236:239], v[212:215], a[76:79]\n"
"ds_read_b128 a[224:227], v3 offset:4096\n"
"v_cvt_pk_bf16_f32 v194, v144, v145\n"
"v_mfma_f32_16x16x32_bf16 a[108:111], a[232:235], v[216:219], a[108:111]\n"
"v_exp_f32 v146, v146\n"
"v_mfma_f32_16x16x32_bf16 a[108:111], a[236:239], v[220:223], a[108:111]\n"
"ds_read_b128 a[228:231], v3 offset:5120\n"
"buffer_load_dwordx4 v1, s[4:7], s60 offen offset:1024 sc1 lds\n"
"v_mfma_f32_16x16x32_bf16 a[80:83], a[240:243], v[208:211], a[80:83]\n"
"v_exp_f32 v147, v147\n"
"v_mfma_f32_16x16x32_bf16 a[80:83], a[244:247], v[212:215], a[80:83]\n"
"ds_read_b128 a[232:235], v3 offset:6144\n"
"v_cvt_pk_bf16_f32 v195, v146, v147\n"
"v_mfma_f32_16x16x32_bf16 a[112:115], a[240:243], v[216:219], a[112:115]\n"
"v_exp_f32 v148, v148\n"
"v_mfma_f32_16x16x32_bf16 a[112:115], a[244:247], v[220:223], a[112:115]\n"
"ds_read_b128 a[236:239], v3 offset:7168\n"
"v_lshrrev_b32 v53, 8, v54\n"
"v_mfma_f32_16x16x32_bf16 a[84:87], a[248:251], v[208:211], a[84:87]\n"
"v_exp_f32 v149, v149\n"
"v_mfma_f32_16x16x32_bf16 a[84:87], a[252:255], v[212:215], a[84:87]\n"
"ds_read_b128 a[240:243], v3 offset:8192\n"
"v_cvt_pk_bf16_f32 v202, v148, v149\n"
"v_mfma_f32_16x16x32_bf16 a[116:119], a[248:251], v[216:219], a[116:119]\n"
"v_exp_f32 v150, v150\n"
"v_mfma_f32_16x16x32_bf16 a[116:119], a[252:255], v[220:223], a[116:119]\n"
"ds_read_b128 a[244:247], v3 offset:9216\n"
"buffer_load_dwordx4 v1, s[4:7], s60 offen offset:2048 sc1 lds\n"
"v_mfma_f32_16x16x32_bf16 a[88:91], v[24:27], v[208:211], a[88:91]\n"
"v_exp_f32 v151, v151\n"
"v_mfma_f32_16x16x32_bf16 a[88:91], v[28:31], v[212:215], a[88:91]\n"
"ds_read_b128 a[248:251], v3 offset:10240\n"
"v_cvt_pk_bf16_f32 v203, v150, v151\n"
"v_mfma_f32_16x16x32_bf16 a[120:123], v[24:27], v[216:219], a[120:123]\n"
"v_exp_f32 v160, v160\n"
"v_mfma_f32_16x16x32_bf16 a[120:123], v[28:31], v[220:223], a[120:123]\n"
"ds_read_b128 a[252:255], v3 offset:11264\n"
"v_and_b32 v53, 0x2000, v53\n"
"v_mfma_f32_16x16x32_bf16 a[92:95], v[32:35], v[208:211], a[92:95]\n"
"v_exp_f32 v161, v161\n"
"v_mfma_f32_16x16x32_bf16 a[92:95], v[36:39], v[212:215], a[92:95]\n"
"ds_read_b128 v[24:27], v3 offset:12288\n"
"v_cvt_pk_bf16_f32 v196, v160, v161\n"
"v_mfma_f32_16x16x32_bf16 a[124:127], v[32:35], v[216:219], a[124:127]\n"
"v_exp_f32 v162, v162\n"
"v_mfma_f32_16x16x32_bf16 a[124:127], v[36:39], v[220:223], a[124:127]\n"
"ds_read_b128 v[28:31], v3 offset:13312\n"
"buffer_load_dwordx4 v1, s[4:7], s60 offen offset:3072 sc1 lds\n"
"v_mfma_f32_16x16x32_bf16 a[136:139], v[4:7], v[208:211], a[136:139]\n"
"v_exp_f32 v163, v163\n"
"v_mfma_f32_16x16x32_bf16 a[136:139], v[4:7], v[212:215], a[136:139]\n"
"ds_read_b128 v[32:35], v3 offset:14336\n"
"v_cvt_pk_bf16_f32 v197, v162, v163\n"
"v_mfma_f32_16x16x32_bf16 a[140:143], v[4:7], v[216:219], a[140:143]\n"
"v_exp_f32 v164, v164\n"
"v_mfma_f32_16x16x32_bf16 a[140:143], v[4:7], v[220:223], a[140:143]\n"
"ds_read_b128 v[36:39], v3 offset:15360\n"
"v_xor_b32 v54, v54, v53\n"
"v_mfma_f32_16x16x32_bf16 v[136:139], a[144:147], v[88:91], v[232:235]\n"
"v_exp_f32 v165, v165\n"
"v_mfma_f32_16x16x32_bf16 v[136:139], a[148:151], v[92:95], v[136:139]\n"
"v_xor_b32 v2, 0x8000, v2\n"
"v_cvt_pk_bf16_f32 v204, v164, v165\n"
"v_mfma_f32_16x16x32_bf16 v[136:139], a[152:155], v[96:99], v[136:139]\n"
"v_exp_f32 v166, v166\n"
"v_mfma_f32_16x16x32_bf16 v[136:139], a[156:159], v[100:103], v[136:139]\n"
"v_xor_b32 v54, s101, v54\n"
"s_mov_b32 s59, s27\n"
"v_mfma_f32_16x16x32_bf16 v[140:143], a[144:147], v[104:107], v[236:239]\n"
"v_exp_f32 v167, v167\n"
"v_mfma_f32_16x16x32_bf16 v[140:143], a[148:151], v[108:111], v[140:143]\n"
"v_cvt_pk_bf16_f32 v205, v166, v167\n"
"v_add_u32 v10, 0x2000, v1\n"
"v_mfma_f32_16x16x32_bf16 v[140:143], a[152:155], v[112:115], v[140:143]\n"
"s_add_u32 s29, s29, 0x140000\n"
"s_waitcnt vmcnt(12) lgkmcnt(0)\n"
"v_mfma_f32_16x16x32_bf16 v[140:143], a[156:159], v[116:119], v[140:143]\n"
"s_barrier\n"
"s_mul_i32 s27, s26, 0x97b5\n"
"v_mfma_f32_16x16x32_bf16 v[152:155], a[160:163], v[88:91], v[232:235]\n"
"v_exp_f32 v176, v176\n"
"v_mfma_f32_16x16x32_bf16 v[152:155], a[164:167], v[92:95], v[152:155]\n"
"ds_read_b128 a[144:147], v2\n"
"s_lshr_b32 s27, s27, 20\n"
"v_mfma_f32_16x16x32_bf16 v[152:155], a[168:171], v[96:99], v[152:155]\n"
"v_exp_f32 v177, v177\n"
"v_mfma_f32_16x16x32_bf16 v[152:155], a[172:175], v[100:103], v[152:155]\n"
"ds_read_b128 a[148:151], v2 offset:1024\n"
"v_cvt_pk_bf16_f32 v198, v176, v177\n"
"v_mfma_f32_16x16x32_bf16 v[156:159], a[160:163], v[104:107], v[236:239]\n"
"v_exp_f32 v178, v178\n"
"v_mfma_f32_16x16x32_bf16 v[156:159], a[164:167], v[108:111], v[156:159]\n"
"ds_read_b128 a[152:155], v2 offset:2048\n"
"s_mul_i32 s30, s27, 27\n"
"v_mfma_f32_16x16x32_bf16 v[156:159], a[168:171], v[112:115], v[156:159]\n"
"v_exp_f32 v179, v179\n"
"v_mfma_f32_16x16x32_bf16 v[156:159], a[172:175], v[116:119], v[156:159]\n"
"ds_read_b128 a[156:159], v2 offset:3072\n"
"v_cvt_pk_bf16_f32 v199, v178, v179\n"
"v_mfma_f32_16x16x32_bf16 v[168:171], a[176:179], v[88:91], v[232:235]\n"
"v_exp_f32 v180, v180\n"
"v_mfma_f32_16x16x32_bf16 v[168:171], a[180:183], v[92:95], v[168:171]\n"
"ds_read_b128 a[160:163], v2 offset:4096\n"
"s_sub_u32 s31, s26, s30\n"
"v_mfma_f32_16x16x32_bf16 v[168:171], a[184:187], v[96:99], v[168:171]\n"
"v_exp_f32 v181, v181\n"
"v_mfma_f32_16x16x32_bf16 v[168:171], a[188:191], v[100:103], v[168:171]\n"
"ds_read_b128 a[164:167], v2 offset:5120\n"
"v_cvt_pk_bf16_f32 v206, v180, v181\n"
"v_mfma_f32_16x16x32_bf16 v[172:175], a[176:179], v[104:107], v[236:239]\n"
"v_exp_f32 v182, v182\n"
"v_mfma_f32_16x16x32_bf16 v[172:175], a[180:183], v[108:111], v[172:175]\n"
"ds_read_b128 a[168:171], v2 offset:6144\n"
"s_mul_i32 s30, s27, 0x1b000\n"
"v_mfma_f32_16x16x32_bf16 v[172:175], a[184:187], v[112:115], v[172:175]\n"
"v_exp_f32 v183, v183\n"
"v_mfma_f32_16x16x32_bf16 v[172:175], a[188:191], v[116:119], v[172:175]\n"
"ds_read_b128 a[172:175], v2 offset:7168\n"
"v_cvt_pk_bf16_f32 v207, v182, v183\n"
"v_mfma_f32_16x16x32_bf16 v[184:187], a[192:195], v[88:91], v[232:235]\n"
"s_lshl_b32 s28, s31, 7\n"
"s_add_u32 s30, s30, s28\n"
"v_mfma_f32_16x16x32_bf16 v[184:187], a[196:199], v[92:95], v[184:187]\n"
"ds_read_b128 a[176:179], v2 offset:8192\n"
"s_mov_b32 s28, 0x2000000\n"
"v_mfma_f32_16x16x32_bf16 v[184:187], a[200:203], v[96:99], v[184:187]\n"
"v_exp_f32 v136, v136\n"
"v_mfma_f32_16x16x32_bf16 v[184:187], a[204:207], v[100:103], v[184:187]\n"
"ds_read_b128 a[180:183], v2 offset:9216\n"
"s_cmp_ge_u32 s26, 0x21c\n"
"v_mfma_f32_16x16x32_bf16 v[188:191], a[192:195], v[104:107], v[236:239]\n"
"v_exp_f32 v137, v137\n"
"v_mfma_f32_16x16x32_bf16 v[188:191], a[196:199], v[108:111], v[188:191]\n"
"ds_read_b128 a[184:187], v2 offset:10240\n"
"v_cvt_pk_bf16_f32 v208, v136, v137\n"
"v_mfma_f32_16x16x32_bf16 v[188:191], a[200:203], v[112:115], v[188:191]\n"
"v_exp_f32 v138, v138\n"
"v_mfma_f32_16x16x32_bf16 v[188:191], a[204:207], v[116:119], v[188:191]\n"
"ds_read_b128 a[188:191], v2 offset:11264\n"
"s_cmov_b32 s28, 0x40000000\n"
"v_mfma_f32_16x16x32_bf16 a[0:3], a[208:211], v[192:195], a[0:3]\n"
"v_exp_f32 v139, v139\n"
"v_mfma_f32_16x16x32_bf16 a[0:3], a[212:215], v[196:199], a[0:3]\n"
"ds_read_b128 a[192:195], v2 offset:12288\n"
"v_cvt_pk_bf16_f32 v209, v138, v139\n"
"v_mfma_f32_16x16x32_bf16 a[32:35], a[208:211], v[200:203], a[32:35]\n"
"v_exp_f32 v140, v140\n"
"v_mfma_f32_16x16x32_bf16 a[32:35], a[212:215], v[204:207], a[32:35]\n"
"ds_read_b128 a[196:199], v2 offset:13312\n"
"s_mov_b32 m0, s63\n"
"v_mfma_f32_16x16x32_bf16 a[4:7], a[216:219], v[192:195], a[4:7]\n"
"v_exp_f32 v141, v141\n"
"v_mfma_f32_16x16x32_bf16 a[4:7], a[220:223], v[196:199], a[4:7]\n"
"ds_read_b128 a[200:203], v2 offset:14336\n"
"v_cvt_pk_bf16_f32 v216, v140, v141\n"
"v_mfma_f32_16x16x32_bf16 a[36:39], a[216:219], v[200:203], a[36:39]\n"
"v_exp_f32 v142, v142\n"
"v_mfma_f32_16x16x32_bf16 a[36:39], a[220:223], v[204:207], a[36:39]\n"
"ds_read_b128 a[204:207], v2 offset:15360\n"
"buffer_load_dwordx4 v1, s[16:19], s60 offen sc1 lds\n"
"v_mfma_f32_16x16x32_bf16 a[8:11], a[224:227], v[192:195], a[8:11]\n"
"v_exp_f32 v143, v143\n"
"v_mfma_f32_16x16x32_bf16 a[8:11], a[228:231], v[196:199], a[8:11]\n"
"buffer_load_dwordx4 v1, s[16:19], s60 offen offset:1024 sc1 lds\n"
"v_cvt_pk_bf16_f32 v217, v142, v143\n"
"v_mfma_f32_16x16x32_bf16 a[40:43], a[224:227], v[200:203], a[40:43]\n"
"v_exp_f32 v152, v152\n"
"v_mfma_f32_16x16x32_bf16 a[40:43], a[228:231], v[204:207], a[40:43]\n"
"buffer_load_dwordx4 v1, s[16:19], s60 offen offset:2048 sc1 lds\n"
"s_mul_i32 s33, s31, 43\n"
"v_mfma_f32_16x16x32_bf16 a[12:15], a[232:235], v[192:195], a[12:15]\n"
"v_exp_f32 v153, v153\n"
"v_mfma_f32_16x16x32_bf16 a[12:15], a[236:239], v[196:199], a[12:15]\n"
"buffer_load_dwordx4 v1, s[16:19], s60 offen offset:3072 sc1 lds\n"
"v_cvt_pk_bf16_f32 v210, v152, v153\n"
"v_mfma_f32_16x16x32_bf16 a[44:47], a[232:235], v[200:203], a[44:47]\n"
"v_exp_f32 v154, v154\n"
"v_mfma_f32_16x16x32_bf16 a[44:47], a[236:239], v[204:207], a[44:47]\n"
"s_lshr_b32 s33, s33, 8\n"
"s_mul_i32 s32, s33, 6\n"
"v_mfma_f32_16x16x32_bf16 a[16:19], a[240:243], v[192:195], a[16:19]\n"
"v_exp_f32 v155, v155\n"
"v_mfma_f32_16x16x32_bf16 a[16:19], a[244:247], v[196:199], a[16:19]\n"
"v_cvt_pk_bf16_f32 v211, v154, v155\n"
"s_sub_u32 s32, s31, s32\n"
"v_mfma_f32_16x16x32_bf16 a[48:51], a[240:243], v[200:203], a[48:51]\n"
"v_exp_f32 v156, v156\n"
"v_mfma_f32_16x16x32_bf16 a[48:51], a[244:247], v[204:207], a[48:51]\n"
"v_mfma_f32_16x16x32_bf16 a[20:23], a[248:251], v[192:195], a[20:23]\n"
"v_exp_f32 v157, v157\n"
"v_mfma_f32_16x16x32_bf16 a[20:23], a[252:255], v[196:199], a[20:23]\n"
"v_cvt_pk_bf16_f32 v218, v156, v157\n"
"v_mfma_f32_16x16x32_bf16 a[52:55], a[248:251], v[200:203], a[52:55]\n"
"v_exp_f32 v158, v158\n"
"v_mfma_f32_16x16x32_bf16 a[52:55], a[252:255], v[204:207], a[52:55]\n"
"v_mfma_f32_16x16x32_bf16 a[24:27], v[24:27], v[192:195], a[24:27]\n"
"v_exp_f32 v159, v159\n"
"v_mfma_f32_16x16x32_bf16 a[24:27], v[28:31], v[196:199], a[24:27]\n"
"v_cvt_pk_bf16_f32 v219, v158, v159\n"
"v_mfma_f32_16x16x32_bf16 a[56:59], v[24:27], v[200:203], a[56:59]\n"
"v_exp_f32 v168, v168\n"
"v_mfma_f32_16x16x32_bf16 a[56:59], v[28:31], v[204:207], a[56:59]\n"
"v_mfma_f32_16x16x32_bf16 a[28:31], v[32:35], v[192:195], a[28:31]\n"
"v_exp_f32 v169, v169\n"
"v_mfma_f32_16x16x32_bf16 a[28:31], v[36:39], v[196:199], a[28:31]\n"
"v_cvt_pk_bf16_f32 v212, v168, v169\n"
"v_mfma_f32_16x16x32_bf16 a[60:63], v[32:35], v[200:203], a[60:63]\n"
"v_exp_f32 v170, v170\n"
"v_mfma_f32_16x16x32_bf16 a[60:63], v[36:39], v[204:207], a[60:63]\n"
"v_mfma_f32_16x16x32_bf16 a[128:131], v[4:7], v[192:195], a[128:131]\n"
"v_exp_f32 v171, v171\n"
"v_mfma_f32_16x16x32_bf16 a[128:131], v[4:7], v[196:199], a[128:131]\n"
"v_cvt_pk_bf16_f32 v213, v170, v171\n"
"s_waitcnt lgkmcnt(0)\n"
"v_mfma_f32_16x16x32_bf16 a[132:135], v[4:7], v[200:203], a[132:135]\n"
"v_mfma_f32_16x16x32_bf16 a[132:135], v[4:7], v[204:207], a[132:135]\n"
"v_mfma_f32_16x16x32_bf16 v[128:131], a[144:147], v[56:59], v[224:227]\n"
"v_exp_f32 v172, v172\n"
"v_mfma_f32_16x16x32_bf16 v[128:131], a[148:151], v[60:63], v[128:131]\n"
"s_add_u32 s60, s60, 0x8000\n"
"s_xor_b32 s62, s62, 0x8000\n"
"v_mfma_f32_16x16x32_bf16 v[128:131], a[152:155], v[64:67], v[128:131]\n"
"v_exp_f32 v173, v173\n"
"v_mfma_f32_16x16x32_bf16 v[128:131], a[156:159], v[68:71], v[128:131]\n"
"v_cvt_pk_bf16_f32 v220, v172, v173\n"
"s_xor_b32 s63, s63, 0x8000\n"
"v_mfma_f32_16x16x32_bf16 v[132:135], a[144:147], v[72:75], v[228:231]\n"
"v_exp_f32 v174, v174\n"
"v_mfma_f32_16x16x32_bf16 v[132:135], a[148:151], v[76:79], v[132:135]\n"
"s_mov_b32 s61, s60\n"
"s_add_u32 m0, s78, 0x1c000\n"
"v_mfma_f32_16x16x32_bf16 v[132:135], a[152:155], v[80:83], v[132:135]\n"
"v_exp_f32 v175, v175\n"
"v_mfma_f32_16x16x32_bf16 v[132:135], a[156:159], v[84:87], v[132:135]\n"
"buffer_load_dwordx4 v12, s[20:23], s29 offen lds\n"
"v_cvt_pk_bf16_f32 v221, v174, v175\n"
"v_mfma_f32_16x16x32_bf16 v[144:147], a[160:163], v[56:59], v[224:227]\n"
"v_exp_f32 v184, v184\n"
"v_mfma_f32_16x16x32_bf16 v[144:147], a[164:167], v[60:63], v[144:147]\n"
"buffer_load_dwordx4 v13, s[20:23], s29 offen offset:1024 lds\n"
"v_readlane_b32 s40, v54, 0\n"
"v_mfma_f32_16x16x32_bf16 v[144:147], a[168:171], v[64:67], v[144:147]\n"
"v_exp_f32 v185, v185\n"
"v_mfma_f32_16x16x32_bf16 v[144:147], a[172:175], v[68:71], v[144:147]\n"
"buffer_load_dwordx4 v14, s[20:23], s29 offen offset:2048 lds\n"
"v_cvt_pk_bf16_f32 v214, v184, v185\n"
"v_mfma_f32_16x16x32_bf16 v[148:151], a[160:163], v[72:75], v[228:231]\n"
"v_exp_f32 v186, v186\n"
"v_mfma_f32_16x16x32_bf16 v[148:151], a[164:167], v[76:79], v[148:151]\n"
"s_waitcnt vmcnt(11)\n"
"v_readlane_b32 s41, v54, 1\n"
"v_mfma_f32_16x16x32_bf16 v[148:151], a[168:171], v[80:83], v[148:151]\n"
"v_exp_f32 v187, v187\n"
"v_mfma_f32_16x16x32_bf16 v[148:151], a[172:175], v[84:87], v[148:151]\n"
"buffer_load_dwordx4 v15, s[20:23], s29 offen offset:3072 lds\n"
"v_cvt_pk_bf16_f32 v215, v186, v187\n"
"v_mfma_f32_16x16x32_bf16 v[160:163], a[176:179], v[56:59], v[224:227]\n"
"v_exp_f32 v188, v188\n"
"v_mfma_f32_16x16x32_bf16 v[160:163], a[180:183], v[60:63], v[160:163]\n"
"ds_read_b64_tr_b16 v[240:241], v16\n"
"ds_read_b64_tr_b16 v[242:243], v16 offset:2048\n"
"v_mfma_f32_16x16x32_bf16 v[160:163], a[184:187], v[64:67], v[160:163]\n"
"v_exp_f32 v189, v189\n"
"v_mfma_f32_16x16x32_bf16 v[160:163], a[188:191], v[68:71], v[160:163]\n"
"ds_read_b64_tr_b16 v[244:245], v16 offset:32\n"
"v_cvt_pk_bf16_f32 v222, v188, v189\n"
"v_mfma_f32_16x16x32_bf16 v[164:167], a[176:179], v[72:75], v[228:231]\n"
"v_exp_f32 v190, v190\n"
"v_mfma_f32_16x16x32_bf16 v[164:167], a[180:183], v[76:79], v[164:167]\n"
"ds_read_b64_tr_b16 v[246:247], v16 offset:2080\n"
"ds_read_b64_tr_b16 v[248:249], v16 offset:64\n"
"v_mfma_f32_16x16x32_bf16 v[164:167], a[184:187], v[80:83], v[164:167]\n"
"v_exp_f32 v191, v191\n"
"v_mfma_f32_16x16x32_bf16 v[164:167], a[188:191], v[84:87], v[164:167]\n"
"ds_read_b64_tr_b16 v[250:251], v16 offset:2112\n"
"v_cvt_pk_bf16_f32 v223, v190, v191\n"
"v_mfma_f32_16x16x32_bf16 v[176:179], a[192:195], v[56:59], v[224:227]\n"
"v_exp_f32 v128, v128\n"
"v_mfma_f32_16x16x32_bf16 v[176:179], a[196:199], v[60:63], v[176:179]\n"
"ds_read_b64_tr_b16 v[252:253], v16 offset:96\n"
"ds_read_b64_tr_b16 v[254:255], v16 offset:2144\n"
"v_mfma_f32_16x16x32_bf16 v[176:179], a[200:203], v[64:67], v[176:179]\n"
"v_exp_f32 v129, v129\n"
"v_mfma_f32_16x16x32_bf16 v[176:179], a[204:207], v[68:71], v[176:179]\n"
"v_xor_b32 v3, 0x8000, v3\n"
"v_cvt_pk_bf16_f32 v192, v128, v129\n"
"v_mfma_f32_16x16x32_bf16 v[180:183], a[192:195], v[72:75], v[228:231]\n"
"v_exp_f32 v130, v130\n"
"v_mfma_f32_16x16x32_bf16 v[180:183], a[196:199], v[76:79], v[180:183]\n"
"v_readlane_b32 s42, v54, 2\n"
"v_readlane_b32 s43, v54, 3\n"
"v_mfma_f32_16x16x32_bf16 v[180:183], a[200:203], v[80:83], v[180:183]\n"
"v_exp_f32 v131, v131\n"
"v_mfma_f32_16x16x32_bf16 v[180:183], a[204:207], v[84:87], v[180:183]\n"
"v_cvt_pk_bf16_f32 v193, v130, v131\n"
"s_waitcnt lgkmcnt(0)\n"
"v_mfma_f32_16x16x32_bf16 a[64:67], a[208:211], v[208:211], a[64:67]\n"
"v_exp_f32 v132, v132\n"
"v_mfma_f32_16x16x32_bf16 a[64:67], a[212:215], v[212:215], a[64:67]\n"
"v_add_u32 v54, 0x90000, v54\n"
"s_and_b32 s58, s59, 32\n"
"v_mfma_f32_16x16x32_bf16 a[96:99], a[208:211], v[216:219], a[96:99]\n"
"s_add_u32 s58, s58, 32\n"
"s_nop 0\n"
"v_mfma_f32_16x16x32_bf16 a[96:99], a[212:215], v[220:223], a[96:99]\n"
"s_barrier\n"
"s_mul_i32 s58, s58, 0xa000\n"
"v_mfma_f32_16x16x32_bf16 a[68:71], a[216:219], v[208:211], a[68:71]\n"
"v_exp_f32 v133, v133\n"
"v_mfma_f32_16x16x32_bf16 a[68:71], a[220:223], v[212:215], a[68:71]\n"
"ds_read_b128 a[208:211], v3\n"
"v_cvt_pk_bf16_f32 v200, v132, v133\n"
"v_mfma_f32_16x16x32_bf16 a[100:103], a[216:219], v[216:219], a[100:103]\n"
"v_exp_f32 v134, v134\n"
"v_mfma_f32_16x16x32_bf16 a[100:103], a[220:223], v[220:223], a[100:103]\n"
"ds_read_b128 a[212:215], v3 offset:1024\n"
"s_mov_b32 m0, s62\n"
"v_mfma_f32_16x16x32_bf16 a[72:75], a[224:227], v[208:211], a[72:75]\n"
"v_exp_f32 v135, v135\n"
"v_mfma_f32_16x16x32_bf16 a[72:75], a[228:231], v[212:215], a[72:75]\n"
"ds_read_b128 a[216:219], v3 offset:2048\n"
"v_cvt_pk_bf16_f32 v201, v134, v135\n"
"v_mfma_f32_16x16x32_bf16 a[104:107], a[224:227], v[216:219], a[104:107]\n"
"v_exp_f32 v144, v144\n"
"v_mfma_f32_16x16x32_bf16 a[104:107], a[228:231], v[220:223], a[104:107]\n"
"ds_read_b128 a[220:223], v3 offset:3072\n"
"buffer_load_dwordx4 v1, s[4:7], s60 offen sc1 lds\n"
"v_mfma_f32_16x16x32_bf16 a[76:79], a[232:235], v[208:211], a[76:79]\n"
"v_exp_f32 v145, v145\n"
"v_mfma_f32_16x16x32_bf16 a[76:79], a[236:239], v[212:215], a[76:79]\n"
"ds_read_b128 a[224:227], v3 offset:4096\n"
"v_cvt_pk_bf16_f32 v194, v144, v145\n"
"v_mfma_f32_16x16x32_bf16 a[108:111], a[232:235], v[216:219], a[108:111]\n"
"v_exp_f32 v146, v146\n"
"v_mfma_f32_16x16x32_bf16 a[108:111], a[236:239], v[220:223], a[108:111]\n"
"ds_read_b128 a[228:231], v3 offset:5120\n"
"buffer_load_dwordx4 v1, s[4:7], s60 offen offset:1024 sc1 lds\n"
"v_mfma_f32_16x16x32_bf16 a[80:83], a[240:243], v[208:211], a[80:83]\n"
"v_exp_f32 v147, v147\n"
"v_mfma_f32_16x16x32_bf16 a[80:83], a[244:247], v[212:215], a[80:83]\n"
"ds_read_b128 a[232:235], v3 offset:6144\n"
"v_cvt_pk_bf16_f32 v195, v146, v147\n"
"v_mfma_f32_16x16x32_bf16 a[112:115], a[240:243], v[216:219], a[112:115]\n"
"v_exp_f32 v148, v148\n"
"v_mfma_f32_16x16x32_bf16 a[112:115], a[244:247], v[220:223], a[112:115]\n"
"ds_read_b128 a[236:239], v3 offset:7168\n"
"s_sub_u32 s29, s29, s58\n"
"v_mfma_f32_16x16x32_bf16 a[84:87], a[248:251], v[208:211], a[84:87]\n"
"v_exp_f32 v149, v149\n"
"v_mfma_f32_16x16x32_bf16 a[84:87], a[252:255], v[212:215], a[84:87]\n"
"ds_read_b128 a[240:243], v3 offset:8192\n"
"v_cvt_pk_bf16_f32 v202, v148, v149\n"
"v_mfma_f32_16x16x32_bf16 a[116:119], a[248:251], v[216:219], a[116:119]\n"
"v_exp_f32 v150, v150\n"
"v_mfma_f32_16x16x32_bf16 a[116:119], a[252:255], v[220:223], a[116:119]\n"
"ds_read_b128 a[244:247], v3 offset:9216\n"
"buffer_load_dwordx4 v1, s[4:7], s60 offen offset:2048 sc1 lds\n"
"v_mfma_f32_16x16x32_bf16 a[88:91], v[24:27], v[208:211], a[88:91]\n"
"v_exp_f32 v151, v151\n"
"v_mfma_f32_16x16x32_bf16 a[88:91], v[28:31], v[212:215], a[88:91]\n"
"ds_read_b128 a[248:251], v3 offset:10240\n"
"v_cvt_pk_bf16_f32 v203, v150, v151\n"
"v_mfma_f32_16x16x32_bf16 a[120:123], v[24:27], v[216:219], a[120:123]\n"
"v_exp_f32 v160, v160\n"
"v_mfma_f32_16x16x32_bf16 a[120:123], v[28:31], v[220:223], a[120:123]\n"
"ds_read_b128 a[252:255], v3 offset:11264\n"
"s_mov_b32 s20, s38\n"
"v_mfma_f32_16x16x32_bf16 a[92:95], v[32:35], v[208:211], a[92:95]\n"
"v_exp_f32 v161, v161\n"
"v_mfma_f32_16x16x32_bf16 a[92:95], v[36:39], v[212:215], a[92:95]\n"
"ds_read_b128 v[24:27], v3 offset:12288\n"
"v_cvt_pk_bf16_f32 v196, v160, v161\n"
"v_mfma_f32_16x16x32_bf16 a[124:127], v[32:35], v[216:219], a[124:127]\n"
"v_exp_f32 v162, v162\n"
"v_mfma_f32_16x16x32_bf16 a[124:127], v[36:39], v[220:223], a[124:127]\n"
"ds_read_b128 v[28:31], v3 offset:13312\n"
"buffer_load_dwordx4 v1, s[4:7], s60 offen offset:3072 sc1 lds\n"
"v_mfma_f32_16x16x32_bf16 a[136:139], v[4:7], v[208:211], a[136:139]\n"
"v_exp_f32 v163, v163\n"
"v_mfma_f32_16x16x32_bf16 a[136:139], v[4:7], v[212:215], a[136:139]\n"
"ds_read_b128 v[32:35], v3 offset:14336\n"
"v_cvt_pk_bf16_f32 v197, v162, v163\n"
"v_mfma_f32_16x16x32_bf16 a[140:143], v[4:7], v[216:219], a[140:143]\n"
"v_exp_f32 v164, v164\n"
"v_mfma_f32_16x16x32_bf16 a[140:143], v[4:7], v[220:223], a[140:143]\n"
"ds_read_b128 v[36:39], v3 offset:15360\n"
"s_and_b32 s21, s39, 0xffff\n"
"v_mfma_f32_16x16x32_bf16 v[136:139], a[144:147], v[88:91], v[232:235]\n"
"v_exp_f32 v165, v165\n"
"v_mfma_f32_16x16x32_bf16 v[136:139], a[148:151], v[92:95], v[136:139]\n"
"v_xor_b32 v2, 0x8000, v2\n"
"v_cvt_pk_bf16_f32 v204, v164, v165\n"
"v_mfma_f32_16x16x32_bf16 v[136:139], a[152:155], v[96:99], v[136:139]\n"
"v_exp_f32 v166, v166\n"
"v_mfma_f32_16x16x32_bf16 v[136:139], a[156:159], v[100:103], v[136:139]\n"
"s_mov_b32 s22, 0x21c000\n"
"s_lshl_b32 s33, s33, 2\n"
"v_mfma_f32_16x16x32_bf16 v[140:143], a[144:147], v[104:107], v[236:239]\n"
"v_exp_f32 v167, v167\n"
"v_mfma_f32_16x16x32_bf16 v[140:143], a[148:151], v[108:111], v[140:143]\n"
"v_cvt_pk_bf16_f32 v205, v166, v167\n"
"s_add_u32 s33, s33, s32\n"
"v_mfma_f32_16x16x32_bf16 v[140:143], a[152:155], v[112:115], v[140:143]\n"
"s_add_u32 s11, s33, s32\n"
"s_waitcnt vmcnt(12) lgkmcnt(0)\n"
"v_mfma_f32_16x16x32_bf16 v[140:143], a[156:159], v[116:119], v[140:143]\n"
"s_barrier\n"
"s_sub_u32 s11, s11, 8\n"
"v_mfma_f32_16x16x32_bf16 v[152:155], a[160:163], v[88:91], v[232:235]\n"
"v_exp_f32 v176, v176\n"
"v_mfma_f32_16x16x32_bf16 v[152:155], a[164:167], v[92:95], v[152:155]\n"
"ds_read_b128 a[144:147], v2\n"
"s_cmp_ge_u32 s32, 4\n"
"v_mfma_f32_16x16x32_bf16 v[152:155], a[168:171], v[96:99], v[152:155]\n"
"v_exp_f32 v177, v177\n"
"v_mfma_f32_16x16x32_bf16 v[152:155], a[172:175], v[100:103], v[152:155]\n"
"ds_read_b128 a[148:151], v2 offset:1024\n"
"v_cvt_pk_bf16_f32 v198, v176, v177\n"
"v_mfma_f32_16x16x32_bf16 v[156:159], a[160:163], v[104:107], v[236:239]\n"
"v_exp_f32 v178, v178\n"
"v_mfma_f32_16x16x32_bf16 v[156:159], a[164:167], v[108:111], v[156:159]\n"
"ds_read_b128 a[152:155], v2 offset:2048\n"
"s_cmov_b32 s33, s11\n"
"v_mfma_f32_16x16x32_bf16 v[156:159], a[168:171], v[112:115], v[156:159]\n"
"v_exp_f32 v179, v179\n"
"v_mfma_f32_16x16x32_bf16 v[156:159], a[172:175], v[116:119], v[156:159]\n"
"ds_read_b128 a[156:159], v2 offset:3072\n"
"v_cvt_pk_bf16_f32 v199, v178, v179\n"
"v_mfma_f32_16x16x32_bf16 v[168:171], a[176:179], v[88:91], v[232:235]\n"
"v_exp_f32 v180, v180\n"
"v_mfma_f32_16x16x32_bf16 v[168:171], a[180:183], v[92:95], v[168:171]\n"
"ds_read_b128 a[160:163], v2 offset:4096\n"
"s_cmp_ge_u32 s32, 4\n"
"v_mfma_f32_16x16x32_bf16 v[168:171], a[184:187], v[96:99], v[168:171]\n"
"v_exp_f32 v181, v181\n"
"v_mfma_f32_16x16x32_bf16 v[168:171], a[188:191], v[100:103], v[168:171]\n"
"ds_read_b128 a[164:167], v2 offset:5120\n"
"v_cvt_pk_bf16_f32 v206, v180, v181\n"
"v_mfma_f32_16x16x32_bf16 v[172:175], a[176:179], v[104:107], v[236:239]\n"
"v_exp_f32 v182, v182\n"
"v_mfma_f32_16x16x32_bf16 v[172:175], a[180:183], v[108:111], v[172:175]\n"
"ds_read_b128 a[168:171], v2 offset:6144\n"
"s_cselect_b32 s32, 1, 2\n"
"v_mfma_f32_16x16x32_bf16 v[172:175], a[184:187], v[112:115], v[172:175]\n"
"v_exp_f32 v183, v183\n"
"v_mfma_f32_16x16x32_bf16 v[172:175], a[188:191], v[116:119], v[172:175]\n"
"ds_read_b128 a[172:175], v2 offset:7168\n"
"v_cvt_pk_bf16_f32 v207, v182, v183\n"
"v_mfma_f32_16x16x32_bf16 v[184:187], a[192:195], v[88:91], v[232:235]\n"
"s_sub_u32 s11, s31, 24\n"
"s_lshl_b32 s11, s11, 2\n"
"v_mfma_f32_16x16x32_bf16 v[184:187], a[196:199], v[92:95], v[184:187]\n"
"ds_read_b128 a[176:179], v2 offset:8192\n"
"s_cmp_ge_u32 s31, 24\n"
"v_mfma_f32_16x16x32_bf16 v[184:187], a[200:203], v[96:99], v[184:187]\n"
"v_exp_f32 v136, v136\n"
"v_mfma_f32_16x16x32_bf16 v[184:187], a[204:207], v[100:103], v[184:187]\n"
"ds_read_b128 a[180:183], v2 offset:9216\n"
"s_cmov_b32 s33, s11\n"
"v_mfma_f32_16x16x32_bf16 v[188:191], a[192:195], v[104:107], v[236:239]\n"
"v_exp_f32 v137, v137\n"
"v_mfma_f32_16x16x32_bf16 v[188:191], a[196:199], v[108:111], v[188:191]\n"
"ds_read_b128 a[184:187], v2 offset:10240\n"
"v_cvt_pk_bf16_f32 v208, v136, v137\n"
"v_mfma_f32_16x16x32_bf16 v[188:191], a[200:203], v[112:115], v[188:191]\n"
"v_exp_f32 v138, v138\n"
"v_mfma_f32_16x16x32_bf16 v[188:191], a[204:207], v[116:119], v[188:191]\n"
"ds_read_b128 a[188:191], v2 offset:11264\n"
"s_cmp_ge_u32 s31, 24\n"
"v_mfma_f32_16x16x32_bf16 a[0:3], a[208:211], v[192:195], a[0:3]\n"
"v_exp_f32 v139, v139\n"
"v_mfma_f32_16x16x32_bf16 a[0:3], a[212:215], v[196:199], a[0:3]\n"
"ds_read_b128 a[192:195], v2 offset:12288\n"
"v_cvt_pk_bf16_f32 v209, v138, v139\n"
"v_mfma_f32_16x16x32_bf16 a[32:35], a[208:211], v[200:203], a[32:35]\n"
"v_exp_f32 v140, v140\n"
"v_mfma_f32_16x16x32_bf16 a[32:35], a[212:215], v[204:207], a[32:35]\n"
"ds_read_b128 a[196:199], v2 offset:13312\n"
"s_mov_b32 m0, s63\n"
"v_mfma_f32_16x16x32_bf16 a[4:7], a[216:219], v[192:195], a[4:7]\n"
"v_exp_f32 v141, v141\n"
"v_mfma_f32_16x16x32_bf16 a[4:7], a[220:223], v[196:199], a[4:7]\n"
"ds_read_b128 a[200:203], v2 offset:14336\n"
"v_cvt_pk_bf16_f32 v216, v140, v141\n"
"v_mfma_f32_16x16x32_bf16 a[36:39], a[216:219], v[200:203], a[36:39]\n"
"v_exp_f32 v142, v142\n"
"v_mfma_f32_16x16x32_bf16 a[36:39], a[220:223], v[204:207], a[36:39]\n"
"ds_read_b128 a[204:207], v2 offset:15360\n"
"buffer_load_dwordx4 v1, s[16:19], s60 offen sc1 lds\n"
"v_mfma_f32_16x16x32_bf16 a[8:11], a[224:227], v[192:195], a[8:11]\n"
"v_exp_f32 v143, v143\n"
"v_mfma_f32_16x16x32_bf16 a[8:11], a[228:231], v[196:199], a[8:11]\n"
"buffer_load_dwordx4 v1, s[16:19], s60 offen offset:1024 sc1 lds\n"
"v_cvt_pk_bf16_f32 v217, v142, v143\n"
"v_mfma_f32_16x16x32_bf16 a[40:43], a[224:227], v[200:203], a[40:43]\n"
"v_exp_f32 v152, v152\n"
"v_mfma_f32_16x16x32_bf16 a[40:43], a[228:231], v[204:207], a[40:43]\n"
"buffer_load_dwordx4 v1, s[16:19], s60 offen offset:2048 sc1 lds\n"
"s_cmov_b32 s32, 0\n"
"v_mfma_f32_16x16x32_bf16 a[12:15], a[232:235], v[192:195], a[12:15]\n"
"v_exp_f32 v153, v153\n"
"v_mfma_f32_16x16x32_bf16 a[12:15], a[236:239], v[196:199], a[12:15]\n"
"buffer_load_dwordx4 v1, s[16:19], s60 offen offset:3072 sc1 lds\n"
"v_cvt_pk_bf16_f32 v210, v152, v153\n"
"v_mfma_f32_16x16x32_bf16 a[44:47], a[232:235], v[200:203], a[44:47]\n"
"v_exp_f32 v154, v154\n"
"v_mfma_f32_16x16x32_bf16 a[44:47], a[236:239], v[204:207], a[44:47]\n"
"s_lshl_b32 s11, 2, s32\n"
"s_sub_u32 s11, 8, s11\n"
"v_mfma_f32_16x16x32_bf16 a[16:19], a[240:243], v[192:195], a[16:19]\n"
"v_exp_f32 v155, v155\n"
"v_mfma_f32_16x16x32_bf16 a[16:19], a[244:247], v[196:199], a[16:19]\n"
"buffer_store_dwordx4 v[240:243], v1, s[4:7], s40 offen sc1\n"
"v_cvt_pk_bf16_f32 v211, v154, v155\n"
"v_mfma_f32_16x16x32_bf16 a[48:51], a[240:243], v[200:203], a[48:51]\n"
"v_exp_f32 v156, v156\n"
"v_mfma_f32_16x16x32_bf16 a[48:51], a[244:247], v[204:207], a[48:51]\n"
"v_lshrrev_b32 v17, s32, v0\n"
"v_mfma_f32_16x16x32_bf16 a[20:23], a[248:251], v[192:195], a[20:23]\n"
"v_exp_f32 v157, v157\n"
"v_mfma_f32_16x16x32_bf16 a[20:23], a[252:255], v[196:199], a[20:23]\n"
"buffer_store_dwordx4 v[244:247], v1, s[4:7], s41 offen sc1\n"
"v_cvt_pk_bf16_f32 v218, v156, v157\n"
"v_mfma_f32_16x16x32_bf16 a[52:55], a[248:251], v[200:203], a[52:55]\n"
"v_exp_f32 v158, v158\n"
"v_mfma_f32_16x16x32_bf16 a[52:55], a[252:255], v[204:207], a[52:55]\n"
"v_mfma_f32_16x16x32_bf16 a[24:27], v[24:27], v[192:195], a[24:27]\n"
"v_exp_f32 v159, v159\n"
"v_mfma_f32_16x16x32_bf16 a[24:27], v[28:31], v[196:199], a[24:27]\n"
"buffer_store_dwordx4 v[248:251], v1, s[4:7], s42 offen sc1\n"
"v_cvt_pk_bf16_f32 v219, v158, v159\n"
"v_mfma_f32_16x16x32_bf16 a[56:59], v[24:27], v[200:203], a[56:59]\n"
"v_exp_f32 v168, v168\n"
"v_mfma_f32_16x16x32_bf16 a[56:59], v[28:31], v[204:207], a[56:59]\n"
"v_mfma_f32_16x16x32_bf16 a[28:31], v[32:35], v[192:195], a[28:31]\n"
"v_exp_f32 v169, v169\n"
"v_mfma_f32_16x16x32_bf16 a[28:31], v[36:39], v[196:199], a[28:31]\n"
"buffer_store_dwordx4 v[252:255], v1, s[4:7], s43 offen sc1\n"
"v_cvt_pk_bf16_f32 v212, v168, v169\n"
"v_mfma_f32_16x16x32_bf16 a[60:63], v[32:35], v[200:203], a[60:63]\n"
"v_exp_f32 v170, v170\n"
"v_mfma_f32_16x16x32_bf16 a[60:63], v[36:39], v[204:207], a[60:63]\n"
"v_mfma_f32_16x16x32_bf16 a[128:131], v[4:7], v[192:195], a[128:131]\n"
"v_exp_f32 v171, v171\n"
"v_mfma_f32_16x16x32_bf16 a[128:131], v[4:7], v[196:199], a[128:131]\n"
"v_cvt_pk_bf16_f32 v213, v170, v171\n"
"s_waitcnt lgkmcnt(0)\n"
"v_mfma_f32_16x16x32_bf16 a[132:135], v[4:7], v[200:203], a[132:135]\n"
"v_mfma_f32_16x16x32_bf16 a[132:135], v[4:7], v[204:207], a[132:135]\n"
"v_mfma_f32_16x16x32_bf16 v[128:131], a[144:147], v[56:59], v[224:227]\n"
"v_exp_f32 v172, v172\n"
"v_mfma_f32_16x16x32_bf16 v[128:131], a[148:151], v[60:63], v[128:131]\n"
"s_add_u32 s60, s60, 0x8000\n"
"s_xor_b32 s62, s62, 0x8000\n"
"v_mfma_f32_16x16x32_bf16 v[128:131], a[152:155], v[64:67], v[128:131]\n"
"v_exp_f32 v173, v173\n"
"v_mfma_f32_16x16x32_bf16 v[128:131], a[156:159], v[68:71], v[128:131]\n"
"v_cvt_pk_bf16_f32 v220, v172, v173\n"
"s_xor_b32 s63, s63, 0x8000\n"
"v_mfma_f32_16x16x32_bf16 v[132:135], a[144:147], v[72:75], v[228:231]\n"
"v_exp_f32 v174, v174\n"
"v_mfma_f32_16x16x32_bf16 v[132:135], a[148:151], v[76:79], v[132:135]\n"
"s_mov_b32 s61, s60\n"
"s_add_u32 m0, s78, 0x14000\n"
"v_mfma_f32_16x16x32_bf16 v[132:135], a[152:155], v[80:83], v[132:135]\n"
"v_exp_f32 v175, v175\n"
"v_mfma_f32_16x16x32_bf16 v[132:135], a[156:159], v[84:87], v[132:135]\n"
"buffer_load_dwordx4 v12, s[20:23], s29 offen lds\n"
"v_cvt_pk_bf16_f32 v221, v174, v175\n"
"v_mfma_f32_16x16x32_bf16 v[144:147], a[160:163], v[56:59], v[224:227]\n"
"v_exp_f32 v184, v184\n"
"v_mfma_f32_16x16x32_bf16 v[144:147], a[164:167], v[60:63], v[144:147]\n"
"buffer_load_dwordx4 v13, s[20:23], s29 offen offset:1024 lds\n"
"v_readlane_b32 s40, v54, 0\n"
"v_mfma_f32_16x16x32_bf16 v[144:147], a[168:171], v[64:67], v[144:147]\n"
"v_exp_f32 v185, v185\n"
"v_mfma_f32_16x16x32_bf16 v[144:147], a[172:175], v[68:71], v[144:147]\n"
"buffer_load_dwordx4 v14, s[20:23], s29 offen offset:2048 lds\n"
"v_cvt_pk_bf16_f32 v214, v184, v185\n"
"v_mfma_f32_16x16x32_bf16 v[148:151], a[160:163], v[72:75], v[228:231]\n"
"v_exp_f32 v186, v186\n"
"v_mfma_f32_16x16x32_bf16 v[148:151], a[164:167], v[76:79], v[148:151]\n"
"s_waitcnt vmcnt(15)\n"
"v_readlane_b32 s41, v54, 1\n"
"v_mfma_f32_16x16x32_bf16 v[148:151], a[168:171], v[80:83], v[148:151]\n"
"v_exp_f32 v187, v187\n"
"v_mfma_f32_16x16x32_bf16 v[148:151], a[172:175], v[84:87], v[148:151]\n"
"buffer_load_dwordx4 v15, s[20:23], s29 offen offset:3072 lds\n"
"v_cvt_pk_bf16_f32 v215, v186, v187\n"
"v_mfma_f32_16x16x32_bf16 v[160:163], a[176:179], v[56:59], v[224:227]\n"
"v_exp_f32 v188, v188\n"
"v_mfma_f32_16x16x32_bf16 v[160:163], a[180:183], v[60:63], v[160:163]\n"
"ds_read_b64_tr_b16 v[240:241], v16 offset:32768\n"
"ds_read_b64_tr_b16 v[242:243], v16 offset:34816\n"
"v_mfma_f32_16x16x32_bf16 v[160:163], a[184:187], v[64:67], v[160:163]\n"
"v_exp_f32 v189, v189\n"
"v_mfma_f32_16x16x32_bf16 v[160:163], a[188:191], v[68:71], v[160:163]\n"
"ds_read_b64_tr_b16 v[244:245], v16 offset:32800\n"
"v_cvt_pk_bf16_f32 v222, v188, v189\n"
"v_mfma_f32_16x16x32_bf16 v[164:167], a[176:179], v[72:75], v[228:231]\n"
"v_exp_f32 v190, v190\n"
"v_mfma_f32_16x16x32_bf16 v[164:167], a[180:183], v[76:79], v[164:167]\n"
"ds_read_b64_tr_b16 v[246:247], v16 offset:34848\n"
"ds_read_b64_tr_b16 v[248:249], v16 offset:32832\n"
"v_mfma_f32_16x16x32_bf16 v[164:167], a[184:187], v[80:83], v[164:167]\n"
"v_exp_f32 v191, v191\n"
"v_mfma_f32_16x16x32_bf16 v[164:167], a[188:191], v[84:87], v[164:167]\n"
"ds_read_b64_tr_b16 v[250:251], v16 offset:34880\n"
"v_cvt_pk_bf16_f32 v223, v190, v191\n"
"v_mfma_f32_16x16x32_bf16 v[176:179], a[192:195], v[56:59], v[224:227]\n"
"v_exp_f32 v128, v128\n"
"v_mfma_f32_16x16x32_bf16 v[176:179], a[196:199], v[60:63], v[176:179]\n"
"ds_read_b64_tr_b16 v[252:253], v16 offset:32864\n"
"ds_read_b64_tr_b16 v[254:255], v16 offset:34912\n"
"v_mfma_f32_16x16x32_bf16 v[176:179], a[200:203], v[64:67], v[176:179]\n"
"v_exp_f32 v129, v129\n"
"v_mfma_f32_16x16x32_bf16 v[176:179], a[204:207], v[68:71], v[176:179]\n"
"v_xor_b32 v3, 0x8000, v3\n"
"v_cvt_pk_bf16_f32 v192, v128, v129\n"
"v_mfma_f32_16x16x32_bf16 v[180:183], a[192:195], v[72:75], v[228:231]\n"
"v_exp_f32 v130, v130\n"
"v_mfma_f32_16x16x32_bf16 v[180:183], a[196:199], v[76:79], v[180:183]\n"
"v_readlane_b32 s42, v54, 2\n"
"v_readlane_b32 s43, v54, 3\n"
"v_mfma_f32_16x16x32_bf16 v[180:183], a[200:203], v[80:83], v[180:183]\n"
"v_exp_f32 v131, v131\n"
"v_mfma_f32_16x16x32_bf16 v[180:183], a[204:207], v[84:87], v[180:183]\n"
"v_cvt_pk_bf16_f32 v193, v130, v131\n"
"s_waitcnt lgkmcnt(0)\n"
"v_mfma_f32_16x16x32_bf16 a[64:67], a[208:211], v[208:211], a[64:67]\n"
"v_exp_f32 v132, v132\n"
"v_mfma_f32_16x16x32_bf16 a[64:67], a[212:215], v[212:215], a[64:67]\n"
"s_mul_i32 s58, s59, 43\n"
"s_lshr_b32 s58, s58, 9\n"
"v_mfma_f32_16x16x32_bf16 a[96:99], a[208:211], v[216:219], a[96:99]\n"
"s_mul_i32 s83, s58, 12\n"
"s_nop 0\n"
"v_mfma_f32_16x16x32_bf16 a[96:99], a[212:215], v[220:223], a[96:99]\n"
"s_barrier\n"
"s_sub_u32 s83, s59, s83\n"
"v_mfma_f32_16x16x32_bf16 a[68:71], a[216:219], v[208:211], a[68:71]\n"
"v_exp_f32 v133, v133\n"
"v_mfma_f32_16x16x32_bf16 a[68:71], a[220:223], v[212:215], a[68:71]\n"
"ds_read_b128 a[208:211], v3\n"
"v_cvt_pk_bf16_f32 v200, v132, v133\n"
"v_mfma_f32_16x16x32_bf16 a[100:103], a[216:219], v[216:219], a[100:103]\n"
"v_exp_f32 v134, v134\n"
"v_mfma_f32_16x16x32_bf16 a[100:103], a[220:223], v[220:223], a[100:103]\n"
"ds_read_b128 a[212:215], v3 offset:1024\n"
"s_mov_b32 m0, s62\n"
"v_mfma_f32_16x16x32_bf16 a[72:75], a[224:227], v[208:211], a[72:75]\n"
"v_exp_f32 v135, v135\n"
"v_mfma_f32_16x16x32_bf16 a[72:75], a[228:231], v[212:215], a[72:75]\n"
"ds_read_b128 a[216:219], v3 offset:2048\n"
"v_cvt_pk_bf16_f32 v201, v134, v135\n"
"v_mfma_f32_16x16x32_bf16 a[104:107], a[224:227], v[216:219], a[104:107]\n"
"v_exp_f32 v144, v144\n"
"v_mfma_f32_16x16x32_bf16 a[104:107], a[228:231], v[220:223], a[104:107]\n"
"ds_read_b128 a[220:223], v3 offset:3072\n"
"buffer_load_dwordx4 v1, s[4:7], s60 offen sc1 lds\n"
"v_mfma_f32_16x16x32_bf16 a[76:79], a[232:235], v[208:211], a[76:79]\n"
"v_exp_f32 v145, v145\n"
"v_mfma_f32_16x16x32_bf16 a[76:79], a[236:239], v[212:215], a[76:79]\n"
"ds_read_b128 a[224:227], v3 offset:4096\n"
"v_cvt_pk_bf16_f32 v194, v144, v145\n"
"v_mfma_f32_16x16x32_bf16 a[108:111], a[232:235], v[216:219], a[108:111]\n"
"v_exp_f32 v146, v146\n"
"v_mfma_f32_16x16x32_bf16 a[108:111], a[236:239], v[220:223], a[108:111]\n"
"ds_read_b128 a[228:231], v3 offset:5120\n"
"buffer_load_dwordx4 v1, s[4:7], s60 offen offset:1024 sc1 lds\n"
"v_mfma_f32_16x16x32_bf16 a[80:83], a[240:243], v[208:211], a[80:83]\n"
"v_exp_f32 v147, v147\n"
"v_mfma_f32_16x16x32_bf16 a[80:83], a[244:247], v[212:215], a[80:83]\n"
"ds_read_b128 a[232:235], v3 offset:6144\n"
"v_cvt_pk_bf16_f32 v195, v146, v147\n"
"v_mfma_f32_16x16x32_bf16 a[112:115], a[240:243], v[216:219], a[112:115]\n"
"v_exp_f32 v148, v148\n"
"v_mfma_f32_16x16x32_bf16 a[112:115], a[244:247], v[220:223], a[112:115]\n"
"ds_read_b128 a[236:239], v3 offset:7168\n"
"s_lshr_b32 s59, s83, 1\n"
"v_mfma_f32_16x16x32_bf16 a[84:87], a[248:251], v[208:211], a[84:87]\n"
"v_exp_f32 v149, v149\n"
"v_mfma_f32_16x16x32_bf16 a[84:87], a[252:255], v[212:215], a[84:87]\n"
"ds_read_b128 a[240:243], v3 offset:8192\n"
"v_cvt_pk_bf16_f32 v202, v148, v149\n"
"v_mfma_f32_16x16x32_bf16 a[116:119], a[248:251], v[216:219], a[116:119]\n"
"v_exp_f32 v150, v150\n"
"v_mfma_f32_16x16x32_bf16 a[116:119], a[252:255], v[220:223], a[116:119]\n"
"ds_read_b128 a[244:247], v3 offset:9216\n"
"buffer_load_dwordx4 v1, s[4:7], s60 offen offset:2048 sc1 lds\n"
"v_mfma_f32_16x16x32_bf16 a[88:91], v[24:27], v[208:211], a[88:91]\n"
"v_exp_f32 v151, v151\n"
"v_mfma_f32_16x16x32_bf16 a[88:91], v[28:31], v[212:215], a[88:91]\n"
"ds_read_b128 a[248:251], v3 offset:10240\n"
"v_cvt_pk_bf16_f32 v203, v150, v151\n"
"v_mfma_f32_16x16x32_bf16 a[120:123], v[24:27], v[216:219], a[120:123]\n"
"v_exp_f32 v160, v160\n"
"v_mfma_f32_16x16x32_bf16 a[120:123], v[28:31], v[220:223], a[120:123]\n"
"ds_read_b128 a[252:255], v3 offset:11264\n"
"s_cmp_ge_u32 s58, 4\n"
"v_mfma_f32_16x16x32_bf16 a[92:95], v[32:35], v[208:211], a[92:95]\n"
"v_exp_f32 v161, v161\n"
"v_mfma_f32_16x16x32_bf16 a[92:95], v[36:39], v[212:215], a[92:95]\n"
"ds_read_b128 v[24:27], v3 offset:12288\n"
"v_cvt_pk_bf16_f32 v196, v160, v161\n"
"v_mfma_f32_16x16x32_bf16 a[124:127], v[32:35], v[216:219], a[124:127]\n"
"v_exp_f32 v162, v162\n"
"v_mfma_f32_16x16x32_bf16 a[124:127], v[36:39], v[220:223], a[124:127]\n"
"ds_read_b128 v[28:31], v3 offset:13312\n"
"buffer_load_dwordx4 v1, s[4:7], s60 offen offset:3072 sc1 lds\n"
"v_mfma_f32_16x16x32_bf16 a[136:139], v[4:7], v[208:211], a[136:139]\n"
"v_exp_f32 v163, v163\n"
"v_mfma_f32_16x16x32_bf16 a[136:139], v[4:7], v[212:215], a[136:139]\n"
"ds_read_b128 v[32:35], v3 offset:14336\n"
"v_cvt_pk_bf16_f32 v197, v162, v163\n"
"v_mfma_f32_16x16x32_bf16 a[140:143], v[4:7], v[216:219], a[140:143]\n"
"v_exp_f32 v164, v164\n"
"v_mfma_f32_16x16x32_bf16 a[140:143], v[4:7], v[220:223], a[140:143]\n"
"ds_read_b128 v[36:39], v3 offset:15360\n"
"s_cmov_b32 s58, s59\n"
"v_mfma_f32_16x16x32_bf16 v[136:139], a[144:147], v[88:91], v[232:235]\n"
"v_exp_f32 v165, v165\n"
"v_mfma_f32_16x16x32_bf16 v[136:139], a[148:151], v[92:95], v[136:139]\n"
"v_xor_b32 v2, 0x8000, v2\n"
"v_cvt_pk_bf16_f32 v204, v164, v165\n"
"v_mfma_f32_16x16x32_bf16 v[136:139], a[152:155], v[96:99], v[136:139]\n"
"v_exp_f32 v166, v166\n"
"v_mfma_f32_16x16x32_bf16 v[136:139], a[156:159], v[100:103], v[136:139]\n"
"s_and_b32 s59, s83, 1\n"
"s_add_u32 s59, s59, 12\n"
"v_mfma_f32_16x16x32_bf16 v[140:143], a[144:147], v[104:107], v[236:239]\n"
"v_exp_f32 v167, v167\n"
"v_mfma_f32_16x16x32_bf16 v[140:143], a[148:151], v[108:111], v[140:143]\n"
"v_cvt_pk_bf16_f32 v205, v166, v167\n"
"s_cmp_ge_u32 s26, 0x1e0\n"
"v_mfma_f32_16x16x32_bf16 v[140:143], a[152:155], v[112:115], v[140:143]\n"
"s_cmov_b32 s83, s59\n"
"s_waitcnt vmcnt(16) lgkmcnt(0)\n"
"v_mfma_f32_16x16x32_bf16 v[140:143], a[156:159], v[116:119], v[140:143]\n"
"s_barrier\n"
"s_mul_i32 s83, s83, 0x1400\n"
"v_mfma_f32_16x16x32_bf16 v[152:155], a[160:163], v[88:91], v[232:235]\n"
"v_exp_f32 v176, v176\n"
"v_mfma_f32_16x16x32_bf16 v[152:155], a[164:167], v[92:95], v[152:155]\n"
"ds_read_b128 a[144:147], v2\n"
"s_mul_i32 s58, s58, 0x48000\n"
"v_mfma_f32_16x16x32_bf16 v[152:155], a[168:171], v[96:99], v[152:155]\n"
"v_exp_f32 v177, v177\n"
"v_mfma_f32_16x16x32_bf16 v[152:155], a[172:175], v[100:103], v[152:155]\n"
"ds_read_b128 a[148:151], v2 offset:1024\n"
"v_cvt_pk_bf16_f32 v198, v176, v177\n"
"v_mfma_f32_16x16x32_bf16 v[156:159], a[160:163], v[104:107], v[236:239]\n"
"v_exp_f32 v178, v178\n"
"v_mfma_f32_16x16x32_bf16 v[156:159], a[164:167], v[108:111], v[156:159]\n"
"ds_read_b128 a[152:155], v2 offset:2048\n"
"s_add_u32 s58, s58, 0x2800000\n"
"v_mfma_f32_16x16x32_bf16 v[156:159], a[168:171], v[112:115], v[156:159]\n"
"v_exp_f32 v179, v179\n"
"v_mfma_f32_16x16x32_bf16 v[156:159], a[172:175], v[116:119], v[156:159]\n"
"ds_read_b128 a[156:159], v2 offset:3072\n"
"v_cvt_pk_bf16_f32 v199, v178, v179\n"
"v_mfma_f32_16x16x32_bf16 v[168:171], a[176:179], v[88:91], v[232:235]\n"
"v_exp_f32 v180, v180\n"
"v_mfma_f32_16x16x32_bf16 v[168:171], a[180:183], v[92:95], v[168:171]\n"
"ds_read_b128 a[160:163], v2 offset:4096\n"
"s_cmp_ge_u32 s26, 0x21c\n"
"v_mfma_f32_16x16x32_bf16 v[168:171], a[184:187], v[96:99], v[168:171]\n"
"v_exp_f32 v181, v181\n"
"v_mfma_f32_16x16x32_bf16 v[168:171], a[188:191], v[100:103], v[168:171]\n"
"ds_read_b128 a[164:167], v2 offset:5120\n"
"v_cvt_pk_bf16_f32 v206, v180, v181\n"
"v_mfma_f32_16x16x32_bf16 v[172:175], a[176:179], v[104:107], v[236:239]\n"
"v_exp_f32 v182, v182\n"
"v_mfma_f32_16x16x32_bf16 v[172:175], a[180:183], v[108:111], v[172:175]\n"
"ds_read_b128 a[168:171], v2 offset:6144\n"
"s_cmov_b32 s58, 0x40000000\n"
"v_mfma_f32_16x16x32_bf16 v[172:175], a[184:187], v[112:115], v[172:175]\n"
"v_exp_f32 v183, v183\n"
"v_mfma_f32_16x16x32_bf16 v[172:175], a[188:191], v[116:119], v[172:175]\n"
"ds_read_b128 a[172:175], v2 offset:7168\n"
"v_cvt_pk_bf16_f32 v207, v182, v183\n"
"v_mfma_f32_16x16x32_bf16 v[184:187], a[192:195], v[88:91], v[232:235]\n"
"v_lshl_add_u32 v54, v19, 10, s83\n"
"v_and_b32 v19, 0xffffe000, v54\n"
"v_mfma_f32_16x16x32_bf16 v[184:187], a[196:199], v[92:95], v[184:187]\n"
"ds_read_b128 a[176:179], v2 offset:8192\n"
"v_add_u32 v54, v54, v19\n"
"v_mfma_f32_16x16x32_bf16 v[184:187], a[200:203], v[96:99], v[184:187]\n"
"v_exp_f32 v136, v136\n"
"v_mfma_f32_16x16x32_bf16 v[184:187], a[204:207], v[100:103], v[184:187]\n"
"ds_read_b128 a[180:183], v2 offset:9216\n"
"v_lshl_add_u32 v19, v52, 20, s58\n"
"v_mfma_f32_16x16x32_bf16 v[188:191], a[192:195], v[104:107], v[236:239]\n"
"v_exp_f32 v137, v137\n"
"v_mfma_f32_16x16x32_bf16 v[188:191], a[196:199], v[108:111], v[188:191]\n"
"ds_read_b128 a[184:187], v2 offset:10240\n"
"v_cvt_pk_bf16_f32 v208, v136, v137\n"
"v_mfma_f32_16x16x32_bf16 v[188:191], a[200:203], v[112:115], v[188:191]\n"
"v_exp_f32 v138, v138\n"
"v_mfma_f32_16x16x32_bf16 v[188:191], a[204:207], v[116:119], v[188:191]\n"
"ds_read_b128 a[188:191], v2 offset:11264\n"
"v_add_u32 v54, v54, v19\n"
"v_mfma_f32_16x16x32_bf16 a[0:3], a[208:211], v[192:195], a[0:3]\n"
"v_exp_f32 v139, v139\n"
"v_mfma_f32_16x16x32_bf16 a[0:3], a[212:215], v[196:199], a[0:3]\n"
"ds_read_b128 a[192:195], v2 offset:12288\n"
"v_cvt_pk_bf16_f32 v209, v138, v139\n"
"v_mfma_f32_16x16x32_bf16 a[32:35], a[208:211], v[200:203], a[32:35]\n"
"v_exp_f32 v140, v140\n"
"v_mfma_f32_16x16x32_bf16 a[32:35], a[212:215], v[204:207], a[32:35]\n"
"ds_read_b128 a[196:199], v2 offset:13312\n"
"s_mov_b32 m0, s63\n"
"v_mfma_f32_16x16x32_bf16 a[4:7], a[216:219], v[192:195], a[4:7]\n"
"v_exp_f32 v141, v141\n"
"v_mfma_f32_16x16x32_bf16 a[4:7], a[220:223], v[196:199], a[4:7]\n"
"ds_read_b128 a[200:203], v2 offset:14336\n"
"v_cvt_pk_bf16_f32 v216, v140, v141\n"
"v_mfma_f32_16x16x32_bf16 a[36:39], a[216:219], v[200:203], a[36:39]\n"
"v_exp_f32 v142, v142\n"
"v_mfma_f32_16x16x32_bf16 a[36:39], a[220:223], v[204:207], a[36:39]\n"
"ds_read_b128 a[204:207], v2 offset:15360\n"
"buffer_load_dwordx4 v1, s[16:19], s60 offen sc1 lds\n"
"v_mfma_f32_16x16x32_bf16 a[8:11], a[224:227], v[192:195], a[8:11]\n"
"v_exp_f32 v143, v143\n"
"v_mfma_f32_16x16x32_bf16 a[8:11], a[228:231], v[196:199], a[8:11]\n"
"buffer_load_dwordx4 v1, s[16:19], s60 offen offset:1024 sc1 lds\n"
"v_cvt_pk_bf16_f32 v217, v142, v143\n"
"v_mfma_f32_16x16x32_bf16 a[40:43], a[224:227], v[200:203], a[40:43]\n"
"v_exp_f32 v152, v152\n"
"v_mfma_f32_16x16x32_bf16 a[40:43], a[228:231], v[204:207], a[40:43]\n"
"buffer_load_dwordx4 v1, s[16:19], s60 offen offset:2048 sc1 lds\n"
"s_mov_b32 s20, s36\n"
"v_mfma_f32_16x16x32_bf16 a[12:15], a[232:235], v[192:195], a[12:15]\n"
"v_exp_f32 v153, v153\n"
"v_mfma_f32_16x16x32_bf16 a[12:15], a[236:239], v[196:199], a[12:15]\n"
"buffer_load_dwordx4 v1, s[16:19], s60 offen offset:3072 sc1 lds\n"
"v_cvt_pk_bf16_f32 v210, v152, v153\n"
"v_mfma_f32_16x16x32_bf16 a[44:47], a[232:235], v[200:203], a[44:47]\n"
"v_exp_f32 v154, v154\n"
"v_mfma_f32_16x16x32_bf16 a[44:47], a[236:239], v[204:207], a[44:47]\n"
"s_and_b32 s21, s37, 0xffff\n"
"s_mov_b32 s22, 0x21c000\n"
"v_mfma_f32_16x16x32_bf16 a[16:19], a[240:243], v[192:195], a[16:19]\n"
"v_exp_f32 v155, v155\n"
"v_mfma_f32_16x16x32_bf16 a[16:19], a[244:247], v[196:199], a[16:19]\n"
"buffer_store_dwordx4 v[240:243], v1, s[4:7], s40 offen sc1\n"
"v_cvt_pk_bf16_f32 v211, v154, v155\n"
"v_mfma_f32_16x16x32_bf16 a[48:51], a[240:243], v[200:203], a[48:51]\n"
"v_exp_f32 v156, v156\n"
"v_mfma_f32_16x16x32_bf16 a[48:51], a[244:247], v[204:207], a[48:51]\n"
"v_lshrrev_b32 v53, 3, v0\n"
"v_mul_u32_u24 v12, 0xd80, v53\n"
"v_mfma_f32_16x16x32_bf16 a[20:23], a[248:251], v[192:195], a[20:23]\n"
"v_exp_f32 v157, v157\n"
"v_mfma_f32_16x16x32_bf16 a[20:23], a[252:255], v[196:199], a[20:23]\n"
"buffer_store_dwordx4 v[244:247], v1, s[4:7], s41 offen sc1\n"
"v_cvt_pk_bf16_f32 v218, v156, v157\n"
"v_mfma_f32_16x16x32_bf16 a[52:55], a[248:251], v[200:203], a[52:55]\n"
"v_exp_f32 v158, v158\n"
"v_mfma_f32_16x16x32_bf16 a[52:55], a[252:255], v[204:207], a[52:55]\n"
"v_and_b32 v53, 7, v0\n"
"v_lshl_add_u32 v12, v53, 4, v12\n"
"v_mfma_f32_16x16x32_bf16 a[24:27], v[24:27], v[192:195], a[24:27]\n"
"v_exp_f32 v159, v159\n"
"v_mfma_f32_16x16x32_bf16 a[24:27], v[28:31], v[196:199], a[24:27]\n"
"buffer_store_dwordx4 v[248:251], v1, s[4:7], s42 offen sc1\n"
"v_cvt_pk_bf16_f32 v219, v158, v159\n"
"v_mfma_f32_16x16x32_bf16 a[56:59], v[24:27], v[200:203], a[56:59]\n"
"v_exp_f32 v168, v168\n"
"v_mfma_f32_16x16x32_bf16 a[56:59], v[28:31], v[204:207], a[56:59]\n"
"v_add_u32 v13, 0x6800, v12\n"
"v_add_u32 v14, 0xd000, v12\n"
"v_mfma_f32_16x16x32_bf16 a[28:31], v[32:35], v[192:195], a[28:31]\n"
"v_exp_f32 v169, v169\n"
"v_mfma_f32_16x16x32_bf16 a[28:31], v[36:39], v[196:199], a[28:31]\n"
"buffer_store_dwordx4 v[252:255], v1, s[4:7], s43 offen sc1\n"
"v_cvt_pk_bf16_f32 v212, v168, v169\n"
"v_mfma_f32_16x16x32_bf16 a[60:63], v[32:35], v[200:203], a[60:63]\n"
"v_exp_f32 v170, v170\n"
"v_mfma_f32_16x16x32_bf16 a[60:63], v[36:39], v[204:207], a[60:63]\n"
"v_add_u32 v15, 0x13800, v12\n"
"v_lshlrev_b32 v18, s32, v17\n"
"v_mfma_f32_16x16x32_bf16 a[128:131], v[4:7], v[192:195], a[128:131]\n"
"v_exp_f32 v171, v171\n"
"v_mfma_f32_16x16x32_bf16 a[128:131], v[4:7], v[196:199], a[128:131]\n"
"v_cvt_pk_bf16_f32 v213, v170, v171\n"
"s_waitcnt lgkmcnt(0)\n"
"v_mfma_f32_16x16x32_bf16 a[132:135], v[4:7], v[200:203], a[132:135]\n"
"v_mfma_f32_16x16x32_bf16 a[132:135], v[4:7], v[204:207], a[132:135]\n"
"v_mfma_f32_16x16x32_bf16 v[128:131], a[144:147], v[56:59], v[224:227]\n"
"v_exp_f32 v172, v172\n"
"v_mfma_f32_16x16x32_bf16 v[128:131], a[148:151], v[60:63], v[128:131]\n"
"s_add_u32 s60, s60, 0x8000\n"
"s_xor_b32 s62, s62, 0x8000\n"
"v_mfma_f32_16x16x32_bf16 v[128:131], a[152:155], v[64:67], v[128:131]\n"
"v_exp_f32 v173, v173\n"
"v_mfma_f32_16x16x32_bf16 v[128:131], a[156:159], v[68:71], v[128:131]\n"
"v_cvt_pk_bf16_f32 v220, v172, v173\n"
"s_xor_b32 s63, s63, 0x8000\n"
"v_mfma_f32_16x16x32_bf16 v[132:135], a[144:147], v[72:75], v[228:231]\n"
"v_exp_f32 v174, v174\n"
"v_mfma_f32_16x16x32_bf16 v[132:135], a[148:151], v[76:79], v[132:135]\n"
"s_mov_b32 s61, s60\n"
"s_add_u32 m0, s78, 0x1c000\n"
"v_mfma_f32_16x16x32_bf16 v[132:135], a[152:155], v[80:83], v[132:135]\n"
"v_exp_f32 v175, v175\n"
"v_mfma_f32_16x16x32_bf16 v[132:135], a[156:159], v[84:87], v[132:135]\n"
"buffer_load_dwordx4 v12, s[20:23], s30 offen lds\n"
"v_cvt_pk_bf16_f32 v221, v174, v175\n"
"v_mfma_f32_16x16x32_bf16 v[144:147], a[160:163], v[56:59], v[224:227]\n"
"v_exp_f32 v184, v184\n"
"v_mfma_f32_16x16x32_bf16 v[144:147], a[164:167], v[60:63], v[144:147]\n"
"buffer_load_dwordx4 v13, s[20:23], s30 offen offset:1024 lds\n"
"v_readlane_b32 s40, v54, 0\n"
"v_mfma_f32_16x16x32_bf16 v[144:147], a[168:171], v[64:67], v[144:147]\n"
"v_exp_f32 v185, v185\n"
"v_mfma_f32_16x16x32_bf16 v[144:147], a[172:175], v[68:71], v[144:147]\n"
"buffer_load_dwordx4 v14, s[20:23], s30 offen offset:2048 lds\n"
"v_cvt_pk_bf16_f32 v214, v184, v185\n"
"v_mfma_f32_16x16x32_bf16 v[148:151], a[160:163], v[72:75], v[228:231]\n"
"v_exp_f32 v186, v186\n"
"v_mfma_f32_16x16x32_bf16 v[148:151], a[164:167], v[76:79], v[148:151]\n"
"s_waitcnt vmcnt(15)\n"
"v_readlane_b32 s41, v54, 1\n"
"v_mfma_f32_16x16x32_bf16 v[148:151], a[168:171], v[80:83], v[148:151]\n"
"v_exp_f32 v187, v187\n"
"v_mfma_f32_16x16x32_bf16 v[148:151], a[172:175], v[84:87], v[148:151]\n"
"buffer_load_dwordx4 v15, s[20:23], s30 offen offset:3072 lds\n"
"v_cvt_pk_bf16_f32 v215, v186, v187\n"
"v_mfma_f32_16x16x32_bf16 v[160:163], a[176:179], v[56:59], v[224:227]\n"
"v_exp_f32 v188, v188\n"
"v_mfma_f32_16x16x32_bf16 v[160:163], a[180:183], v[60:63], v[160:163]\n"
"ds_read_b64_tr_b16 v[240:241], v16\n"
"ds_read_b64_tr_b16 v[242:243], v16 offset:2048\n"
"v_mfma_f32_16x16x32_bf16 v[160:163], a[184:187], v[64:67], v[160:163]\n"
"v_exp_f32 v189, v189\n"
"v_mfma_f32_16x16x32_bf16 v[160:163], a[188:191], v[68:71], v[160:163]\n"
"ds_read_b64_tr_b16 v[244:245], v16 offset:32\n"
"v_cvt_pk_bf16_f32 v222, v188, v189\n"
"v_mfma_f32_16x16x32_bf16 v[164:167], a[176:179], v[72:75], v[228:231]\n"
"v_exp_f32 v190, v190\n"
"v_mfma_f32_16x16x32_bf16 v[164:167], a[180:183], v[76:79], v[164:167]\n"
"ds_read_b64_tr_b16 v[246:247], v16 offset:2080\n"
"ds_read_b64_tr_b16 v[248:249], v16 offset:64\n"
"v_mfma_f32_16x16x32_bf16 v[164:167], a[184:187], v[80:83], v[164:167]\n"
"v_exp_f32 v191, v191\n"
"v_mfma_f32_16x16x32_bf16 v[164:167], a[188:191], v[84:87], v[164:167]\n"
"ds_read_b64_tr_b16 v[250:251], v16 offset:2112\n"
"v_cvt_pk_bf16_f32 v223, v190, v191\n"
"v_mfma_f32_16x16x32_bf16 v[176:179], a[192:195], v[56:59], v[224:227]\n"
"v_exp_f32 v128, v128\n"
"v_mfma_f32_16x16x32_bf16 v[176:179], a[196:199], v[60:63], v[176:179]\n"
"ds_read_b64_tr_b16 v[252:253], v16 offset:96\n"
"ds_read_b64_tr_b16 v[254:255], v16 offset:2144\n"
"v_mfma_f32_16x16x32_bf16 v[176:179], a[200:203], v[64:67], v[176:179]\n"
"v_exp_f32 v129, v129\n"
"v_mfma_f32_16x16x32_bf16 v[176:179], a[204:207], v[68:71], v[176:179]\n"
"v_xor_b32 v3, 0x8000, v3\n"
"v_cvt_pk_bf16_f32 v192, v128, v129\n"
"v_mfma_f32_16x16x32_bf16 v[180:183], a[192:195], v[72:75], v[228:231]\n"
"v_exp_f32 v130, v130\n"
"v_mfma_f32_16x16x32_bf16 v[180:183], a[196:199], v[76:79], v[180:183]\n"
"v_readlane_b32 s42, v54, 2\n"
"v_readlane_b32 s43, v54, 3\n"
"v_mfma_f32_16x16x32_bf16 v[180:183], a[200:203], v[80:83], v[180:183]\n"
"v_exp_f32 v131, v131\n"
"v_mfma_f32_16x16x32_bf16 v[180:183], a[204:207], v[84:87], v[180:183]\n"
"v_cvt_pk_bf16_f32 v193, v130, v131\n"
"s_waitcnt lgkmcnt(0)\n"
"v_mfma_f32_16x16x32_bf16 a[64:67], a[208:211], v[208:211], a[64:67]\n"
"v_exp_f32 v132, v132\n"
"v_mfma_f32_16x16x32_bf16 a[64:67], a[212:215], v[212:215], a[64:67]\n"
"v_sub_u32 v55, v0, v18\n"
"v_add_u32 v55, s11, v55\n"
"v_mfma_f32_16x16x32_bf16 a[96:99], a[208:211], v[216:219], a[96:99]\n"
"v_add_u32 v17, s33, v17\n"
"s_nop 0\n"
"v_mfma_f32_16x16x32_bf16 a[96:99], a[212:215], v[220:223], a[96:99]\n"
"s_barrier\n"
"s_mul_i32 s11, s27, 0x1c00\n"
"v_mfma_f32_16x16x32_bf16 a[68:71], a[216:219], v[208:211], a[68:71]\n"
"v_exp_f32 v133, v133\n"
"v_mfma_f32_16x16x32_bf16 a[68:71], a[220:223], v[212:215], a[68:71]\n"
"ds_read_b128 a[208:211], v3\n"
"v_cvt_pk_bf16_f32 v200, v132, v133\n"
"v_mfma_f32_16x16x32_bf16 a[100:103], a[216:219], v[216:219], a[100:103]\n"
"v_exp_f32 v134, v134\n"
"v_mfma_f32_16x16x32_bf16 a[100:103], a[220:223], v[220:223], a[100:103]\n"
"ds_read_b128 a[212:215], v3 offset:1024\n"
"s_mov_b32 m0, s62\n"
"v_mfma_f32_16x16x32_bf16 a[72:75], a[224:227], v[208:211], a[72:75]\n"
"v_exp_f32 v135, v135\n"
"v_mfma_f32_16x16x32_bf16 a[72:75], a[228:231], v[212:215], a[72:75]\n"
"ds_read_b128 a[216:219], v3 offset:2048\n"
"v_cvt_pk_bf16_f32 v201, v134, v135\n"
"v_mfma_f32_16x16x32_bf16 a[104:107], a[224:227], v[216:219], a[104:107]\n"
"v_exp_f32 v144, v144\n"
"v_mfma_f32_16x16x32_bf16 a[104:107], a[228:231], v[220:223], a[104:107]\n"
"ds_read_b128 a[220:223], v3 offset:3072\n"
"buffer_load_dwordx4 v1, s[4:7], s60 offen sc1 lds\n"
"v_mfma_f32_16x16x32_bf16 a[76:79], a[232:235], v[208:211], a[76:79]\n"
"v_exp_f32 v145, v145\n"
"v_mfma_f32_16x16x32_bf16 a[76:79], a[236:239], v[212:215], a[76:79]\n"
"ds_read_b128 a[224:227], v3 offset:4096\n"
"v_cvt_pk_bf16_f32 v194, v144, v145\n"
"v_mfma_f32_16x16x32_bf16 a[108:111], a[232:235], v[216:219], a[108:111]\n"
"v_exp_f32 v146, v146\n"
"v_mfma_f32_16x16x32_bf16 a[108:111], a[236:239], v[220:223], a[108:111]\n"
"ds_read_b128 a[228:231], v3 offset:5120\n"
"buffer_load_dwordx4 v1, s[4:7], s60 offen offset:1024 sc1 lds\n"
"v_mfma_f32_16x16x32_bf16 a[80:83], a[240:243], v[208:211], a[80:83]\n"
"v_exp_f32 v147, v147\n"
"v_mfma_f32_16x16x32_bf16 a[80:83], a[244:247], v[212:215], a[80:83]\n"
"ds_read_b128 a[232:235], v3 offset:6144\n"
"v_cvt_pk_bf16_f32 v195, v146, v147\n"
"v_mfma_f32_16x16x32_bf16 a[112:115], a[240:243], v[216:219], a[112:115]\n"
"v_exp_f32 v148, v148\n"
"v_mfma_f32_16x16x32_bf16 a[112:115], a[244:247], v[220:223], a[112:115]\n"
"ds_read_b128 a[236:239], v3 offset:7168\n"
"v_lshl_add_u32 v55, v55, 10, s11\n"
"v_mfma_f32_16x16x32_bf16 a[84:87], a[248:251], v[208:211], a[84:87]\n"
"v_exp_f32 v149, v149\n"
"v_mfma_f32_16x16x32_bf16 a[84:87], a[252:255], v[212:215], a[84:87]\n"
"ds_read_b128 a[240:243], v3 offset:8192\n"
"v_cvt_pk_bf16_f32 v202, v148, v149\n"
"v_mfma_f32_16x16x32_bf16 a[116:119], a[248:251], v[216:219], a[116:119]\n"
"v_exp_f32 v150, v150\n"
"v_mfma_f32_16x16x32_bf16 a[116:119], a[252:255], v[220:223], a[116:119]\n"
"ds_read_b128 a[244:247], v3 offset:9216\n"
"buffer_load_dwordx4 v1, s[4:7], s60 offen offset:2048 sc1 lds\n"
"v_mfma_f32_16x16x32_bf16 a[88:91], v[24:27], v[208:211], a[88:91]\n"
"v_exp_f32 v151, v151\n"
"v_mfma_f32_16x16x32_bf16 a[88:91], v[28:31], v[212:215], a[88:91]\n"
"ds_read_b128 a[248:251], v3 offset:10240\n"
"v_cvt_pk_bf16_f32 v203, v150, v151\n"
"v_mfma_f32_16x16x32_bf16 a[120:123], v[24:27], v[216:219], a[120:123]\n"
"v_exp_f32 v160, v160\n"
"v_mfma_f32_16x16x32_bf16 a[120:123], v[28:31], v[220:223], a[120:123]\n"
"ds_read_b128 a[252:255], v3 offset:11264\n"
"v_and_b32 v18, 0xffffe000, v55\n"
"v_mfma_f32_16x16x32_bf16 a[92:95], v[32:35], v[208:211], a[92:95]\n"
"v_exp_f32 v161, v161\n"
"v_mfma_f32_16x16x32_bf16 a[92:95], v[36:39], v[212:215], a[92:95]\n"
"ds_read_b128 v[24:27], v3 offset:12288\n"
"v_cvt_pk_bf16_f32 v196, v160, v161\n"
"v_mfma_f32_16x16x32_bf16 a[124:127], v[32:35], v[216:219], a[124:127]\n"
"v_exp_f32 v162, v162\n"
"v_mfma_f32_16x16x32_bf16 a[124:127], v[36:39], v[220:223], a[124:127]\n"
"ds_read_b128 v[28:31], v3 offset:13312\n"
"buffer_load_dwordx4 v1, s[4:7], s60 offen offset:3072 sc1 lds\n"
"v_mfma_f32_16x16x32_bf16 a[136:139], v[4:7], v[208:211], a[136:139]\n"
"v_exp_f32 v163, v163\n"
"v_mfma_f32_16x16x32_bf16 a[136:139], v[4:7], v[212:215], a[136:139]\n"
"ds_read_b128 v[32:35], v3 offset:14336\n"
"v_cvt_pk_bf16_f32 v197, v162, v163\n"
"v_mfma_f32_16x16x32_bf16 a[140:143], v[4:7], v[216:219], a[140:143]\n"
"v_exp_f32 v164, v164\n"
"v_mfma_f32_16x16x32_bf16 a[140:143], v[4:7], v[220:223], a[140:143]\n"
"ds_read_b128 v[36:39], v3 offset:15360\n"
"v_add_u32 v55, v55, v18\n"
"v_mfma_f32_16x16x32_bf16 v[136:139], a[144:147], v[88:91], v[232:235]\n"
"v_exp_f32 v165, v165\n"
"v_mfma_f32_16x16x32_bf16 v[136:139], a[148:151], v[92:95], v[136:139]\n"
"v_xor_b32 v2, 0x8000, v2\n"
"v_cvt_pk_bf16_f32 v204, v164, v165\n"
"v_mfma_f32_16x16x32_bf16 v[136:139], a[152:155], v[96:99], v[136:139]\n"
"v_exp_f32 v166, v166\n"
"v_mfma_f32_16x16x32_bf16 v[136:139], a[156:159], v[100:103], v[136:139]\n"
"v_and_b32 v18, 7, v17\n"
"v_lshl_add_u32 v55, v18, 15, v55\n"
"v_mfma_f32_16x16x32_bf16 v[140:143], a[144:147], v[104:107], v[236:239]\n"
"v_exp_f32 v167, v167\n"
"v_mfma_f32_16x16x32_bf16 v[140:143], a[148:151], v[108:111], v[140:143]\n"
"v_cvt_pk_bf16_f32 v205, v166, v167\n"
"v_lshl_add_u32 v17, v17, 19, s28\n"
"v_mfma_f32_16x16x32_bf16 v[140:143], a[152:155], v[112:115], v[140:143]\n"
"v_add_u32 v55, v55, v17\n"
"s_waitcnt vmcnt(16) lgkmcnt(0)\n"
"v_mfma_f32_16x16x32_bf16 v[140:143], a[156:159], v[116:119], v[140:143]\n"
"s_barrier\n"
"v_mfma_f32_16x16x32_bf16 v[152:155], a[160:163], v[88:91], v[232:235]\n"
"v_exp_f32 v176, v176\n"
"v_mfma_f32_16x16x32_bf16 v[152:155], a[164:167], v[92:95], v[152:155]\n"
"ds_read_b128 a[144:147], v2\n"
"v_mfma_f32_16x16x32_bf16 v[152:155], a[168:171], v[96:99], v[152:155]\n"
"v_exp_f32 v177, v177\n"
"v_mfma_f32_16x16x32_bf16 v[152:155], a[172:175], v[100:103], v[152:155]\n"
"ds_read_b128 a[148:151], v2 offset:1024\n"
"v_cvt_pk_bf16_f32 v198, v176, v177\n"
"v_mfma_f32_16x16x32_bf16 v[156:159], a[160:163], v[104:107], v[236:239]\n"
"v_exp_f32 v178, v178\n"
"v_mfma_f32_16x16x32_bf16 v[156:159], a[164:167], v[108:111], v[156:159]\n"
"ds_read_b128 a[152:155], v2 offset:2048\n"
"v_mfma_f32_16x16x32_bf16 v[156:159], a[168:171], v[112:115], v[156:159]\n"
"v_exp_f32 v179, v179\n"
"v_mfma_f32_16x16x32_bf16 v[156:159], a[172:175], v[116:119], v[156:159]\n"
"ds_read_b128 a[156:159], v2 offset:3072\n"
"v_cvt_pk_bf16_f32 v199, v178, v179\n"
"v_mfma_f32_16x16x32_bf16 v[168:171], a[176:179], v[88:91], v[232:235]\n"
"v_exp_f32 v180, v180\n"
"v_mfma_f32_16x16x32_bf16 v[168:171], a[180:183], v[92:95], v[168:171]\n"
"ds_read_b128 a[160:163], v2 offset:4096\n"
"v_mfma_f32_16x16x32_bf16 v[168:171], a[184:187], v[96:99], v[168:171]\n"
"v_exp_f32 v181, v181\n"
"v_mfma_f32_16x16x32_bf16 v[168:171], a[188:191], v[100:103], v[168:171]\n"
"ds_read_b128 a[164:167], v2 offset:5120\n"
"v_cvt_pk_bf16_f32 v206, v180, v181\n"
"v_mfma_f32_16x16x32_bf16 v[172:175], a[176:179], v[104:107], v[236:239]\n"
"v_exp_f32 v182, v182\n"
"v_mfma_f32_16x16x32_bf16 v[172:175], a[180:183], v[108:111], v[172:175]\n"
"ds_read_b128 a[168:171], v2 offset:6144\n"
"v_mfma_f32_16x16x32_bf16 v[172:175], a[184:187], v[112:115], v[172:175]\n"
"v_exp_f32 v183, v183\n"
"v_mfma_f32_16x16x32_bf16 v[172:175], a[188:191], v[116:119], v[172:175]\n"
"ds_read_b128 a[172:175], v2 offset:7168\n"
"v_cvt_pk_bf16_f32 v207, v182, v183\n"
"v_mfma_f32_16x16x32_bf16 v[184:187], a[192:195], v[88:91], v[232:235]\n"
"v_mfma_f32_16x16x32_bf16 v[184:187], a[196:199], v[92:95], v[184:187]\n"
"ds_read_b128 a[176:179], v2 offset:8192\n"
"v_mfma_f32_16x16x32_bf16 v[184:187], a[200:203], v[96:99], v[184:187]\n"
"v_exp_f32 v136, v136\n"
"v_mfma_f32_16x16x32_bf16 v[184:187], a[204:207], v[100:103], v[184:187]\n"
"ds_read_b128 a[180:183], v2 offset:9216\n"
"v_mfma_f32_16x16x32_bf16 v[188:191], a[192:195], v[104:107], v[236:239]\n"
"v_exp_f32 v137, v137\n"
"v_mfma_f32_16x16x32_bf16 v[188:191], a[196:199], v[108:111], v[188:191]\n"
"ds_read_b128 a[184:187], v2 offset:10240\n"
"v_cvt_pk_bf16_f32 v208, v136, v137\n"
"v_mfma_f32_16x16x32_bf16 v[188:191], a[200:203], v[112:115], v[188:191]\n"
"v_exp_f32 v138, v138\n"
"v_mfma_f32_16x16x32_bf16 v[188:191], a[204:207], v[116:119], v[188:191]\n"
"ds_read_b128 a[188:191], v2 offset:11264\n"
"v_mfma_f32_16x16x32_bf16 a[0:3], a[208:211], v[192:195], a[0:3]\n"
"v_exp_f32 v139, v139\n"
"v_mfma_f32_16x16x32_bf16 a[0:3], a[212:215], v[196:199], a[0:3]\n"
"ds_read_b128 a[192:195], v2 offset:12288\n"
"v_cvt_pk_bf16_f32 v209, v138, v139\n"
"v_mfma_f32_16x16x32_bf16 a[32:35], a[208:211], v[200:203], a[32:35]\n"
"v_exp_f32 v140, v140\n"
"v_mfma_f32_16x16x32_bf16 a[32:35], a[212:215], v[204:207], a[32:35]\n"
"ds_read_b128 a[196:199], v2 offset:13312\n"
"s_mov_b32 m0, s63\n"
"v_mfma_f32_16x16x32_bf16 a[4:7], a[216:219], v[192:195], a[4:7]\n"
"v_exp_f32 v141, v141\n"
"v_mfma_f32_16x16x32_bf16 a[4:7], a[220:223], v[196:199], a[4:7]\n"
"ds_read_b128 a[200:203], v2 offset:14336\n"
"v_cvt_pk_bf16_f32 v216, v140, v141\n"
"v_mfma_f32_16x16x32_bf16 a[36:39], a[216:219], v[200:203], a[36:39]\n"
"v_exp_f32 v142, v142\n"
"v_mfma_f32_16x16x32_bf16 a[36:39], a[220:223], v[204:207], a[36:39]\n"
"ds_read_b128 a[204:207], v2 offset:15360\n"
"buffer_load_dwordx4 v1, s[16:19], s60 offen sc1 lds\n"
"v_mfma_f32_16x16x32_bf16 a[8:11], a[224:227], v[192:195], a[8:11]\n"
"v_exp_f32 v143, v143\n"
"v_mfma_f32_16x16x32_bf16 a[8:11], a[228:231], v[196:199], a[8:11]\n"
"buffer_load_dwordx4 v1, s[16:19], s60 offen offset:1024 sc1 lds\n"
"v_cvt_pk_bf16_f32 v217, v142, v143\n"
"v_mfma_f32_16x16x32_bf16 a[40:43], a[224:227], v[200:203], a[40:43]\n"
"v_exp_f32 v152, v152\n"
"v_mfma_f32_16x16x32_bf16 a[40:43], a[228:231], v[204:207], a[40:43]\n"
"buffer_load_dwordx4 v1, s[16:19], s60 offen offset:2048 sc1 lds\n"
"v_mfma_f32_16x16x32_bf16 a[12:15], a[232:235], v[192:195], a[12:15]\n"
"v_exp_f32 v153, v153\n"
"v_mfma_f32_16x16x32_bf16 a[12:15], a[236:239], v[196:199], a[12:15]\n"
"buffer_load_dwordx4 v1, s[16:19], s60 offen offset:3072 sc1 lds\n"
"v_cvt_pk_bf16_f32 v210, v152, v153\n"
"v_mfma_f32_16x16x32_bf16 a[44:47], a[232:235], v[200:203], a[44:47]\n"
"v_exp_f32 v154, v154\n"
"v_mfma_f32_16x16x32_bf16 a[44:47], a[236:239], v[204:207], a[44:47]\n"
"v_mfma_f32_16x16x32_bf16 a[16:19], a[240:243], v[192:195], a[16:19]\n"
"v_exp_f32 v155, v155\n"
"v_mfma_f32_16x16x32_bf16 a[16:19], a[244:247], v[196:199], a[16:19]\n"
"buffer_store_dwordx4 v[240:243], v1, s[4:7], s40 offen sc1\n"
"v_cvt_pk_bf16_f32 v211, v154, v155\n"
"v_mfma_f32_16x16x32_bf16 a[48:51], a[240:243], v[200:203], a[48:51]\n"
"v_exp_f32 v156, v156\n"
"v_mfma_f32_16x16x32_bf16 a[48:51], a[244:247], v[204:207], a[48:51]\n"
"v_mfma_f32_16x16x32_bf16 a[20:23], a[248:251], v[192:195], a[20:23]\n"
"v_exp_f32 v157, v157\n"
"v_mfma_f32_16x16x32_bf16 a[20:23], a[252:255], v[196:199], a[20:23]\n"
"buffer_store_dwordx4 v[244:247], v1, s[4:7], s41 offen sc1\n"
"v_cvt_pk_bf16_f32 v218, v156, v157\n"
"v_mfma_f32_16x16x32_bf16 a[52:55], a[248:251], v[200:203], a[52:55]\n"
"v_exp_f32 v158, v158\n"
"v_mfma_f32_16x16x32_bf16 a[52:55], a[252:255], v[204:207], a[52:55]\n"
"v_mfma_f32_16x16x32_bf16 a[24:27], v[24:27], v[192:195], a[24:27]\n"
"v_exp_f32 v159, v159\n"
"v_mfma_f32_16x16x32_bf16 a[24:27], v[28:31], v[196:199], a[24:27]\n"
"buffer_store_dwordx4 v[248:251], v1, s[4:7], s42 offen sc1\n"
"v_cvt_pk_bf16_f32 v219, v158, v159\n"
"v_mfma_f32_16x16x32_bf16 a[56:59], v[24:27], v[200:203], a[56:59]\n"
"v_exp_f32 v168, v168\n"
"v_mfma_f32_16x16x32_bf16 a[56:59], v[28:31], v[204:207], a[56:59]\n"
"v_mfma_f32_16x16x32_bf16 a[28:31], v[32:35], v[192:195], a[28:31]\n"
"v_exp_f32 v169, v169\n"
"v_mfma_f32_16x16x32_bf16 a[28:31], v[36:39], v[196:199], a[28:31]\n"
"buffer_store_dwordx4 v[252:255], v1, s[4:7], s43 offen sc1\n"
"v_cvt_pk_bf16_f32 v212, v168, v169\n"
"v_mfma_f32_16x16x32_bf16 a[60:63], v[32:35], v[200:203], a[60:63]\n"
"v_exp_f32 v170, v170\n"
"v_mfma_f32_16x16x32_bf16 a[60:63], v[36:39], v[204:207], a[60:63]\n"
"v_mfma_f32_16x16x32_bf16 a[128:131], v[4:7], v[192:195], a[128:131]\n"
"v_exp_f32 v171, v171\n"
"v_mfma_f32_16x16x32_bf16 a[128:131], v[4:7], v[196:199], a[128:131]\n"
"v_cvt_pk_bf16_f32 v213, v170, v171\n"
"s_waitcnt lgkmcnt(0)\n"
"v_mfma_f32_16x16x32_bf16 a[132:135], v[4:7], v[200:203], a[132:135]\n"
"v_mfma_f32_16x16x32_bf16 a[132:135], v[4:7], v[204:207], a[132:135]\n"
"v_mfma_f32_16x16x32_bf16 v[128:131], a[144:147], v[56:59], v[224:227]\n"
"v_exp_f32 v172, v172\n"
"v_mfma_f32_16x16x32_bf16 v[128:131], a[148:151], v[60:63], v[128:131]\n"
"s_add_u32 s60, s60, 0x8000\n"
"s_xor_b32 s62, s62, 0x8000\n"
"v_mfma_f32_16x16x32_bf16 v[128:131], a[152:155], v[64:67], v[128:131]\n"
"v_exp_f32 v173, v173\n"
"v_mfma_f32_16x16x32_bf16 v[128:131], a[156:159], v[68:71], v[128:131]\n"
"v_cvt_pk_bf16_f32 v220, v172, v173\n"
"s_xor_b32 s63, s63, 0x8000\n"
"v_mfma_f32_16x16x32_bf16 v[132:135], a[144:147], v[72:75], v[228:231]\n"
"v_exp_f32 v174, v174\n"
"v_mfma_f32_16x16x32_bf16 v[132:135], a[148:151], v[76:79], v[132:135]\n"
"s_mov_b32 s61, s60\n"
"v_readlane_b32 s40, v55, 0\n"
"v_mfma_f32_16x16x32_bf16 v[132:135], a[152:155], v[80:83], v[132:135]\n"
"v_exp_f32 v175, v175\n"
"v_mfma_f32_16x16x32_bf16 v[132:135], a[156:159], v[84:87], v[132:135]\n"
"v_cvt_pk_bf16_f32 v221, v174, v175\n"
"v_readlane_b32 s41, v55, 1\n"
"v_mfma_f32_16x16x32_bf16 v[144:147], a[160:163], v[56:59], v[224:227]\n"
"v_exp_f32 v184, v184\n"
"v_mfma_f32_16x16x32_bf16 v[144:147], a[164:167], v[60:63], v[144:147]\n"
"v_readlane_b32 s42, v55, 2\n"
"v_readlane_b32 s43, v55, 3\n"
"v_mfma_f32_16x16x32_bf16 v[144:147], a[168:171], v[64:67], v[144:147]\n"
"v_exp_f32 v185, v185\n"
"v_mfma_f32_16x16x32_bf16 v[144:147], a[172:175], v[68:71], v[144:147]\n"
"v_cvt_pk_bf16_f32 v214, v184, v185\n"
"v_mfma_f32_16x16x32_bf16 v[148:151], a[160:163], v[72:75], v[228:231]\n"
"v_exp_f32 v186, v186\n"
"v_mfma_f32_16x16x32_bf16 v[148:151], a[164:167], v[76:79], v[148:151]\n"
"s_waitcnt vmcnt(12)\n"
"v_mfma_f32_16x16x32_bf16 v[148:151], a[168:171], v[80:83], v[148:151]\n"
"v_exp_f32 v187, v187\n"
"v_mfma_f32_16x16x32_bf16 v[148:151], a[172:175], v[84:87], v[148:151]\n"
"ds_read_b64_tr_b16 v[240:241], v16 offset:32768\n"
"v_cvt_pk_bf16_f32 v215, v186, v187\n"
"v_mfma_f32_16x16x32_bf16 v[160:163], a[176:179], v[56:59], v[224:227]\n"
"v_exp_f32 v188, v188\n"
"v_mfma_f32_16x16x32_bf16 v[160:163], a[180:183], v[60:63], v[160:163]\n"
"ds_read_b64_tr_b16 v[242:243], v16 offset:34816\n"
"ds_read_b64_tr_b16 v[244:245], v16 offset:32800\n"
"v_mfma_f32_16x16x32_bf16 v[160:163], a[184:187], v[64:67], v[160:163]\n"
"v_exp_f32 v189, v189\n"
"v_mfma_f32_16x16x32_bf16 v[160:163], a[188:191], v[68:71], v[160:163]\n"
"ds_read_b64_tr_b16 v[246:247], v16 offset:34848\n"
"v_cvt_pk_bf16_f32 v222, v188, v189\n"
"v_mfma_f32_16x16x32_bf16 v[164:167], a[176:179], v[72:75], v[228:231]\n"
"v_exp_f32 v190, v190\n"
"v_mfma_f32_16x16x32_bf16 v[164:167], a[180:183], v[76:79], v[164:167]\n"
"ds_read_b64_tr_b16 v[248:249], v16 offset:32832\n"
"ds_read_b64_tr_b16 v[250:251], v16 offset:34880\n"
"v_mfma_f32_16x16x32_bf16 v[164:167], a[184:187], v[80:83], v[164:167]\n"
"v_exp_f32 v191, v191\n"
"v_mfma_f32_16x16x32_bf16 v[164:167], a[188:191], v[84:87], v[164:167]\n"
"ds_read_b64_tr_b16 v[252:253], v16 offset:32864\n"
"v_cvt_pk_bf16_f32 v223, v190, v191\n"
"v_mfma_f32_16x16x32_bf16 v[176:179], a[192:195], v[56:59], v[224:227]\n"
"v_exp_f32 v128, v128\n"
"v_mfma_f32_16x16x32_bf16 v[176:179], a[196:199], v[60:63], v[176:179]\n"
"ds_read_b64_tr_b16 v[254:255], v16 offset:34912\n"
"v_mfma_f32_16x16x32_bf16 v[176:179], a[200:203], v[64:67], v[176:179]\n"
"v_exp_f32 v129, v129\n"
"v_mfma_f32_16x16x32_bf16 v[176:179], a[204:207], v[68:71], v[176:179]\n"
"v_xor_b32 v3, 0x8000, v3\n"
"v_cvt_pk_bf16_f32 v192, v128, v129\n"
"v_mfma_f32_16x16x32_bf16 v[180:183], a[192:195], v[72:75], v[228:231]\n"
"v_exp_f32 v130, v130\n"
"v_mfma_f32_16x16x32_bf16 v[180:183], a[196:199], v[76:79], v[180:183]\n"
"v_mfma_f32_16x16x32_bf16 v[180:183], a[200:203], v[80:83], v[180:183]\n"
"v_exp_f32 v131, v131\n"
"v_mfma_f32_16x16x32_bf16 v[180:183], a[204:207], v[84:87], v[180:183]\n"
"v_cvt_pk_bf16_f32 v193, v130, v131\n"
"s_waitcnt lgkmcnt(0)\n"
"v_mfma_f32_16x16x32_bf16 a[64:67], a[208:211], v[208:211], a[64:67]\n"
"v_exp_f32 v132, v132\n"
"v_mfma_f32_16x16x32_bf16 a[64:67], a[212:215], v[212:215], a[64:67]\n"
"v_mfma_f32_16x16x32_bf16 a[96:99], a[208:211], v[216:219], a[96:99]\n"
"s_nop 0\n"
"v_mfma_f32_16x16x32_bf16 a[96:99], a[212:215], v[220:223], a[96:99]\n"
"s_barrier\n"
"v_mfma_f32_16x16x32_bf16 a[68:71], a[216:219], v[208:211], a[68:71]\n"
"v_exp_f32 v133, v133\n"
"v_mfma_f32_16x16x32_bf16 a[68:71], a[220:223], v[212:215], a[68:71]\n"
"ds_read_b128 a[208:211], v3\n"
"v_cvt_pk_bf16_f32 v200, v132, v133\n"
"v_mfma_f32_16x16x32_bf16 a[100:103], a[216:219], v[216:219], a[100:103]\n"
"v_exp_f32 v134, v134\n"
"v_mfma_f32_16x16x32_bf16 a[100:103], a[220:223], v[220:223], a[100:103]\n"
"ds_read_b128 a[212:215], v3 offset:1024\n"
"s_mov_b32 m0, s62\n"
"v_mfma_f32_16x16x32_bf16 a[72:75], a[224:227], v[208:211], a[72:75]\n"
"v_exp_f32 v135, v135\n"
"v_mfma_f32_16x16x32_bf16 a[72:75], a[228:231], v[212:215], a[72:75]\n"
"ds_read_b128 a[216:219], v3 offset:2048\n"
"v_cvt_pk_bf16_f32 v201, v134, v135\n"
"v_mfma_f32_16x16x32_bf16 a[104:107], a[224:227], v[216:219], a[104:107]\n"
"v_exp_f32 v144, v144\n"
"v_mfma_f32_16x16x32_bf16 a[104:107], a[228:231], v[220:223], a[104:107]\n"
"ds_read_b128 a[220:223], v3 offset:3072\n"
"buffer_load_dwordx4 v1, s[4:7], s60 offen sc1 lds\n"
"v_mfma_f32_16x16x32_bf16 a[76:79], a[232:235], v[208:211], a[76:79]\n"
"v_exp_f32 v145, v145\n"
"v_mfma_f32_16x16x32_bf16 a[76:79], a[236:239], v[212:215], a[76:79]\n"
"ds_read_b128 a[224:227], v3 offset:4096\n"
"v_cvt_pk_bf16_f32 v194, v144, v145\n"
"v_mfma_f32_16x16x32_bf16 a[108:111], a[232:235], v[216:219], a[108:111]\n"
"v_exp_f32 v146, v146\n"
"v_mfma_f32_16x16x32_bf16 a[108:111], a[236:239], v[220:223], a[108:111]\n"
"ds_read_b128 a[228:231], v3 offset:5120\n"
"buffer_store_dwordx4 v[240:243], v1, s[4:7], s40 offen sc1\n"
"v_mfma_f32_16x16x32_bf16 a[80:83], a[240:243], v[208:211], a[80:83]\n"
"v_exp_f32 v147, v147\n"
"v_mfma_f32_16x16x32_bf16 a[80:83], a[244:247], v[212:215], a[80:83]\n"
"ds_read_b128 a[232:235], v3 offset:6144\n"
"v_cvt_pk_bf16_f32 v195, v146, v147\n"
"v_mfma_f32_16x16x32_bf16 a[112:115], a[240:243], v[216:219], a[112:115]\n"
"v_exp_f32 v148, v148\n"
"v_mfma_f32_16x16x32_bf16 a[112:115], a[244:247], v[220:223], a[112:115]\n"
"ds_read_b128 a[236:239], v3 offset:7168\n"
"v_mfma_f32_16x16x32_bf16 a[84:87], a[248:251], v[208:211], a[84:87]\n"
"v_exp_f32 v149, v149\n"
"v_mfma_f32_16x16x32_bf16 a[84:87], a[252:255], v[212:215], a[84:87]\n"
"ds_read_b128 a[240:243], v3 offset:8192\n"
"v_cvt_pk_bf16_f32 v202, v148, v149\n"
"v_mfma_f32_16x16x32_bf16 a[116:119], a[248:251], v[216:219], a[116:119]\n"
"v_exp_f32 v150, v150\n"
"v_mfma_f32_16x16x32_bf16 a[116:119], a[252:255], v[220:223], a[116:119]\n"
"ds_read_b128 a[244:247], v3 offset:9216\n"
"buffer_load_dwordx4 v1, s[4:7], s60 offen offset:1024 sc1 lds\n"
"v_mfma_f32_16x16x32_bf16 a[88:91], v[24:27], v[208:211], a[88:91]\n"
"v_exp_f32 v151, v151\n"
"v_mfma_f32_16x16x32_bf16 a[88:91], v[28:31], v[212:215], a[88:91]\n"
"ds_read_b128 a[248:251], v3 offset:10240\n"
"v_cvt_pk_bf16_f32 v203, v150, v151\n"
"v_mfma_f32_16x16x32_bf16 a[120:123], v[24:27], v[216:219], a[120:123]\n"
"v_exp_f32 v160, v160\n"
"v_mfma_f32_16x16x32_bf16 a[120:123], v[28:31], v[220:223], a[120:123]\n"
"ds_read_b128 a[252:255], v3 offset:11264\n"
"buffer_store_dwordx4 v[244:247], v1, s[4:7], s41 offen sc1\n"
"v_mfma_f32_16x16x32_bf16 a[92:95], v[32:35], v[208:211], a[92:95]\n"
"v_exp_f32 v161, v161\n"
"v_mfma_f32_16x16x32_bf16 a[92:95], v[36:39], v[212:215], a[92:95]\n"
"ds_read_b128 v[24:27], v3 offset:12288\n"
"v_cvt_pk_bf16_f32 v196, v160, v161\n"
"v_mfma_f32_16x16x32_bf16 a[124:127], v[32:35], v[216:219], a[124:127]\n"
"v_exp_f32 v162, v162\n"
"v_mfma_f32_16x16x32_bf16 a[124:127], v[36:39], v[220:223], a[124:127]\n"
"ds_read_b128 v[28:31], v3 offset:13312\n"
"v_mfma_f32_16x16x32_bf16 a[136:139], v[4:7], v[208:211], a[136:139]\n"
"v_exp_f32 v163, v163\n"
"v_mfma_f32_16x16x32_bf16 a[136:139], v[4:7], v[212:215], a[136:139]\n"
"ds_read_b128 v[32:35], v3 offset:14336\n"
"v_cvt_pk_bf16_f32 v197, v162, v163\n"
"v_mfma_f32_16x16x32_bf16 a[140:143], v[4:7], v[216:219], a[140:143]\n"
"v_exp_f32 v164, v164\n"
"v_mfma_f32_16x16x32_bf16 a[140:143], v[4:7], v[220:223], a[140:143]\n"
"ds_read_b128 v[36:39], v3 offset:15360\n"
"buffer_store_dwordx4 v[248:251], v1, s[4:7], s42 offen sc1\n"
"v_mfma_f32_16x16x32_bf16 v[136:139], a[144:147], v[88:91], v[232:235]\n"
"v_exp_f32 v165, v165\n"
"v_mfma_f32_16x16x32_bf16 v[136:139], a[148:151], v[92:95], v[136:139]\n"
"v_xor_b32 v2, 0x8000, v2\n"
"v_cvt_pk_bf16_f32 v204, v164, v165\n"
"v_mfma_f32_16x16x32_bf16 v[136:139], a[152:155], v[96:99], v[136:139]\n"
"v_exp_f32 v166, v166\n"
"v_mfma_f32_16x16x32_bf16 v[136:139], a[156:159], v[100:103], v[136:139]\n"
"buffer_load_dwordx4 v1, s[4:7], s60 offen offset:2048 sc1 lds\n"
"v_mfma_f32_16x16x32_bf16 v[140:143], a[144:147], v[104:107], v[236:239]\n"
"v_exp_f32 v167, v167\n"
"v_mfma_f32_16x16x32_bf16 v[140:143], a[148:151], v[108:111], v[140:143]\n"
"v_cvt_pk_bf16_f32 v205, v166, v167\n"
"v_mfma_f32_16x16x32_bf16 v[140:143], a[152:155], v[112:115], v[140:143]\n"
"s_waitcnt vmcnt(14) lgkmcnt(0)\n"
"v_mfma_f32_16x16x32_bf16 v[140:143], a[156:159], v[116:119], v[140:143]\n"
"s_barrier\n"
"v_mfma_f32_16x16x32_bf16 v[152:155], a[160:163], v[88:91], v[232:235]\n"
"v_exp_f32 v176, v176\n"
"v_mfma_f32_16x16x32_bf16 v[152:155], a[164:167], v[92:95], v[152:155]\n"
"ds_read_b128 a[144:147], v2\n"
"buffer_store_dwordx4 v[252:255], v1, s[4:7], s43 offen sc1\n"
"v_mfma_f32_16x16x32_bf16 v[152:155], a[168:171], v[96:99], v[152:155]\n"
"v_exp_f32 v177, v177\n"
"v_mfma_f32_16x16x32_bf16 v[152:155], a[172:175], v[100:103], v[152:155]\n"
"ds_read_b128 a[148:151], v2 offset:1024\n"
"v_cvt_pk_bf16_f32 v198, v176, v177\n"
"v_mfma_f32_16x16x32_bf16 v[156:159], a[160:163], v[104:107], v[236:239]\n"
"v_exp_f32 v178, v178\n"
"v_mfma_f32_16x16x32_bf16 v[156:159], a[164:167], v[108:111], v[156:159]\n"
"ds_read_b128 a[152:155], v2 offset:2048\n"
"v_mfma_f32_16x16x32_bf16 v[156:159], a[168:171], v[112:115], v[156:159]\n"
"v_exp_f32 v179, v179\n"
"v_mfma_f32_16x16x32_bf16 v[156:159], a[172:175], v[116:119], v[156:159]\n"
"ds_read_b128 a[156:159], v2 offset:3072\n"
"v_cvt_pk_bf16_f32 v199, v178, v179\n"
"v_mfma_f32_16x16x32_bf16 v[168:171], a[176:179], v[88:91], v[232:235]\n"
"v_exp_f32 v180, v180\n"
"v_mfma_f32_16x16x32_bf16 v[168:171], a[180:183], v[92:95], v[168:171]\n"
"ds_read_b128 a[160:163], v2 offset:4096\n"
"v_mfma_f32_16x16x32_bf16 v[168:171], a[184:187], v[96:99], v[168:171]\n"
"v_exp_f32 v181, v181\n"
"v_mfma_f32_16x16x32_bf16 v[168:171], a[188:191], v[100:103], v[168:171]\n"
"ds_read_b128 a[164:167], v2 offset:5120\n"
"v_cvt_pk_bf16_f32 v206, v180, v181\n"
"v_mfma_f32_16x16x32_bf16 v[172:175], a[176:179], v[104:107], v[236:239]\n"
"v_exp_f32 v182, v182\n"
"v_mfma_f32_16x16x32_bf16 v[172:175], a[180:183], v[108:111], v[172:175]\n"
"ds_read_b128 a[168:171], v2 offset:6144\n"
"buffer_load_dwordx4 v1, s[4:7], s60 offen offset:3072 sc1 lds\n"
"v_mfma_f32_16x16x32_bf16 v[172:175], a[184:187], v[112:115], v[172:175]\n"
"v_exp_f32 v183, v183\n"
"v_mfma_f32_16x16x32_bf16 v[172:175], a[188:191], v[116:119], v[172:175]\n"
"ds_read_b128 a[172:175], v2 offset:7168\n"
"v_cvt_pk_bf16_f32 v207, v182, v183\n"
"v_mfma_f32_16x16x32_bf16 v[184:187], a[192:195], v[88:91], v[232:235]\n"
"v_mfma_f32_16x16x32_bf16 v[184:187], a[196:199], v[92:95], v[184:187]\n"
"ds_read_b128 a[176:179], v2 offset:8192\n"
"v_mfma_f32_16x16x32_bf16 v[184:187], a[200:203], v[96:99], v[184:187]\n"
"v_exp_f32 v136, v136\n"
"v_mfma_f32_16x16x32_bf16 v[184:187], a[204:207], v[100:103], v[184:187]\n"
"ds_read_b128 a[180:183], v2 offset:9216\n"
"v_mfma_f32_16x16x32_bf16 v[188:191], a[192:195], v[104:107], v[236:239]\n"
"v_exp_f32 v137, v137\n"
"v_mfma_f32_16x16x32_bf16 v[188:191], a[196:199], v[108:111], v[188:191]\n"
"ds_read_b128 a[184:187], v2 offset:10240\n"
"v_cvt_pk_bf16_f32 v208, v136, v137\n"
"v_mfma_f32_16x16x32_bf16 v[188:191], a[200:203], v[112:115], v[188:191]\n"
"v_exp_f32 v138, v138\n"
"v_mfma_f32_16x16x32_bf16 v[188:191], a[204:207], v[116:119], v[188:191]\n"
"ds_read_b128 a[188:191], v2 offset:11264\n"
"v_mfma_f32_16x16x32_bf16 a[0:3], a[208:211], v[192:195], a[0:3]\n"
"v_exp_f32 v139, v139\n"
"v_mfma_f32_16x16x32_bf16 a[0:3], a[212:215], v[196:199], a[0:3]\n"
"ds_read_b128 a[192:195], v2 offset:12288\n"
"v_cvt_pk_bf16_f32 v209, v138, v139\n"
"v_mfma_f32_16x16x32_bf16 a[32:35], a[208:211], v[200:203], a[32:35]\n"
"v_exp_f32 v140, v140\n"
"v_mfma_f32_16x16x32_bf16 a[32:35], a[212:215], v[204:207], a[32:35]\n"
"ds_read_b128 a[196:199], v2 offset:13312\n"
"s_mov_b32 m0, s63\n"
"v_mfma_f32_16x16x32_bf16 a[4:7], a[216:219], v[192:195], a[4:7]\n"
"v_exp_f32 v141, v141\n"
"v_mfma_f32_16x16x32_bf16 a[4:7], a[220:223], v[196:199], a[4:7]\n"
"ds_read_b128 a[200:203], v2 offset:14336\n"
"v_cvt_pk_bf16_f32 v216, v140, v141\n"
"v_mfma_f32_16x16x32_bf16 a[36:39], a[216:219], v[200:203], a[36:39]\n"
"v_exp_f32 v142, v142\n"
"v_mfma_f32_16x16x32_bf16 a[36:39], a[220:223], v[204:207], a[36:39]\n"
"ds_read_b128 a[204:207], v2 offset:15360\n"
"buffer_load_dwordx4 v1, s[16:19], s60 offen sc1 lds\n"
"v_mfma_f32_16x16x32_bf16 a[8:11], a[224:227], v[192:195], a[8:11]\n"
"v_exp_f32 v143, v143\n"
"v_mfma_f32_16x16x32_bf16 a[8:11], a[228:231], v[196:199], a[8:11]\n"
"buffer_load_dwordx4 v1, s[16:19], s60 offen offset:1024 sc1 lds\n"
"v_cvt_pk_bf16_f32 v217, v142, v143\n"
"v_mfma_f32_16x16x32_bf16 a[40:43], a[224:227], v[200:203], a[40:43]\n"
"v_exp_f32 v152, v152\n"
"v_mfma_f32_16x16x32_bf16 a[40:43], a[228:231], v[204:207], a[40:43]\n"
"v_mfma_f32_16x16x32_bf16 a[12:15], a[232:235], v[192:195], a[12:15]\n"
"v_exp_f32 v153, v153\n"
"v_mfma_f32_16x16x32_bf16 a[12:15], a[236:239], v[196:199], a[12:15]\n"
"v_cvt_pk_bf16_f32 v210, v152, v153\n"
"v_mfma_f32_16x16x32_bf16 a[44:47], a[232:235], v[200:203], a[44:47]\n"
"v_exp_f32 v154, v154\n"
"v_mfma_f32_16x16x32_bf16 a[44:47], a[236:239], v[204:207], a[44:47]\n"
"v_mfma_f32_16x16x32_bf16 a[16:19], a[240:243], v[192:195], a[16:19]\n"
"v_exp_f32 v155, v155\n"
"v_mfma_f32_16x16x32_bf16 a[16:19], a[244:247], v[196:199], a[16:19]\n"
"buffer_load_dwordx4 v1, s[16:19], s60 offen offset:2048 sc1 lds\n"
"v_cvt_pk_bf16_f32 v211, v154, v155\n"
"v_mfma_f32_16x16x32_bf16 a[48:51], a[240:243], v[200:203], a[48:51]\n"
"v_exp_f32 v156, v156\n"
"v_mfma_f32_16x16x32_bf16 a[48:51], a[244:247], v[204:207], a[48:51]\n"
"v_mfma_f32_16x16x32_bf16 a[20:23], a[248:251], v[192:195], a[20:23]\n"
"v_exp_f32 v157, v157\n"
"v_mfma_f32_16x16x32_bf16 a[20:23], a[252:255], v[196:199], a[20:23]\n"
"v_cvt_pk_bf16_f32 v218, v156, v157\n"
"v_mfma_f32_16x16x32_bf16 a[52:55], a[248:251], v[200:203], a[52:55]\n"
"v_exp_f32 v158, v158\n"
"v_mfma_f32_16x16x32_bf16 a[52:55], a[252:255], v[204:207], a[52:55]\n"
"v_mfma_f32_16x16x32_bf16 a[24:27], v[24:27], v[192:195], a[24:27]\n"
"v_exp_f32 v159, v159\n"
"v_mfma_f32_16x16x32_bf16 a[24:27], v[28:31], v[196:199], a[24:27]\n"
"buffer_load_dwordx4 v1, s[16:19], s60 offen offset:3072 sc1 lds\n"
"v_cvt_pk_bf16_f32 v219, v158, v159\n"
"v_mfma_f32_16x16x32_bf16 a[56:59], v[24:27], v[200:203], a[56:59]\n"
"v_exp_f32 v168, v168\n"
"v_mfma_f32_16x16x32_bf16 a[56:59], v[28:31], v[204:207], a[56:59]\n"
"v_mfma_f32_16x16x32_bf16 a[28:31], v[32:35], v[192:195], a[28:31]\n"
"v_exp_f32 v169, v169\n"
"v_mfma_f32_16x16x32_bf16 a[28:31], v[36:39], v[196:199], a[28:31]\n"
"v_cvt_pk_bf16_f32 v212, v168, v169\n"
"v_mfma_f32_16x16x32_bf16 a[60:63], v[32:35], v[200:203], a[60:63]\n"
"v_exp_f32 v170, v170\n"
"v_mfma_f32_16x16x32_bf16 a[60:63], v[36:39], v[204:207], a[60:63]\n"
"v_mfma_f32_16x16x32_bf16 a[128:131], v[4:7], v[192:195], a[128:131]\n"
"v_exp_f32 v171, v171\n"
"v_mfma_f32_16x16x32_bf16 a[128:131], v[4:7], v[196:199], a[128:131]\n"
"v_cvt_pk_bf16_f32 v213, v170, v171\n"
"s_waitcnt lgkmcnt(0)\n"
"v_mfma_f32_16x16x32_bf16 a[132:135], v[4:7], v[200:203], a[132:135]\n"
"v_mfma_f32_16x16x32_bf16 a[132:135], v[4:7], v[204:207], a[132:135]\n"
"s_lshr_b32 s27, s26, 2\n"
"s_lshl_b32 s27, s27, 7\n"
"s_add_u32 s27, s27, 0xe8000\n"
"s_waitcnt vmcnt(5)\n"
"s_barrier\n"
"buffer_store_dword v22, v23, s[4:7], s27 offen sc1\n"
"s_mov_b32 s69, s76\n"
"s_mov_b32 s76, -1\n"
"s_cmp_lg_u32 s69, 0\n"
"s_cbranch_scc1 .Ladaln_block_A_FULL1\n"
".Ladaln_block_A_MODE2:\n"
"s_mov_b32 s69, s75\n"
".Ladaln_block_A_CHK:\n"
"s_cmp_eq_u32 s69, 0\n"
"s_cbranch_scc1 .Ladaln_block_A_NEXT\n"
".p2alignl 6, 0xbf800000\n"
"s_nop 0\n"
"s_nop 0\n"
"s_nop 0\n"
".Ladaln_block_A_FULL2:\n"
"v_mfma_f32_16x16x32_bf16 v[128:131], a[144:147], v[56:59], v[224:227]\n"
"v_exp_f32 v172, v172\n"
"v_mfma_f32_16x16x32_bf16 v[128:131], a[148:151], v[60:63], v[128:131]\n"
"s_add_u32 s60, s60, s64\n"
"s_mov_b32 s64, s65\n"
"v_mfma_f32_16x16x32_bf16 v[128:131], a[152:155], v[64:67], v[128:131]\n"
"v_exp_f32 v173, v173\n"
"v_mfma_f32_16x16x32_bf16 v[128:131], a[156:159], v[68:71], v[128:131]\n"
"v_cvt_pk_bf16_f32 v220, v172, v173\n"
"s_add_u32 m0, s63, 0x4000\n"
"v_mfma_f32_16x16x32_bf16 v[132:135], a[144:147], v[72:75], v[228:231]\n"
"v_exp_f32 v174, v174\n"
"v_mfma_f32_16x16x32_bf16 v[132:135], a[148:151], v[76:79], v[132:135]\n"
"buffer_load_dwordx4 v9, s[16:19], s61 offen sc1 lds\n"
"s_mov_b32 s65, 0x10000\n"
"v_mfma_f32_16x16x32_bf16 v[132:135], a[152:155], v[80:83], v[132:135]\n"
"v_exp_f32 v175, v175\n"
"v_mfma_f32_16x16x32_bf16 v[132:135], a[156:159], v[84:87], v[132:135]\n"
"v_cvt_pk_bf16_f32 v221, v174, v175\n"
"v_mfma_f32_16x16x32_bf16 v[144:147], a[160:163], v[56:59], v[224:227]\n"
"v_exp_f32 v184, v184\n"
"v_mfma_f32_16x16x32_bf16 v[144:147], a[164:167], v[60:63], v[144:147]\n"
"s_cmp_eq_u32 s8, 15\n"
"s_cmov_b32 s60, s9\n"
"v_mfma_f32_16x16x32_bf16 v[144:147], a[168:171], v[64:67], v[144:147]\n"
"v_exp_f32 v185, v185\n"
"v_mfma_f32_16x16x32_bf16 v[144:147], a[172:175], v[68:71], v[144:147]\n"
"buffer_load_dwordx4 v9, s[16:19], s61 offen offset:1024 sc1 lds\n"
"v_cvt_pk_bf16_f32 v214, v184, v185\n"
"v_mfma_f32_16x16x32_bf16 v[148:151], a[160:163], v[72:75], v[228:231]\n"
"v_exp_f32 v186, v186\n"
"v_mfma_f32_16x16x32_bf16 v[148:151], a[164:167], v[76:79], v[148:151]\n"
"s_xor_b32 s62, s62, 0x8000\n"
"v_mfma_f32_16x16x32_bf16 v[148:151], a[168:171], v[80:83], v[148:151]\n"
"v_exp_f32 v187, v187\n"
"v_mfma_f32_16x16x32_bf16 v[148:151], a[172:175], v[84:87], v[148:151]\n"
"v_cvt_pk_bf16_f32 v215, v186, v187\n"
"v_mfma_f32_16x16x32_bf16 v[160:163], a[176:179], v[56:59], v[224:227]\n"
"v_exp_f32 v188, v188\n"
"v_mfma_f32_16x16x32_bf16 v[160:163], a[180:183], v[60:63], v[160:163]\n"
"s_cmp_eq_u32 s8, 14\n"
"s_cselect_b32 s30, s10, 0x40000000\n"
"v_mfma_f32_16x16x32_bf16 v[160:163], a[184:187], v[64:67], v[160:163]\n"
"v_exp_f32 v189, v189\n"
"v_mfma_f32_16x16x32_bf16 v[160:163], a[188:191], v[68:71], v[160:163]\n"
"buffer_load_dwordx4 v9, s[16:19], s61 offen offset:2048 sc1 lds\n"
"v_cvt_pk_bf16_f32 v222, v188, v189\n"
"v_mfma_f32_16x16x32_bf16 v[164:167], a[176:179], v[72:75], v[228:231]\n"
"v_exp_f32 v190, v190\n"
"v_mfma_f32_16x16x32_bf16 v[164:167], a[180:183], v[76:79], v[164:167]\n"
"s_cmp_eq_u32 s8, 14\n"
"s_cselect_b32 s31, s82, 0\n"
"v_mfma_f32_16x16x32_bf16 v[164:167], a[184:187], v[80:83], v[164:167]\n"
"v_exp_f32 v191, v191\n"
"v_mfma_f32_16x16x32_bf16 v[164:167], a[188:191], v[84:87], v[164:167]\n"
"buffer_load_dword v21, v20, s[4:7], s30 offen sc1\n"
"v_cvt_pk_bf16_f32 v223, v190, v191\n"
"v_mfma_f32_16x16x32_bf16 v[176:179], a[192:195], v[56:59], v[224:227]\n"
"v_exp_f32 v128, v128\n"
"v_mfma_f32_16x16x32_bf16 v[176:179], a[196:199], v[60:63], v[176:179]\n"
"v_mfma_f32_16x16x32_bf16 v[176:179], a[200:203], v[64:67], v[176:179]\n"
"v_exp_f32 v129, v129\n"
"v_mfma_f32_16x16x32_bf16 v[176:179], a[204:207], v[68:71], v[176:179]\n"
"v_xor_b32 v3, s67, v3\n"
"v_cvt_pk_bf16_f32 v192, v128, v129\n"
"v_mfma_f32_16x16x32_bf16 v[180:183], a[192:195], v[72:75], v[228:231]\n"
"v_exp_f32 v130, v130\n"
"v_mfma_f32_16x16x32_bf16 v[180:183], a[196:199], v[76:79], v[180:183]\n"
"buffer_load_dwordx4 v9, s[16:19], s61 offen offset:3072 sc1 lds\n"
"v_mfma_f32_16x16x32_bf16 v[180:183], a[200:203], v[80:83], v[180:183]\n"
"v_exp_f32 v131, v131\n"
"v_mfma_f32_16x16x32_bf16 v[180:183], a[204:207], v[84:87], v[180:183]\n"
"v_cvt_pk_bf16_f32 v193, v130, v131\n"
"s_xor_b32 s63, s63, 0x8000\n"
"v_mfma_f32_16x16x32_bf16 a[64:67], a[208:211], v[208:211], a[64:67]\n"
"v_exp_f32 v132, v132\n"
"v_mfma_f32_16x16x32_bf16 a[64:67], a[212:215], v[212:215], a[64:67]\n"
"s_mov_b32 s61, s60\n"
"s_cmp_eq_u32 s8, 15\n"
"v_mfma_f32_16x16x32_bf16 a[96:99], a[208:211], v[216:219], a[96:99]\n"
"s_cmov_b64 s[16:17], s[0:1]\n"
"s_waitcnt vmcnt(13)\n"
"v_mfma_f32_16x16x32_bf16 a[96:99], a[212:215], v[220:223], a[96:99]\n"
"s_barrier\n"
"s_cmov_b32 s63, s47\n"
"v_mfma_f32_16x16x32_bf16 a[68:71], a[216:219], v[208:211], a[68:71]\n"
"v_exp_f32 v133, v133\n"
"v_mfma_f32_16x16x32_bf16 a[68:71], a[220:223], v[212:215], a[68:71]\n"
"ds_read_b128 a[208:211], v3\n"
"v_cvt_pk_bf16_f32 v200, v132, v133\n"
"v_mfma_f32_16x16x32_bf16 a[100:103], a[216:219], v[216:219], a[100:103]\n"
"v_exp_f32 v134, v134\n"
"v_mfma_f32_16x16x32_bf16 a[100:103], a[220:223], v[220:223], a[100:103]\n"
"ds_read_b128 a[212:215], v3 offset:1024\n"
"s_mov_b32 m0, s62\n"
"v_mfma_f32_16x16x32_bf16 a[72:75], a[224:227], v[208:211], a[72:75]\n"
"v_exp_f32 v135, v135\n"
"v_mfma_f32_16x16x32_bf16 a[72:75], a[228:231], v[212:215], a[72:75]\n"
"ds_read_b128 a[216:219], v3 offset:2048\n"
"v_cvt_pk_bf16_f32 v201, v134, v135\n"
"v_mfma_f32_16x16x32_bf16 a[104:107], a[224:227], v[216:219], a[104:107]\n"
"v_exp_f32 v144, v144\n"
"v_mfma_f32_16x16x32_bf16 a[104:107], a[228:231], v[220:223], a[104:107]\n"
"ds_read_b128 a[220:223], v3 offset:3072\n"
"buffer_load_dwordx4 v1, s[4:7], s60 offen sc1 lds\n"
"v_mfma_f32_16x16x32_bf16 a[76:79], a[232:235], v[208:211], a[76:79]\n"
"v_exp_f32 v145, v145\n"
"v_mfma_f32_16x16x32_bf16 a[76:79], a[236:239], v[212:215], a[76:79]\n"
"ds_read_b128 a[224:227], v3 offset:4096\n"
"v_cvt_pk_bf16_f32 v194, v144, v145\n"
"v_mfma_f32_16x16x32_bf16 a[108:111], a[232:235], v[216:219], a[108:111]\n"
"v_exp_f32 v146, v146\n"
"v_mfma_f32_16x16x32_bf16 a[108:111], a[236:239], v[220:223], a[108:111]\n"
"ds_read_b128 a[228:231], v3 offset:5120\n"
"buffer_load_dwordx4 v1, s[4:7], s60 offen offset:1024 sc1 lds\n"
"v_mfma_f32_16x16x32_bf16 a[80:83], a[240:243], v[208:211], a[80:83]\n"
"v_exp_f32 v147, v147\n"
"v_mfma_f32_16x16x32_bf16 a[80:83], a[244:247], v[212:215], a[80:83]\n"
"ds_read_b128 a[232:235], v3 offset:6144\n"
"v_cvt_pk_bf16_f32 v195, v146, v147\n"
"v_mfma_f32_16x16x32_bf16 a[112:115], a[240:243], v[216:219], a[112:115]\n"
"v_exp_f32 v148, v148\n"
"v_mfma_f32_16x16x32_bf16 a[112:115], a[244:247], v[220:223], a[112:115]\n"
"ds_read_b128 a[236:239], v3 offset:7168\n"
"s_cmp_eq_u32 s8, 16\n"
"v_mfma_f32_16x16x32_bf16 a[84:87], a[248:251], v[208:211], a[84:87]\n"
"v_exp_f32 v149, v149\n"
"v_mfma_f32_16x16x32_bf16 a[84:87], a[252:255], v[212:215], a[84:87]\n"
"ds_read_b128 a[240:243], v3 offset:8192\n"
"v_cvt_pk_bf16_f32 v202, v148, v149\n"
"v_mfma_f32_16x16x32_bf16 a[116:119], a[248:251], v[216:219], a[116:119]\n"
"v_exp_f32 v150, v150\n"
"v_mfma_f32_16x16x32_bf16 a[116:119], a[252:255], v[220:223], a[116:119]\n"
"ds_read_b128 a[244:247], v3 offset:9216\n"
"buffer_load_dwordx4 v1, s[4:7], s60 offen offset:2048 sc1 lds\n"
"v_mfma_f32_16x16x32_bf16 a[88:91], v[24:27], v[208:211], a[88:91]\n"
"v_exp_f32 v151, v151\n"
"v_mfma_f32_16x16x32_bf16 a[88:91], v[28:31], v[212:215], a[88:91]\n"
"ds_read_b128 a[248:251], v3 offset:10240\n"
"v_cvt_pk_bf16_f32 v203, v150, v151\n"
"v_mfma_f32_16x16x32_bf16 a[120:123], v[24:27], v[216:219], a[120:123]\n"
"v_exp_f32 v160, v160\n"
"v_mfma_f32_16x16x32_bf16 a[120:123], v[28:31], v[220:223], a[120:123]\n"
"ds_read_b128 a[252:255], v3 offset:11264\n"
"s_cmov_b32 s18, 0\n"
"v_mfma_f32_16x16x32_bf16 a[92:95], v[32:35], v[208:211], a[92:95]\n"
"v_exp_f32 v161, v161\n"
"v_mfma_f32_16x16x32_bf16 a[92:95], v[36:39], v[212:215], a[92:95]\n"
"ds_read_b128 v[24:27], v3 offset:12288\n"
"v_cvt_pk_bf16_f32 v196, v160, v161\n"
"v_mfma_f32_16x16x32_bf16 a[124:127], v[32:35], v[216:219], a[124:127]\n"
"v_exp_f32 v162, v162\n"
"v_mfma_f32_16x16x32_bf16 a[124:127], v[36:39], v[220:223], a[124:127]\n"
"ds_read_b128 v[28:31], v3 offset:13312\n"
"buffer_load_dwordx4 v1, s[4:7], s60 offen offset:3072 sc1 lds\n"
"v_mfma_f32_16x16x32_bf16 a[136:139], v[4:7], v[208:211], a[136:139]\n"
"v_exp_f32 v163, v163\n"
"v_mfma_f32_16x16x32_bf16 a[136:139], v[4:7], v[212:215], a[136:139]\n"
"ds_read_b128 v[32:35], v3 offset:14336\n"
"v_cvt_pk_bf16_f32 v197, v162, v163\n"
"v_mfma_f32_16x16x32_bf16 a[140:143], v[4:7], v[216:219], a[140:143]\n"
"v_exp_f32 v164, v164\n"
"v_mfma_f32_16x16x32_bf16 a[140:143], v[4:7], v[220:223], a[140:143]\n"
"ds_read_b128 v[36:39], v3 offset:15360\n"
"s_cmov_b32 s63, s78\n"
"v_mfma_f32_16x16x32_bf16 v[136:139], a[144:147], v[88:91], v[232:235]\n"
"v_exp_f32 v165, v165\n"
"v_mfma_f32_16x16x32_bf16 v[136:139], a[148:151], v[92:95], v[136:139]\n"
"v_xor_b32 v2, s66, v2\n"
"v_cvt_pk_bf16_f32 v204, v164, v165\n"
"v_mfma_f32_16x16x32_bf16 v[136:139], a[152:155], v[96:99], v[136:139]\n"
"v_exp_f32 v166, v166\n"
"v_mfma_f32_16x16x32_bf16 v[136:139], a[156:159], v[100:103], v[136:139]\n"
"s_add_u32 m0, s62, 0x4000\n"
"s_add_u32 s8, s8, 1\n"
"v_mfma_f32_16x16x32_bf16 v[140:143], a[144:147], v[104:107], v[236:239]\n"
"v_exp_f32 v167, v167\n"
"v_mfma_f32_16x16x32_bf16 v[140:143], a[148:151], v[108:111], v[140:143]\n"
"buffer_load_dwordx4 v9, s[4:7], s60 offen sc1 lds\n"
"v_cvt_pk_bf16_f32 v205, v166, v167\n"
"v_mfma_f32_16x16x32_bf16 v[140:143], a[152:155], v[112:115], v[140:143]\n"
"s_waitcnt vmcnt(10) lgkmcnt(0)\n"
"v_mfma_f32_16x16x32_bf16 v[140:143], a[156:159], v[116:119], v[140:143]\n"
"s_barrier\n"
"v_mfma_f32_16x16x32_bf16 v[152:155], a[160:163], v[88:91], v[232:235]\n"
"v_exp_f32 v176, v176\n"
"v_mfma_f32_16x16x32_bf16 v[152:155], a[164:167], v[92:95], v[152:155]\n"
"ds_read_b128 a[144:147], v2\n"
"v_mfma_f32_16x16x32_bf16 v[152:155], a[168:171], v[96:99], v[152:155]\n"
"v_exp_f32 v177, v177\n"
"v_mfma_f32_16x16x32_bf16 v[152:155], a[172:175], v[100:103], v[152:155]\n"
"ds_read_b128 a[148:151], v2 offset:1024\n"
"v_cvt_pk_bf16_f32 v198, v176, v177\n"
"v_mfma_f32_16x16x32_bf16 v[156:159], a[160:163], v[104:107], v[236:239]\n"
"v_exp_f32 v178, v178\n"
"v_mfma_f32_16x16x32_bf16 v[156:159], a[164:167], v[108:111], v[156:159]\n"
"ds_read_b128 a[152:155], v2 offset:2048\n"
"buffer_load_dwordx4 v9, s[4:7], s60 offen offset:1024 sc1 lds\n"
"v_mfma_f32_16x16x32_bf16 v[156:159], a[168:171], v[112:115], v[156:159]\n"
"v_exp_f32 v179, v179\n"
"v_mfma_f32_16x16x32_bf16 v[156:159], a[172:175], v[116:119], v[156:159]\n"
"ds_read_b128 a[156:159], v2 offset:3072\n"
"v_cvt_pk_bf16_f32 v199, v178, v179\n"
"v_mfma_f32_16x16x32_bf16 v[168:171], a[176:179], v[88:91], v[232:235]\n"
"v_exp_f32 v180, v180\n"
"v_mfma_f32_16x16x32_bf16 v[168:171], a[180:183], v[92:95], v[168:171]\n"
"ds_read_b128 a[160:163], v2 offset:4096\n"
"s_waitcnt vmcnt(7)\n"
"v_mfma_f32_16x16x32_bf16 v[168:171], a[184:187], v[96:99], v[168:171]\n"
"v_exp_f32 v181, v181\n"
"v_mfma_f32_16x16x32_bf16 v[168:171], a[188:191], v[100:103], v[168:171]\n"
"ds_read_b128 a[164:167], v2 offset:5120\n"
"v_cvt_pk_bf16_f32 v206, v180, v181\n"
"v_mfma_f32_16x16x32_bf16 v[172:175], a[176:179], v[104:107], v[236:239]\n"
"v_exp_f32 v182, v182\n"
"v_mfma_f32_16x16x32_bf16 v[172:175], a[180:183], v[108:111], v[172:175]\n"
"ds_read_b128 a[168:171], v2 offset:6144\n"
"buffer_load_dwordx4 v9, s[4:7], s60 offen offset:2048 sc1 lds\n"
"v_mfma_f32_16x16x32_bf16 v[172:175], a[184:187], v[112:115], v[172:175]\n"
"v_exp_f32 v183, v183\n"
"v_mfma_f32_16x16x32_bf16 v[172:175], a[188:191], v[116:119], v[172:175]\n"
"ds_read_b128 a[172:175], v2 offset:7168\n"
"v_cvt_pk_bf16_f32 v207, v182, v183\n"
"v_mfma_f32_16x16x32_bf16 v[184:187], a[192:195], v[88:91], v[232:235]\n"
"v_cmp_ne_u32 vcc, s31, v21\n"
"v_mfma_f32_16x16x32_bf16 v[184:187], a[196:199], v[92:95], v[184:187]\n"
"ds_read_b128 a[176:179], v2 offset:8192\n"
"s_cbranch_vccnz .Ladaln_block_P_prep1\n"
".Ladaln_block_P_prep1_R:\n"
"v_mfma_f32_16x16x32_bf16 v[184:187], a[200:203], v[96:99], v[184:187]\n"
"v_exp_f32 v136, v136\n"
"v_mfma_f32_16x16x32_bf16 v[184:187], a[204:207], v[100:103], v[184:187]\n"
"ds_read_b128 a[180:183], v2 offset:9216\n"
"buffer_load_dwordx4 v9, s[4:7], s60 offen offset:3072 sc1 lds\n"
"v_mfma_f32_16x16x32_bf16 v[188:191], a[192:195], v[104:107], v[236:239]\n"
"v_exp_f32 v137, v137\n"
"v_mfma_f32_16x16x32_bf16 v[188:191], a[196:199], v[108:111], v[188:191]\n"
"ds_read_b128 a[184:187], v2 offset:10240\n"
"v_cvt_pk_bf16_f32 v208, v136, v137\n"
"v_mfma_f32_16x16x32_bf16 v[188:191], a[200:203], v[112:115], v[188:191]\n"
"v_exp_f32 v138, v138\n"
"v_mfma_f32_16x16x32_bf16 v[188:191], a[204:207], v[116:119], v[188:191]\n"
"ds_read_b128 a[188:191], v2 offset:11264\n"
"v_mfma_f32_16x16x32_bf16 a[0:3], a[208:211], v[192:195], a[0:3]\n"
"v_exp_f32 v139, v139\n"
"v_mfma_f32_16x16x32_bf16 a[0:3], a[212:215], v[196:199], a[0:3]\n"
"ds_read_b128 a[192:195], v2 offset:12288\n"
"v_cvt_pk_bf16_f32 v209, v138, v139\n"
"v_mfma_f32_16x16x32_bf16 a[32:35], a[208:211], v[200:203], a[32:35]\n"
"v_exp_f32 v140, v140\n"
"v_mfma_f32_16x16x32_bf16 a[32:35], a[212:215], v[204:207], a[32:35]\n"
"ds_read_b128 a[196:199], v2 offset:13312\n"
"s_mov_b32 m0, s63\n"
"v_mfma_f32_16x16x32_bf16 a[4:7], a[216:219], v[192:195], a[4:7]\n"
"v_exp_f32 v141, v141\n"
"v_mfma_f32_16x16x32_bf16 a[4:7], a[220:223], v[196:199], a[4:7]\n"
"ds_read_b128 a[200:203], v2 offset:14336\n"
"v_cvt_pk_bf16_f32 v216, v140, v141\n"
"v_mfma_f32_16x16x32_bf16 a[36:39], a[216:219], v[200:203], a[36:39]\n"
"v_exp_f32 v142, v142\n"
"v_mfma_f32_16x16x32_bf16 a[36:39], a[220:223], v[204:207], a[36:39]\n"
"ds_read_b128 a[204:207], v2 offset:15360\n"
"buffer_load_dwordx4 v1, s[16:19], s60 offen sc1 lds\n"
"v_mfma_f32_16x16x32_bf16 a[8:11], a[224:227], v[192:195], a[8:11]\n"
"v_exp_f32 v143, v143\n"
"v_mfma_f32_16x16x32_bf16 a[8:11], a[228:231], v[196:199], a[8:11]\n"
"v_cvt_pk_bf16_f32 v217, v142, v143\n"
"s_mov_b32 s67, s66\n"
"v_mfma_f32_16x16x32_bf16 a[40:43], a[224:227], v[200:203], a[40:43]\n"
"v_exp_f32 v152, v152\n"
"v_mfma_f32_16x16x32_bf16 a[40:43], a[228:231], v[204:207], a[40:43]\n"
"buffer_load_dwordx4 v1, s[16:19], s60 offen offset:1024 sc1 lds\n"
"s_mov_b32 s66, s68\n"
"v_mfma_f32_16x16x32_bf16 a[12:15], a[232:235], v[192:195], a[12:15]\n"
"v_exp_f32 v153, v153\n"
"v_mfma_f32_16x16x32_bf16 a[12:15], a[236:239], v[196:199], a[12:15]\n"
"v_cvt_pk_bf16_f32 v210, v152, v153\n"
"s_mov_b32 s68, 0x8000\n"
"v_mfma_f32_16x16x32_bf16 a[44:47], a[232:235], v[200:203], a[44:47]\n"
"v_exp_f32 v154, v154\n"
"v_mfma_f32_16x16x32_bf16 a[44:47], a[236:239], v[204:207], a[44:47]\n"
"s_sub_u32 s69, s69, 1\n"
"s_cmp_lg_u32 s69, 0\n"
"v_mfma_f32_16x16x32_bf16 a[16:19], a[240:243], v[192:195], a[16:19]\n"
"v_exp_f32 v155, v155\n"
"v_mfma_f32_16x16x32_bf16 a[16:19], a[244:247], v[196:199], a[16:19]\n"
"v_cvt_pk_bf16_f32 v211, v154, v155\n"
"v_mfma_f32_16x16x32_bf16 a[48:51], a[240:243], v[200:203], a[48:51]\n"
"v_exp_f32 v156, v156\n"
"v_mfma_f32_16x16x32_bf16 a[48:51], a[244:247], v[204:207], a[48:51]\n"
"buffer_load_dwordx4 v1, s[16:19], s60 offen offset:2048 sc1 lds\n"
"v_mfma_f32_16x16x32_bf16 a[20:23], a[248:251], v[192:195], a[20:23]\n"
"v_exp_f32 v157, v157\n"
"v_mfma_f32_16x16x32_bf16 a[20:23], a[252:255], v[196:199], a[20:23]\n"
"v_cvt_pk_bf16_f32 v218, v156, v157\n"
"v_mfma_f32_16x16x32_bf16 a[52:55], a[248:251], v[200:203], a[52:55]\n"
"v_exp_f32 v158, v158\n"
"v_mfma_f32_16x16x32_bf16 a[52:55], a[252:255], v[204:207], a[52:55]\n"
"v_mfma_f32_16x16x32_bf16 a[24:27], v[24:27], v[192:195], a[24:27]\n"
"v_exp_f32 v159, v159\n"
"v_mfma_f32_16x16x32_bf16 a[24:27], v[28:31], v[196:199], a[24:27]\n"
"v_cvt_pk_bf16_f32 v219, v158, v159\n"
"v_mfma_f32_16x16x32_bf16 a[56:59], v[24:27], v[200:203], a[56:59]\n"
"v_exp_f32 v168, v168\n"
"v_mfma_f32_16x16x32_bf16 a[56:59], v[28:31], v[204:207], a[56:59]\n"
"buffer_load_dwordx4 v1, s[16:19], s60 offen offset:3072 sc1 lds\n"
"v_mfma_f32_16x16x32_bf16 a[28:31], v[32:35], v[192:195], a[28:31]\n"
"v_exp_f32 v169, v169\n"
"v_mfma_f32_16x16x32_bf16 a[28:31], v[36:39], v[196:199], a[28:31]\n"
"v_cvt_pk_bf16_f32 v212, v168, v169\n"
"v_mfma_f32_16x16x32_bf16 a[60:63], v[32:35], v[200:203], a[60:63]\n"
"v_exp_f32 v170, v170\n"
"v_mfma_f32_16x16x32_bf16 a[60:63], v[36:39], v[204:207], a[60:63]\n"
"v_mfma_f32_16x16x32_bf16 a[128:131], v[4:7], v[192:195], a[128:131]\n"
"v_exp_f32 v171, v171\n"
"v_mfma_f32_16x16x32_bf16 a[128:131], v[4:7], v[196:199], a[128:131]\n"
"v_cvt_pk_bf16_f32 v213, v170, v171\n"
"s_waitcnt lgkmcnt(0)\n"
"v_mfma_f32_16x16x32_bf16 a[132:135], v[4:7], v[200:203], a[132:135]\n"
"v_mfma_f32_16x16x32_bf16 a[132:135], v[4:7], v[204:207], a[132:135]\n"
"s_cbranch_scc1 .Ladaln_block_A_FULL2\n"
".Ladaln_block_A_NEXT:\n"
"s_cmp_eq_u32 s71, 0\n"
"s_cbranch_scc1 .Ladaln_block_A_DIAG\n"
"s_sub_u32 s54, s9, 0xd000\n"
"v_exp_f32 v172, v172\n"
"v_exp_f32 v173, v173\n"
"v_exp_f32 v174, v174\n"
"v_cvt_pk_bf16_f32 v220, v172, v173\n"
"v_exp_f32 v175, v175\n"
"v_add_u32 v12, s78, v1\n"
"v_cvt_pk_bf16_f32 v221, v174, v175\n"
"v_exp_f32 v188, v188\n"
"v_exp_f32 v189, v189\n"
"v_exp_f32 v190, v190\n"
"v_cvt_pk_bf16_f32 v222, v188, v189\n"
"v_exp_f32 v191, v191\n"
"v_add_u32 v17, s47, v1\n"
"v_cvt_pk_bf16_f32 v223, v190, v191\n"
"s_waitcnt vmcnt(12)\n"
"ds_read_b128 a[160:163], v17\n"
"v_mfma_f32_16x16x32_bf16 a[96:99], a[208:211], v[216:219], a[96:99]\n"
"v_exp_f32 v184, v184\n"
"v_lshl_add_u32 v14, v0, 2, s47\n"
"v_mfma_f32_16x16x32_bf16 a[96:99], a[212:215], v[220:223], a[96:99]\n"
"ds_write_b128 v12, a[32:35] offset:32768\n"
"buffer_load_dwordx4 a[164:167], v1, s[4:7], s54 offen sc1\n"
"v_accvgpr_read_b32 v224, a0\n"
"v_mfma_f32_16x16x32_bf16 a[100:103], a[216:219], v[216:219], a[100:103]\n"
"v_exp_f32 v185, v185\n"
"v_accvgpr_read_b32 v225, a1\n"
"v_mfma_f32_16x16x32_bf16 a[100:103], a[220:223], v[220:223], a[100:103]\n"
"ds_write_b128 v12, a[36:39] offset:33792\n"
"v_cvt_pk_bf16_f32 v214, v184, v185\n"
"v_accvgpr_read_b32 v226, a2\n"
"v_mfma_f32_16x16x32_bf16 a[104:107], a[224:227], v[216:219], a[104:107]\n"
"v_exp_f32 v186, v186\n"
"v_accvgpr_read_b32 v227, a3\n"
"v_mfma_f32_16x16x32_bf16 a[104:107], a[228:231], v[220:223], a[104:107]\n"
"ds_write_b128 v12, a[40:43] offset:34816\n"
"buffer_load_dwordx4 a[168:171], v1, s[4:7], s54 offen offset:1024 sc1\n"
"v_accvgpr_read_b32 v228, a4\n"
"v_mfma_f32_16x16x32_bf16 a[108:111], a[232:235], v[216:219], a[108:111]\n"
"v_exp_f32 v187, v187\n"
"v_accvgpr_read_b32 v229, a5\n"
"v_mfma_f32_16x16x32_bf16 a[108:111], a[236:239], v[220:223], a[108:111]\n"
"ds_write_b128 v12, a[44:47] offset:35840\n"
"v_cvt_pk_bf16_f32 v215, v186, v187\n"
"v_accvgpr_read_b32 v230, a6\n"
"v_mfma_f32_16x16x32_bf16 a[112:115], a[240:243], v[216:219], a[112:115]\n"
"v_accvgpr_read_b32 v231, a7\n"
"v_accvgpr_read_b32 v232, a8\n"
"v_accvgpr_read_b32 v233, a9\n"
"v_mfma_f32_16x16x32_bf16 a[112:115], a[244:247], v[220:223], a[112:115]\n"
"ds_write_b128 v12, a[48:51] offset:49152\n"
"buffer_load_dwordx4 a[172:175], v1, s[4:7], s54 offen offset:2048 sc1\n"
"v_accvgpr_read_b32 v234, a10\n"
"v_mfma_f32_16x16x32_bf16 a[116:119], a[248:251], v[216:219], a[116:119]\n"
"v_accvgpr_read_b32 v235, a11\n"
"v_accvgpr_read_b32 v236, a12\n"
"v_accvgpr_read_b32 v237, a13\n"
"v_mfma_f32_16x16x32_bf16 a[116:119], a[252:255], v[220:223], a[116:119]\n"
"ds_write_b128 v12, a[52:55] offset:50176\n"
"v_accvgpr_read_b32 v238, a14\n"
"v_accvgpr_read_b32 v239, a15\n"
"v_mfma_f32_16x16x32_bf16 a[120:123], v[24:27], v[216:219], a[120:123]\n"
"v_accvgpr_read_b32 v240, a16\n"
"v_accvgpr_read_b32 v241, a17\n"
"v_accvgpr_read_b32 v242, a18\n"
"v_mfma_f32_16x16x32_bf16 a[120:123], v[28:31], v[220:223], a[120:123]\n"
"ds_write_b128 v12, a[56:59] offset:51200\n"
"buffer_load_dwordx4 a[176:179], v1, s[4:7], s54 offen offset:3072 sc1\n"
"s_add_u32 s54, s54, 0x1000\n"
"v_mfma_f32_16x16x32_bf16 a[124:127], v[32:35], v[216:219], a[124:127]\n"
"v_accvgpr_read_b32 v243, a19\n"
"v_accvgpr_read_b32 v244, a20\n"
"v_accvgpr_read_b32 v245, a21\n"
"v_mfma_f32_16x16x32_bf16 a[124:127], v[36:39], v[220:223], a[124:127]\n"
"ds_write_b128 v12, a[60:63] offset:52224\n"
"v_accvgpr_read_b32 v246, a22\n"
"v_accvgpr_read_b32 v247, a23\n"
"v_mfma_f32_16x16x32_bf16 a[140:143], v[4:7], v[216:219], a[140:143]\n"
"v_accvgpr_read_b32 v248, a24\n"
"v_accvgpr_read_b32 v249, a25\n"
"v_accvgpr_read_b32 v250, a26\n"
"v_mfma_f32_16x16x32_bf16 a[140:143], v[4:7], v[220:223], a[140:143]\n"
"ds_write_b32 v14, a132\n"
"buffer_load_dwordx4 a[180:183], v1, s[4:7], s54 offen sc1\n"
"v_accvgpr_read_b32 v251, a27\n"
"v_mfma_f32_16x16x32_bf16 a[64:67], a[208:211], v[208:211], a[64:67]\n"
"v_accvgpr_read_b32 v252, a28\n"
"v_accvgpr_read_b32 v253, a29\n"
"v_accvgpr_read_b32 v254, a30\n"
"v_mfma_f32_16x16x32_bf16 a[64:67], a[212:215], v[212:215], a[64:67]\n"
"ds_write_b128 v12, a[96:99] offset:16384\n"
"v_accvgpr_read_b32 v255, a31\n"
"v_accvgpr_read_b32 v48, a128\n"
"v_mfma_f32_16x16x32_bf16 a[68:71], a[216:219], v[208:211], a[68:71]\n"
"v_xor_b32 v13, 0x3000, v12\n"
"v_xor_b32 v15, 0x3000, v14\n"
"s_lshr_b32 s83, s2, 3\n"
"v_mfma_f32_16x16x32_bf16 a[68:71], a[220:223], v[212:215], a[68:71]\n"
"ds_write_b128 v12, a[100:103] offset:17408\n"
"buffer_load_dwordx4 a[184:187], v1, s[4:7], s54 offen offset:1024 sc1\n"
"s_lshl2_add_u32 s83, s83, s3\n"
"v_mfma_f32_16x16x32_bf16 a[72:75], a[224:227], v[208:211], a[72:75]\n"
"s_lshl_b32 s83, s83, 10\n"
"v_lshlrev_b32 v19, 7, v0\n"
"v_min_u32 v19, 0x380, v19\n"
"v_mfma_f32_16x16x32_bf16 a[72:75], a[228:231], v[212:215], a[72:75]\n"
"ds_write_b128 v12, a[104:107] offset:18432\n"
"buffer_load_dword v18, v19, s[12:15], s83 offen\n"
"v_mfma_f32_16x16x32_bf16 a[76:79], a[232:235], v[208:211], a[76:79]\n"
"v_mfma_f32_16x16x32_bf16 a[76:79], a[236:239], v[212:215], a[76:79]\n"
"ds_write_b128 v12, a[108:111] offset:19456\n"
"buffer_load_dwordx4 a[188:191], v1, s[4:7], s54 offen offset:2048 sc1\n"
"v_mfma_f32_16x16x32_bf16 a[80:83], a[240:243], v[208:211], a[80:83]\n"
"s_waitcnt vmcnt(8)\n"
"v_mfma_f32_16x16x32_bf16 a[80:83], a[244:247], v[212:215], a[80:83]\n"
"ds_write_b128 v12, a[112:115]\n"
"v_mfma_f32_16x16x32_bf16 a[84:87], a[248:251], v[208:211], a[84:87]\n"
"ds_write_b128 v12, a[116:119] offset:1024\n"
"v_mfma_f32_16x16x32_bf16 a[84:87], a[252:255], v[212:215], a[84:87]\n"
"ds_write_b128 v12, a[120:123] offset:2048\n"
"buffer_load_dwordx4 a[192:195], v1, s[4:7], s54 offen offset:3072 sc1\n"
"s_add_u32 s54, s54, 0x3000\n"
"v_mfma_f32_16x16x32_bf16 a[88:91], v[24:27], v[208:211], a[88:91]\n"
"ds_write_b128 v12, a[124:127] offset:3072\n"
"v_mfma_f32_16x16x32_bf16 a[88:91], v[28:31], v[212:215], a[88:91]\n"
"ds_write_b32 v14, a140 offset:256\n"
"v_mfma_f32_16x16x32_bf16 a[92:95], v[32:35], v[208:211], a[92:95]\n"
"v_mfma_f32_16x16x32_bf16 a[92:95], v[36:39], v[212:215], a[92:95]\n"
"v_mfma_f32_16x16x32_bf16 a[136:139], v[4:7], v[208:211], a[136:139]\n"
"v_mfma_f32_16x16x32_bf16 a[136:139], v[4:7], v[212:215], a[136:139]\n"
"s_waitcnt lgkmcnt(0)\n"
"s_barrier\n"
"ds_read_b32 v49, v15\n"
"ds_read_b32 v51, v15 offset:256\n"
"ds_read_b128 v[128:131], v13 offset:32768\n"
"ds_read_b128 v[132:135], v13 offset:33792\n"
"ds_read_b128 v[136:139], v13 offset:34816\n"
"ds_read_b128 v[140:143], v13 offset:35840\n"
"ds_read_b128 v[144:147], v13 offset:49152\n"
"ds_read_b128 v[148:151], v13 offset:50176\n"
"ds_read_b128 v[152:155], v13 offset:51200\n"
"ds_read_b128 v[156:159], v13 offset:52224\n"
"ds_read_b128 v[160:163], v13 offset:16384\n"
"ds_read_b128 v[164:167], v13 offset:17408\n"
"ds_read_b128 v[168:171], v13 offset:18432\n"
"ds_read_b128 v[172:175], v13 offset:19456\n"
"ds_read_b128 v[176:179], v13\n"
"ds_read_b128 v[180:183], v13 offset:1024\n"
"ds_read_b128 v[184:187], v13 offset:2048\n"
"ds_read_b128 v[188:191], v13 offset:3072\n"
"buffer_load_dwordx4 a[196:199], v1, s[4:7], s54 offen sc1\n"
"v_accvgpr_read_b32 v192, a64\n"
"v_accvgpr_read_b32 v193, a65\n"
"v_accvgpr_read_b32 v194, a66\n"
"v_accvgpr_read_b32 v195, a67\n"
"v_accvgpr_read_b32 v196, a68\n"
"v_accvgpr_read_b32 v197, a69\n"
"v_accvgpr_read_b32 v198, a70\n"
"v_accvgpr_read_b32 v199, a71\n"
"v_accvgpr_read_b32 v200, a72\n"
"v_accvgpr_read_b32 v201, a73\n"
"v_accvgpr_read_b32 v202, a74\n"
"v_accvgpr_read_b32 v203, a75\n"
"v_accvgpr_read_b32 v204, a76\n"
"v_accvgpr_read_b32 v205, a77\n"
"v_accvgpr_read_b32 v206, a78\n"
"v_accvgpr_read_b32 v207, a79\n"
"v_accvgpr_read_b32 v208, a80\n"
"v_accvgpr_read_b32 v209, a81\n"
"v_accvgpr_read_b32 v210, a82\n"
"v_accvgpr_read_b32 v211, a83\n"
"v_accvgpr_read_b32 v212, a84\n"
"v_accvgpr_read_b32 v213, a85\n"
"v_accvgpr_read_b32 v214, a86\n"
"v_accvgpr_read_b32 v215, a87\n"
"v_accvgpr_read_b32 v216, a88\n"
"v_accvgpr_read_b32 v217, a89\n"
"v_accvgpr_read_b32 v218, a90\n"
"v_accvgpr_read_b32 v219, a91\n"
"v_accvgpr_read_b32 v220, a92\n"
"v_accvgpr_read_b32 v221, a93\n"
"v_accvgpr_read_b32 v222, a94\n"
"v_accvgpr_read_b32 v223, a95\n"
"v_accvgpr_read_b32 v50, a136\n"
"s_waitcnt lgkmcnt(8)\n"
"buffer_load_dwordx4 a[200:203], v1, s[4:7], s54 offen offset:1024 sc1\n"
"v_add_f32 v48, v48, v49\n"
"v_add_f32 v50, v50, v51\n"
"v_add_f32 v120, v48, v50\n"
"v_cmp_nge_f32 vcc, 0x67800000, v120\n"
"s_nop 0\n"
"s_cbranch_vccnz .Ladaln_block_S_F\n"
".Ladaln_block_S_F_R:\n"
"v_rcp_f32 v48, v48\n"
"v_rcp_f32 v50, v50\n"
"v_mov_b32 v49, v48\n"
"v_mov_b32 v51, v50\n"
"v_pk_add_f32 v[224:225], v[224:225], v[128:129]\n"
"v_pk_add_f32 v[226:227], v[226:227], v[130:131]\n"
"v_pk_add_f32 v[228:229], v[228:229], v[132:133]\n"
"v_pk_add_f32 v[230:231], v[230:231], v[134:135]\n"
"v_pk_mul_f32 v[224:225], v[224:225], v[48:49]\n"
"v_pk_mul_f32 v[226:227], v[226:227], v[48:49]\n"
"v_pk_mul_f32 v[228:229], v[228:229], v[48:49]\n"
"v_pk_mul_f32 v[230:231], v[230:231], v[48:49]\n"
"v_cvt_pk_bf16_f32 v224, v224, v225\n"
"v_cvt_pk_bf16_f32 v225, v226, v227\n"
"v_cvt_pk_bf16_f32 v226, v228, v229\n"
"v_cvt_pk_bf16_f32 v227, v230, v231\n"
"s_add_u32 s74, s73, 0\n"
"buffer_store_dwordx4 v[224:227], v1, s[4:7], s74 offen sc1\n"
"v_pk_add_f32 v[232:233], v[232:233], v[136:137]\n"
"v_pk_add_f32 v[234:235], v[234:235], v[138:139]\n"
"v_pk_add_f32 v[236:237], v[236:237], v[140:141]\n"
"v_pk_add_f32 v[238:239], v[238:239], v[142:143]\n"
"v_pk_mul_f32 v[232:233], v[232:233], v[48:49]\n"
"v_pk_mul_f32 v[234:235], v[234:235], v[48:49]\n"
"v_pk_mul_f32 v[236:237], v[236:237], v[48:49]\n"
"v_pk_mul_f32 v[238:239], v[238:239], v[48:49]\n"
"v_cvt_pk_bf16_f32 v232, v232, v233\n"
"v_cvt_pk_bf16_f32 v233, v234, v235\n"
"v_cvt_pk_bf16_f32 v234, v236, v237\n"
"v_cvt_pk_bf16_f32 v235, v238, v239\n"
"s_add_u32 s74, s73, 0x1000\n"
"buffer_store_dwordx4 v[232:235], v1, s[4:7], s74 offen sc1\n"
"v_pk_add_f32 v[240:241], v[240:241], v[144:145]\n"
"v_pk_add_f32 v[242:243], v[242:243], v[146:147]\n"
"v_pk_add_f32 v[244:245], v[244:245], v[148:149]\n"
"v_pk_add_f32 v[246:247], v[246:247], v[150:151]\n"
"v_pk_mul_f32 v[240:241], v[240:241], v[48:49]\n"
"v_pk_mul_f32 v[242:243], v[242:243], v[48:49]\n"
"v_pk_mul_f32 v[244:245], v[244:245], v[48:49]\n"
"v_pk_mul_f32 v[246:247], v[246:247], v[48:49]\n"
"v_cvt_pk_bf16_f32 v240, v240, v241\n"
"v_cvt_pk_bf16_f32 v241, v242, v243\n"
"v_cvt_pk_bf16_f32 v242, v244, v245\n"
"v_cvt_pk_bf16_f32 v243, v246, v247\n"
"s_add_u32 s74, s73, 0x4000\n"
"buffer_store_dwordx4 v[240:243], v1, s[4:7], s74 offen sc1\n"
"v_pk_add_f32 v[248:249], v[248:249], v[152:153]\n"
"v_pk_add_f32 v[250:251], v[250:251], v[154:155]\n"
"v_pk_add_f32 v[252:253], v[252:253], v[156:157]\n"
"v_pk_add_f32 v[254:255], v[254:255], v[158:159]\n"
"v_pk_mul_f32 v[248:249], v[248:249], v[48:49]\n"
"v_pk_mul_f32 v[250:251], v[250:251], v[48:49]\n"
"v_pk_mul_f32 v[252:253], v[252:253], v[48:49]\n"
"v_pk_mul_f32 v[254:255], v[254:255], v[48:49]\n"
"v_cvt_pk_bf16_f32 v248, v248, v249\n"
"v_cvt_pk_bf16_f32 v249, v250, v251\n"
"v_cvt_pk_bf16_f32 v250, v252, v253\n"
"v_cvt_pk_bf16_f32 v251, v254, v255\n"
"s_add_u32 s74, s73, 0x5000\n"
"buffer_store_dwordx4 v[248:251], v1, s[4:7], s74 offen sc1\n"
"s_waitcnt lgkmcnt(0)\n"
"v_pk_add_f32 v[192:193], v[192:193], v[160:161]\n"
"v_pk_add_f32 v[194:195], v[194:195], v[162:163]\n"
"v_pk_add_f32 v[196:197], v[196:197], v[164:165]\n"
"v_pk_add_f32 v[198:199], v[198:199], v[166:167]\n"
"v_pk_mul_f32 v[192:193], v[192:193], v[50:51]\n"
"v_pk_mul_f32 v[194:195], v[194:195], v[50:51]\n"
"v_pk_mul_f32 v[196:197], v[196:197], v[50:51]\n"
"v_pk_mul_f32 v[198:199], v[198:199], v[50:51]\n"
"v_cvt_pk_bf16_f32 v192, v192, v193\n"
"v_cvt_pk_bf16_f32 v193, v194, v195\n"
"v_cvt_pk_bf16_f32 v194, v196, v197\n"
"v_cvt_pk_bf16_f32 v195, v198, v199\n"
"s_add_u32 s74, s73, 0\n"
"buffer_store_dwordx4 v[192:195], v1, s[4:7], s74 offen offset:2048 sc1\n"
"v_pk_add_f32 v[200:201], v[200:201], v[168:169]\n"
"v_pk_add_f32 v[202:203], v[202:203], v[170:171]\n"
"v_pk_add_f32 v[204:205], v[204:205], v[172:173]\n"
"v_pk_add_f32 v[206:207], v[206:207], v[174:175]\n"
"v_pk_mul_f32 v[200:201], v[200:201], v[50:51]\n"
"v_pk_mul_f32 v[202:203], v[202:203], v[50:51]\n"
"v_pk_mul_f32 v[204:205], v[204:205], v[50:51]\n"
"v_pk_mul_f32 v[206:207], v[206:207], v[50:51]\n"
"v_cvt_pk_bf16_f32 v200, v200, v201\n"
"v_cvt_pk_bf16_f32 v201, v202, v203\n"
"v_cvt_pk_bf16_f32 v202, v204, v205\n"
"v_cvt_pk_bf16_f32 v203, v206, v207\n"
"s_add_u32 s74, s73, 0x1000\n"
"buffer_store_dwordx4 v[200:203], v1, s[4:7], s74 offen offset:2048 sc1\n"
"v_pk_add_f32 v[208:209], v[208:209], v[176:177]\n"
"v_pk_add_f32 v[210:211], v[210:211], v[178:179]\n"
"v_pk_add_f32 v[212:213], v[212:213], v[180:181]\n"
"v_pk_add_f32 v[214:215], v[214:215], v[182:183]\n"
"v_pk_mul_f32 v[208:209], v[208:209], v[50:51]\n"
"v_pk_mul_f32 v[210:211], v[210:211], v[50:51]\n"
"v_pk_mul_f32 v[212:213], v[212:213], v[50:51]\n"
"v_pk_mul_f32 v[214:215], v[214:215], v[50:51]\n"
"v_cvt_pk_bf16_f32 v208, v208, v209\n"
"v_cvt_pk_bf16_f32 v209, v210, v211\n"
"v_cvt_pk_bf16_f32 v210, v212, v213\n"
"v_cvt_pk_bf16_f32 v211, v214, v215\n"
"s_add_u32 s74, s73, 0x4000\n"
"buffer_store_dwordx4 v[208:211], v1, s[4:7], s74 offen offset:2048 sc1\n"
"v_pk_add_f32 v[216:217], v[216:217], v[184:185]\n"
"v_pk_add_f32 v[218:219], v[218:219], v[186:187]\n"
"v_pk_add_f32 v[220:221], v[220:221], v[188:189]\n"
"v_pk_add_f32 v[222:223], v[222:223], v[190:191]\n"
"v_pk_mul_f32 v[216:217], v[216:217], v[50:51]\n"
"v_pk_mul_f32 v[218:219], v[218:219], v[50:51]\n"
"v_pk_mul_f32 v[220:221], v[220:221], v[50:51]\n"
"v_pk_mul_f32 v[222:223], v[222:223], v[50:51]\n"
"v_cvt_pk_bf16_f32 v216, v216, v217\n"
"v_cvt_pk_bf16_f32 v217, v218, v219\n"
"v_cvt_pk_bf16_f32 v218, v220, v221\n"
"v_cvt_pk_bf16_f32 v219, v222, v223\n"
"s_add_u32 s74, s73, 0x5000\n"
"buffer_store_dwordx4 v[216:219], v1, s[4:7], s74 offen offset:2048 sc1\n"
"buffer_load_dwordx4 a[204:207], v1, s[4:7], s54 offen offset:2048 sc1\n"
"buffer_load_dwordx4 a[208:211], v1, s[4:7], s54 offen offset:3072 sc1\n"
"s_add_u32 s54, s54, 0x1000\n"
"buffer_load_dwordx4 a[212:215], v1, s[4:7], s54 offen sc1\n"
"buffer_load_dwordx4 a[216:219], v1, s[4:7], s54 offen offset:1024 sc1\n"
"buffer_load_dwordx4 a[220:223], v1, s[4:7], s54 offen offset:2048 sc1\n"
"buffer_load_dwordx4 a[224:227], v1, s[4:7], s54 offen offset:3072 sc1\n"
"s_add_u32 s54, s54, 0x3000\n"
"v_add_u32 v16, 0x10000, v12\n"
"s_and_b32 s56, s100, 4\n"
"s_lshr_b32 s52, s100, 6\n"
"s_add_u32 s52, s56, s52\n"
"s_bfe_u32 s56, s100, 0x10001\n"
"s_lshl3_add_u32 s53, s56, s49\n"
"s_bfe_u32 s56, s100, 0x10002\n"
"s_lshr_b32 s50, s100, 5\n"
"s_lshl3_add_u32 s50, s56, s50\n"
"s_and_b32 s56, s100, 1\n"
"s_lshl1_add_u32 s50, s50, s56\n"
"s_bfe_u32 s56, s100, 0x10001\n"
"s_bfe_u32 s51, s100, 0x20003\n"
"s_lshl2_add_u32 s51, s56, s51\n"
"s_lshl2_add_u32 s55, s50, s3\n"
"s_lshl_b32 s55, s55, 17\n"
"s_add_u32 s55, s55, 0x6000000\n"
"s_lshr_b32 s56, s55, 8\n"
"s_and_b32 s56, s56, 0x2000\n"
"s_xor_b32 s55, s55, s56\n"
"s_xor_b32 s55, s55, s101\n"
"s_xor_b32 s68, s55, 0x80000\n"
"s_lshl_b32 s56, s3, 10\n"
"v_add_u32 v2, s56, v1\n"
"v_xor_b32 v3, 0x400, v2\n"
"v_xor_b32 v4, 0x800, v2\n"
"v_xor_b32 v5, 0xc00, v2\n"
"s_lshl_b32 s56, s3, 14\n"
"v_add_u32 v10, s56, v1\n"
"v_add_u32 v6, 0x18000, v10\n"
"v_xor_b32 v7, 0x4000, v10\n"
"v_add_u32 v7, 0x18000, v7\n"
"v_xor_b32 v8, 0x8000, v10\n"
"v_add_u32 v8, 0x18000, v8\n"
"v_xor_b32 v9, 0xc000, v10\n"
"v_add_u32 v9, 0x18000, v9\n"
"s_mov_b32 s8, s24\n"
"s_and_b32 s9, s25, 0xffff\n"
"s_mov_b32 s10, 0x280000\n"
"s_mov_b32 s11, 0x20000\n"
"s_lshl2_add_u32 s56, s50, s3\n"
"s_mul_i32 s56, s56, 0x5000\n"
"s_mul_i32 s63, s51, 0xa0\n"
"s_add_u32 s63, s63, s56\n"
"s_and_b32 s58, s51, 1\n"
"s_mul_i32 s58, s58, 32\n"
"s_add_u32 s62, s63, s58\n"
"s_lshl_b32 s58, s58, 2\n"
"s_sub_u32 s63, s63, s58\n"
"s_add_u32 s63, s63, 0x80\n"
"v_and_b32 v10, 15, v0\n"
"v_lshrrev_b32 v11, 4, v0\n"
"v_mul_u32_u24 v21, 0x500, v10\n"
"v_lshl_add_u32 v21, v11, 3, v21\n"
"v_and_b32 v10, 3, v0\n"
"s_and_b32 s65, s3, 3\n"
"v_lshl_add_u32 v10, s65, 2, v10\n"
"v_lshlrev_b32 v10, 7, v10\n"
"v_add_u32 v29, 0, v0\n"
"v_mul_u32_u24 v24, 0x199a, v29\n"
"v_lshrrev_b32 v24, 16, v24\n"
"v_mul_u32_u24 v24, 0x460, v24\n"
"v_add_u32 v24, v24, v1\n"
"v_add_u32 v29, 64, v0\n"
"v_mul_u32_u24 v25, 0x199a, v29\n"
"v_lshrrev_b32 v25, 16, v25\n"
"v_mul_u32_u24 v25, 0x460, v25\n"
"v_add_u32 v25, v25, v1\n"
"v_add_u32 v29, 0x80, v0\n"
"v_mul_u32_u24 v26, 0x199a, v29\n"
"v_lshrrev_b32 v26, 16, v26\n"
"v_mul_u32_u24 v26, 0x460, v26\n"
"v_add_u32 v26, v26, v1\n"
"v_lshrrev_b32 v29, 5, v0\n"
"v_lshl_add_u32 v26, v29, 30, v26\n"
"s_min_u32 s61, s62, s63\n"
"v_and_b32 v29, 15, v0\n"
"v_mul_u32_u24 v29, 0xa0, v29\n"
"v_lshl_add_u32 v29, v11, 3, v29\n"
"s_sub_u32 s57, s78, s61\n"
"s_add_u32 s56, s57, s62\n"
"v_add_u32 v27, s56, v29\n"
"s_add_u32 s56, s57, s63\n"
"v_add_u32 v28, s56, v29\n"
"s_mov_b32 m0, s78\n"
"s_lshr_b32 s57, s50, 1\n"
"s_sub_u32 s56, 15, s57\n"
"s_min_u32 s56, s56, s57\n"
"s_lshl_b32 s58, s56, 11\n"
"s_and_b32 s57, s58, 0x2000\n"
"s_add_u32 s58, s58, s57\n"
"s_add_u32 s58, s58, 0xd0000\n"
"s_xor_b32 s58, s58, s101\n"
"s_and_b32 s56, s48, 15\n"
"s_lshl4_add_u32 s56, s49, s56\n"
"s_lshr_b32 s72, s48, 4\n"
"s_xor_b32 s72, s72, s100\n"
"s_and_b32 s72, s72, 1\n"
"s_lshl_b32 s72, s72, 13\n"
"s_xor_b32 s72, s72, s101\n"
"s_lshl_b32 s56, s56, 7\n"
"s_and_b32 s57, s56, 0x2000\n"
"s_add_u32 s56, s56, s57\n"
"s_add_u32 s56, s56, 0xd0000\n"
"s_xor_b32 s56, s56, s72\n"
"s_waitcnt vmcnt(6) lgkmcnt(0)\n"
"s_barrier\n"
"buffer_store_dword v22, v23, s[4:7], s56 offen sc1\n"
"buffer_load_dwordx4 v24, s[8:11], s61 offen lds\n"
"buffer_load_dwordx4 v25, s[8:11], s61 offen offset:1024 lds\n"
"buffer_load_dwordx4 v26, s[8:11], s61 offen offset:2048 lds\n"
"buffer_load_dwordx4 a[228:231], v1, s[4:7], s54 offen sc1\n"
"buffer_load_dwordx4 a[232:235], v1, s[4:7], s54 offen offset:1024 sc1\n"
"buffer_load_dwordx4 a[236:239], v1, s[4:7], s54 offen offset:2048 sc1\n"
"buffer_load_dwordx4 a[240:243], v1, s[4:7], s54 offen offset:3072 sc1\n"
"s_add_u32 s54, s54, 0x1000\n"
"buffer_load_dwordx4 a[244:247], v1, s[4:7], s54 offen sc1\n"
"buffer_load_dwordx4 a[248:251], v1, s[4:7], s54 offen offset:1024 sc1\n"
"buffer_load_dwordx4 a[252:255], v1, s[4:7], s54 offen offset:2048 sc1\n"
"buffer_load_dwordx4 v[44:47], v1, s[4:7], s54 offen offset:3072 sc1\n"
"s_add_u32 s54, s54, 0x3000\n"
"buffer_load_dwordx4 v[48:51], v1, s[4:7], s54 offen sc1\n"
"buffer_load_dwordx4 v[52:55], v1, s[4:7], s54 offen offset:1024 sc1\n"
"buffer_load_dwordx4 v[56:59], v1, s[4:7], s54 offen offset:2048 sc1\n"
"buffer_load_dwordx4 v[60:63], v1, s[4:7], s54 offen offset:3072 sc1\n"
"s_add_u32 s54, s54, 0x5000\n"
"buffer_load_dwordx4 v[64:67], v1, s[4:7], s54 offen sc1\n"
"buffer_load_dwordx4 v[68:71], v1, s[4:7], s54 offen offset:1024 sc1\n"
"buffer_load_dwordx4 v[72:75], v1, s[4:7], s54 offen offset:2048 sc1\n"
"buffer_load_dwordx4 v[76:79], v1, s[4:7], s54 offen offset:3072 sc1\n"
"s_add_u32 s54, s54, 0x3000\n"
"buffer_load_dwordx4 v[80:83], v1, s[4:7], s54 offen sc1\n"
"buffer_load_dwordx4 v[84:87], v1, s[4:7], s54 offen offset:1024 sc1\n"
"buffer_load_dwordx4 v[88:91], v1, s[4:7], s54 offen offset:2048 sc1\n"
"buffer_load_dwordx4 v[92:95], v1, s[4:7], s54 offen offset:3072 sc1\n"
"s_add_u32 s54, s54, 0x5000\n"
"s_mov_b32 s83, 0\n"
".Ladaln_block_P_ctx:\n"
"buffer_load_dword v20, v10, s[4:7], s58 offen sc1\n"
"s_add_u32 s83, s83, 1\n"
"s_waitcnt vmcnt(0)\n"
"v_cmp_ne_u32 vcc, s82, v20\n"
"s_cmp_lt_u32 s83, 0x20000\n"
"s_cbranch_vccz .Ladaln_block_P_ctx_R\n"
"s_sleep 0\n"
"s_cbranch_scc1 .Ladaln_block_P_ctx\n"
".Ladaln_block_P_ctx_R:\n"
"buffer_load_dwordx4 v[96:99], v2, s[4:7], s68 offen sc1\n"
"buffer_load_dwordx4 v[100:103], v3, s[4:7], s68 offen sc1\n"
"buffer_load_dwordx4 v[104:107], v4, s[4:7], s68 offen sc1\n"
"buffer_load_dwordx4 v[108:111], v5, s[4:7], s68 offen sc1\n"
"s_add_u32 s68, s68, 0x1000\n"
"buffer_load_dwordx4 v[112:115], v2, s[4:7], s68 offen sc1\n"
"buffer_load_dwordx4 v[116:119], v3, s[4:7], s68 offen sc1\n"
"buffer_load_dwordx4 v[120:123], v4, s[4:7], s68 offen sc1\n"
"buffer_load_dwordx4 v[124:127], v5, s[4:7], s68 offen sc1\n"
"s_add_u32 s68, s68, 0x3000\n"
"buffer_load_dwordx4 v[128:131], v2, s[4:7], s55 offen sc1\n"
"buffer_load_dwordx4 v[132:135], v3, s[4:7], s55 offen sc1\n"
"buffer_load_dwordx4 v[136:139], v4, s[4:7], s55 offen sc1\n"
"buffer_load_dwordx4 v[140:143], v5, s[4:7], s55 offen sc1\n"
"s_add_u32 s55, s55, 0x1000\n"
"s_waitcnt vmcnt(8)\n"
"v_mfma_f32_16x16x32_bf16 a[80:83], a[164:167], v[96:99], 0\n"
"buffer_load_dwordx4 v[144:147], v2, s[4:7], s55 offen sc1\n"
"v_mfma_f32_16x16x32_bf16 a[84:87], a[164:167], v[100:103], 0\n"
"v_mfma_f32_16x16x32_bf16 a[88:91], a[164:167], v[104:107], 0\n"
"buffer_load_dwordx4 v[148:151], v3, s[4:7], s55 offen sc1\n"
"v_mfma_f32_16x16x32_bf16 a[92:95], a[164:167], v[108:111], 0\n"
"v_mfma_f32_16x16x32_bf16 a[96:99], a[168:171], v[96:99], 0\n"
"ds_read_b64 v[34:35], v27\n"
"v_mfma_f32_16x16x32_bf16 a[100:103], a[168:171], v[100:103], 0\n"
"buffer_load_dwordx4 v[152:155], v4, s[4:7], s55 offen sc1\n"
"v_mfma_f32_16x16x32_bf16 a[104:107], a[168:171], v[104:107], 0\n"
"ds_read_b64 v[36:37], v27 offset:32\n"
"v_mfma_f32_16x16x32_bf16 a[108:111], a[168:171], v[108:111], 0\n"
"v_mfma_f32_16x16x32_bf16 a[112:115], a[172:175], v[96:99], 0\n"
"buffer_load_dwordx4 v[156:159], v5, s[4:7], s55 offen sc1\n"
"s_add_u32 s55, s55, 0x3000\n"
"v_mfma_f32_16x16x32_bf16 a[116:119], a[172:175], v[100:103], 0\n"
"ds_read_b64 v[38:39], v27 offset:64\n"
"v_mfma_f32_16x16x32_bf16 a[120:123], a[172:175], v[104:107], 0\n"
"v_mfma_f32_16x16x32_bf16 a[124:127], a[172:175], v[108:111], 0\n"
"buffer_load_dwordx4 v[160:163], v2, s[4:7], s68 offen sc1\n"
"v_mfma_f32_16x16x32_bf16 a[128:131], a[176:179], v[96:99], 0\n"
"ds_read_b64 v[40:41], v27 offset:96\n"
"v_mfma_f32_16x16x32_bf16 a[132:135], a[176:179], v[100:103], 0\n"
"v_mfma_f32_16x16x32_bf16 a[136:139], a[176:179], v[104:107], 0\n"
"buffer_load_dwordx4 v[164:167], v3, s[4:7], s68 offen sc1\n"
"v_mfma_f32_16x16x32_bf16 a[140:143], a[176:179], v[108:111], 0\n"
"ds_read_b64 v[42:43], v28\n"
"v_mfma_f32_16x16x32_bf16 a[144:147], a[180:183], v[96:99], 0\n"
"v_mfma_f32_16x16x32_bf16 a[148:151], a[180:183], v[100:103], 0\n"
"buffer_load_dwordx4 v[168:171], v4, s[4:7], s68 offen sc1\n"
"v_mfma_f32_16x16x32_bf16 a[152:155], a[180:183], v[104:107], 0\n"
"v_mfma_f32_16x16x32_bf16 a[156:159], a[180:183], v[108:111], 0\n"
"s_waitcnt vmcnt(11)\n"
"v_mfma_f32_16x16x32_bf16 a[80:83], a[184:187], v[112:115], a[80:83]\n"
"buffer_load_dwordx4 v[172:175], v5, s[4:7], s68 offen sc1\n"
"s_add_u32 s68, s68, 0x1000\n"
"v_mfma_f32_16x16x32_bf16 a[84:87], a[184:187], v[116:119], a[84:87]\n"
"v_mfma_f32_16x16x32_bf16 a[88:91], a[184:187], v[120:123], a[88:91]\n"
"v_mfma_f32_16x16x32_bf16 a[92:95], a[184:187], v[124:127], a[92:95]\n"
"buffer_load_dwordx4 v[176:179], v2, s[4:7], s68 offen sc1\n"
"v_mfma_f32_16x16x32_bf16 a[96:99], a[188:191], v[112:115], a[96:99]\n"
"v_mfma_f32_16x16x32_bf16 a[100:103], a[188:191], v[116:119], a[100:103]\n"
"v_mfma_f32_16x16x32_bf16 a[104:107], a[188:191], v[120:123], a[104:107]\n"
"buffer_load_dwordx4 v[180:183], v3, s[4:7], s68 offen sc1\n"
"v_mfma_f32_16x16x32_bf16 a[108:111], a[188:191], v[124:127], a[108:111]\n"
"v_mfma_f32_16x16x32_bf16 a[112:115], a[192:195], v[112:115], a[112:115]\n"
"v_mfma_f32_16x16x32_bf16 a[116:119], a[192:195], v[116:119], a[116:119]\n"
"buffer_load_dwordx4 v[184:187], v4, s[4:7], s68 offen sc1\n"
"v_mfma_f32_16x16x32_bf16 a[120:123], a[192:195], v[120:123], a[120:123]\n"
"v_mfma_f32_16x16x32_bf16 a[124:127], a[192:195], v[124:127], a[124:127]\n"
"v_mfma_f32_16x16x32_bf16 a[128:131], a[196:199], v[112:115], a[128:131]\n"
"buffer_load_dwordx4 v[188:191], v5, s[4:7], s68 offen sc1\n"
"s_add_u32 s68, s68, 0x3000\n"
"v_mfma_f32_16x16x32_bf16 a[132:135], a[196:199], v[116:119], a[132:135]\n"
"v_mfma_f32_16x16x32_bf16 a[136:139], a[196:199], v[120:123], a[136:139]\n"
"v_mfma_f32_16x16x32_bf16 a[140:143], a[196:199], v[124:127], a[140:143]\n"
"buffer_load_dwordx4 v[192:195], v1, s[4:7], s54 offen sc1\n"
"v_mfma_f32_16x16x32_bf16 a[144:147], a[200:203], v[112:115], a[144:147]\n"
"v_mfma_f32_16x16x32_bf16 a[148:151], a[200:203], v[116:119], a[148:151]\n"
"v_mfma_f32_16x16x32_bf16 a[152:155], a[200:203], v[120:123], a[152:155]\n"
"v_mfma_f32_16x16x32_bf16 a[156:159], a[200:203], v[124:127], a[156:159]\n"
"s_waitcnt vmcnt(9)\n"
"v_mfma_f32_16x16x32_bf16 a[0:3], a[164:167], v[128:131], 0\n"
"buffer_load_dwordx4 v[96:99], v2, s[4:7], s55 offen sc1\n"
"s_and_b32 s58, s53, 7\n"
"v_mfma_f32_16x16x32_bf16 a[0:3], a[184:187], v[144:147], a[0:3]\n"
"s_lshl4_add_u32 s59, s53, s58\n"
"v_mfma_f32_16x16x32_bf16 a[4:7], a[164:167], v[132:135], 0\n"
"s_lshl_b32 s59, s59, 14\n"
"v_mfma_f32_16x16x32_bf16 a[4:7], a[184:187], v[148:151], a[4:7]\n"
"v_mfma_f32_16x16x32_bf16 a[8:11], a[164:167], v[136:139], 0\n"
"buffer_load_dwordx4 v[100:103], v3, s[4:7], s55 offen sc1\n"
"s_add_u32 s59, s59, s78\n"
"v_mfma_f32_16x16x32_bf16 a[8:11], a[184:187], v[152:155], a[8:11]\n"
"s_andn2_b32 s58, s59, 0x1fff\n"
"v_mfma_f32_16x16x32_bf16 a[12:15], a[164:167], v[140:143], 0\n"
"s_add_u32 s59, s59, s58\n"
"v_mfma_f32_16x16x32_bf16 a[12:15], a[184:187], v[156:159], a[12:15]\n"
"v_mfma_f32_16x16x32_bf16 a[16:19], a[168:171], v[128:131], 0\n"
"buffer_load_dwordx4 v[104:107], v4, s[4:7], s55 offen sc1\n"
"s_add_u32 s59, s59, 0x2000000\n"
"v_mfma_f32_16x16x32_bf16 a[16:19], a[188:191], v[144:147], a[16:19]\n"
"s_lshr_b32 s72, s50, 1\n"
"v_mfma_f32_16x16x32_bf16 a[20:23], a[168:171], v[132:135], 0\n"
"s_lshl2_add_u32 s67, s72, s3\n"
"v_mfma_f32_16x16x32_bf16 a[20:23], a[188:191], v[148:151], a[20:23]\n"
"v_mfma_f32_16x16x32_bf16 a[24:27], a[168:171], v[136:139], 0\n"
"buffer_load_dwordx4 v[108:111], v5, s[4:7], s55 offen sc1\n"
"s_add_u32 s55, s55, 0x1000\n"
"s_lshl3_add_u32 s67, s67, s51\n"
"v_mfma_f32_16x16x32_bf16 a[24:27], a[188:191], v[152:155], a[24:27]\n"
"s_lshl_b32 s67, s67, 8\n"
"v_mfma_f32_16x16x32_bf16 a[28:31], a[168:171], v[140:143], 0\n"
"s_andn2_b32 s58, s67, 0x1fff\n"
"v_mfma_f32_16x16x32_bf16 a[28:31], a[188:191], v[156:159], a[28:31]\n"
"v_mfma_f32_16x16x32_bf16 a[32:35], a[172:175], v[128:131], 0\n"
"buffer_load_dwordx4 v[112:115], v2, s[4:7], s55 offen sc1\n"
"s_add_u32 s67, s67, s58\n"
"v_mfma_f32_16x16x32_bf16 a[32:35], a[192:195], v[144:147], a[32:35]\n"
"s_add_u32 s67, s67, 0x100000\n"
"v_mfma_f32_16x16x32_bf16 a[36:39], a[172:175], v[132:135], 0\n"
"v_mfma_f32_16x16x32_bf16 a[36:39], a[192:195], v[148:151], a[36:39]\n"
"s_lshr_b32 s58, s67, 8\n"
"v_mfma_f32_16x16x32_bf16 a[40:43], a[172:175], v[136:139], 0\n"
"s_and_b32 s58, s58, 0x2000\n"
"v_mfma_f32_16x16x32_bf16 a[40:43], a[192:195], v[152:155], a[40:43]\n"
"buffer_load_dwordx4 v[116:119], v3, s[4:7], s55 offen sc1\n"
"s_xor_b32 s67, s67, s58\n"
"v_mfma_f32_16x16x32_bf16 a[44:47], a[172:175], v[140:143], 0\n"
"v_mfma_f32_16x16x32_bf16 a[44:47], a[192:195], v[156:159], a[44:47]\n"
"s_xor_b32 s67, s67, s101\n"
"v_mfma_f32_16x16x32_bf16 a[48:51], a[176:179], v[128:131], 0\n"
"s_lshl3_add_u32 s70, s72, s51\n"
"v_mfma_f32_16x16x32_bf16 a[48:51], a[196:199], v[144:147], a[48:51]\n"
"buffer_load_dwordx4 v[120:123], v4, s[4:7], s55 offen sc1\n"
"s_lshl2_add_u32 s70, s70, s3\n"
"v_mfma_f32_16x16x32_bf16 a[52:55], a[176:179], v[132:135], 0\n"
"v_mfma_f32_16x16x32_bf16 a[52:55], a[196:199], v[148:151], a[52:55]\n"
"s_lshl_b32 s70, s70, 14\n"
"v_mfma_f32_16x16x32_bf16 a[56:59], a[176:179], v[136:139], 0\n"
"s_add_u32 s70, s70, 0x3000000\n"
"v_mfma_f32_16x16x32_bf16 a[56:59], a[196:199], v[152:155], a[56:59]\n"
"buffer_load_dwordx4 v[124:127], v5, s[4:7], s55 offen sc1\n"
"s_add_u32 s55, s55, 0x3000\n"
"s_lshr_b32 s58, s70, 8\n"
"v_mfma_f32_16x16x32_bf16 a[60:63], a[176:179], v[140:143], 0\n"
"v_mfma_f32_16x16x32_bf16 a[60:63], a[196:199], v[156:159], a[60:63]\n"
"s_and_b32 s58, s58, 0x2000\n"
"v_mfma_f32_16x16x32_bf16 a[64:67], a[180:183], v[128:131], 0\n"
"s_xor_b32 s70, s70, s58\n"
"v_mfma_f32_16x16x32_bf16 a[64:67], a[200:203], v[144:147], a[64:67]\n"
"buffer_load_dwordx4 v[224:227], v1, s[4:7], s54 offen offset:1024 sc1\n"
"s_xor_b32 s70, s70, s101\n"
"v_mfma_f32_16x16x32_bf16 a[68:71], a[180:183], v[132:135], 0\n"
"v_mfma_f32_16x16x32_bf16 a[68:71], a[200:203], v[148:151], a[68:71]\n"
"s_xor_b32 s69, s70, 0x2000\n"
"v_mfma_f32_16x16x32_bf16 a[72:75], a[180:183], v[136:139], 0\n"
"v_add_u32 v13, 0x1000, v1\n"
"v_mfma_f32_16x16x32_bf16 a[72:75], a[200:203], v[152:155], a[72:75]\n"
"v_mfma_f32_16x16x32_bf16 a[76:79], a[180:183], v[140:143], 0\n"
"v_mfma_f32_16x16x32_bf16 a[76:79], a[200:203], v[156:159], a[76:79]\n"
"s_waitcnt vmcnt(10)\n"
"v_mfma_f32_16x16x32_bf16 a[80:83], a[204:207], v[160:163], a[80:83]\n"
"buffer_load_dwordx4 v[128:131], v2, s[4:7], s68 offen sc1\n"
"s_waitcnt lgkmcnt(0)\n"
"v_mfma_f32_16x16x32_bf16 a[80:83], a[224:227], v[176:179], a[80:83]\n"
"v_mfma_f32_16x16x32_bf16 a[84:87], a[204:207], v[164:167], a[84:87]\n"
"v_mfma_f32_16x16x32_bf16 a[84:87], a[224:227], v[180:183], a[84:87]\n"
"v_mfma_f32_16x16x32_bf16 a[88:91], a[204:207], v[168:171], a[88:91]\n"
"buffer_load_dwordx4 v[132:135], v3, s[4:7], s68 offen sc1\n"
"v_mfma_f32_16x16x32_bf16 a[88:91], a[224:227], v[184:187], a[88:91]\n"
"v_mfma_f32_16x16x32_bf16 a[92:95], a[204:207], v[172:175], a[92:95]\n"
"v_mfma_f32_16x16x32_bf16 a[92:95], a[224:227], v[188:191], a[92:95]\n"
"v_mfma_f32_16x16x32_bf16 a[96:99], a[208:211], v[160:163], a[96:99]\n"
"buffer_load_dwordx4 v[136:139], v4, s[4:7], s68 offen sc1\n"
"v_mfma_f32_16x16x32_bf16 a[96:99], a[228:231], v[176:179], a[96:99]\n"
"v_mfma_f32_16x16x32_bf16 a[100:103], a[208:211], v[164:167], a[100:103]\n"
"v_mfma_f32_16x16x32_bf16 a[100:103], a[228:231], v[180:183], a[100:103]\n"
"v_mfma_f32_16x16x32_bf16 a[104:107], a[208:211], v[168:171], a[104:107]\n"
"buffer_load_dwordx4 v[140:143], v5, s[4:7], s68 offen sc1\n"
"s_add_u32 s68, s68, 0x1000\n"
"v_mfma_f32_16x16x32_bf16 a[104:107], a[228:231], v[184:187], a[104:107]\n"
"v_mfma_f32_16x16x32_bf16 a[108:111], a[208:211], v[172:175], a[108:111]\n"
"v_mfma_f32_16x16x32_bf16 a[108:111], a[228:231], v[188:191], a[108:111]\n"
"v_mfma_f32_16x16x32_bf16 a[112:115], a[212:215], v[160:163], a[112:115]\n"
"buffer_load_dwordx4 v[144:147], v2, s[4:7], s68 offen sc1\n"
"v_mfma_f32_16x16x32_bf16 a[112:115], a[232:235], v[176:179], a[112:115]\n"
"v_mfma_f32_16x16x32_bf16 a[116:119], a[212:215], v[164:167], a[116:119]\n"
"v_mfma_f32_16x16x32_bf16 a[116:119], a[232:235], v[180:183], a[116:119]\n"
"v_mfma_f32_16x16x32_bf16 a[120:123], a[212:215], v[168:171], a[120:123]\n"
"v_mfma_f32_16x16x32_bf16 a[120:123], a[232:235], v[184:187], a[120:123]\n"
"buffer_load_dwordx4 v[148:151], v3, s[4:7], s68 offen sc1\n"
"v_mfma_f32_16x16x32_bf16 a[124:127], a[212:215], v[172:175], a[124:127]\n"
"v_mfma_f32_16x16x32_bf16 a[124:127], a[232:235], v[188:191], a[124:127]\n"
"v_mfma_f32_16x16x32_bf16 a[128:131], a[216:219], v[160:163], a[128:131]\n"
"v_mfma_f32_16x16x32_bf16 a[128:131], a[236:239], v[176:179], a[128:131]\n"
"buffer_load_dwordx4 v[152:155], v4, s[4:7], s68 offen sc1\n"
"v_mfma_f32_16x16x32_bf16 a[132:135], a[216:219], v[164:167], a[132:135]\n"
"v_mfma_f32_16x16x32_bf16 a[132:135], a[236:239], v[180:183], a[132:135]\n"
"v_mfma_f32_16x16x32_bf16 a[136:139], a[216:219], v[168:171], a[136:139]\n"
"v_mfma_f32_16x16x32_bf16 a[136:139], a[236:239], v[184:187], a[136:139]\n"
"buffer_load_dwordx4 v[156:159], v5, s[4:7], s68 offen sc1\n"
"s_add_u32 s68, s68, 0x3000\n"
"v_mfma_f32_16x16x32_bf16 a[140:143], a[216:219], v[172:175], a[140:143]\n"
"v_mfma_f32_16x16x32_bf16 a[140:143], a[236:239], v[188:191], a[140:143]\n"
"v_mfma_f32_16x16x32_bf16 a[144:147], a[220:223], v[160:163], a[144:147]\n"
"v_mfma_f32_16x16x32_bf16 a[144:147], a[240:243], v[176:179], a[144:147]\n"
"buffer_load_dwordx4 v[228:231], v1, s[4:7], s54 offen offset:2048 sc1\n"
"v_mfma_f32_16x16x32_bf16 a[148:151], a[220:223], v[164:167], a[148:151]\n"
"v_mfma_f32_16x16x32_bf16 a[148:151], a[240:243], v[180:183], a[148:151]\n"
"v_mfma_f32_16x16x32_bf16 a[152:155], a[220:223], v[168:171], a[152:155]\n"
"v_mfma_f32_16x16x32_bf16 a[152:155], a[240:243], v[184:187], a[152:155]\n"
"v_mfma_f32_16x16x32_bf16 a[156:159], a[220:223], v[172:175], a[156:159]\n"
"v_mfma_f32_16x16x32_bf16 a[156:159], a[240:243], v[188:191], a[156:159]\n"
"s_waitcnt vmcnt(10)\n"
"v_mfma_f32_16x16x32_bf16 a[0:3], a[204:207], v[96:99], a[0:3]\n"
"ds_read_b128 v[160:163], v16 offset:32768\n"
"v_mfma_f32_16x16x32_bf16 a[0:3], a[224:227], v[112:115], a[0:3]\n"
"v_mfma_f32_16x16x32_bf16 a[4:7], a[204:207], v[100:103], a[4:7]\n"
"s_add_u32 m0, s78, 0\n"
"v_mfma_f32_16x16x32_bf16 a[4:7], a[224:227], v[116:119], a[4:7]\n"
"ds_read_b128 v[164:167], v16 offset:33792\n"
"v_mfma_f32_16x16x32_bf16 a[8:11], a[204:207], v[104:107], a[8:11]\n"
"v_mfma_f32_16x16x32_bf16 a[8:11], a[224:227], v[120:123], a[8:11]\n"
"v_mfma_f32_16x16x32_bf16 a[12:15], a[204:207], v[108:111], a[12:15]\n"
"buffer_load_dwordx4 v[168:171], v2, s[4:7], s55 offen sc1\n"
"v_mfma_f32_16x16x32_bf16 a[12:15], a[224:227], v[124:127], a[12:15]\n"
"v_mfma_f32_16x16x32_bf16 a[16:19], a[208:211], v[96:99], a[16:19]\n"
"v_mfma_f32_16x16x32_bf16 a[16:19], a[228:231], v[112:115], a[16:19]\n"
"v_mfma_f32_16x16x32_bf16 a[20:23], a[208:211], v[100:103], a[20:23]\n"
"buffer_load_dwordx4 v[172:175], v3, s[4:7], s55 offen sc1\n"
"v_mfma_f32_16x16x32_bf16 a[20:23], a[228:231], v[116:119], a[20:23]\n"
"v_mfma_f32_16x16x32_bf16 a[24:27], a[208:211], v[104:107], a[24:27]\n"
"v_mfma_f32_16x16x32_bf16 a[24:27], a[228:231], v[120:123], a[24:27]\n"
"buffer_load_dwordx4 v[176:179], v4, s[4:7], s55 offen sc1\n"
"v_mfma_f32_16x16x32_bf16 a[28:31], a[208:211], v[108:111], a[28:31]\n"
"v_mfma_f32_16x16x32_bf16 a[28:31], a[228:231], v[124:127], a[28:31]\n"
"v_mfma_f32_16x16x32_bf16 a[32:35], a[212:215], v[96:99], a[32:35]\n"
"v_mfma_f32_16x16x32_bf16 a[32:35], a[232:235], v[112:115], a[32:35]\n"
"buffer_load_dwordx4 v[180:183], v5, s[4:7], s55 offen sc1\n"
"s_add_u32 s55, s55, 0x1000\n"
"v_mfma_f32_16x16x32_bf16 a[36:39], a[212:215], v[100:103], a[36:39]\n"
"v_mfma_f32_16x16x32_bf16 a[36:39], a[232:235], v[116:119], a[36:39]\n"
"v_mfma_f32_16x16x32_bf16 a[40:43], a[212:215], v[104:107], a[40:43]\n"
"buffer_load_dwordx4 v[184:187], v2, s[4:7], s55 offen sc1\n"
"buffer_load_dwordx4 v1, s[4:7], s59 offen sc1 lds\n"
"v_mfma_f32_16x16x32_bf16 a[40:43], a[232:235], v[120:123], a[40:43]\n"
"v_mfma_f32_16x16x32_bf16 a[44:47], a[212:215], v[108:111], a[44:47]\n"
"v_mfma_f32_16x16x32_bf16 a[44:47], a[232:235], v[124:127], a[44:47]\n"
"v_mfma_f32_16x16x32_bf16 a[48:51], a[216:219], v[96:99], a[48:51]\n"
"buffer_load_dwordx4 v[188:191], v3, s[4:7], s55 offen sc1\n"
"v_mfma_f32_16x16x32_bf16 a[48:51], a[236:239], v[112:115], a[48:51]\n"
"v_mfma_f32_16x16x32_bf16 a[52:55], a[216:219], v[100:103], a[52:55]\n"
"v_mfma_f32_16x16x32_bf16 a[52:55], a[236:239], v[116:119], a[52:55]\n"
"buffer_load_dwordx4 v[232:235], v4, s[4:7], s55 offen sc1\n"
"v_mfma_f32_16x16x32_bf16 a[56:59], a[216:219], v[104:107], a[56:59]\n"
"v_mfma_f32_16x16x32_bf16 a[56:59], a[236:239], v[120:123], a[56:59]\n"
"v_mfma_f32_16x16x32_bf16 a[60:63], a[216:219], v[108:111], a[60:63]\n"
"v_mfma_f32_16x16x32_bf16 a[60:63], a[236:239], v[124:127], a[60:63]\n"
"buffer_load_dwordx4 v[236:239], v5, s[4:7], s55 offen sc1\n"
"s_add_u32 s55, s55, 0x3000\n"
"v_mfma_f32_16x16x32_bf16 a[64:67], a[220:223], v[96:99], a[64:67]\n"
"v_mfma_f32_16x16x32_bf16 a[64:67], a[240:243], v[112:115], a[64:67]\n"
"v_mfma_f32_16x16x32_bf16 a[68:71], a[220:223], v[100:103], a[68:71]\n"
"buffer_load_dwordx4 v[240:243], v1, s[4:7], s54 offen offset:3072 sc1\n"
"s_add_u32 s54, s54, 0x3000\n"
"v_mfma_f32_16x16x32_bf16 a[68:71], a[240:243], v[116:119], a[68:71]\n"
"v_mfma_f32_16x16x32_bf16 a[72:75], a[220:223], v[104:107], a[72:75]\n"
"v_mfma_f32_16x16x32_bf16 a[72:75], a[240:243], v[120:123], a[72:75]\n"
"v_mfma_f32_16x16x32_bf16 a[76:79], a[220:223], v[108:111], a[76:79]\n"
"v_mfma_f32_16x16x32_bf16 a[76:79], a[240:243], v[124:127], a[76:79]\n"
"s_waitcnt vmcnt(11) lgkmcnt(0)\n"
"v_mfma_f32_16x16x32_bf16 a[80:83], a[244:247], v[128:131], a[80:83]\n"
"buffer_load_dwordx4 v[96:99], v2, s[4:7], s68 offen sc1\n"
"v_mfma_f32_16x16x32_bf16 a[80:83], v[52:55], v[144:147], a[80:83]\n"
"v_mfma_f32_16x16x32_bf16 a[84:87], a[244:247], v[132:135], a[84:87]\n"
"buffer_load_dwordx4 v1, s[4:7], s59 offen offset:1024 sc1 lds\n"
"v_mfma_f32_16x16x32_bf16 a[84:87], v[52:55], v[148:151], a[84:87]\n"
"v_mfma_f32_16x16x32_bf16 a[88:91], a[244:247], v[136:139], a[88:91]\n"
"buffer_load_dwordx4 v[100:103], v3, s[4:7], s68 offen sc1\n"
"v_mfma_f32_16x16x32_bf16 a[88:91], v[52:55], v[152:155], a[88:91]\n"
"v_mfma_f32_16x16x32_bf16 a[92:95], a[244:247], v[140:143], a[92:95]\n"
"v_mfma_f32_16x16x32_bf16 a[92:95], v[52:55], v[156:159], a[92:95]\n"
"v_mfma_f32_16x16x32_bf16 a[96:99], a[248:251], v[128:131], a[96:99]\n"
"buffer_load_dwordx4 v[104:107], v4, s[4:7], s68 offen sc1\n"
"v_mfma_f32_16x16x32_bf16 a[96:99], v[56:59], v[144:147], a[96:99]\n"
"v_mfma_f32_16x16x32_bf16 a[100:103], a[248:251], v[132:135], a[100:103]\n"
"v_mfma_f32_16x16x32_bf16 a[100:103], v[56:59], v[148:151], a[100:103]\n"
"v_mfma_f32_16x16x32_bf16 a[104:107], a[248:251], v[136:139], a[104:107]\n"
"buffer_load_dwordx4 v[108:111], v5, s[4:7], s68 offen sc1\n"
"s_add_u32 s68, s68, 0x1000\n"
"v_mfma_f32_16x16x32_bf16 a[104:107], v[56:59], v[152:155], a[104:107]\n"
"v_mfma_f32_16x16x32_bf16 a[108:111], a[248:251], v[140:143], a[108:111]\n"
"v_mfma_f32_16x16x32_bf16 a[108:111], v[56:59], v[156:159], a[108:111]\n"
"v_mfma_f32_16x16x32_bf16 a[112:115], a[252:255], v[128:131], a[112:115]\n"
"buffer_load_dwordx4 v[112:115], v2, s[4:7], s68 offen sc1\n"
"v_mfma_f32_16x16x32_bf16 a[112:115], v[60:63], v[144:147], a[112:115]\n"
"v_mfma_f32_16x16x32_bf16 a[116:119], a[252:255], v[132:135], a[116:119]\n"
"v_mfma_f32_16x16x32_bf16 a[116:119], v[60:63], v[148:151], a[116:119]\n"
"v_mfma_f32_16x16x32_bf16 a[120:123], a[252:255], v[136:139], a[120:123]\n"
"v_mfma_f32_16x16x32_bf16 a[120:123], v[60:63], v[152:155], a[120:123]\n"
"buffer_load_dwordx4 v[116:119], v3, s[4:7], s68 offen sc1\n"
"v_mfma_f32_16x16x32_bf16 a[124:127], a[252:255], v[140:143], a[124:127]\n"
"v_mfma_f32_16x16x32_bf16 a[124:127], v[60:63], v[156:159], a[124:127]\n"
"v_mfma_f32_16x16x32_bf16 a[128:131], v[44:47], v[128:131], a[128:131]\n"
"v_mfma_f32_16x16x32_bf16 a[128:131], v[160:163], v[144:147], a[128:131]\n"
"buffer_load_dwordx4 v[120:123], v4, s[4:7], s68 offen sc1\n"
"v_mfma_f32_16x16x32_bf16 a[132:135], v[44:47], v[132:135], a[132:135]\n"
"v_mfma_f32_16x16x32_bf16 a[132:135], v[160:163], v[148:151], a[132:135]\n"
"v_mfma_f32_16x16x32_bf16 a[136:139], v[44:47], v[136:139], a[136:139]\n"
"v_mfma_f32_16x16x32_bf16 a[136:139], v[160:163], v[152:155], a[136:139]\n"
"buffer_load_dwordx4 v[124:127], v5, s[4:7], s68 offen sc1\n"
"s_add_u32 s68, s68, 0x3000\n"
"v_mfma_f32_16x16x32_bf16 a[140:143], v[44:47], v[140:143], a[140:143]\n"
"v_mfma_f32_16x16x32_bf16 a[140:143], v[160:163], v[156:159], a[140:143]\n"
"v_mfma_f32_16x16x32_bf16 a[144:147], v[48:51], v[128:131], a[144:147]\n"
"v_mfma_f32_16x16x32_bf16 a[144:147], v[164:167], v[144:147], a[144:147]\n"
"buffer_load_dwordx4 v[244:247], v1, s[4:7], s54 offen sc1\n"
"v_mfma_f32_16x16x32_bf16 a[148:151], v[48:51], v[132:135], a[148:151]\n"
"v_mfma_f32_16x16x32_bf16 a[148:151], v[164:167], v[148:151], a[148:151]\n"
"v_mfma_f32_16x16x32_bf16 a[152:155], v[48:51], v[136:139], a[152:155]\n"
"v_mfma_f32_16x16x32_bf16 a[152:155], v[164:167], v[152:155], a[152:155]\n"
"v_mfma_f32_16x16x32_bf16 a[156:159], v[48:51], v[140:143], a[156:159]\n"
"v_mfma_f32_16x16x32_bf16 a[156:159], v[164:167], v[156:159], a[156:159]\n"
"s_waitcnt vmcnt(11)\n"
"v_mfma_f32_16x16x32_bf16 a[0:3], a[244:247], v[168:171], a[0:3]\n"
"ds_read_b128 v[128:131], v16 offset:34816\n"
"s_lshr_b32 s56, s3, 1\n"
"v_mfma_f32_16x16x32_bf16 a[0:3], v[52:55], v[184:187], a[0:3]\n"
"s_lshl1_add_u32 s56, s72, s56\n"
"v_mfma_f32_16x16x32_bf16 a[4:7], a[244:247], v[172:175], a[4:7]\n"
"ds_read_b128 v[132:135], v16 offset:35840\n"
"buffer_load_dwordx4 v1, s[4:7], s59 offen offset:2048 sc1 lds\n"
"s_and_b32 s57, s56, 3\n"
"v_mfma_f32_16x16x32_bf16 a[4:7], v[52:55], v[188:191], a[4:7]\n"
"s_lshl3_add_u32 s56, s56, s57\n"
"v_mfma_f32_16x16x32_bf16 a[8:11], a[244:247], v[176:179], a[8:11]\n"
"s_lshl_b32 s56, s56, 13\n"
"v_mfma_f32_16x16x32_bf16 a[8:11], v[52:55], v[232:235], a[8:11]\n"
"ds_read_b128 v[136:139], v17 offset:1024\n"
"s_and_b32 s57, s3, 1\n"
"v_mfma_f32_16x16x32_bf16 a[12:15], a[244:247], v[180:183], a[12:15]\n"
"s_lshl_b32 s57, s57, 10\n"
"v_mfma_f32_16x16x32_bf16 a[12:15], v[52:55], v[236:239], a[12:15]\n"
"s_add_u32 s56, s56, s57\n"
"v_mfma_f32_16x16x32_bf16 a[16:19], a[248:251], v[168:171], a[16:19]\n"
"ds_read_b128 v[140:143], v17 offset:2048\n"
"s_mul_i32 s57, s51, 5\n"
"v_mfma_f32_16x16x32_bf16 a[16:19], v[56:59], v[184:187], a[16:19]\n"
"s_add_u32 s57, s57, 1\n"
"v_mfma_f32_16x16x32_bf16 a[20:23], a[248:251], v[172:175], a[20:23]\n"
"ds_read_b128 v[144:147], v17 offset:3072\n"
"s_lshr_b32 s57, s57, 1\n"
"v_mfma_f32_16x16x32_bf16 a[20:23], v[56:59], v[188:191], a[20:23]\n"
"s_lshl_b32 s57, s57, 11\n"
"v_mfma_f32_16x16x32_bf16 a[24:27], a[248:251], v[176:179], a[24:27]\n"
"v_mfma_f32_16x16x32_bf16 a[24:27], v[56:59], v[232:235], a[24:27]\n"
"buffer_load_dwordx4 v[148:151], v2, s[4:7], s55 offen sc1\n"
"s_add_u32 s61, s56, s57\n"
"v_mfma_f32_16x16x32_bf16 a[28:31], a[248:251], v[180:183], a[28:31]\n"
"s_add_u32 s71, s61, 0x800\n"
"v_mfma_f32_16x16x32_bf16 a[28:31], v[56:59], v[236:239], a[28:31]\n"
"s_lshr_b32 s57, s51, 1\n"
"v_mfma_f32_16x16x32_bf16 a[32:35], a[252:255], v[168:171], a[32:35]\n"
"buffer_load_dwordx4 v[152:155], v3, s[4:7], s55 offen sc1\n"
"s_mul_i32 s57, s57, 0x2800\n"
"v_mfma_f32_16x16x32_bf16 a[32:35], v[60:63], v[184:187], a[32:35]\n"
"s_add_u32 s56, s56, s57\n"
"v_mfma_f32_16x16x32_bf16 a[36:39], a[252:255], v[172:175], a[36:39]\n"
"s_and_b32 s57, s51, 1\n"
"v_mfma_f32_16x16x32_bf16 a[36:39], v[60:63], v[188:191], a[36:39]\n"
"buffer_load_dwordx4 v[156:159], v4, s[4:7], s55 offen sc1\n"
"s_lshl_b32 s57, s57, 9\n"
"v_mfma_f32_16x16x32_bf16 a[40:43], a[252:255], v[176:179], a[40:43]\n"
"buffer_load_dwordx4 v1, s[4:7], s59 offen offset:3072 sc1 lds\n"
"s_add_u32 s59, s59, 0x8000\n"
"s_add_u32 s56, s56, s57\n"
"v_mfma_f32_16x16x32_bf16 a[40:43], v[60:63], v[232:235], a[40:43]\n"
"buffer_load_dwordx4 v[248:251], v5, s[4:7], s55 offen sc1\n"
"s_add_u32 s55, s55, 0x1000\n"
"s_add_u32 s66, s56, 0x1000\n"
"v_mfma_f32_16x16x32_bf16 a[44:47], a[252:255], v[180:183], a[44:47]\n"
"s_andn2_b32 s58, s61, 0x1fff\n"
"v_mfma_f32_16x16x32_bf16 a[44:47], v[60:63], v[236:239], a[44:47]\n"
"s_add_u32 s61, s61, s58\n"
"v_mfma_f32_16x16x32_bf16 a[48:51], v[44:47], v[168:171], a[48:51]\n"
"buffer_load_dwordx4 v[252:255], v2, s[4:7], s55 offen sc1\n"
"s_add_u32 s61, s61, 0x400000\n"
"v_mfma_f32_16x16x32_bf16 a[48:51], v[160:163], v[184:187], a[48:51]\n"
"v_mfma_f32_16x16x32_bf16 a[52:55], v[44:47], v[172:175], a[52:55]\n"
"s_lshr_b32 s58, s61, 8\n"
"v_mfma_f32_16x16x32_bf16 a[52:55], v[160:163], v[188:191], a[52:55]\n"
"buffer_load_dwordx4 v[196:199], v3, s[4:7], s55 offen sc1\n"
"s_and_b32 s58, s58, 0x2000\n"
"v_mfma_f32_16x16x32_bf16 a[56:59], v[44:47], v[176:179], a[56:59]\n"
"s_xor_b32 s61, s61, s58\n"
"v_mfma_f32_16x16x32_bf16 a[56:59], v[160:163], v[232:235], a[56:59]\n"
"buffer_load_dwordx4 v[200:203], v4, s[4:7], s55 offen sc1\n"
"s_xor_b32 s61, s61, s101\n"
"v_mfma_f32_16x16x32_bf16 a[60:63], v[44:47], v[180:183], a[60:63]\n"
"s_andn2_b32 s58, s71, 0x1fff\n"
"v_mfma_f32_16x16x32_bf16 a[60:63], v[160:163], v[236:239], a[60:63]\n"
"s_add_u32 s71, s71, s58\n"
"v_mfma_f32_16x16x32_bf16 a[64:67], v[48:51], v[168:171], a[64:67]\n"
"buffer_load_dwordx4 v[204:207], v5, s[4:7], s55 offen sc1\n"
"s_add_u32 s55, s55, 0x3000\n"
"s_add_u32 s71, s71, 0x400000\n"
"v_mfma_f32_16x16x32_bf16 a[64:67], v[164:167], v[184:187], a[64:67]\n"
"s_lshr_b32 s58, s71, 8\n"
"v_mfma_f32_16x16x32_bf16 a[68:71], v[48:51], v[172:175], a[68:71]\n"
"s_and_b32 s58, s58, 0x2000\n"
"v_mfma_f32_16x16x32_bf16 a[68:71], v[164:167], v[188:191], a[68:71]\n"
"buffer_load_dwordx4 v[208:211], v1, s[4:7], s54 offen offset:1024 sc1\n"
"s_xor_b32 s71, s71, s58\n"
"v_mfma_f32_16x16x32_bf16 a[72:75], v[48:51], v[176:179], a[72:75]\n"
"s_xor_b32 s71, s71, s101\n"
"v_mfma_f32_16x16x32_bf16 a[72:75], v[164:167], v[232:235], a[72:75]\n"
"v_mfma_f32_16x16x32_bf16 a[76:79], v[48:51], v[180:183], a[76:79]\n"
"v_mfma_f32_16x16x32_bf16 a[76:79], v[164:167], v[236:239], a[76:79]\n"
"s_waitcnt vmcnt(12) lgkmcnt(0)\n"
"v_mfma_f32_16x16x32_bf16 a[80:83], v[128:131], v[96:99], a[80:83]\n"
"buffer_load_dwordx4 v[168:171], v2, s[4:7], s68 offen sc1\n"
"s_andn2_b32 s58, s66, 0x1fff\n"
"v_mfma_f32_16x16x32_bf16 a[80:83], v[144:147], v[112:115], a[80:83]\n"
"v_mfma_f32_16x16x32_bf16 a[84:87], v[128:131], v[100:103], a[84:87]\n"
"s_add_u32 m0, s78, 0x4000\n"
"s_add_u32 s66, s66, s58\n"
"v_mfma_f32_16x16x32_bf16 a[84:87], v[144:147], v[116:119], a[84:87]\n"
"v_mfma_f32_16x16x32_bf16 a[88:91], v[128:131], v[104:107], a[88:91]\n"
"buffer_load_dwordx4 v[172:175], v3, s[4:7], s68 offen sc1\n"
"v_mfma_f32_16x16x32_bf16 a[88:91], v[144:147], v[120:123], a[88:91]\n"
"s_add_u32 s66, s66, 0x400000\n"
"v_mfma_f32_16x16x32_bf16 a[92:95], v[128:131], v[108:111], a[92:95]\n"
"v_mfma_f32_16x16x32_bf16 a[92:95], v[144:147], v[124:127], a[92:95]\n"
"v_mfma_f32_16x16x32_bf16 a[96:99], v[132:135], v[96:99], a[96:99]\n"
"buffer_load_dwordx4 v[160:163], v4, s[4:7], s68 offen sc1\n"
"s_lshr_b32 s58, s66, 8\n"
"v_mfma_f32_16x16x32_bf16 a[96:99], v[64:67], v[112:115], a[96:99]\n"
"v_mfma_f32_16x16x32_bf16 a[100:103], v[132:135], v[100:103], a[100:103]\n"
"v_mfma_f32_16x16x32_bf16 a[100:103], v[64:67], v[116:119], a[100:103]\n"
"s_and_b32 s58, s58, 0x2000\n"
"v_mfma_f32_16x16x32_bf16 a[104:107], v[132:135], v[104:107], a[104:107]\n"
"buffer_load_dwordx4 v[164:167], v5, s[4:7], s68 offen sc1\n"
"s_add_u32 s68, s68, 0x1000\n"
"v_mfma_f32_16x16x32_bf16 a[104:107], v[64:67], v[120:123], a[104:107]\n"
"v_mfma_f32_16x16x32_bf16 a[108:111], v[132:135], v[108:111], a[108:111]\n"
"buffer_load_dwordx4 v1, s[4:7], s59 offen sc1 lds\n"
"s_xor_b32 s66, s66, s58\n"
"v_mfma_f32_16x16x32_bf16 a[108:111], v[64:67], v[124:127], a[108:111]\n"
"v_mfma_f32_16x16x32_bf16 a[112:115], a[160:163], v[96:99], a[112:115]\n"
"buffer_load_dwordx4 v[176:179], v2, s[4:7], s68 offen sc1\n"
"v_mfma_f32_16x16x32_bf16 a[112:115], v[68:71], v[112:115], a[112:115]\n"
"s_xor_b32 s66, s66, s101\n"
"v_mfma_f32_16x16x32_bf16 a[116:119], a[160:163], v[100:103], a[116:119]\n"
"v_mfma_f32_16x16x32_bf16 a[116:119], v[68:71], v[116:119], a[116:119]\n"
"v_mfma_f32_16x16x32_bf16 a[120:123], a[160:163], v[104:107], a[120:123]\n"
"s_lshl3_add_u32 s72, s72, s51\n"
"v_mfma_f32_16x16x32_bf16 a[120:123], v[68:71], v[120:123], a[120:123]\n"
"buffer_load_dwordx4 v[180:183], v3, s[4:7], s68 offen sc1\n"
"v_mfma_f32_16x16x32_bf16 a[124:127], a[160:163], v[108:111], a[124:127]\n"
"v_mfma_f32_16x16x32_bf16 a[124:127], v[68:71], v[124:127], a[124:127]\n"
"s_lshl_b32 s72, s72, 7\n"
"v_mfma_f32_16x16x32_bf16 a[128:131], v[136:139], v[96:99], a[128:131]\n"
"v_mfma_f32_16x16x32_bf16 a[128:131], v[72:75], v[112:115], a[128:131]\n"
"buffer_load_dwordx4 v[184:187], v4, s[4:7], s68 offen sc1\n"
"v_mfma_f32_16x16x32_bf16 a[132:135], v[136:139], v[100:103], a[132:135]\n"
"buffer_load_dwordx4 v1, s[4:7], s59 offen offset:1024 sc1 lds\n"
"s_and_b32 s58, s72, 0x2000\n"
"v_mfma_f32_16x16x32_bf16 a[132:135], v[72:75], v[116:119], a[132:135]\n"
"v_mfma_f32_16x16x32_bf16 a[136:139], v[136:139], v[104:107], a[136:139]\n"
"v_mfma_f32_16x16x32_bf16 a[136:139], v[72:75], v[120:123], a[136:139]\n"
"buffer_load_dwordx4 v[188:191], v5, s[4:7], s68 offen sc1\n"
"s_add_u32 s68, s68, 0x3000\n"
"s_add_u32 s72, s72, s58\n"
"v_mfma_f32_16x16x32_bf16 a[140:143], v[136:139], v[108:111], a[140:143]\n"
"v_mfma_f32_16x16x32_bf16 a[140:143], v[72:75], v[124:127], a[140:143]\n"
"v_mfma_f32_16x16x32_bf16 a[144:147], v[140:143], v[96:99], a[144:147]\n"
"s_add_u32 s72, s72, 0xd8000\n"
"v_mfma_f32_16x16x32_bf16 a[144:147], v[76:79], v[112:115], a[144:147]\n"
"buffer_load_dwordx4 v[56:59], v1, s[4:7], s54 offen offset:2048 sc1\n"
"v_mfma_f32_16x16x32_bf16 a[148:151], v[140:143], v[100:103], a[148:151]\n"
"v_mfma_f32_16x16x32_bf16 a[148:151], v[76:79], v[116:119], a[148:151]\n"
"s_xor_b32 s72, s72, s101\n"
"v_mfma_f32_16x16x32_bf16 a[152:155], v[140:143], v[104:107], a[152:155]\n"
"v_mfma_f32_16x16x32_bf16 a[152:155], v[76:79], v[120:123], a[152:155]\n"
"v_mfma_f32_16x16x32_bf16 a[156:159], v[140:143], v[108:111], a[156:159]\n"
"v_mfma_f32_16x16x32_bf16 a[156:159], v[76:79], v[124:127], a[156:159]\n"
"s_waitcnt vmcnt(12)\n"
"v_mfma_f32_16x16x32_bf16 a[0:3], v[128:131], v[148:151], a[0:3]\n"
"ds_read_b128 v[96:99], v16 offset:49152\n"
"v_lshlrev_b32 v15, 4, v11\n"
"v_mfma_f32_16x16x32_bf16 a[0:3], v[144:147], v[252:255], a[0:3]\n"
"v_lshlrev_b32 v19, 3, v0\n"
"v_mfma_f32_16x16x32_bf16 a[4:7], v[128:131], v[152:155], a[4:7]\n"
"ds_read_b128 v[100:103], v16 offset:50176\n"
"buffer_load_dwordx4 v1, s[4:7], s59 offen offset:2048 sc1 lds\n"
"v_mfma_f32_16x16x32_bf16 a[4:7], v[144:147], v[196:199], a[4:7]\n"
"v_min_u32 v12, 1, v11\n"
"v_mfma_f32_16x16x32_bf16 a[8:11], v[128:131], v[156:159], a[8:11]\n"
"v_lshl_add_u32 v12, v12, 30, v1\n"
"v_mfma_f32_16x16x32_bf16 a[8:11], v[144:147], v[200:203], a[8:11]\n"
"ds_read_b128 v[104:107], v16 offset:51200\n"
"v_mfma_f32_16x16x32_bf16 a[12:15], v[128:131], v[248:251], a[12:15]\n"
"v_lshlrev_b32 v24, 16, v34\n"
"v_mfma_f32_16x16x32_bf16 a[12:15], v[144:147], v[204:207], a[12:15]\n"
"ds_read_b128 v[108:111], v16 offset:52224\n"
"v_and_b32 v25, 0xffff0000, v34\n"
"v_mfma_f32_16x16x32_bf16 a[16:19], v[132:135], v[148:151], a[16:19]\n"
"v_mfma_f32_16x16x32_bf16 a[16:19], v[64:67], v[252:255], a[16:19]\n"
"v_lshlrev_b32 v26, 16, v35\n"
"v_mfma_f32_16x16x32_bf16 a[20:23], v[132:135], v[152:155], a[20:23]\n"
"ds_read_b128 v[112:115], v17 offset:16384\n"
"v_mfma_f32_16x16x32_bf16 a[20:23], v[64:67], v[196:199], a[20:23]\n"
"v_and_b32 v27, 0xffff0000, v35\n"
"v_mfma_f32_16x16x32_bf16 a[24:27], v[132:135], v[156:159], a[24:27]\n"
"ds_read_b128 v[116:119], v17 offset:17408\n"
"v_lshlrev_b32 v28, 16, v36\n"
"v_mfma_f32_16x16x32_bf16 a[24:27], v[64:67], v[200:203], a[24:27]\n"
"v_mfma_f32_16x16x32_bf16 a[28:31], v[132:135], v[248:251], a[28:31]\n"
"v_and_b32 v29, 0xffff0000, v36\n"
"v_mfma_f32_16x16x32_bf16 a[28:31], v[64:67], v[204:207], a[28:31]\n"
"buffer_load_dwordx4 v[120:123], v2, s[4:7], s55 offen sc1\n"
"v_lshlrev_b32 v30, 16, v37\n"
"v_mfma_f32_16x16x32_bf16 a[32:35], a[160:163], v[148:151], a[32:35]\n"
"v_mfma_f32_16x16x32_bf16 a[32:35], v[68:71], v[252:255], a[32:35]\n"
"buffer_load_dwordx4 v[124:127], v3, s[4:7], s55 offen sc1\n"
"v_and_b32 v31, 0xffff0000, v37\n"
"v_mfma_f32_16x16x32_bf16 a[36:39], a[160:163], v[152:155], a[36:39]\n"
"v_mfma_f32_16x16x32_bf16 a[36:39], v[68:71], v[196:199], a[36:39]\n"
"v_lshlrev_b32 v32, 16, v38\n"
"v_mfma_f32_16x16x32_bf16 a[40:43], a[160:163], v[156:159], a[40:43]\n"
"buffer_load_dwordx4 v[60:63], v4, s[4:7], s55 offen sc1\n"
"buffer_load_dwordx4 v1, s[4:7], s59 offen offset:3072 sc1 lds\n"
"s_add_u32 s59, s59, 0x8000\n"
"v_mfma_f32_16x16x32_bf16 a[40:43], v[68:71], v[200:203], a[40:43]\n"
"v_and_b32 v33, 0xffff0000, v38\n"
"v_mfma_f32_16x16x32_bf16 a[44:47], a[160:163], v[248:251], a[44:47]\n"
"buffer_load_dwordx4 v[212:215], v5, s[4:7], s55 offen sc1\n"
"s_add_u32 s55, s55, 0x1000\n"
"v_lshlrev_b32 v34, 16, v39\n"
"v_mfma_f32_16x16x32_bf16 a[44:47], v[68:71], v[204:207], a[44:47]\n"
"v_and_b32 v35, 0xffff0000, v39\n"
"v_mfma_f32_16x16x32_bf16 a[48:51], v[136:139], v[148:151], a[48:51]\n"
"v_mfma_f32_16x16x32_bf16 a[48:51], v[72:75], v[252:255], a[48:51]\n"
"buffer_load_dwordx4 v[216:219], v2, s[4:7], s55 offen sc1\n"
"v_lshlrev_b32 v36, 16, v40\n"
"v_mfma_f32_16x16x32_bf16 a[52:55], v[136:139], v[152:155], a[52:55]\n"
"v_and_b32 v37, 0xffff0000, v40\n"
"v_mfma_f32_16x16x32_bf16 a[52:55], v[72:75], v[196:199], a[52:55]\n"
"buffer_load_dwordx4 v[220:223], v3, s[4:7], s55 offen sc1\n"
"v_mfma_f32_16x16x32_bf16 a[56:59], v[136:139], v[156:159], a[56:59]\n"
"v_lshlrev_b32 v38, 16, v41\n"
"v_mfma_f32_16x16x32_bf16 a[56:59], v[72:75], v[200:203], a[56:59]\n"
"v_mfma_f32_16x16x32_bf16 a[60:63], v[136:139], v[248:251], a[60:63]\n"
"buffer_load_dwordx4 a[164:167], v4, s[4:7], s55 offen sc1\n"
"v_and_b32 v39, 0xffff0000, v41\n"
"v_mfma_f32_16x16x32_bf16 a[60:63], v[72:75], v[204:207], a[60:63]\n"
"v_lshlrev_b32 v40, 16, v42\n"
"v_mfma_f32_16x16x32_bf16 a[64:67], v[140:143], v[148:151], a[64:67]\n"
"buffer_load_dwordx4 a[168:171], v5, s[4:7], s55 offen sc1\n"
"s_add_u32 s55, s55, 0x3000\n"
"v_mfma_f32_16x16x32_bf16 a[64:67], v[76:79], v[252:255], a[64:67]\n"
"v_and_b32 v41, 0xffff0000, v42\n"
"v_mfma_f32_16x16x32_bf16 a[68:71], v[140:143], v[152:155], a[68:71]\n"
"v_lshlrev_b32 v42, 16, v43\n"
"v_mfma_f32_16x16x32_bf16 a[68:71], v[76:79], v[196:199], a[68:71]\n"
"buffer_load_dwordx4 a[172:175], v1, s[4:7], s54 offen offset:3072 sc1\n"
"s_add_u32 s54, s54, 0x4000\n"
"v_mfma_f32_16x16x32_bf16 a[72:75], v[140:143], v[156:159], a[72:75]\n"
"v_and_b32 v43, 0xffff0000, v43\n"
"v_mfma_f32_16x16x32_bf16 a[72:75], v[76:79], v[200:203], a[72:75]\n"
"v_mfma_f32_16x16x32_bf16 a[76:79], v[140:143], v[248:251], a[76:79]\n"
"v_mfma_f32_16x16x32_bf16 a[76:79], v[76:79], v[204:207], a[76:79]\n"
"s_waitcnt vmcnt(12) lgkmcnt(0)\n"
"v_mfma_f32_16x16x32_bf16 a[80:83], v[80:83], v[168:171], a[80:83]\n"
"buffer_load_dwordx4 v[148:151], v2, s[4:7], s68 offen sc1\n"
"s_lshl2_add_u32 s60, s51, s3\n"
"v_mfma_f32_16x16x32_bf16 a[80:83], v[100:103], v[176:179], a[80:83]\n"
"v_mfma_f32_16x16x32_bf16 a[84:87], v[80:83], v[172:175], a[84:87]\n"
"s_add_u32 m0, s78, 0x8000\n"
"v_mfma_f32_16x16x32_bf16 a[84:87], v[100:103], v[180:183], a[84:87]\n"
"v_mfma_f32_16x16x32_bf16 a[88:91], v[80:83], v[160:163], a[88:91]\n"
"buffer_load_dwordx4 v[152:155], v3, s[4:7], s68 offen sc1\n"
"v_mfma_f32_16x16x32_bf16 a[88:91], v[100:103], v[184:187], a[88:91]\n"
"v_mfma_f32_16x16x32_bf16 a[92:95], v[80:83], v[164:167], a[92:95]\n"
"s_lshl3_add_u32 s60, s60, s3\n"
"v_mfma_f32_16x16x32_bf16 a[92:95], v[100:103], v[188:191], a[92:95]\n"
"v_mfma_f32_16x16x32_bf16 a[96:99], v[84:87], v[168:171], a[96:99]\n"
"buffer_load_dwordx4 v[156:159], v4, s[4:7], s68 offen sc1\n"
"v_mfma_f32_16x16x32_bf16 a[96:99], v[104:107], v[176:179], a[96:99]\n"
"v_mfma_f32_16x16x32_bf16 a[100:103], v[84:87], v[172:175], a[100:103]\n"
"v_mfma_f32_16x16x32_bf16 a[100:103], v[104:107], v[180:183], a[100:103]\n"
"v_mfma_f32_16x16x32_bf16 a[104:107], v[84:87], v[160:163], a[104:107]\n"
"buffer_load_dwordx4 v[128:131], v5, s[4:7], s68 offen sc1\n"
"s_add_u32 s68, s68, 0x1000\n"
"s_lshl_b32 s60, s60, 15\n"
"v_mfma_f32_16x16x32_bf16 a[104:107], v[104:107], v[184:187], a[104:107]\n"
"v_mfma_f32_16x16x32_bf16 a[108:111], v[84:87], v[164:167], a[108:111]\n"
"buffer_load_dwordx4 v1, s[4:7], s59 offen sc1 lds\n"
"v_mfma_f32_16x16x32_bf16 a[108:111], v[104:107], v[188:191], a[108:111]\n"
"v_mfma_f32_16x16x32_bf16 a[112:115], v[88:91], v[168:171], a[112:115]\n"
"buffer_load_dwordx4 v[132:135], v2, s[4:7], s68 offen sc1\n"
"v_mfma_f32_16x16x32_bf16 a[112:115], v[108:111], v[176:179], a[112:115]\n"
"v_mfma_f32_16x16x32_bf16 a[116:119], v[88:91], v[172:175], a[116:119]\n"
"v_mfma_f32_16x16x32_bf16 a[116:119], v[108:111], v[180:183], a[116:119]\n"
"s_add_u32 s60, s60, 0x2800000\n"
"v_mfma_f32_16x16x32_bf16 a[120:123], v[88:91], v[160:163], a[120:123]\n"
"v_mfma_f32_16x16x32_bf16 a[120:123], v[108:111], v[184:187], a[120:123]\n"
"buffer_load_dwordx4 v[136:139], v3, s[4:7], s68 offen sc1\n"
"v_mfma_f32_16x16x32_bf16 a[124:127], v[88:91], v[164:167], a[124:127]\n"
"v_mfma_f32_16x16x32_bf16 a[124:127], v[108:111], v[188:191], a[124:127]\n"
"v_mfma_f32_16x16x32_bf16 a[128:131], v[92:95], v[168:171], a[128:131]\n"
"v_mfma_f32_16x16x32_bf16 a[128:131], v[112:115], v[176:179], a[128:131]\n"
"buffer_load_dwordx4 v[140:143], v4, s[4:7], s68 offen sc1\n"
"s_add_u32 s64, s60, 0x1d000\n"
"v_mfma_f32_16x16x32_bf16 a[132:135], v[92:95], v[172:175], a[132:135]\n"
"buffer_load_dwordx4 v1, s[4:7], s59 offen offset:1024 sc1 lds\n"
"v_mfma_f32_16x16x32_bf16 a[132:135], v[112:115], v[180:183], a[132:135]\n"
"v_mfma_f32_16x16x32_bf16 a[136:139], v[92:95], v[160:163], a[136:139]\n"
"v_mfma_f32_16x16x32_bf16 a[136:139], v[112:115], v[184:187], a[136:139]\n"
"buffer_load_dwordx4 v[144:147], v5, s[4:7], s68 offen sc1\n"
"s_add_u32 s68, s68, 0x3000\n"
"v_mfma_f32_16x16x32_bf16 a[140:143], v[92:95], v[164:167], a[140:143]\n"
"v_mfma_f32_16x16x32_bf16 a[140:143], v[112:115], v[188:191], a[140:143]\n"
"s_cmp_eq_u32 s3, 3\n"
"s_cmov_b32 s64, 0x40000000\n"
"v_mfma_f32_16x16x32_bf16 a[144:147], v[96:99], v[168:171], a[144:147]\n"
"v_mfma_f32_16x16x32_bf16 a[144:147], v[116:119], v[176:179], a[144:147]\n"
"buffer_load_dwordx4 v[64:67], v1, s[4:7], s54 offen sc1\n"
"v_mfma_f32_16x16x32_bf16 a[148:151], v[96:99], v[172:175], a[148:151]\n"
"v_mfma_f32_16x16x32_bf16 a[148:151], v[116:119], v[180:183], a[148:151]\n"
"v_mfma_f32_16x16x32_bf16 a[152:155], v[96:99], v[160:163], a[152:155]\n"
"v_mfma_f32_16x16x32_bf16 a[152:155], v[116:119], v[184:187], a[152:155]\n"
"v_mfma_f32_16x16x32_bf16 a[156:159], v[96:99], v[164:167], a[156:159]\n"
"v_mfma_f32_16x16x32_bf16 a[156:159], v[116:119], v[188:191], a[156:159]\n"
"s_waitcnt vmcnt(12)\n"
"v_mfma_f32_16x16x32_bf16 a[0:3], v[80:83], v[120:123], a[0:3]\n"
"ds_read_b128 v[68:71], v17 offset:18432\n"
"v_mfma_f32_16x16x32_bf16 a[0:3], v[100:103], v[216:219], a[0:3]\n"
"v_mfma_f32_16x16x32_bf16 a[4:7], v[80:83], v[124:127], a[4:7]\n"
"ds_read_b128 v[72:75], v17 offset:19456\n"
"buffer_load_dwordx4 v1, s[4:7], s59 offen offset:2048 sc1 lds\n"
"v_mfma_f32_16x16x32_bf16 a[4:7], v[100:103], v[220:223], a[4:7]\n"
"v_mfma_f32_16x16x32_bf16 a[8:11], v[80:83], v[60:63], a[8:11]\n"
"v_mfma_f32_16x16x32_bf16 a[8:11], v[100:103], a[164:167], a[8:11]\n"
"buffer_load_dwordx4 v[168:171], v2, s[4:7], s68 offen sc1\n"
"v_mfma_f32_16x16x32_bf16 a[12:15], v[80:83], v[212:215], a[12:15]\n"
"v_mfma_f32_16x16x32_bf16 a[12:15], v[100:103], a[168:171], a[12:15]\n"
"buffer_load_dwordx4 v[172:175], v3, s[4:7], s68 offen sc1\n"
"v_mfma_f32_16x16x32_bf16 a[16:19], v[84:87], v[120:123], a[16:19]\n"
"v_mfma_f32_16x16x32_bf16 a[16:19], v[104:107], v[216:219], a[16:19]\n"
"v_mfma_f32_16x16x32_bf16 a[20:23], v[84:87], v[124:127], a[20:23]\n"
"buffer_load_dwordx4 v[160:163], v4, s[4:7], s68 offen sc1\n"
"v_mfma_f32_16x16x32_bf16 a[20:23], v[104:107], v[220:223], a[20:23]\n"
"v_mfma_f32_16x16x32_bf16 a[24:27], v[84:87], v[60:63], a[24:27]\n"
"buffer_load_dwordx4 v[164:167], v5, s[4:7], s68 offen sc1\n"
"s_add_u32 s68, s68, 0x1000\n"
"v_mfma_f32_16x16x32_bf16 a[24:27], v[104:107], a[164:167], a[24:27]\n"
"v_mfma_f32_16x16x32_bf16 a[28:31], v[84:87], v[212:215], a[28:31]\n"
"v_mfma_f32_16x16x32_bf16 a[28:31], v[104:107], a[168:171], a[28:31]\n"
"buffer_load_dwordx4 v[76:79], v2, s[4:7], s68 offen sc1\n"
"v_mfma_f32_16x16x32_bf16 a[32:35], v[88:91], v[120:123], a[32:35]\n"
"v_mfma_f32_16x16x32_bf16 a[32:35], v[108:111], v[216:219], a[32:35]\n"
"buffer_load_dwordx4 v[176:179], v3, s[4:7], s68 offen sc1\n"
"v_mfma_f32_16x16x32_bf16 a[36:39], v[88:91], v[124:127], a[36:39]\n"
"v_mfma_f32_16x16x32_bf16 a[36:39], v[108:111], v[220:223], a[36:39]\n"
"v_mfma_f32_16x16x32_bf16 a[40:43], v[88:91], v[60:63], a[40:43]\n"
"buffer_load_dwordx4 v[180:183], v4, s[4:7], s68 offen sc1\n"
"buffer_load_dwordx4 v1, s[4:7], s59 offen offset:3072 sc1 lds\n"
"s_add_u32 s59, s59, 0x8000\n"
"v_mfma_f32_16x16x32_bf16 a[40:43], v[108:111], a[164:167], a[40:43]\n"
"v_mfma_f32_16x16x32_bf16 a[44:47], v[88:91], v[212:215], a[44:47]\n"
"buffer_load_dwordx4 v[184:187], v5, s[4:7], s68 offen sc1\n"
"s_add_u32 s68, s68, 0x3000\n"
"v_mfma_f32_16x16x32_bf16 a[44:47], v[108:111], a[168:171], a[44:47]\n"
"v_mfma_f32_16x16x32_bf16 a[48:51], v[92:95], v[120:123], a[48:51]\n"
"v_mfma_f32_16x16x32_bf16 a[48:51], v[112:115], v[216:219], a[48:51]\n"
"buffer_load_dwordx4 v[188:191], v1, s[4:7], s54 offen offset:1024 sc1\n"
"v_mfma_f32_16x16x32_bf16 a[52:55], v[92:95], v[124:127], a[52:55]\n"
"v_mfma_f32_16x16x32_bf16 a[52:55], v[112:115], v[220:223], a[52:55]\n"
"buffer_load_dwordx4 a[176:179], v1, s[4:7], s54 offen offset:2048 sc1\n"
"v_mfma_f32_16x16x32_bf16 a[56:59], v[92:95], v[60:63], a[56:59]\n"
"v_mfma_f32_16x16x32_bf16 a[56:59], v[112:115], a[164:167], a[56:59]\n"
"v_mfma_f32_16x16x32_bf16 a[60:63], v[92:95], v[212:215], a[60:63]\n"
"buffer_load_dwordx4 a[180:183], v1, s[4:7], s54 offen offset:3072 sc1\n"
"s_add_u32 s54, s54, 0x1000\n"
"v_mfma_f32_16x16x32_bf16 a[60:63], v[112:115], a[168:171], a[60:63]\n"
"v_mfma_f32_16x16x32_bf16 a[64:67], v[96:99], v[120:123], a[64:67]\n"
"buffer_load_dwordx4 a[184:187], v1, s[4:7], s54 offen sc1\n"
"v_mfma_f32_16x16x32_bf16 a[64:67], v[116:119], v[216:219], a[64:67]\n"
"v_mfma_f32_16x16x32_bf16 a[68:71], v[96:99], v[124:127], a[68:71]\n"
"v_mfma_f32_16x16x32_bf16 a[68:71], v[116:119], v[220:223], a[68:71]\n"
"buffer_load_dwordx4 a[188:191], v1, s[4:7], s54 offen offset:1024 sc1\n"
"v_mfma_f32_16x16x32_bf16 a[72:75], v[96:99], v[60:63], a[72:75]\n"
"v_mfma_f32_16x16x32_bf16 a[72:75], v[116:119], a[164:167], a[72:75]\n"
"v_mfma_f32_16x16x32_bf16 a[76:79], v[96:99], v[212:215], a[76:79]\n"
"v_mfma_f32_16x16x32_bf16 a[76:79], v[116:119], a[168:171], a[76:79]\n"
"s_waitcnt vmcnt(16) lgkmcnt(0)\n"
"v_mfma_f32_16x16x32_bf16 a[80:83], v[68:71], v[148:151], a[80:83]\n"
"ds_read_b128 v[60:63], v16\n"
"s_nop 0\n"
"v_mfma_f32_16x16x32_bf16 a[80:83], v[240:243], v[132:135], a[80:83]\n"
"v_mfma_f32_16x16x32_bf16 a[84:87], v[68:71], v[152:155], a[84:87]\n"
"ds_read_b128 v[80:83], v16 offset:1024\n"
"s_add_u32 m0, s78, 0xc000\n"
"v_mfma_f32_16x16x32_bf16 a[84:87], v[240:243], v[136:139], a[84:87]\n"
"v_mfma_f32_16x16x32_bf16 a[88:91], v[68:71], v[156:159], a[88:91]\n"
"ds_read_b128 v[84:87], v16 offset:2048\n"
"v_mfma_f32_16x16x32_bf16 a[88:91], v[240:243], v[140:143], a[88:91]\n"
"v_mfma_f32_16x16x32_bf16 a[92:95], v[68:71], v[128:131], a[92:95]\n"
"ds_read_b128 v[88:91], v16 offset:3072\n"
"v_mfma_f32_16x16x32_bf16 a[92:95], v[240:243], v[144:147], a[92:95]\n"
"v_mfma_f32_16x16x32_bf16 a[96:99], v[72:75], v[148:151], a[96:99]\n"
"buffer_load_dwordx4 v[120:123], v2, s[4:7], s68 offen sc1\n"
"v_mfma_f32_16x16x32_bf16 a[96:99], v[244:247], v[132:135], a[96:99]\n"
"v_mfma_f32_16x16x32_bf16 a[100:103], v[72:75], v[152:155], a[100:103]\n"
"buffer_load_dwordx4 v[124:127], v3, s[4:7], s68 offen sc1\n"
"v_mfma_f32_16x16x32_bf16 a[100:103], v[244:247], v[136:139], a[100:103]\n"
"v_mfma_f32_16x16x32_bf16 a[104:107], v[72:75], v[156:159], a[104:107]\n"
"buffer_load_dwordx4 v[96:99], v4, s[4:7], s68 offen sc1\n"
"v_mfma_f32_16x16x32_bf16 a[104:107], v[244:247], v[140:143], a[104:107]\n"
"v_mfma_f32_16x16x32_bf16 a[108:111], v[72:75], v[128:131], a[108:111]\n"
"buffer_load_dwordx4 v[100:103], v5, s[4:7], s68 offen sc1\n"
"buffer_load_dwordx4 v1, s[4:7], s59 offen sc1 lds\n"
"s_add_u32 s68, s68, 0x1000\n"
"v_mfma_f32_16x16x32_bf16 a[108:111], v[244:247], v[144:147], a[108:111]\n"
"v_mfma_f32_16x16x32_bf16 a[112:115], v[192:195], v[148:151], a[112:115]\n"
"buffer_load_dwordx4 v[104:107], v2, s[4:7], s68 offen sc1\n"
"v_mfma_f32_16x16x32_bf16 a[112:115], v[208:211], v[132:135], a[112:115]\n"
"v_mfma_f32_16x16x32_bf16 a[116:119], v[192:195], v[152:155], a[116:119]\n"
"v_mfma_f32_16x16x32_bf16 a[116:119], v[208:211], v[136:139], a[116:119]\n"
"buffer_load_dwordx4 v[108:111], v3, s[4:7], s68 offen sc1\n"
"v_mfma_f32_16x16x32_bf16 a[120:123], v[192:195], v[156:159], a[120:123]\n"
"v_mfma_f32_16x16x32_bf16 a[120:123], v[208:211], v[140:143], a[120:123]\n"
"buffer_load_dwordx4 v[112:115], v4, s[4:7], s68 offen sc1\n"
"v_mfma_f32_16x16x32_bf16 a[124:127], v[192:195], v[128:131], a[124:127]\n"
"v_mfma_f32_16x16x32_bf16 a[124:127], v[208:211], v[144:147], a[124:127]\n"
"buffer_load_dwordx4 v[116:119], v5, s[4:7], s68 offen sc1\n"
"s_add_u32 s68, s68, 0x3000\n"
"v_mfma_f32_16x16x32_bf16 a[128:131], v[224:227], v[148:151], a[128:131]\n"
"v_mfma_f32_16x16x32_bf16 a[128:131], v[56:59], v[132:135], a[128:131]\n"
"buffer_load_dwordx4 a[192:195], v1, s[4:7], s54 offen offset:2048 sc1\n"
"v_mfma_f32_16x16x32_bf16 a[132:135], v[224:227], v[152:155], a[132:135]\n"
"buffer_load_dwordx4 v1, s[4:7], s59 offen offset:1024 sc1 lds\n"
"v_mfma_f32_16x16x32_bf16 a[132:135], v[56:59], v[136:139], a[132:135]\n"
"buffer_load_dwordx4 a[196:199], v1, s[4:7], s54 offen offset:3072 sc1\n"
"s_add_u32 s54, s54, 0x3000\n"
"v_mfma_f32_16x16x32_bf16 a[136:139], v[224:227], v[156:159], a[136:139]\n"
"v_mfma_f32_16x16x32_bf16 a[136:139], v[56:59], v[140:143], a[136:139]\n"
"buffer_load_dwordx4 a[200:203], v1, s[4:7], s54 offen sc1\n"
"v_mfma_f32_16x16x32_bf16 a[140:143], v[224:227], v[128:131], a[140:143]\n"
"v_mfma_f32_16x16x32_bf16 a[140:143], v[56:59], v[144:147], a[140:143]\n"
"buffer_load_dwordx4 a[204:207], v1, s[4:7], s54 offen offset:1024 sc1\n"
"v_mfma_f32_16x16x32_bf16 a[144:147], v[228:231], v[148:151], a[144:147]\n"
"v_mfma_f32_16x16x32_bf16 a[144:147], a[172:175], v[132:135], a[144:147]\n"
"buffer_load_dwordx4 a[208:211], v1, s[4:7], s54 offen offset:2048 sc1\n"
"v_mfma_f32_16x16x32_bf16 a[148:151], v[228:231], v[152:155], a[148:151]\n"
"v_mfma_f32_16x16x32_bf16 a[148:151], a[172:175], v[136:139], a[148:151]\n"
"buffer_load_dwordx4 a[212:215], v1, s[4:7], s54 offen offset:3072 sc1\n"
"s_add_u32 s54, s54, 0x4000\n"
"v_mfma_f32_16x16x32_bf16 a[152:155], v[228:231], v[156:159], a[152:155]\n"
"v_mfma_f32_16x16x32_bf16 a[152:155], a[172:175], v[140:143], a[152:155]\n"
"v_mfma_f32_16x16x32_bf16 a[156:159], v[228:231], v[128:131], a[156:159]\n"
"v_mfma_f32_16x16x32_bf16 a[156:159], a[172:175], v[144:147], a[156:159]\n"
"s_waitcnt vmcnt(16) lgkmcnt(0)\n"
"v_mfma_f32_16x16x32_bf16 a[80:83], v[60:63], v[168:171], a[80:83]\n"
"ds_read_b128 a[216:219], v16 offset:16384\n"
"v_mfma_f32_16x16x32_bf16 a[80:83], v[188:191], v[76:79], a[80:83]\n"
"v_mfma_f32_16x16x32_bf16 a[84:87], v[60:63], v[172:175], a[84:87]\n"
"buffer_load_dwordx4 v1, s[4:7], s59 offen offset:2048 sc1 lds\n"
"v_mfma_f32_16x16x32_bf16 a[84:87], v[188:191], v[176:179], a[84:87]\n"
"ds_read_b128 a[220:223], v16 offset:17408\n"
"v_mfma_f32_16x16x32_bf16 a[88:91], v[60:63], v[160:163], a[88:91]\n"
"v_mfma_f32_16x16x32_bf16 a[88:91], v[188:191], v[180:183], a[88:91]\n"
"v_mfma_f32_16x16x32_bf16 a[92:95], v[60:63], v[164:167], a[92:95]\n"
"ds_read_b128 a[224:227], v16 offset:18432\n"
"v_mfma_f32_16x16x32_bf16 a[92:95], v[188:191], v[184:187], a[92:95]\n"
"v_mfma_f32_16x16x32_bf16 a[96:99], v[80:83], v[168:171], a[96:99]\n"
"v_mfma_f32_16x16x32_bf16 a[96:99], a[176:179], v[76:79], a[96:99]\n"
"ds_read_b128 a[228:231], v16 offset:19456\n"
"v_mfma_f32_16x16x32_bf16 a[100:103], v[80:83], v[172:175], a[100:103]\n"
"v_mfma_f32_16x16x32_bf16 a[100:103], a[176:179], v[176:179], a[100:103]\n"
"v_mfma_f32_16x16x32_bf16 a[104:107], v[80:83], v[160:163], a[104:107]\n"
"buffer_load_dwordx4 v[92:95], v2, s[4:7], s55 offen sc1\n"
"v_mfma_f32_16x16x32_bf16 a[104:107], a[176:179], v[180:183], a[104:107]\n"
"v_mfma_f32_16x16x32_bf16 a[108:111], v[80:83], v[164:167], a[108:111]\n"
"v_mfma_f32_16x16x32_bf16 a[108:111], a[176:179], v[184:187], a[108:111]\n"
"buffer_load_dwordx4 a[232:235], v3, s[4:7], s55 offen sc1\n"
"v_mfma_f32_16x16x32_bf16 a[112:115], v[84:87], v[168:171], a[112:115]\n"
"v_mfma_f32_16x16x32_bf16 a[112:115], a[180:183], v[76:79], a[112:115]\n"
"v_mfma_f32_16x16x32_bf16 a[116:119], v[84:87], v[172:175], a[116:119]\n"
"v_mfma_f32_16x16x32_bf16 a[116:119], a[180:183], v[176:179], a[116:119]\n"
"buffer_load_dwordx4 a[236:239], v4, s[4:7], s55 offen sc1\n"
"v_mfma_f32_16x16x32_bf16 a[120:123], v[84:87], v[160:163], a[120:123]\n"
"buffer_load_dwordx4 v1, s[4:7], s59 offen offset:3072 sc1 lds\n"
"s_add_u32 s59, s59, 0x8000\n"
"v_mfma_f32_16x16x32_bf16 a[120:123], a[180:183], v[180:183], a[120:123]\n"
"v_mfma_f32_16x16x32_bf16 a[124:127], v[84:87], v[164:167], a[124:127]\n"
"buffer_load_dwordx4 a[240:243], v5, s[4:7], s55 offen sc1\n"
"s_add_u32 s55, s55, 0x1000\n"
"v_mfma_f32_16x16x32_bf16 a[124:127], a[180:183], v[184:187], a[124:127]\n"
"v_mfma_f32_16x16x32_bf16 a[128:131], v[88:91], v[168:171], a[128:131]\n"
"v_mfma_f32_16x16x32_bf16 a[128:131], a[184:187], v[76:79], a[128:131]\n"
"buffer_load_dwordx4 v[232:235], v2, s[4:7], s55 offen sc1\n"
"v_mfma_f32_16x16x32_bf16 a[132:135], v[88:91], v[172:175], a[132:135]\n"
"v_mfma_f32_16x16x32_bf16 a[132:135], a[184:187], v[176:179], a[132:135]\n"
"v_mfma_f32_16x16x32_bf16 a[136:139], v[88:91], v[160:163], a[136:139]\n"
"buffer_load_dwordx4 v[236:239], v3, s[4:7], s55 offen sc1\n"
"v_mfma_f32_16x16x32_bf16 a[136:139], a[184:187], v[180:183], a[136:139]\n"
"v_mfma_f32_16x16x32_bf16 a[140:143], v[88:91], v[164:167], a[140:143]\n"
"v_mfma_f32_16x16x32_bf16 a[140:143], a[184:187], v[184:187], a[140:143]\n"
"buffer_load_dwordx4 a[244:247], v4, s[4:7], s55 offen sc1\n"
"v_mfma_f32_16x16x32_bf16 a[144:147], v[64:67], v[168:171], a[144:147]\n"
"v_mfma_f32_16x16x32_bf16 a[144:147], a[188:191], v[76:79], a[144:147]\n"
"v_mfma_f32_16x16x32_bf16 a[148:151], v[64:67], v[172:175], a[148:151]\n"
"buffer_load_dwordx4 a[248:251], v5, s[4:7], s55 offen sc1\n"
"s_add_u32 s55, s55, 0x3000\n"
"v_mfma_f32_16x16x32_bf16 a[148:151], a[188:191], v[176:179], a[148:151]\n"
"v_mfma_f32_16x16x32_bf16 a[152:155], v[64:67], v[160:163], a[152:155]\n"
"v_mfma_f32_16x16x32_bf16 a[152:155], a[188:191], v[180:183], a[152:155]\n"
"v_mfma_f32_16x16x32_bf16 a[156:159], v[64:67], v[164:167], a[156:159]\n"
"v_mfma_f32_16x16x32_bf16 a[156:159], a[188:191], v[184:187], a[156:159]\n"
"s_waitcnt vmcnt(10) lgkmcnt(0)\n"
"v_mfma_f32_16x16x32_bf16 a[84:87], a[192:195], v[124:127], a[84:87]\n"
"buffer_load_dwordx4 v[76:79], v2, s[4:7], s55 offen sc1\n"
"v_mfma_f32_16x16x32_bf16 a[84:87], a[212:215], v[108:111], a[84:87]\n"
"s_barrier\n"
"v_mfma_f32_16x16x32_bf16 a[100:103], a[196:199], v[124:127], a[100:103]\n"
"v_mul_u32_u24 v14, 0xccd, v0\n"
"v_mfma_f32_16x16x32_bf16 a[100:103], a[216:219], v[108:111], a[100:103]\n"
"v_lshrrev_b32 v14, 16, v14\n"
"v_mfma_f32_16x16x32_bf16 a[116:119], a[200:203], v[124:127], a[116:119]\n"
"buffer_load_dwordx4 v[176:179], v3, s[4:7], s55 offen sc1\n"
"v_mul_u32_u24 v16, 20, v14\n"
"v_mfma_f32_16x16x32_bf16 a[116:119], a[220:223], v[108:111], a[116:119]\n"
"v_sub_u32 v16, v0, v16\n"
"v_mfma_f32_16x16x32_bf16 a[132:135], a[204:207], v[124:127], a[132:135]\n"
"s_and_b32 s58, s51, 1\n"
"v_mfma_f32_16x16x32_bf16 a[132:135], a[224:227], v[108:111], a[132:135]\n"
"v_lshl_add_u32 v16, s58, 2, v16\n"
"v_mfma_f32_16x16x32_bf16 a[148:151], a[208:211], v[124:127], a[148:151]\n"
"v_subrev_u32 v17, 20, v16\n"
"v_mfma_f32_16x16x32_bf16 a[148:151], a[228:231], v[108:111], a[148:151]\n"
"ds_write_b128 v7, a[84:87]\n"
"v_min_u32 v16, v16, v17\n"
"v_mfma_f32_16x16x32_bf16 a[88:91], a[192:195], v[96:99], a[88:91]\n"
"buffer_load_dwordx4 v[180:183], v4, s[4:7], s55 offen sc1\n"
"v_min_u32 v14, 1, v14\n"
"v_mfma_f32_16x16x32_bf16 a[88:91], a[212:215], v[112:115], a[88:91]\n"
"ds_write_b128 v7, a[100:103] offset:1024\n"
"v_mul_u32_u24 v14, 0x1400, v14\n"
"v_mfma_f32_16x16x32_bf16 a[104:107], a[196:199], v[96:99], a[104:107]\n"
"v_lshl_add_u32 v14, v16, 4, v14\n"
"v_mfma_f32_16x16x32_bf16 a[104:107], a[216:219], v[112:115], a[104:107]\n"
"ds_write_b128 v7, a[116:119] offset:2048\n"
"s_mul_i32 s56, s51, 0x140\n"
"v_mfma_f32_16x16x32_bf16 a[120:123], a[200:203], v[96:99], a[120:123]\n"
"buffer_load_dwordx4 v[184:187], v5, s[4:7], s55 offen sc1\n"
"s_add_u32 s55, s55, 0x1000\n"
"s_add_u32 s56, s56, 0x21400\n"
"v_mfma_f32_16x16x32_bf16 a[120:123], a[220:223], v[112:115], a[120:123]\n"
"ds_write_b128 v7, a[132:135] offset:3072\n"
"s_lshl_b32 s57, s3, 14\n"
"v_mfma_f32_16x16x32_bf16 a[136:139], a[204:207], v[96:99], a[136:139]\n"
"s_add_u32 s57, s57, 0x1bc00\n"
"v_mfma_f32_16x16x32_bf16 a[136:139], a[224:227], v[112:115], a[136:139]\n"
"ds_write_b128 v7, a[148:151] offset:4096\n"
"v_add_u32 v16, s57, v15\n"
"v_mfma_f32_16x16x32_bf16 a[152:155], a[208:211], v[96:99], a[152:155]\n"
"s_mov_b32 m0, s57\n"
"v_mfma_f32_16x16x32_bf16 a[152:155], a[228:231], v[112:115], a[152:155]\n"
"ds_write_b128 v8, a[88:91] offset:5120\n"
"buffer_load_dwordx4 v14, s[4:7], s56 offen sc1 lds\n"
"v_mfma_f32_16x16x32_bf16 a[92:95], a[192:195], v[100:103], a[92:95]\n"
"buffer_load_dwordx4 a[252:255], v2, s[4:7], s55 offen sc1\n"
"v_mfma_f32_16x16x32_bf16 a[92:95], a[212:215], v[116:119], a[92:95]\n"
"ds_write_b128 v8, a[104:107] offset:6144\n"
"v_mfma_f32_16x16x32_bf16 a[108:111], a[196:199], v[100:103], a[108:111]\n"
"v_mfma_f32_16x16x32_bf16 a[108:111], a[216:219], v[116:119], a[108:111]\n"
"ds_write_b128 v8, a[120:123] offset:7168\n"
"v_mfma_f32_16x16x32_bf16 a[124:127], a[200:203], v[100:103], a[124:127]\n"
"buffer_load_dwordx4 v[44:47], v3, s[4:7], s55 offen sc1\n"
"v_mfma_f32_16x16x32_bf16 a[124:127], a[220:223], v[116:119], a[124:127]\n"
"ds_write_b128 v8, a[136:139] offset:8192\n"
"v_mfma_f32_16x16x32_bf16 a[140:143], a[204:207], v[100:103], a[140:143]\n"
"v_mfma_f32_16x16x32_bf16 a[140:143], a[224:227], v[116:119], a[140:143]\n"
"ds_write_b128 v8, a[152:155] offset:9216\n"
"v_mfma_f32_16x16x32_bf16 a[156:159], a[208:211], v[100:103], a[156:159]\n"
"buffer_load_dwordx4 v[48:51], v4, s[4:7], s55 offen sc1\n"
"v_mfma_f32_16x16x32_bf16 a[156:159], a[228:231], v[116:119], a[156:159]\n"
"ds_write_b128 v9, a[92:95] offset:10240\n"
"v_mfma_f32_16x16x32_bf16 a[80:83], a[192:195], v[120:123], a[80:83]\n"
"v_mfma_f32_16x16x32_bf16 a[80:83], a[212:215], v[104:107], a[80:83]\n"
"ds_write_b128 v9, a[108:111] offset:11264\n"
"v_mfma_f32_16x16x32_bf16 a[96:99], a[196:199], v[120:123], a[96:99]\n"
"v_mfma_f32_16x16x32_bf16 a[96:99], a[216:219], v[104:107], a[96:99]\n"
"ds_write_b128 v9, a[124:127] offset:12288\n"
"v_mfma_f32_16x16x32_bf16 a[112:115], a[200:203], v[120:123], a[112:115]\n"
"buffer_load_dwordx4 v[52:55], v5, s[4:7], s55 offen sc1\n"
"s_add_u32 s55, s55, 0x3000\n"
"v_mfma_f32_16x16x32_bf16 a[112:115], a[220:223], v[104:107], a[112:115]\n"
"ds_write_b128 v9, a[140:143] offset:13312\n"
"v_mfma_f32_16x16x32_bf16 a[128:131], a[204:207], v[120:123], a[128:131]\n"
"v_mfma_f32_16x16x32_bf16 a[128:131], a[224:227], v[104:107], a[128:131]\n"
"ds_write_b128 v9, a[156:159] offset:14336\n"
"v_mfma_f32_16x16x32_bf16 a[144:147], a[208:211], v[120:123], a[144:147]\n"
"v_mfma_f32_16x16x32_bf16 a[144:147], a[228:231], v[104:107], a[144:147]\n"
"s_waitcnt vmcnt(9)\n"
"v_mfma_f32_16x16x32_bf16 a[0:3], v[68:71], v[92:95], a[0:3]\n"
"buffer_load_dwordx4 v[248:251], v2, s[4:7], s55 offen sc1\n"
"v_mfma_f32_16x16x32_bf16 a[0:3], v[240:243], v[232:235], a[0:3]\n"
"v_mfma_f32_16x16x32_bf16 a[4:7], v[68:71], a[232:235], a[4:7]\n"
"v_mfma_f32_16x16x32_bf16 a[4:7], v[240:243], v[236:239], a[4:7]\n"
"v_mfma_f32_16x16x32_bf16 a[8:11], v[68:71], a[236:239], a[8:11]\n"
"buffer_load_dwordx4 v[252:255], v3, s[4:7], s55 offen sc1\n"
"v_mfma_f32_16x16x32_bf16 a[8:11], v[240:243], a[244:247], a[8:11]\n"
"v_mfma_f32_16x16x32_bf16 a[12:15], v[68:71], a[240:243], a[12:15]\n"
"s_waitcnt lgkmcnt(0)\n"
"s_barrier\n"
"ds_read_b128 v[116:119], v6\n"
"v_mfma_f32_16x16x32_bf16 a[12:15], v[240:243], a[248:251], a[12:15]\n"
"ds_read_b128 v[136:139], v6 offset:5120\n"
"ds_read_b128 v[156:159], v6 offset:10240\n"
"ds_read_b128 v[120:123], v6 offset:1024\n"
"v_mfma_f32_16x16x32_bf16 a[16:19], v[72:75], v[92:95], a[16:19]\n"
"ds_read_b128 v[140:143], v6 offset:6144\n"
"ds_read_b128 v[160:163], v6 offset:11264\n"
"ds_read_b128 v[124:127], v6 offset:2048\n"
"v_mfma_f32_16x16x32_bf16 a[16:19], v[244:247], v[232:235], a[16:19]\n"
"buffer_load_dwordx4 v[196:199], v4, s[4:7], s55 offen sc1\n"
"ds_read_b128 v[144:147], v6 offset:7168\n"
"ds_read_b128 v[164:167], v6 offset:12288\n"
"v_mfma_f32_16x16x32_bf16 a[20:23], v[72:75], a[232:235], a[20:23]\n"
"ds_read_b128 v[128:131], v6 offset:3072\n"
"ds_read_b128 v[148:151], v6 offset:8192\n"
"ds_read_b128 v[168:171], v6 offset:13312\n"
"v_mfma_f32_16x16x32_bf16 a[20:23], v[244:247], v[236:239], a[20:23]\n"
"ds_read_b128 v[132:135], v6 offset:4096\n"
"ds_read_b128 v[152:155], v6 offset:9216\n"
"ds_read_b128 v[172:175], v6 offset:14336\n"
"v_mfma_f32_16x16x32_bf16 a[24:27], v[72:75], a[236:239], a[24:27]\n"
"v_accvgpr_read_b32 v96, a80\n"
"v_accvgpr_read_b32 v97, a81\n"
"v_accvgpr_read_b32 v98, a82\n"
"v_mfma_f32_16x16x32_bf16 a[24:27], v[244:247], a[244:247], a[24:27]\n"
"v_accvgpr_read_b32 v99, a83\n"
"v_accvgpr_read_b32 v100, a96\n"
"v_accvgpr_read_b32 v101, a97\n"
"v_mfma_f32_16x16x32_bf16 a[28:31], v[72:75], a[240:243], a[28:31]\n"
"buffer_load_dwordx4 v[200:203], v5, s[4:7], s55 offen sc1\n"
"s_add_u32 s55, s55, 0x1000\n"
"v_accvgpr_read_b32 v102, a98\n"
"v_mfma_f32_16x16x32_bf16 a[28:31], v[244:247], a[248:251], a[28:31]\n"
"v_accvgpr_read_b32 v103, a99\n"
"v_accvgpr_read_b32 v104, a112\n"
"v_accvgpr_read_b32 v105, a113\n"
"v_mfma_f32_16x16x32_bf16 a[32:35], v[192:195], v[92:95], a[32:35]\n"
"v_accvgpr_read_b32 v106, a114\n"
"v_accvgpr_read_b32 v107, a115\n"
"v_accvgpr_read_b32 v108, a128\n"
"v_mfma_f32_16x16x32_bf16 a[32:35], v[208:211], v[232:235], a[32:35]\n"
"v_accvgpr_read_b32 v109, a129\n"
"v_accvgpr_read_b32 v110, a130\n"
"v_accvgpr_read_b32 v111, a131\n"
"v_mfma_f32_16x16x32_bf16 a[36:39], v[192:195], a[232:235], a[36:39]\n"
"v_accvgpr_read_b32 v112, a144\n"
"v_accvgpr_read_b32 v113, a145\n"
"v_accvgpr_read_b32 v114, a146\n"
"v_mfma_f32_16x16x32_bf16 a[36:39], v[208:211], v[236:239], a[36:39]\n"
"buffer_load_dwordx4 v[204:207], v2, s[4:7], s55 offen sc1\n"
"v_accvgpr_read_b32 v115, a147\n"
"s_waitcnt lgkmcnt(12)\n"
"v_mfma_f32_16x16x32_bf16 a[40:43], v[192:195], a[236:239], a[40:43]\n"
"v_add_f32 v96, v96, v116\n"
"v_add_f32 v136, v136, v156\n"
"v_add_f32 v97, v97, v117\n"
"v_mfma_f32_16x16x32_bf16 a[40:43], v[208:211], a[244:247], a[40:43]\n"
"v_add_f32 v137, v137, v157\n"
"v_add_f32 v98, v98, v118\n"
"v_add_f32 v138, v138, v158\n"
"v_mfma_f32_16x16x32_bf16 a[44:47], v[192:195], a[240:243], a[44:47]\n"
"v_add_f32 v99, v99, v119\n"
"v_add_f32 v139, v139, v159\n"
"v_add_f32 v96, v96, v136\n"
"v_mfma_f32_16x16x32_bf16 a[44:47], v[208:211], a[248:251], a[44:47]\n"
"buffer_load_dwordx4 a[160:163], v3, s[4:7], s55 offen sc1\n"
"v_add_f32 v97, v97, v137\n"
"v_add_f32 v98, v98, v138\n"
"v_mfma_f32_16x16x32_bf16 a[48:51], v[224:227], v[92:95], a[48:51]\n"
"v_add_f32 v99, v99, v139\n"
"v_cvt_pk_bf16_f32 v116, v96, v97\n"
"v_cvt_pk_bf16_f32 v118, v98, v99\n"
"v_mfma_f32_16x16x32_bf16 a[48:51], v[56:59], v[232:235], a[48:51]\n"
"v_mov_b32 v117, v22\n"
"v_mov_b32 v119, v22\n"
"v_mfma_f32_16x16x32_bf16 a[52:55], v[224:227], a[232:235], a[52:55]\n"
"buffer_store_dwordx4 v[116:119], v1, s[4:7], s69 offen sc1\n"
"s_waitcnt lgkmcnt(9)\n"
"v_add_f32 v100, v100, v120\n"
"v_mfma_f32_16x16x32_bf16 a[52:55], v[56:59], v[236:239], a[52:55]\n"
"v_add_f32 v140, v140, v160\n"
"v_add_f32 v101, v101, v121\n"
"v_add_f32 v141, v141, v161\n"
"v_mfma_f32_16x16x32_bf16 a[56:59], v[224:227], a[236:239], a[56:59]\n"
"buffer_load_dwordx4 v[212:215], v4, s[4:7], s55 offen sc1\n"
"v_add_f32 v102, v102, v122\n"
"v_add_f32 v142, v142, v162\n"
"v_mfma_f32_16x16x32_bf16 a[56:59], v[56:59], a[244:247], a[56:59]\n"
"v_add_f32 v103, v103, v123\n"
"v_add_f32 v143, v143, v163\n"
"v_add_f32 v100, v100, v140\n"
"v_mfma_f32_16x16x32_bf16 a[60:63], v[224:227], a[240:243], a[60:63]\n"
"v_add_f32 v101, v101, v141\n"
"v_add_f32 v102, v102, v142\n"
"v_add_f32 v103, v103, v143\n"
"v_mfma_f32_16x16x32_bf16 a[60:63], v[56:59], a[248:251], a[60:63]\n"
"v_cvt_pk_bf16_f32 v120, v100, v101\n"
"v_cvt_pk_bf16_f32 v122, v102, v103\n"
"v_mov_b32 v121, v22\n"
"v_mfma_f32_16x16x32_bf16 a[64:67], v[228:231], v[92:95], a[64:67]\n"
"v_mov_b32 v123, v22\n"
"v_mfma_f32_16x16x32_bf16 a[64:67], a[172:175], v[232:235], a[64:67]\n"
"buffer_load_dwordx4 v[216:219], v5, s[4:7], s55 offen sc1\n"
"buffer_store_dwordx4 v[120:123], v1, s[4:7], s69 offen offset:1024 sc1\n"
"s_add_u32 s55, s55, 0x3000\n"
"v_mfma_f32_16x16x32_bf16 a[68:71], v[228:231], a[232:235], a[68:71]\n"
"s_waitcnt lgkmcnt(6)\n"
"v_add_f32 v104, v104, v124\n"
"v_add_f32 v144, v144, v164\n"
"v_mfma_f32_16x16x32_bf16 a[68:71], a[172:175], v[236:239], a[68:71]\n"
"v_add_f32 v105, v105, v125\n"
"v_add_f32 v145, v145, v165\n"
"v_add_f32 v106, v106, v126\n"
"v_mfma_f32_16x16x32_bf16 a[72:75], v[228:231], a[236:239], a[72:75]\n"
"v_add_f32 v146, v146, v166\n"
"v_add_f32 v107, v107, v127\n"
"v_add_f32 v147, v147, v167\n"
"v_mfma_f32_16x16x32_bf16 a[72:75], a[172:175], a[244:247], a[72:75]\n"
"v_add_f32 v104, v104, v144\n"
"v_add_f32 v105, v105, v145\n"
"v_add_f32 v106, v106, v146\n"
"v_mfma_f32_16x16x32_bf16 a[76:79], v[228:231], a[240:243], a[76:79]\n"
"v_add_f32 v107, v107, v147\n"
"v_mfma_f32_16x16x32_bf16 a[76:79], a[172:175], a[248:251], a[76:79]\n"
"s_waitcnt vmcnt(10)\n"
"v_mfma_f32_16x16x32_bf16 a[0:3], v[60:63], v[76:79], a[0:3]\n"
"v_cvt_pk_bf16_f32 v124, v104, v105\n"
"v_cvt_pk_bf16_f32 v126, v106, v107\n"
"v_mov_b32 v125, v22\n"
"v_mfma_f32_16x16x32_bf16 a[0:3], v[188:191], a[252:255], a[0:3]\n"
"v_mov_b32 v127, v22\n"
"v_mfma_f32_16x16x32_bf16 a[4:7], v[60:63], v[176:179], a[4:7]\n"
"buffer_store_dwordx4 v[124:127], v1, s[4:7], s69 offen offset:2048 sc1\n"
"s_add_u32 m0, s78, 0x10000\n"
"s_waitcnt lgkmcnt(3)\n"
"v_mfma_f32_16x16x32_bf16 a[4:7], v[188:191], v[44:47], a[4:7]\n"
"v_add_f32 v108, v108, v128\n"
"v_add_f32 v148, v148, v168\n"
"v_add_f32 v109, v109, v129\n"
"v_mfma_f32_16x16x32_bf16 a[8:11], v[60:63], v[180:183], a[8:11]\n"
"v_add_f32 v149, v149, v169\n"
"v_add_f32 v110, v110, v130\n"
"v_add_f32 v150, v150, v170\n"
"v_mfma_f32_16x16x32_bf16 a[8:11], v[188:191], v[48:51], a[8:11]\n"
"buffer_load_dwordx4 v1, s[4:7], s59 offen sc1 lds\n"
"v_add_f32 v111, v111, v131\n"
"v_add_f32 v151, v151, v171\n"
"v_mfma_f32_16x16x32_bf16 a[12:15], v[60:63], v[184:187], a[12:15]\n"
"v_add_f32 v108, v108, v148\n"
"v_add_f32 v109, v109, v149\n"
"v_add_f32 v110, v110, v150\n"
"v_mfma_f32_16x16x32_bf16 a[12:15], v[188:191], v[52:55], a[12:15]\n"
"v_add_f32 v111, v111, v151\n"
"v_cvt_pk_bf16_f32 v128, v108, v109\n"
"v_cvt_pk_bf16_f32 v130, v110, v111\n"
"v_mfma_f32_16x16x32_bf16 a[16:19], v[80:83], v[76:79], a[16:19]\n"
"buffer_load_dwordx4 v1, s[4:7], s59 offen offset:1024 sc1 lds\n"
"v_mov_b32 v129, v22\n"
"v_mov_b32 v131, v22\n"
"v_mfma_f32_16x16x32_bf16 a[16:19], a[176:179], a[252:255], a[16:19]\n"
"buffer_store_dwordx4 v[128:131], v1, s[4:7], s69 offen offset:3072 sc1\n"
"s_waitcnt lgkmcnt(0)\n"
"v_add_f32 v112, v112, v132\n"
"v_mfma_f32_16x16x32_bf16 a[20:23], v[80:83], v[176:179], a[20:23]\n"
"v_add_f32 v152, v152, v172\n"
"v_add_f32 v113, v113, v133\n"
"v_add_f32 v153, v153, v173\n"
"v_mfma_f32_16x16x32_bf16 a[20:23], a[176:179], v[44:47], a[20:23]\n"
"buffer_load_dwordx4 v1, s[4:7], s59 offen offset:2048 sc1 lds\n"
"v_add_f32 v114, v114, v134\n"
"v_add_f32 v154, v154, v174\n"
"v_mfma_f32_16x16x32_bf16 a[24:27], v[80:83], v[180:183], a[24:27]\n"
"v_add_f32 v115, v115, v135\n"
"v_add_f32 v155, v155, v175\n"
"v_add_f32 v112, v112, v152\n"
"v_mfma_f32_16x16x32_bf16 a[24:27], a[176:179], v[48:51], a[24:27]\n"
"v_add_f32 v113, v113, v153\n"
"v_add_f32 v114, v114, v154\n"
"v_add_f32 v115, v115, v155\n"
"v_mfma_f32_16x16x32_bf16 a[28:31], v[80:83], v[184:187], a[28:31]\n"
"v_cvt_pk_bf16_f32 v132, v112, v113\n"
"v_cvt_pk_bf16_f32 v134, v114, v115\n"
"v_mov_b32 v133, v22\n"
"v_mfma_f32_16x16x32_bf16 a[28:31], a[176:179], v[52:55], a[28:31]\n"
"buffer_load_dwordx4 v1, s[4:7], s59 offen offset:3072 sc1 lds\n"
"s_add_u32 s59, s59, 0x8000\n"
"v_mov_b32 v135, v22\n"
"v_mfma_f32_16x16x32_bf16 a[32:35], v[84:87], v[76:79], a[32:35]\n"
"buffer_store_dwordx4 v[132:135], v13, s[4:7], s69 offen sc1\n"
"v_mfma_f32_16x16x32_bf16 a[32:35], a[180:183], a[252:255], a[32:35]\n"
"v_mfma_f32_16x16x32_bf16 a[36:39], v[84:87], v[176:179], a[36:39]\n"
"buffer_load_dwordx4 a[80:83], v1, s[4:7], s60 offen sc1\n"
"v_mfma_f32_16x16x32_bf16 a[36:39], a[180:183], v[44:47], a[36:39]\n"
"v_mfma_f32_16x16x32_bf16 a[40:43], v[84:87], v[180:183], a[40:43]\n"
"v_mfma_f32_16x16x32_bf16 a[40:43], a[180:183], v[48:51], a[40:43]\n"
"buffer_load_dwordx4 a[84:87], v1, s[4:7], s60 offen offset:1024 sc1\n"
"v_mfma_f32_16x16x32_bf16 a[44:47], v[84:87], v[184:187], a[44:47]\n"
"v_mfma_f32_16x16x32_bf16 a[44:47], a[180:183], v[52:55], a[44:47]\n"
"v_mfma_f32_16x16x32_bf16 a[48:51], v[88:91], v[76:79], a[48:51]\n"
"buffer_load_dwordx4 a[88:91], v1, s[4:7], s60 offen offset:2048 sc1\n"
"v_mfma_f32_16x16x32_bf16 a[48:51], a[184:187], a[252:255], a[48:51]\n"
"v_mfma_f32_16x16x32_bf16 a[52:55], v[88:91], v[176:179], a[52:55]\n"
"v_mfma_f32_16x16x32_bf16 a[52:55], a[184:187], v[44:47], a[52:55]\n"
"v_mfma_f32_16x16x32_bf16 a[56:59], v[88:91], v[180:183], a[56:59]\n"
"buffer_load_dwordx4 a[92:95], v1, s[4:7], s60 offen offset:3072 sc1\n"
"s_add_u32 s60, s60, 0x1000\n"
"v_mfma_f32_16x16x32_bf16 a[56:59], a[184:187], v[48:51], a[56:59]\n"
"v_mfma_f32_16x16x32_bf16 a[60:63], v[88:91], v[184:187], a[60:63]\n"
"v_mfma_f32_16x16x32_bf16 a[60:63], a[184:187], v[52:55], a[60:63]\n"
"buffer_load_dwordx4 a[96:99], v1, s[4:7], s60 offen sc1\n"
"v_mfma_f32_16x16x32_bf16 a[64:67], v[64:67], v[76:79], a[64:67]\n"
"v_mfma_f32_16x16x32_bf16 a[64:67], a[188:191], a[252:255], a[64:67]\n"
"v_mfma_f32_16x16x32_bf16 a[68:71], v[64:67], v[176:179], a[68:71]\n"
"buffer_load_dwordx4 a[100:103], v1, s[4:7], s60 offen offset:1024 sc1\n"
"v_mfma_f32_16x16x32_bf16 a[68:71], a[188:191], v[44:47], a[68:71]\n"
"v_mfma_f32_16x16x32_bf16 a[72:75], v[64:67], v[180:183], a[72:75]\n"
"v_mfma_f32_16x16x32_bf16 a[72:75], a[188:191], v[48:51], a[72:75]\n"
"v_mfma_f32_16x16x32_bf16 a[76:79], v[64:67], v[184:187], a[76:79]\n"
"v_mfma_f32_16x16x32_bf16 a[76:79], a[188:191], v[52:55], a[76:79]\n"
"s_barrier\n"
"s_waitcnt vmcnt(14)\n"
"v_mfma_f32_16x16x32_bf16 a[4:7], a[192:195], v[252:255], a[4:7]\n"
"s_nop 0\n"
"ds_read_b128 v[56:59], v16\n"
"ds_read_b128 v[60:63], v16 offset:64\n"
"v_mfma_f32_16x16x32_bf16 a[4:7], a[212:215], a[160:163], a[4:7]\n"
"ds_read_b128 v[64:67], v16 offset:128\n"
"ds_read_b128 v[68:71], v16 offset:192\n"
"ds_read_b128 v[72:75], v16 offset:256\n"
"v_mfma_f32_16x16x32_bf16 a[20:23], a[196:199], v[252:255], a[20:23]\n"
"ds_read_b128 v[76:79], v16 offset:320\n"
"ds_read_b128 v[80:83], v16 offset:384\n"
"ds_read_b128 v[84:87], v16 offset:448\n"
"v_mfma_f32_16x16x32_bf16 a[20:23], a[216:219], a[160:163], a[20:23]\n"
"ds_read_b128 v[88:91], v16 offset:512\n"
"ds_read_b128 v[92:95], v16 offset:576\n"
"v_mfma_f32_16x16x32_bf16 a[36:39], a[200:203], v[252:255], a[36:39]\n"
"v_mfma_f32_16x16x32_bf16 a[36:39], a[220:223], a[160:163], a[36:39]\n"
"v_mfma_f32_16x16x32_bf16 a[52:55], a[204:207], v[252:255], a[52:55]\n"
"v_mfma_f32_16x16x32_bf16 a[52:55], a[224:227], a[160:163], a[52:55]\n"
"v_mfma_f32_16x16x32_bf16 a[68:71], a[208:211], v[252:255], a[68:71]\n"
"v_mfma_f32_16x16x32_bf16 a[68:71], a[228:231], a[160:163], a[68:71]\n"
"ds_write_b128 v7, a[4:7]\n"
"v_mfma_f32_16x16x32_bf16 a[8:11], a[192:195], v[196:199], a[8:11]\n"
"v_mfma_f32_16x16x32_bf16 a[8:11], a[212:215], v[212:215], a[8:11]\n"
"ds_write_b128 v7, a[20:23] offset:1024\n"
"v_mfma_f32_16x16x32_bf16 a[24:27], a[196:199], v[196:199], a[24:27]\n"
"buffer_load_dwordx4 a[104:107], v1, s[4:7], s60 offen offset:2048 sc1\n"
"v_mfma_f32_16x16x32_bf16 a[24:27], a[216:219], v[212:215], a[24:27]\n"
"ds_write_b128 v7, a[36:39] offset:2048\n"
"v_mfma_f32_16x16x32_bf16 a[40:43], a[200:203], v[196:199], a[40:43]\n"
"buffer_load_dwordx4 a[108:111], v1, s[4:7], s60 offen offset:3072 sc1\n"
"s_add_u32 s60, s60, 0x3000\n"
"v_mfma_f32_16x16x32_bf16 a[40:43], a[220:223], v[212:215], a[40:43]\n"
"ds_write_b128 v7, a[52:55] offset:3072\n"
"v_mfma_f32_16x16x32_bf16 a[56:59], a[204:207], v[196:199], a[56:59]\n"
"v_mfma_f32_16x16x32_bf16 a[56:59], a[224:227], v[212:215], a[56:59]\n"
"ds_write_b128 v7, a[68:71] offset:4096\n"
"buffer_load_dwordx4 a[112:115], v1, s[4:7], s60 offen sc1\n"
"v_mfma_f32_16x16x32_bf16 a[72:75], a[208:211], v[196:199], a[72:75]\n"
"v_mfma_f32_16x16x32_bf16 a[72:75], a[228:231], v[212:215], a[72:75]\n"
"ds_write_b128 v8, a[8:11] offset:5120\n"
"v_mfma_f32_16x16x32_bf16 a[12:15], a[192:195], v[200:203], a[12:15]\n"
"buffer_load_dwordx4 a[116:119], v1, s[4:7], s60 offen offset:1024 sc1\n"
"v_mfma_f32_16x16x32_bf16 a[12:15], a[212:215], v[216:219], a[12:15]\n"
"ds_write_b128 v8, a[24:27] offset:6144\n"
"v_mfma_f32_16x16x32_bf16 a[28:31], a[196:199], v[200:203], a[28:31]\n"
"v_mfma_f32_16x16x32_bf16 a[28:31], a[216:219], v[216:219], a[28:31]\n"
"ds_write_b128 v8, a[40:43] offset:7168\n"
"buffer_load_dwordx4 v[176:179], v1, s[4:7], s70 offen sc1\n"
"v_mfma_f32_16x16x32_bf16 a[44:47], a[200:203], v[200:203], a[44:47]\n"
"v_mfma_f32_16x16x32_bf16 a[44:47], a[220:223], v[216:219], a[44:47]\n"
"ds_write_b128 v8, a[56:59] offset:8192\n"
"v_mfma_f32_16x16x32_bf16 a[60:63], a[204:207], v[200:203], a[60:63]\n"
"buffer_load_dwordx4 v[180:183], v1, s[4:7], s70 offen offset:1024 sc1\n"
"v_mfma_f32_16x16x32_bf16 a[60:63], a[224:227], v[216:219], a[60:63]\n"
"ds_write_b128 v8, a[72:75] offset:9216\n"
"v_mfma_f32_16x16x32_bf16 a[76:79], a[208:211], v[200:203], a[76:79]\n"
"v_mfma_f32_16x16x32_bf16 a[76:79], a[228:231], v[216:219], a[76:79]\n"
"ds_write_b128 v9, a[12:15] offset:10240\n"
"buffer_load_dwordx4 v[184:187], v1, s[4:7], s70 offen offset:2048 sc1\n"
"v_mfma_f32_16x16x32_bf16 a[0:3], a[192:195], v[248:251], a[0:3]\n"
"v_mfma_f32_16x16x32_bf16 a[0:3], a[212:215], v[204:207], a[0:3]\n"
"ds_write_b128 v9, a[28:31] offset:11264\n"
"v_mfma_f32_16x16x32_bf16 a[16:19], a[196:199], v[248:251], a[16:19]\n"
"buffer_load_dwordx4 v[188:191], v1, s[4:7], s70 offen offset:3072 sc1\n"
"v_mfma_f32_16x16x32_bf16 a[16:19], a[216:219], v[204:207], a[16:19]\n"
"ds_write_b128 v9, a[44:47] offset:12288\n"
"v_mfma_f32_16x16x32_bf16 a[32:35], a[200:203], v[248:251], a[32:35]\n"
"v_mfma_f32_16x16x32_bf16 a[32:35], a[220:223], v[204:207], a[32:35]\n"
"ds_write_b128 v9, a[60:63] offset:13312\n"
"buffer_load_dwordx4 v[192:195], v13, s[4:7], s70 offen sc1\n"
"v_mfma_f32_16x16x32_bf16 a[48:51], a[204:207], v[248:251], a[48:51]\n"
"v_mfma_f32_16x16x32_bf16 a[48:51], a[224:227], v[204:207], a[48:51]\n"
"ds_write_b128 v9, a[76:79] offset:14336\n"
"v_mfma_f32_16x16x32_bf16 a[64:67], a[208:211], v[248:251], a[64:67]\n"
"v_mfma_f32_16x16x32_bf16 a[64:67], a[228:231], v[204:207], a[64:67]\n"
".Ladaln_block_T_O:\n"
"s_waitcnt lgkmcnt(0)\n"
"s_barrier\n"
"ds_read_b128 v[116:119], v6\n"
"ds_read_b128 v[136:139], v6 offset:5120\n"
"ds_read_b128 v[156:159], v6 offset:10240\n"
"ds_read_b128 v[120:123], v6 offset:1024\n"
"ds_read_b128 v[140:143], v6 offset:6144\n"
"ds_read_b128 v[160:163], v6 offset:11264\n"
"ds_read_b128 v[124:127], v6 offset:2048\n"
"ds_read_b128 v[144:147], v6 offset:7168\n"
"ds_read_b128 v[164:167], v6 offset:12288\n"
"ds_read_b128 v[128:131], v6 offset:3072\n"
"ds_read_b128 v[148:151], v6 offset:8192\n"
"ds_read_b128 v[168:171], v6 offset:13312\n"
"ds_read_b128 v[132:135], v6 offset:4096\n"
"ds_read_b128 v[152:155], v6 offset:9216\n"
"ds_read_b128 v[172:175], v6 offset:14336\n"
"buffer_load_dwordx4 a[120:123], v1, s[4:7], s60 offen offset:2048 sc1\n"
"buffer_load_dwordx4 a[124:127], v1, s[4:7], s60 offen offset:3072 sc1\n"
"s_add_u32 s60, s60, 0x1000\n"
"buffer_load_dwordx4 a[128:131], v1, s[4:7], s60 offen sc1\n"
"buffer_load_dwordx4 a[132:135], v1, s[4:7], s60 offen offset:1024 sc1\n"
"buffer_load_dwordx4 a[136:139], v1, s[4:7], s60 offen offset:2048 sc1\n"
"buffer_load_dwordx4 a[140:143], v1, s[4:7], s60 offen offset:3072 sc1\n"
"s_add_u32 s60, s60, 0x3000\n"
"buffer_load_dwordx4 a[144:147], v1, s[4:7], s60 offen sc1\n"
"buffer_load_dwordx4 a[148:151], v1, s[4:7], s60 offen offset:1024 sc1\n"
"v_accvgpr_read_b32 v96, a0\n"
"v_accvgpr_read_b32 v97, a1\n"
"v_accvgpr_read_b32 v98, a2\n"
"v_accvgpr_read_b32 v99, a3\n"
"v_accvgpr_read_b32 v100, a16\n"
"v_accvgpr_read_b32 v101, a17\n"
"v_accvgpr_read_b32 v102, a18\n"
"v_accvgpr_read_b32 v103, a19\n"
"v_accvgpr_read_b32 v104, a32\n"
"v_accvgpr_read_b32 v105, a33\n"
"v_accvgpr_read_b32 v106, a34\n"
"v_accvgpr_read_b32 v107, a35\n"
"v_accvgpr_read_b32 v108, a48\n"
"v_accvgpr_read_b32 v109, a49\n"
"v_accvgpr_read_b32 v110, a50\n"
"v_accvgpr_read_b32 v111, a51\n"
"v_accvgpr_read_b32 v112, a64\n"
"v_accvgpr_read_b32 v113, a65\n"
"v_accvgpr_read_b32 v114, a66\n"
"v_accvgpr_read_b32 v115, a67\n"
"s_mov_b32 s83, 0\n"
"s_waitcnt vmcnt(8)\n"
".Ladaln_block_P_xch:\n"
"v_xor_b32 v177, s82, v177\n"
"v_xor_b32 v179, s82, v179\n"
"v_xor_b32 v181, s82, v181\n"
"v_xor_b32 v183, s82, v183\n"
"v_xor_b32 v185, s82, v185\n"
"v_xor_b32 v187, s82, v187\n"
"v_xor_b32 v189, s82, v189\n"
"v_xor_b32 v191, s82, v191\n"
"v_xor_b32 v193, s82, v193\n"
"v_xor_b32 v195, s82, v195\n"
"v_or3_b32 v177, v177, v179, v181\n"
"v_or3_b32 v183, v183, v185, v187\n"
"v_or3_b32 v189, v189, v191, v193\n"
"v_or3_b32 v177, v177, v183, v189\n"
"v_or_b32 v177, v177, v195\n"
"v_cmp_ne_u32 vcc, 0, v177\n"
"s_nop 0\n"
"s_cbranch_vccz .Ladaln_block_P_xch_R\n"
"s_sleep 0\n"
"buffer_load_dwordx4 v[176:179], v1, s[4:7], s70 offen sc1\n"
"buffer_load_dwordx4 v[180:183], v1, s[4:7], s70 offen offset:1024 sc1\n"
"buffer_load_dwordx4 v[184:187], v1, s[4:7], s70 offen offset:2048 sc1\n"
"buffer_load_dwordx4 v[188:191], v1, s[4:7], s70 offen offset:3072 sc1\n"
"buffer_load_dwordx4 v[192:195], v13, s[4:7], s70 offen sc1\n"
"s_add_u32 s83, s83, 1\n"
"s_waitcnt vmcnt(0)\n"
"s_cmp_lt_u32 s83, 0x20000\n"
"s_cbranch_scc1 .Ladaln_block_P_xch\n"
".Ladaln_block_P_xch_R:\n"
"v_and_b32 v177, 0xffff0000, v176\n"
"s_add_u32 m0, s78, 0x14000\n"
"s_nop 0\n"
"buffer_load_dwordx4 v1, s[4:7], s59 offen sc1 lds\n"
"v_lshlrev_b32 v176, 16, v176\n"
"v_and_b32 v179, 0xffff0000, v178\n"
"v_lshlrev_b32 v178, 16, v178\n"
"v_and_b32 v181, 0xffff0000, v180\n"
"v_lshlrev_b32 v180, 16, v180\n"
"v_and_b32 v183, 0xffff0000, v182\n"
"v_lshlrev_b32 v182, 16, v182\n"
"v_and_b32 v185, 0xffff0000, v184\n"
"v_lshlrev_b32 v184, 16, v184\n"
"v_and_b32 v187, 0xffff0000, v186\n"
"v_lshlrev_b32 v186, 16, v186\n"
"v_and_b32 v189, 0xffff0000, v188\n"
"v_lshlrev_b32 v188, 16, v188\n"
"v_and_b32 v191, 0xffff0000, v190\n"
"v_lshlrev_b32 v190, 16, v190\n"
"v_and_b32 v193, 0xffff0000, v192\n"
"v_lshlrev_b32 v192, 16, v192\n"
"buffer_load_dwordx4 v1, s[4:7], s59 offen offset:1024 sc1 lds\n"
"v_and_b32 v195, 0xffff0000, v194\n"
"v_lshlrev_b32 v194, 16, v194\n"
"v_fmac_f32 v24, v56, v176\n"
"v_fmac_f32 v25, v57, v177\n"
"v_fmac_f32 v26, v58, v178\n"
"v_fmac_f32 v27, v59, v179\n"
"v_fmac_f32 v28, v60, v180\n"
"v_fmac_f32 v29, v61, v181\n"
"v_fmac_f32 v30, v62, v182\n"
"v_fmac_f32 v31, v63, v183\n"
"v_fmac_f32 v32, v64, v184\n"
"v_fmac_f32 v33, v65, v185\n"
"v_fmac_f32 v34, v66, v186\n"
"v_fmac_f32 v35, v67, v187\n"
"v_fmac_f32 v36, v68, v188\n"
"v_fmac_f32 v37, v69, v189\n"
"v_fmac_f32 v38, v70, v190\n"
"v_fmac_f32 v39, v71, v191\n"
"v_fmac_f32 v40, v72, v192\n"
"buffer_load_dwordx4 v1, s[4:7], s59 offen offset:2048 sc1 lds\n"
"v_fmac_f32 v41, v73, v193\n"
"v_fmac_f32 v42, v74, v194\n"
"v_fmac_f32 v43, v75, v195\n"
"s_nop 0\n"
"s_waitcnt lgkmcnt(12)\n"
"v_pk_add_f32 v[96:97], v[96:97], v[116:117]\n"
"v_pk_add_f32 v[136:137], v[136:137], v[156:157]\n"
"v_pk_add_f32 v[98:99], v[98:99], v[118:119]\n"
"v_pk_add_f32 v[138:139], v[138:139], v[158:159]\n"
"v_pk_add_f32 v[96:97], v[96:97], v[136:137]\n"
"v_pk_add_f32 v[98:99], v[98:99], v[138:139]\n"
"v_fmac_f32 v24, v56, v96\n"
"v_fmac_f32 v25, v57, v97\n"
"v_cvt_pk_bf16_f32 v96, v24, v25\n"
"v_lshlrev_b32 v24, 16, v96\n"
"v_and_b32 v25, 0xffff0000, v96\n"
"v_mul_f32 v96, v24, v76\n"
"v_mul_f32 v97, v25, v77\n"
"v_cvt_pk_bf16_f32 v176, v96, v97\n"
"buffer_load_dwordx4 v1, s[4:7], s59 offen offset:3072 sc1 lds\n"
"s_add_u32 s59, s59, 0x8000\n"
"v_fmac_f32 v26, v58, v98\n"
"v_fmac_f32 v27, v59, v99\n"
"v_cvt_pk_bf16_f32 v98, v26, v27\n"
"v_lshlrev_b32 v26, 16, v98\n"
"v_and_b32 v27, 0xffff0000, v98\n"
"v_mul_f32 v98, v26, v78\n"
"v_mul_f32 v99, v27, v79\n"
"v_cvt_pk_bf16_f32 v177, v98, v99\n"
"s_waitcnt lgkmcnt(9)\n"
"v_pk_add_f32 v[100:101], v[100:101], v[120:121]\n"
"v_pk_add_f32 v[140:141], v[140:141], v[160:161]\n"
"v_pk_add_f32 v[102:103], v[102:103], v[122:123]\n"
"v_pk_add_f32 v[142:143], v[142:143], v[162:163]\n"
"v_pk_add_f32 v[100:101], v[100:101], v[140:141]\n"
"v_pk_add_f32 v[102:103], v[102:103], v[142:143]\n"
"v_fmac_f32 v28, v60, v100\n"
"v_fmac_f32 v29, v61, v101\n"
"v_cvt_pk_bf16_f32 v100, v28, v29\n"
"v_lshlrev_b32 v28, 16, v100\n"
"v_and_b32 v29, 0xffff0000, v100\n"
"v_mul_f32 v100, v28, v80\n"
"v_mul_f32 v101, v29, v81\n"
"v_cvt_pk_bf16_f32 v178, v100, v101\n"
"v_fmac_f32 v30, v62, v102\n"
"v_fmac_f32 v31, v63, v103\n"
"v_cvt_pk_bf16_f32 v102, v30, v31\n"
"v_lshlrev_b32 v30, 16, v102\n"
"v_and_b32 v31, 0xffff0000, v102\n"
"v_mul_f32 v102, v30, v82\n"
"v_mul_f32 v103, v31, v83\n"
"v_cvt_pk_bf16_f32 v179, v102, v103\n"
"s_nop 0\n"
"buffer_store_dwordx4 v[176:179], v1, s[4:7], s61 offen sc1\n"
"s_waitcnt lgkmcnt(6)\n"
"v_pk_add_f32 v[104:105], v[104:105], v[124:125]\n"
"v_pk_add_f32 v[144:145], v[144:145], v[164:165]\n"
"v_pk_add_f32 v[106:107], v[106:107], v[126:127]\n"
"v_pk_add_f32 v[146:147], v[146:147], v[166:167]\n"
"v_pk_add_f32 v[104:105], v[104:105], v[144:145]\n"
"v_pk_add_f32 v[106:107], v[106:107], v[146:147]\n"
"v_fmac_f32 v32, v64, v104\n"
"v_fmac_f32 v33, v65, v105\n"
"v_cvt_pk_bf16_f32 v104, v32, v33\n"
"v_lshlrev_b32 v32, 16, v104\n"
"v_and_b32 v33, 0xffff0000, v104\n"
"v_mul_f32 v104, v32, v84\n"
"v_mul_f32 v105, v33, v85\n"
"v_cvt_pk_bf16_f32 v180, v104, v105\n"
"v_fmac_f32 v34, v66, v106\n"
"v_fmac_f32 v35, v67, v107\n"
"v_cvt_pk_bf16_f32 v106, v34, v35\n"
"v_lshlrev_b32 v34, 16, v106\n"
"v_and_b32 v35, 0xffff0000, v106\n"
"v_mul_f32 v106, v34, v86\n"
"v_mul_f32 v107, v35, v87\n"
"v_cvt_pk_bf16_f32 v181, v106, v107\n"
"s_waitcnt lgkmcnt(3)\n"
"v_pk_add_f32 v[108:109], v[108:109], v[128:129]\n"
"v_pk_add_f32 v[148:149], v[148:149], v[168:169]\n"
"v_pk_add_f32 v[110:111], v[110:111], v[130:131]\n"
"v_pk_add_f32 v[150:151], v[150:151], v[170:171]\n"
"v_pk_add_f32 v[108:109], v[108:109], v[148:149]\n"
"v_pk_add_f32 v[110:111], v[110:111], v[150:151]\n"
"v_fmac_f32 v36, v68, v108\n"
"v_fmac_f32 v37, v69, v109\n"
"v_cvt_pk_bf16_f32 v108, v36, v37\n"
"v_lshlrev_b32 v36, 16, v108\n"
"v_and_b32 v37, 0xffff0000, v108\n"
"v_mul_f32 v108, v36, v88\n"
"v_mul_f32 v109, v37, v89\n"
"v_cvt_pk_bf16_f32 v182, v108, v109\n"
"v_fmac_f32 v38, v70, v110\n"
"v_fmac_f32 v39, v71, v111\n"
"v_cvt_pk_bf16_f32 v110, v38, v39\n"
"v_lshlrev_b32 v38, 16, v110\n"
"v_and_b32 v39, 0xffff0000, v110\n"
"v_mul_f32 v110, v38, v90\n"
"v_mul_f32 v111, v39, v91\n"
"v_cvt_pk_bf16_f32 v183, v110, v111\n"
"s_nop 0\n"
"buffer_store_dwordx4 v[180:183], v1, s[4:7], s71 offen sc1\n"
"s_waitcnt lgkmcnt(0)\n"
"v_pk_add_f32 v[112:113], v[112:113], v[132:133]\n"
"v_pk_add_f32 v[152:153], v[152:153], v[172:173]\n"
"v_pk_add_f32 v[114:115], v[114:115], v[134:135]\n"
"v_pk_add_f32 v[154:155], v[154:155], v[174:175]\n"
"v_pk_add_f32 v[112:113], v[112:113], v[152:153]\n"
"v_pk_add_f32 v[114:115], v[114:115], v[154:155]\n"
"v_fmac_f32 v40, v72, v112\n"
"v_fmac_f32 v41, v73, v113\n"
"v_cvt_pk_bf16_f32 v112, v40, v41\n"
"v_lshlrev_b32 v40, 16, v112\n"
"v_and_b32 v41, 0xffff0000, v112\n"
"v_mul_f32 v112, v40, v92\n"
"v_mul_f32 v113, v41, v93\n"
"v_cvt_pk_bf16_f32 v184, v112, v113\n"
"v_fmac_f32 v42, v74, v114\n"
"v_fmac_f32 v43, v75, v115\n"
"v_cvt_pk_bf16_f32 v114, v42, v43\n"
"v_lshlrev_b32 v42, 16, v114\n"
"v_and_b32 v43, 0xffff0000, v114\n"
"v_mul_f32 v114, v42, v94\n"
"v_mul_f32 v115, v43, v95\n"
"v_cvt_pk_bf16_f32 v185, v114, v115\n"
"s_nop 0\n"
"buffer_store_dwordx2 v[184:185], v19, s[4:7], s66 offen sc1\n"
"v_add_f32 v96, v24, v25\n"
"v_mul_f32 v98, v24, v24\n"
"v_fmac_f32 v98, v25, v25\n"
"v_add_f32 v96, v96, v26\n"
"v_fmac_f32 v98, v26, v26\n"
"v_add_f32 v96, v96, v27\n"
"v_fmac_f32 v98, v27, v27\n"
"v_add_f32 v96, v96, v28\n"
"v_fmac_f32 v98, v28, v28\n"
"v_add_f32 v96, v96, v29\n"
"v_fmac_f32 v98, v29, v29\n"
"v_add_f32 v96, v96, v30\n"
"v_fmac_f32 v98, v30, v30\n"
"v_add_f32 v96, v96, v31\n"
"v_fmac_f32 v98, v31, v31\n"
"v_add_f32 v96, v96, v32\n"
"v_fmac_f32 v98, v32, v32\n"
"v_add_f32 v96, v96, v33\n"
"v_fmac_f32 v98, v33, v33\n"
"v_add_f32 v96, v96, v34\n"
"v_fmac_f32 v98, v34, v34\n"
"v_add_f32 v96, v96, v35\n"
"v_fmac_f32 v98, v35, v35\n"
"v_add_f32 v96, v96, v36\n"
"v_fmac_f32 v98, v36, v36\n"
"v_add_f32 v96, v96, v37\n"
"v_fmac_f32 v98, v37, v37\n"
"v_add_f32 v96, v96, v38\n"
"v_fmac_f32 v98, v38, v38\n"
"v_add_f32 v96, v96, v39\n"
"v_fmac_f32 v98, v39, v39\n"
"v_add_f32 v96, v96, v40\n"
"v_fmac_f32 v98, v40, v40\n"
"v_add_f32 v96, v96, v41\n"
"v_fmac_f32 v98, v41, v41\n"
"v_add_f32 v96, v96, v42\n"
"v_fmac_f32 v98, v42, v42\n"
"v_add_f32 v96, v96, v43\n"
"v_fmac_f32 v98, v43, v43\n"
"v_mov_b32 v97, v96\n"
"v_mov_b32 v99, v98\n"
"s_nop 0\n"
"v_permlane16_swap_b32 v96, v97\n"
"v_permlane16_swap_b32 v98, v99\n"
"v_add_f32 v96, v96, v97\n"
"v_add_f32 v98, v98, v99\n"
"v_mov_b32 v97, v96\n"
"v_mov_b32 v99, v98\n"
"s_nop 0\n"
"v_permlane32_swap_b32 v96, v97\n"
"v_permlane32_swap_b32 v98, v99\n"
"v_add_f32 v96, v96, v97\n"
"v_add_f32 v98, v98, v99\n"
"v_mov_b32 v97, v22\n"
"v_mov_b32 v99, v22\n"
"s_nop 0\n"
"buffer_store_dwordx4 v[96:99], v12, s[4:7], s67 offen sc1\n"
"s_waitcnt vmcnt(1)\n"
"s_barrier\n"
"buffer_store_dword v22, v23, s[4:7], s72 offen sc1\n"
"s_lshl_b32 s56, s3, 10\n"
"s_add_u32 m0, s56, 0x26000\n"
"s_add_u32 s56, s56, 0x10f00\n"
"buffer_load_dwordx4 v1, s[4:7], s56 offen sc1 lds\n"
"s_add_u32 m0, s78, 0x18000\n"
"s_nop 0\n"
"buffer_load_dwordx4 v1, s[4:7], s59 offen sc1 lds\n"
"buffer_load_dwordx4 v1, s[4:7], s59 offen offset:1024 sc1 lds\n"
"buffer_load_dwordx4 v1, s[4:7], s59 offen offset:2048 sc1 lds\n"
"buffer_load_dwordx4 v1, s[4:7], s59 offen offset:3072 sc1 lds\n"
"s_add_u32 s59, s59, 0x8000\n"
"s_add_u32 m0, s78, 0x1c000\n"
"s_nop 0\n"
"buffer_load_dwordx4 v1, s[4:7], s59 offen sc1 lds\n"
"buffer_load_dwordx4 v1, s[4:7], s59 offen offset:1024 sc1 lds\n"
"buffer_load_dwordx4 v1, s[4:7], s59 offen offset:2048 sc1 lds\n"
"buffer_load_dwordx4 v1, s[4:7], s59 offen offset:3072 sc1 lds\n"
"s_add_u32 s59, s59, 0x8000\n"
"s_add_u32 m0, s78, 0x20000\n"
"s_cmp_eq_u32 s3, 3\n"
"s_cselect_b32 s59, 0x40000000, s59\n"
"buffer_load_dwordx4 v1, s[4:7], s59 offen sc1 lds\n"
"buffer_load_dwordx4 v1, s[4:7], s59 offen offset:1024 sc1 lds\n"
"buffer_load_dwordx4 v1, s[4:7], s59 offen offset:2048 sc1 lds\n"
"buffer_load_dwordx4 v1, s[4:7], s59 offen offset:3072 sc1 lds\n"
"s_add_u32 s59, s59, 0x8000\n"
"v_bfe_u32 v16, v0, 3, 1\n"
"v_mul_u32_u24 v16, 0x500, v16\n"
"v_sub_u32 v16, v15, v16\n"
"v_add_u32 v16, 0x26500, v16\n"
"s_lshl_b32 s58, s52, 11\n"
"s_and_b32 s56, s58, 0x2000\n"
"s_add_u32 s58, s58, s56\n"
"s_add_u32 s58, s58, 0xd8000\n"
"s_xor_b32 s58, s58, s101\n"
"v_and_b32 v18, 7, v0\n"
"s_and_b32 s65, s3, 2\n"
"v_lshl_add_u32 v18, s65, 2, v18\n"
"v_lshlrev_b32 v18, 7, v18\n"
"s_lshl2_add_u32 s67, s52, s3\n"
"s_lshl3_add_u32 s57, s67, s3\n"
"s_lshl_b32 s57, s57, 14\n"
"s_add_u32 s57, s57, 0x400000\n"
"s_lshl_b32 s67, s67, 12\n"
"s_lshr_b32 s56, s57, 8\n"
"s_and_b32 s56, s56, 0x2000\n"
"s_xor_b32 s57, s57, s56\n"
"s_xor_b32 s57, s57, s101\n"
"s_andn2_b32 s56, s67, 0x1fff\n"
"s_add_u32 s67, s67, s56\n"
"s_add_u32 s67, s67, 0x100000\n"
"s_lshr_b32 s56, s67, 8\n"
"s_and_b32 s56, s56, 0x2000\n"
"s_xor_b32 s67, s67, s56\n"
"s_xor_b32 s67, s67, s101\n"
"v_mov_b32 v10, v1\n"
"v_add_u32 v11, 0xe000, v1\n"
"v_add_u32 v12, 0x1c000, v1\n"
"s_lshl_b32 s56, s3, 11\n"
"v_add_u32 v13, s56, v1\n"
"ds_read_b128 v[100:103], v10\n"
"ds_read_b128 v[104:107], v10 offset:7168\n"
"ds_read_b128 v[108:111], v10 offset:14336\n"
"ds_read_b128 v[112:115], v10 offset:21504\n"
"ds_read_b128 v[116:119], v10 offset:1024\n"
"ds_read_b128 v[120:123], v10 offset:8192\n"
"ds_read_b128 v[124:127], v10 offset:15360\n"
"ds_read_b128 v[128:131], v10 offset:22528\n"
"buffer_load_dwordx4 a[152:155], v1, s[4:7], s60 offen offset:2048 sc1\n"
"buffer_load_dwordx4 a[156:159], v1, s[4:7], s60 offen offset:3072 sc1\n"
"s_add_u32 s60, s60, 0x1000\n"
"buffer_load_dwordx4 a[160:163], v1, s[4:7], s60 offen sc1\n"
"buffer_load_dwordx4 a[164:167], v1, s[4:7], s60 offen offset:1024 sc1\n"
"buffer_load_dwordx4 a[168:171], v1, s[4:7], s60 offen offset:2048 sc1\n"
"buffer_load_dwordx4 a[172:175], v1, s[4:7], s60 offen offset:3072 sc1\n"
"s_add_u32 s60, s60, 0x3000\n"
"s_mov_b32 s83, 0\n"
".Ladaln_block_P_x1:\n"
"buffer_load_dword v20, v18, s[4:7], s58 offen sc1\n"
"s_add_u32 s83, s83, 1\n"
"s_waitcnt vmcnt(0)\n"
"v_cmp_ne_u32 vcc, s82, v20\n"
"s_cmp_lt_u32 s83, 0x20000\n"
"s_cbranch_vccz .Ladaln_block_P_x1_R\n"
"s_sleep 0\n"
"s_cbranch_scc1 .Ladaln_block_P_x1\n"
".Ladaln_block_P_x1_R:\n"
"buffer_load_dwordx4 a[0:3], v1, s[4:7], s57 offen sc1\n"
"buffer_load_dwordx4 a[4:7], v1, s[4:7], s57 offen offset:1024 sc1\n"
"buffer_load_dwordx4 a[8:11], v1, s[4:7], s57 offen offset:2048 sc1\n"
"buffer_load_dwordx4 a[12:15], v1, s[4:7], s57 offen offset:3072 sc1\n"
"s_add_u32 s57, s57, 0x1000\n"
"buffer_load_dwordx2 a[16:17], v19, s[4:7], s57 offen sc1\n"
"buffer_load_dwordx2 a[18:19], v19, s[4:7], s57 offen offset:512 sc1\n"
"buffer_load_dwordx2 a[20:21], v19, s[4:7], s57 offen offset:1024 sc1\n"
"buffer_load_dwordx2 a[22:23], v19, s[4:7], s57 offen offset:1536 sc1\n"
"buffer_load_dwordx4 a[24:27], v1, s[4:7], s57 offen offset:2048 sc1\n"
"buffer_load_dwordx4 a[28:31], v1, s[4:7], s57 offen offset:3072 sc1\n"
"s_add_u32 s57, s57, 0x3000\n"
"buffer_load_dwordx4 a[32:35], v1, s[4:7], s57 offen sc1\n"
"buffer_load_dwordx4 a[36:39], v1, s[4:7], s57 offen offset:1024 sc1\n"
"buffer_load_dwordx4 a[40:43], v1, s[4:7], s57 offen offset:2048 sc1\n"
"buffer_load_dwordx4 a[44:47], v1, s[4:7], s57 offen offset:3072 sc1\n"
"s_add_u32 s57, s57, 0x1000\n"
"s_waitcnt vmcnt(4) lgkmcnt(4)\n"
"v_mfma_f32_16x16x32_bf16 v[44:47], v[100:103], a[0:3], 0\n"
"ds_read_b128 v[132:135], v10 offset:2048\n"
"buffer_load_dwordx4 a[48:51], v1, s[4:7], s57 offen sc1\n"
"v_mfma_f32_16x16x32_bf16 v[44:47], v[104:107], a[8:11], v[44:47]\n"
"s_lshr_b32 s56, s3, 1\n"
"s_lshl1_add_u32 s56, s52, s56\n"
"v_mfma_f32_16x16x32_bf16 v[44:47], v[108:111], a[16:19], v[44:47]\n"
"ds_read_b128 v[136:139], v10 offset:9216\n"
"s_lshl_b32 s56, s56, 18\n"
"v_mfma_f32_16x16x32_bf16 v[44:47], v[112:115], a[24:27], v[44:47]\n"
"s_and_b32 s72, s3, 1\n"
"s_lshl_b32 s72, s72, 11\n"
"v_mfma_f32_16x16x32_bf16 v[48:51], v[100:103], a[4:7], 0\n"
"ds_read_b128 v[140:143], v10 offset:16384\n"
"buffer_load_dwordx4 a[52:55], v1, s[4:7], s57 offen offset:1024 sc1\n"
"v_mfma_f32_16x16x32_bf16 v[48:51], v[104:107], a[12:15], v[48:51]\n"
"s_add_u32 s56, s56, s72\n"
"s_lshr_b32 s72, s53, 2\n"
"v_mfma_f32_16x16x32_bf16 v[48:51], v[108:111], a[20:23], v[48:51]\n"
"ds_read_b128 v[144:147], v10 offset:23552\n"
"s_mul_i32 s72, s72, 12\n"
"v_mfma_f32_16x16x32_bf16 v[48:51], v[112:115], a[28:31], v[48:51]\n"
"s_and_b32 s61, s53, 3\n"
"s_waitcnt lgkmcnt(4)\n"
"v_mfma_f32_16x16x32_bf16 v[52:55], v[116:119], a[0:3], 0\n"
"ds_read_b128 v[100:103], v10 offset:3072\n"
"buffer_load_dwordx2 a[56:57], v19, s[4:7], s57 offen offset:2048 sc1\n"
"v_mfma_f32_16x16x32_bf16 v[52:55], v[120:123], a[8:11], v[52:55]\n"
"s_add_u32 s72, s72, s61\n"
"s_add_u32 s61, s61, s72\n"
"v_mfma_f32_16x16x32_bf16 v[52:55], v[124:127], a[16:19], v[52:55]\n"
"ds_read_b128 v[104:107], v10 offset:10240\n"
"buffer_load_dwordx2 a[58:59], v19, s[4:7], s57 offen offset:2560 sc1\n"
"v_mfma_f32_16x16x32_bf16 v[52:55], v[128:131], a[24:27], v[52:55]\n"
"s_lshl_b32 s61, s61, 12\n"
"s_add_u32 s61, s61, s56\n"
"v_mfma_f32_16x16x32_bf16 v[56:59], v[116:119], a[4:7], 0\n"
"ds_read_b128 v[108:111], v10 offset:17408\n"
"buffer_load_dwordx2 a[60:61], v19, s[4:7], s57 offen offset:3072 sc1\n"
"v_mfma_f32_16x16x32_bf16 v[56:59], v[120:123], a[12:15], v[56:59]\n"
"s_add_u32 s72, s72, 8\n"
"s_lshl_b32 s72, s72, 12\n"
"v_mfma_f32_16x16x32_bf16 v[56:59], v[124:127], a[20:23], v[56:59]\n"
"ds_read_b128 v[112:115], v10 offset:24576\n"
"s_add_u32 s66, s72, s56\n"
"v_mfma_f32_16x16x32_bf16 v[56:59], v[128:131], a[28:31], v[56:59]\n"
"s_lshr_b32 s72, s53, 1\n"
"s_waitcnt lgkmcnt(4)\n"
"v_mfma_f32_16x16x32_bf16 v[60:63], v[132:135], a[0:3], 0\n"
"ds_read_b128 v[116:119], v10 offset:4096\n"
"s_add_u32 s72, s72, 48\n"
"v_mfma_f32_16x16x32_bf16 v[60:63], v[136:139], a[8:11], v[60:63]\n"
"buffer_load_dwordx2 a[62:63], v19, s[4:7], s57 offen offset:3584 sc1\n"
"s_add_u32 s57, s57, 0x3000\n"
"v_mfma_f32_16x16x32_bf16 v[60:63], v[140:143], a[16:19], v[60:63]\n"
"ds_read_b128 v[120:123], v10 offset:11264\n"
"s_lshl_b32 s72, s72, 12\n"
"v_mfma_f32_16x16x32_bf16 v[60:63], v[144:147], a[24:27], v[60:63]\n"
"buffer_load_dwordx4 a[64:67], v1, s[4:7], s57 offen sc1\n"
"s_add_u32 s56, s56, s72\n"
"v_mfma_f32_16x16x32_bf16 v[64:67], v[132:135], a[4:7], 0\n"
"ds_read_b128 v[124:127], v10 offset:18432\n"
"s_and_b32 s72, s53, 1\n"
"v_mfma_f32_16x16x32_bf16 v[64:67], v[136:139], a[12:15], v[64:67]\n"
"s_lshl_b32 s72, s72, 9\n"
"s_add_u32 s58, s56, s72\n"
"v_mfma_f32_16x16x32_bf16 v[64:67], v[140:143], a[20:23], v[64:67]\n"
"ds_read_b128 v[128:131], v10 offset:25600\n"
"s_andn2_b32 s72, s61, 0x1fff\n"
"v_mfma_f32_16x16x32_bf16 v[64:67], v[144:147], a[28:31], v[64:67]\n"
"buffer_load_dwordx4 a[68:71], v1, s[4:7], s57 offen offset:1024 sc1\n"
"s_waitcnt lgkmcnt(4)\n"
"v_mfma_f32_16x16x32_bf16 v[68:71], v[100:103], a[0:3], 0\n"
"ds_read_b128 v[132:135], v10 offset:5120\n"
"s_add_u32 s61, s61, s72\n"
"v_mfma_f32_16x16x32_bf16 v[68:71], v[104:107], a[8:11], v[68:71]\n"
"s_add_u32 s61, s61, 0x1000000\n"
"s_lshr_b32 s72, s61, 8\n"
"v_mfma_f32_16x16x32_bf16 v[68:71], v[108:111], a[16:19], v[68:71]\n"
"ds_read_b128 v[136:139], v10 offset:12288\n"
"s_and_b32 s72, s72, 0x2000\n"
"v_mfma_f32_16x16x32_bf16 v[68:71], v[112:115], a[24:27], v[68:71]\n"
"buffer_load_dwordx4 a[72:75], v1, s[4:7], s57 offen offset:2048 sc1\n"
"s_xor_b32 s61, s61, s72\n"
"v_mfma_f32_16x16x32_bf16 v[72:75], v[100:103], a[4:7], 0\n"
"ds_read_b128 v[140:143], v10 offset:19456\n"
"s_xor_b32 s61, s61, s101\n"
"v_mfma_f32_16x16x32_bf16 v[72:75], v[104:107], a[12:15], v[72:75]\n"
"s_andn2_b32 s72, s66, 0x1fff\n"
"s_add_u32 s66, s66, s72\n"
"v_mfma_f32_16x16x32_bf16 v[72:75], v[108:111], a[20:23], v[72:75]\n"
"ds_read_b128 v[144:147], v10 offset:26624\n"
"s_add_u32 s66, s66, 0x1000000\n"
"v_mfma_f32_16x16x32_bf16 v[72:75], v[112:115], a[28:31], v[72:75]\n"
"s_lshr_b32 s72, s66, 8\n"
"s_waitcnt lgkmcnt(4)\n"
"v_mfma_f32_16x16x32_bf16 v[76:79], v[116:119], a[0:3], 0\n"
"ds_read_b128 v[100:103], v10 offset:6144\n"
"s_and_b32 s72, s72, 0x2000\n"
"v_mfma_f32_16x16x32_bf16 v[76:79], v[120:123], a[8:11], v[76:79]\n"
"buffer_load_dwordx4 a[76:79], v1, s[4:7], s57 offen offset:3072 sc1\n"
"s_add_u32 s57, s57, 0x1000\n"
"v_mfma_f32_16x16x32_bf16 v[76:79], v[124:127], a[16:19], v[76:79]\n"
"ds_read_b128 v[104:107], v10 offset:13312\n"
"s_xor_b32 s66, s66, s72\n"
"v_mfma_f32_16x16x32_bf16 v[76:79], v[128:131], a[24:27], v[76:79]\n"
"s_xor_b32 s66, s66, s101\n"
"s_andn2_b32 s72, s58, 0x1fff\n"
"v_mfma_f32_16x16x32_bf16 v[80:83], v[116:119], a[4:7], 0\n"
"ds_read_b128 v[108:111], v10 offset:20480\n"
"s_add_u32 s58, s58, s72\n"
"v_mfma_f32_16x16x32_bf16 v[80:83], v[120:123], a[12:15], v[80:83]\n"
"buffer_load_dwordx4 a[176:179], v1, s[4:7], s57 offen sc1\n"
"s_add_u32 s58, s58, 0x1000000\n"
"v_mfma_f32_16x16x32_bf16 v[80:83], v[124:127], a[20:23], v[80:83]\n"
"ds_read_b128 v[112:115], v10 offset:27648\n"
"s_lshr_b32 s72, s58, 8\n"
"v_mfma_f32_16x16x32_bf16 v[80:83], v[128:131], a[28:31], v[80:83]\n"
"s_and_b32 s72, s72, 0x2000\n"
"s_waitcnt lgkmcnt(4)\n"
"v_mfma_f32_16x16x32_bf16 v[84:87], v[132:135], a[0:3], 0\n"
"ds_read_b128 v[116:119], v10 offset:28672\n"
"s_xor_b32 s58, s58, s72\n"
"v_mfma_f32_16x16x32_bf16 v[84:87], v[136:139], a[8:11], v[84:87]\n"
"buffer_load_dwordx4 a[180:183], v1, s[4:7], s57 offen offset:1024 sc1\n"
"s_xor_b32 s58, s58, s101\n"
"v_mfma_f32_16x16x32_bf16 v[84:87], v[140:143], a[16:19], v[84:87]\n"
"ds_read_b128 v[120:123], v10 offset:35840\n"
"v_mfma_f32_16x16x32_bf16 v[84:87], v[144:147], a[24:27], v[84:87]\n"
"s_cmp_ge_u32 s53, 12\n"
"s_cmov_b32 s58, 0x40000000\n"
"v_mfma_f32_16x16x32_bf16 v[88:91], v[132:135], a[4:7], 0\n"
"ds_read_b128 v[124:127], v10 offset:43008\n"
"s_lshl4_add_u32 s69, s52, s53\n"
"v_mfma_f32_16x16x32_bf16 v[88:91], v[136:139], a[12:15], v[88:91]\n"
"buffer_load_dwordx4 a[184:187], v1, s[4:7], s57 offen offset:2048 sc1\n"
"s_lshl_b32 s69, s69, 7\n"
"v_mfma_f32_16x16x32_bf16 v[88:91], v[140:143], a[20:23], v[88:91]\n"
"ds_read_b128 v[128:131], v10 offset:50176\n"
"s_and_b32 s72, s69, 0x2000\n"
"v_mfma_f32_16x16x32_bf16 v[88:91], v[144:147], a[28:31], v[88:91]\n"
"s_add_u32 s69, s69, s72\n"
"s_waitcnt lgkmcnt(4)\n"
"v_mfma_f32_16x16x32_bf16 v[92:95], v[100:103], a[0:3], 0\n"
"ds_read_b128 v[132:135], v10 offset:29696\n"
"s_add_u32 s69, s69, 0xf8000\n"
"v_mfma_f32_16x16x32_bf16 v[92:95], v[104:107], a[8:11], v[92:95]\n"
"buffer_load_dwordx4 a[188:191], v1, s[4:7], s57 offen offset:3072 sc1\n"
"s_add_u32 s57, s57, 0x3000\n"
"v_mfma_f32_16x16x32_bf16 v[92:95], v[108:111], a[16:19], v[92:95]\n"
"ds_read_b128 v[136:139], v10 offset:36864\n"
"s_xor_b32 s69, s69, s101\n"
"v_mfma_f32_16x16x32_bf16 v[92:95], v[112:115], a[24:27], v[92:95]\n"
"s_lshr_b32 s70, s50, 2\n"
"s_lshl_b32 s70, s70, 11\n"
"v_mfma_f32_16x16x32_bf16 v[96:99], v[100:103], a[4:7], 0\n"
"ds_read_b128 v[140:143], v10 offset:44032\n"
"s_and_b32 s72, s70, 0x2000\n"
"v_mfma_f32_16x16x32_bf16 v[96:99], v[104:107], a[12:15], v[96:99]\n"
"buffer_load_dwordx4 v[148:151], v1, s[4:7], s67 offen sc1\n"
"s_add_u32 s70, s70, s72\n"
"v_mfma_f32_16x16x32_bf16 v[96:99], v[108:111], a[20:23], v[96:99]\n"
"ds_read_b128 v[144:147], v10 offset:51200\n"
"s_add_u32 s70, s70, 0xf8000\n"
"v_mfma_f32_16x16x32_bf16 v[96:99], v[112:115], a[28:31], v[96:99]\n"
"s_xor_b32 s70, s70, s101\n"
"s_waitcnt vmcnt(9) lgkmcnt(4)\n"
"v_mfma_f32_16x16x32_bf16 v[44:47], v[116:119], a[32:35], v[44:47]\n"
"ds_read_b128 v[100:103], v10 offset:30720\n"
"v_mfma_f32_16x16x32_bf16 v[44:47], v[120:123], a[40:43], v[44:47]\n"
"buffer_load_dwordx4 v[152:155], v1, s[4:7], s67 offen offset:1024 sc1\n"
"v_mfma_f32_16x16x32_bf16 v[44:47], v[124:127], a[48:51], v[44:47]\n"
"ds_read_b128 v[104:107], v10 offset:37888\n"
"v_mfma_f32_16x16x32_bf16 v[44:47], v[128:131], a[56:59], v[44:47]\n"
"v_mfma_f32_16x16x32_bf16 v[48:51], v[116:119], a[36:39], v[48:51]\n"
"ds_read_b128 v[108:111], v10 offset:45056\n"
"v_mfma_f32_16x16x32_bf16 v[48:51], v[120:123], a[44:47], v[48:51]\n"
"buffer_load_dwordx4 v[156:159], v1, s[4:7], s67 offen offset:2048 sc1\n"
"v_mfma_f32_16x16x32_bf16 v[48:51], v[124:127], a[52:55], v[48:51]\n"
"ds_read_b128 v[112:115], v10 offset:52224\n"
"v_mfma_f32_16x16x32_bf16 v[48:51], v[128:131], a[60:63], v[48:51]\n"
"s_waitcnt lgkmcnt(4)\n"
"v_mfma_f32_16x16x32_bf16 v[52:55], v[132:135], a[32:35], v[52:55]\n"
"ds_read_b128 v[116:119], v10 offset:31744\n"
"v_mfma_f32_16x16x32_bf16 v[52:55], v[136:139], a[40:43], v[52:55]\n"
"buffer_load_dwordx4 v[160:163], v1, s[4:7], s67 offen offset:3072 sc1\n"
"v_mfma_f32_16x16x32_bf16 v[52:55], v[140:143], a[48:51], v[52:55]\n"
"ds_read_b128 v[120:123], v10 offset:38912\n"
"v_mfma_f32_16x16x32_bf16 v[52:55], v[144:147], a[56:59], v[52:55]\n"
"v_mfma_f32_16x16x32_bf16 v[56:59], v[132:135], a[36:39], v[56:59]\n"
"ds_read_b128 v[124:127], v10 offset:46080\n"
"v_mfma_f32_16x16x32_bf16 v[56:59], v[136:139], a[44:47], v[56:59]\n"
"buffer_load_dwordx2 a[192:193], v19, s[4:7], s57 offen sc1\n"
"v_mfma_f32_16x16x32_bf16 v[56:59], v[140:143], a[52:55], v[56:59]\n"
"ds_read_b128 v[128:131], v10 offset:53248\n"
"v_mfma_f32_16x16x32_bf16 v[56:59], v[144:147], a[60:63], v[56:59]\n"
"buffer_load_dwordx2 a[194:195], v19, s[4:7], s57 offen offset:512 sc1\n"
"s_waitcnt lgkmcnt(4)\n"
"v_mfma_f32_16x16x32_bf16 v[60:63], v[100:103], a[32:35], v[60:63]\n"
"s_barrier\n"
"v_mfma_f32_16x16x32_bf16 v[60:63], v[104:107], a[40:43], v[60:63]\n"
"ds_read_b128 v[132:135], v13\n"
"buffer_load_dwordx2 a[196:197], v19, s[4:7], s57 offen offset:1024 sc1\n"
"v_mfma_f32_16x16x32_bf16 v[60:63], v[108:111], a[48:51], v[60:63]\n"
"v_mfma_f32_16x16x32_bf16 v[60:63], v[112:115], a[56:59], v[60:63]\n"
"ds_read_b128 v[136:139], v13 offset:7168\n"
"buffer_load_dwordx2 a[198:199], v19, s[4:7], s57 offen offset:1536 sc1\n"
"v_mfma_f32_16x16x32_bf16 v[64:67], v[100:103], a[36:39], v[64:67]\n"
"v_mfma_f32_16x16x32_bf16 v[64:67], v[104:107], a[44:47], v[64:67]\n"
"ds_read_b128 v[140:143], v13 offset:14336\n"
"buffer_load_dwordx4 a[200:203], v1, s[4:7], s57 offen offset:2048 sc1\n"
"v_mfma_f32_16x16x32_bf16 v[64:67], v[108:111], a[52:55], v[64:67]\n"
"v_mfma_f32_16x16x32_bf16 v[64:67], v[112:115], a[60:63], v[64:67]\n"
"ds_read_b128 v[144:147], v13 offset:21504\n"
"s_waitcnt lgkmcnt(4)\n"
"v_mfma_f32_16x16x32_bf16 v[68:71], v[116:119], a[32:35], v[68:71]\n"
"ds_read_b128 v[100:103], v13 offset:1024\n"
"v_mfma_f32_16x16x32_bf16 v[68:71], v[120:123], a[40:43], v[68:71]\n"
"ds_read_b128 v[176:179], v16\n"
"v_mfma_f32_16x16x32_bf16 v[68:71], v[124:127], a[48:51], v[68:71]\n"
"ds_read_b128 v[104:107], v13 offset:8192\n"
"v_mfma_f32_16x16x32_bf16 v[68:71], v[128:131], a[56:59], v[68:71]\n"
"ds_read_b128 v[180:183], v16 offset:64\n"
"v_mfma_f32_16x16x32_bf16 v[72:75], v[116:119], a[36:39], v[72:75]\n"
"ds_read_b128 v[108:111], v13 offset:15360\n"
"v_mfma_f32_16x16x32_bf16 v[72:75], v[120:123], a[44:47], v[72:75]\n"
"ds_read_b128 v[184:187], v16 offset:128\n"
"v_mfma_f32_16x16x32_bf16 v[72:75], v[124:127], a[52:55], v[72:75]\n"
"ds_read_b128 v[112:115], v13 offset:22528\n"
"v_mfma_f32_16x16x32_bf16 v[72:75], v[128:131], a[60:63], v[72:75]\n"
"ds_read_b128 v[188:191], v16 offset:192\n"
"s_waitcnt lgkmcnt(0)\n"
"v_mfma_f32_16x16x32_bf16 v[164:167], v[132:135], v[176:179], 0\n"
"ds_read_b128 v[116:119], v11\n"
"v_mfma_f32_16x16x32_bf16 v[164:167], v[136:139], v[180:183], v[164:167]\n"
"ds_read_b128 v[120:123], v11 offset:7168\n"
"v_mfma_f32_16x16x32_bf16 v[164:167], v[140:143], v[184:187], v[164:167]\n"
"ds_read_b128 v[124:127], v11 offset:14336\n"
"v_mfma_f32_16x16x32_bf16 v[164:167], v[144:147], v[188:191], v[164:167]\n"
"ds_read_b128 v[128:131], v11 offset:21504\n"
"s_nop 0\n"
"v_mfma_f32_16x16x32_bf16 v[168:171], v[100:103], v[176:179], 0\n"
"ds_read_b128 v[132:135], v11 offset:1024\n"
"v_mfma_f32_16x16x32_bf16 v[168:171], v[104:107], v[180:183], v[168:171]\n"
"ds_read_b128 v[136:139], v11 offset:8192\n"
"v_mfma_f32_16x16x32_bf16 v[168:171], v[108:111], v[184:187], v[168:171]\n"
"ds_read_b128 v[140:143], v11 offset:15360\n"
"v_mfma_f32_16x16x32_bf16 v[168:171], v[112:115], v[188:191], v[168:171]\n"
"ds_read_b128 v[144:147], v11 offset:22528\n"
"s_waitcnt vmcnt(9) lgkmcnt(4)\n"
"v_mfma_f32_16x16x32_bf16 v[44:47], v[116:119], a[64:67], v[44:47]\n"
"ds_read_b128 v[100:103], v13 offset:28672\n"
"v_mfma_f32_16x16x32_bf16 v[44:47], v[120:123], a[72:75], v[44:47]\n"
"ds_read_b128 v[176:179], v16 offset:256\n"
"v_mfma_f32_16x16x32_bf16 v[44:47], v[124:127], a[176:179], v[44:47]\n"
"ds_read_b128 v[104:107], v13 offset:35840\n"
"v_mfma_f32_16x16x32_bf16 v[44:47], v[128:131], a[184:187], v[44:47]\n"
"ds_read_b128 v[180:183], v16 offset:320\n"
"v_mfma_f32_16x16x32_bf16 v[48:51], v[116:119], a[68:71], v[48:51]\n"
"ds_read_b128 v[108:111], v13 offset:43008\n"
"v_mfma_f32_16x16x32_bf16 v[48:51], v[120:123], a[76:79], v[48:51]\n"
"ds_read_b128 v[184:187], v16 offset:384\n"
"v_mfma_f32_16x16x32_bf16 v[48:51], v[124:127], a[180:183], v[48:51]\n"
"ds_read_b128 v[112:115], v13 offset:50176\n"
"v_mfma_f32_16x16x32_bf16 v[48:51], v[128:131], a[188:191], v[48:51]\n"
"ds_read_b128 v[188:191], v16 offset:448\n"
"s_waitcnt lgkmcnt(8)\n"
"v_mfma_f32_16x16x32_bf16 v[52:55], v[132:135], a[64:67], v[52:55]\n"
"ds_read_b128 v[116:119], v13 offset:29696\n"
"v_mfma_f32_16x16x32_bf16 v[52:55], v[136:139], a[72:75], v[52:55]\n"
"buffer_load_dwordx4 a[204:207], v1, s[4:7], s57 offen offset:3072 sc1\n"
"s_add_u32 s57, s57, 0x1000\n"
"s_waitcnt vmcnt(6)\n"
"v_mfma_f32_16x16x32_bf16 v[52:55], v[140:143], a[176:179], v[52:55]\n"
"ds_read_b128 v[120:123], v13 offset:36864\n"
"v_xor_b32 v149, s82, v149\n"
"v_mfma_f32_16x16x32_bf16 v[52:55], v[144:147], a[184:187], v[52:55]\n"
"v_xor_b32 v151, s82, v151\n"
"v_mfma_f32_16x16x32_bf16 v[56:59], v[132:135], a[68:71], v[56:59]\n"
"ds_read_b128 v[124:127], v13 offset:44032\n"
"v_xor_b32 v153, s82, v153\n"
"v_mfma_f32_16x16x32_bf16 v[56:59], v[136:139], a[76:79], v[56:59]\n"
"buffer_load_dwordx4 a[208:211], v1, s[4:7], s57 offen sc1\n"
"v_xor_b32 v155, s82, v155\n"
"v_mfma_f32_16x16x32_bf16 v[56:59], v[140:143], a[180:183], v[56:59]\n"
"ds_read_b128 v[128:131], v13 offset:51200\n"
"v_xor_b32 v157, s82, v157\n"
"v_mfma_f32_16x16x32_bf16 v[56:59], v[144:147], a[188:191], v[56:59]\n"
"v_xor_b32 v159, s82, v159\n"
"s_waitcnt lgkmcnt(4)\n"
"v_mfma_f32_16x16x32_bf16 v[164:167], v[100:103], v[176:179], v[164:167]\n"
"ds_read_b128 v[132:135], v11 offset:2048\n"
"v_xor_b32 v161, s82, v161\n"
"v_mfma_f32_16x16x32_bf16 v[164:167], v[104:107], v[180:183], v[164:167]\n"
"ds_read_b128 v[136:139], v11 offset:9216\n"
"buffer_load_dwordx4 a[212:215], v1, s[4:7], s57 offen offset:1024 sc1\n"
"v_xor_b32 v163, s82, v163\n"
"v_mfma_f32_16x16x32_bf16 v[164:167], v[108:111], v[184:187], v[164:167]\n"
"ds_read_b128 v[140:143], v11 offset:16384\n"
"v_or3_b32 v149, v149, v151, v153\n"
"v_mfma_f32_16x16x32_bf16 v[164:167], v[112:115], v[188:191], v[164:167]\n"
"ds_read_b128 v[144:147], v11 offset:23552\n"
"v_or3_b32 v155, v155, v157, v159\n"
"s_waitcnt lgkmcnt(4)\n"
"v_mfma_f32_16x16x32_bf16 v[168:171], v[116:119], v[176:179], v[168:171]\n"
"ds_read_b128 v[100:103], v11 offset:3072\n"
"v_or3_b32 v149, v149, v161, v163\n"
"v_mfma_f32_16x16x32_bf16 v[168:171], v[120:123], v[180:183], v[168:171]\n"
"ds_read_b128 v[104:107], v11 offset:10240\n"
"buffer_load_dwordx4 a[216:219], v1, s[4:7], s57 offen offset:2048 sc1\n"
"v_or_b32 v149, v149, v155\n"
"v_mfma_f32_16x16x32_bf16 v[168:171], v[124:127], v[184:187], v[168:171]\n"
"ds_read_b128 v[108:111], v11 offset:17408\n"
"v_mfma_f32_16x16x32_bf16 v[168:171], v[128:131], v[188:191], v[168:171]\n"
"ds_read_b128 v[112:115], v11 offset:24576\n"
"s_waitcnt lgkmcnt(4)\n"
"v_add_u32 v13, 0xe000, v13\n"
"v_mfma_f32_16x16x32_bf16 v[60:63], v[132:135], a[64:67], v[60:63]\n"
"ds_read_b128 v[116:119], v13\n"
"v_mfma_f32_16x16x32_bf16 v[60:63], v[136:139], a[72:75], v[60:63]\n"
"ds_read_b128 v[176:179], v16 offset:512\n"
"v_mfma_f32_16x16x32_bf16 v[60:63], v[140:143], a[176:179], v[60:63]\n"
"ds_read_b128 v[120:123], v13 offset:7168\n"
"v_mfma_f32_16x16x32_bf16 v[60:63], v[144:147], a[184:187], v[60:63]\n"
"ds_read_b128 v[180:183], v16 offset:576\n"
"v_mfma_f32_16x16x32_bf16 v[64:67], v[132:135], a[68:71], v[64:67]\n"
"ds_read_b128 v[124:127], v13 offset:14336\n"
"v_mfma_f32_16x16x32_bf16 v[64:67], v[136:139], a[76:79], v[64:67]\n"
"ds_read_b128 v[184:187], v16 offset:640\n"
"v_mfma_f32_16x16x32_bf16 v[64:67], v[140:143], a[180:183], v[64:67]\n"
"ds_read_b128 v[128:131], v13 offset:21504\n"
"v_mfma_f32_16x16x32_bf16 v[64:67], v[144:147], a[188:191], v[64:67]\n"
"ds_read_b128 v[188:191], v16 offset:704\n"
"s_waitcnt lgkmcnt(8)\n"
"v_mfma_f32_16x16x32_bf16 v[68:71], v[100:103], a[64:67], v[68:71]\n"
"ds_read_b128 v[132:135], v13 offset:1024\n"
"v_mfma_f32_16x16x32_bf16 v[68:71], v[104:107], a[72:75], v[68:71]\n"
"buffer_load_dwordx4 a[220:223], v1, s[4:7], s57 offen offset:3072 sc1\n"
"s_add_u32 s57, s57, 0x3000\n"
"v_mfma_f32_16x16x32_bf16 v[68:71], v[108:111], a[176:179], v[68:71]\n"
"ds_read_b128 v[136:139], v13 offset:8192\n"
"v_mfma_f32_16x16x32_bf16 v[68:71], v[112:115], a[184:187], v[68:71]\n"
"v_cmp_ne_u32 vcc, 0, v149\n"
"s_nop 0\n"
"s_cbranch_vccnz .Ladaln_block_P_st\n"
".Ladaln_block_P_st_R:\n"
"v_mfma_f32_16x16x32_bf16 v[72:75], v[100:103], a[68:71], v[72:75]\n"
"ds_read_b128 v[140:143], v13 offset:15360\n"
"v_add_f32 v148, v148, v152\n"
"v_add_f32 v149, v150, v154\n"
"v_mfma_f32_16x16x32_bf16 v[72:75], v[104:107], a[76:79], v[72:75]\n"
"buffer_load_dwordx4 a[224:227], v1, s[4:7], s57 offen sc1\n"
"v_mov_b32 v150, v148\n"
"v_mov_b32 v151, v149\n"
"v_mfma_f32_16x16x32_bf16 v[72:75], v[108:111], a[180:183], v[72:75]\n"
"ds_read_b128 v[144:147], v13 offset:22528\n"
"v_permlane16_swap_b32 v148, v150\n"
"v_permlane16_swap_b32 v149, v151\n"
"v_mfma_f32_16x16x32_bf16 v[72:75], v[112:115], a[188:191], v[72:75]\n"
"v_add_f32 v148, v148, v150\n"
"v_add_f32 v149, v149, v151\n"
"s_waitcnt lgkmcnt(4)\n"
"v_mfma_f32_16x16x32_bf16 v[164:167], v[116:119], v[176:179], v[164:167]\n"
"ds_read_b128 v[100:103], v11 offset:28672\n"
"v_mov_b32 v150, v148\n"
"v_mov_b32 v151, v149\n"
"v_mfma_f32_16x16x32_bf16 v[164:167], v[120:123], v[180:183], v[164:167]\n"
"ds_read_b128 v[104:107], v11 offset:35840\n"
"buffer_load_dwordx4 a[228:231], v1, s[4:7], s57 offen offset:1024 sc1\n"
"v_mfma_f32_16x16x32_bf16 v[164:167], v[124:127], v[184:187], v[164:167]\n"
"ds_read_b128 v[108:111], v11 offset:43008\n"
"v_permlane32_swap_b32 v148, v150\n"
"v_permlane32_swap_b32 v149, v151\n"
"v_mfma_f32_16x16x32_bf16 v[164:167], v[128:131], v[188:191], v[164:167]\n"
"ds_read_b128 v[112:115], v11 offset:50176\n"
"s_waitcnt lgkmcnt(4)\n"
"v_mfma_f32_16x16x32_bf16 v[168:171], v[132:135], v[176:179], v[168:171]\n"
"ds_read_b128 v[116:119], v11 offset:29696\n"
"v_add_f32 v148, v148, v150\n"
"v_add_f32 v149, v149, v151\n"
"v_mfma_f32_16x16x32_bf16 v[168:171], v[136:139], v[180:183], v[168:171]\n"
"ds_read_b128 v[120:123], v11 offset:36864\n"
"buffer_load_dwordx2 a[232:233], v19, s[4:7], s57 offen offset:2048 sc1\n"
"v_mul_f32 v172, 0xbacccccd, v148\n"
"v_mfma_f32_16x16x32_bf16 v[168:171], v[140:143], v[184:187], v[168:171]\n"
"ds_read_b128 v[124:127], v11 offset:44032\n"
"v_mul_f32 v149, 0x3acccccd, v149\n"
"v_mfma_f32_16x16x32_bf16 v[168:171], v[144:147], v[188:191], v[168:171]\n"
"ds_read_b128 v[128:131], v11 offset:51200\n"
"v_fma_f32 v149, -v172, v172, v149\n"
"s_waitcnt vmcnt(3) lgkmcnt(4)\n"
"v_mfma_f32_16x16x32_bf16 v[44:47], v[100:103], a[192:195], v[44:47]\n"
"ds_read_b128 v[132:135], v13 offset:28672\n"
"buffer_load_dwordx2 a[234:235], v19, s[4:7], s57 offen offset:2560 sc1\n"
"v_max_f32 v149, 0, v149\n"
"v_mfma_f32_16x16x32_bf16 v[44:47], v[104:107], a[200:203], v[44:47]\n"
"ds_read_b128 v[176:179], v16 offset:768\n"
"v_add_f32 v149, 0x3727c5ac, v149\n"
"v_mfma_f32_16x16x32_bf16 v[44:47], v[108:111], a[208:211], v[44:47]\n"
"ds_read_b128 v[136:139], v13 offset:35840\n"
"buffer_load_dwordx2 a[236:237], v19, s[4:7], s57 offen offset:3072 sc1\n"
"v_mfma_f32_16x16x32_bf16 v[44:47], v[112:115], a[216:219], v[44:47]\n"
"ds_read_b128 v[180:183], v16 offset:832\n"
"v_rsq_f32 v174, v149\n"
"v_mfma_f32_16x16x32_bf16 v[48:51], v[100:103], a[196:199], v[48:51]\n"
"ds_read_b128 v[140:143], v13 offset:43008\n"
"v_add_f32 v156, v156, v160\n"
"v_add_f32 v157, v158, v162\n"
"v_mfma_f32_16x16x32_bf16 v[48:51], v[104:107], a[204:207], v[48:51]\n"
"ds_read_b128 v[184:187], v16 offset:896\n"
"v_mov_b32 v158, v156\n"
"v_mov_b32 v159, v157\n"
"v_mfma_f32_16x16x32_bf16 v[48:51], v[108:111], a[212:215], v[48:51]\n"
"ds_read_b128 v[144:147], v13 offset:50176\n"
"v_permlane16_swap_b32 v156, v158\n"
"v_permlane16_swap_b32 v157, v159\n"
"v_mfma_f32_16x16x32_bf16 v[48:51], v[112:115], a[220:223], v[48:51]\n"
"ds_read_b128 v[188:191], v16 offset:960\n"
"s_waitcnt lgkmcnt(8)\n"
"v_mfma_f32_16x16x32_bf16 v[52:55], v[116:119], a[192:195], v[52:55]\n"
"ds_read_b128 v[100:103], v13 offset:29696\n"
"v_add_f32 v156, v156, v158\n"
"v_add_f32 v157, v157, v159\n"
"v_mfma_f32_16x16x32_bf16 v[52:55], v[120:123], a[200:203], v[52:55]\n"
"buffer_load_dwordx2 a[238:239], v19, s[4:7], s57 offen offset:3584 sc1\n"
"s_add_u32 s57, s57, 0x1000\n"
"v_mfma_f32_16x16x32_bf16 v[52:55], v[124:127], a[208:211], v[52:55]\n"
"ds_read_b128 v[104:107], v13 offset:36864\n"
"v_mov_b32 v158, v156\n"
"v_mov_b32 v159, v157\n"
"v_mfma_f32_16x16x32_bf16 v[52:55], v[128:131], a[216:219], v[52:55]\n"
"buffer_load_dwordx4 a[240:243], v1, s[4:7], s57 offen sc1\n"
"v_permlane32_swap_b32 v156, v158\n"
"v_permlane32_swap_b32 v157, v159\n"
"v_mfma_f32_16x16x32_bf16 v[56:59], v[116:119], a[196:199], v[56:59]\n"
"ds_read_b128 v[108:111], v13 offset:44032\n"
"v_add_f32 v156, v156, v158\n"
"v_add_f32 v157, v157, v159\n"
"v_mfma_f32_16x16x32_bf16 v[56:59], v[120:123], a[204:207], v[56:59]\n"
"v_mul_f32 v173, 0xbacccccd, v156\n"
"v_mfma_f32_16x16x32_bf16 v[56:59], v[124:127], a[212:215], v[56:59]\n"
"ds_read_b128 v[112:115], v13 offset:51200\n"
"v_mul_f32 v157, 0x3acccccd, v157\n"
"v_mfma_f32_16x16x32_bf16 v[56:59], v[128:131], a[220:223], v[56:59]\n"
"buffer_load_dwordx4 a[244:247], v1, s[4:7], s57 offen offset:1024 sc1\n"
"v_fma_f32 v157, -v173, v173, v157\n"
"s_waitcnt lgkmcnt(4)\n"
"v_mfma_f32_16x16x32_bf16 v[164:167], v[132:135], v[176:179], v[164:167]\n"
"ds_read_b128 v[116:119], v11 offset:30720\n"
"v_max_f32 v157, 0, v157\n"
"v_mfma_f32_16x16x32_bf16 v[164:167], v[136:139], v[180:183], v[164:167]\n"
"ds_read_b128 v[120:123], v11 offset:37888\n"
"v_add_f32 v157, 0x3727c5ac, v157\n"
"v_mfma_f32_16x16x32_bf16 v[164:167], v[140:143], v[184:187], v[164:167]\n"
"ds_read_b128 v[124:127], v11 offset:45056\n"
"v_rsq_f32 v175, v157\n"
"v_mfma_f32_16x16x32_bf16 v[164:167], v[144:147], v[188:191], v[164:167]\n"
"ds_read_b128 v[128:131], v11 offset:52224\n"
"s_waitcnt lgkmcnt(4)\n"
"v_mfma_f32_16x16x32_bf16 v[168:171], v[100:103], v[176:179], v[168:171]\n"
"ds_read_b128 v[132:135], v11 offset:31744\n"
"buffer_load_dwordx4 a[248:251], v1, s[4:7], s57 offen offset:2048 sc1\n"
"v_mfma_f32_16x16x32_bf16 v[168:171], v[104:107], v[180:183], v[168:171]\n"
"ds_read_b128 v[136:139], v11 offset:38912\n"
"v_mov_b32 v192, v172\n"
"v_mfma_f32_16x16x32_bf16 v[168:171], v[108:111], v[184:187], v[168:171]\n"
"ds_read_b128 v[140:143], v11 offset:46080\n"
"v_mov_b32 v193, v172\n"
"v_mfma_f32_16x16x32_bf16 v[168:171], v[112:115], v[188:191], v[168:171]\n"
"ds_read_b128 v[144:147], v11 offset:53248\n"
"s_waitcnt lgkmcnt(4)\n"
"v_add_u32 v13, 0xe000, v13\n"
"v_mfma_f32_16x16x32_bf16 v[60:63], v[116:119], a[192:195], v[60:63]\n"
"ds_read_b128 v[100:103], v13\n"
"v_mov_b32 v194, v173\n"
"v_mfma_f32_16x16x32_bf16 v[60:63], v[120:123], a[200:203], v[60:63]\n"
"ds_read_b128 v[176:179], v16 offset:1024\n"
"v_mov_b32 v195, v173\n"
"v_mfma_f32_16x16x32_bf16 v[60:63], v[124:127], a[208:211], v[60:63]\n"
"ds_read_b128 v[104:107], v13 offset:7168\n"
"v_mov_b32 v196, v174\n"
"v_mfma_f32_16x16x32_bf16 v[60:63], v[128:131], a[216:219], v[60:63]\n"
"ds_read_b128 v[180:183], v16 offset:1088\n"
"v_mov_b32 v197, v174\n"
"v_mfma_f32_16x16x32_bf16 v[64:67], v[116:119], a[196:199], v[64:67]\n"
"ds_read_b128 v[108:111], v13 offset:14336\n"
"v_mov_b32 v198, v175\n"
"v_mfma_f32_16x16x32_bf16 v[64:67], v[120:123], a[204:207], v[64:67]\n"
"ds_read_b128 v[184:187], v16 offset:1152\n"
"v_mov_b32 v199, v175\n"
"v_mfma_f32_16x16x32_bf16 v[64:67], v[124:127], a[212:215], v[64:67]\n"
"ds_read_b128 v[112:115], v13 offset:21504\n"
"v_mfma_f32_16x16x32_bf16 v[64:67], v[128:131], a[220:223], v[64:67]\n"
"ds_read_b128 v[188:191], v16 offset:1216\n"
"s_waitcnt lgkmcnt(8)\n"
"v_mfma_f32_16x16x32_bf16 v[68:71], v[132:135], a[192:195], v[68:71]\n"
"ds_read_b128 v[116:119], v13 offset:1024\n"
"v_mfma_f32_16x16x32_bf16 v[68:71], v[136:139], a[200:203], v[68:71]\n"
"buffer_load_dwordx4 a[252:255], v1, s[4:7], s57 offen offset:3072 sc1\n"
"s_add_u32 s57, s57, 0x3000\n"
"v_mfma_f32_16x16x32_bf16 v[68:71], v[140:143], a[208:211], v[68:71]\n"
"ds_read_b128 v[120:123], v13 offset:8192\n"
"v_mfma_f32_16x16x32_bf16 v[68:71], v[144:147], a[216:219], v[68:71]\n"
"v_mfma_f32_16x16x32_bf16 v[72:75], v[132:135], a[196:199], v[72:75]\n"
"ds_read_b128 v[124:127], v13 offset:15360\n"
"v_mfma_f32_16x16x32_bf16 v[72:75], v[136:139], a[204:207], v[72:75]\n"
"buffer_load_dwordx4 v[200:203], v1, s[4:7], s60 offen sc1\n"
"v_mfma_f32_16x16x32_bf16 v[72:75], v[140:143], a[212:215], v[72:75]\n"
"ds_read_b128 v[128:131], v13 offset:22528\n"
"v_mfma_f32_16x16x32_bf16 v[72:75], v[144:147], a[220:223], v[72:75]\n"
"s_waitcnt lgkmcnt(4)\n"
"v_mfma_f32_16x16x32_bf16 v[164:167], v[100:103], v[176:179], v[164:167]\n"
"ds_read_b128 v[132:135], v12\n"
"v_mfma_f32_16x16x32_bf16 v[164:167], v[104:107], v[180:183], v[164:167]\n"
"ds_read_b128 v[136:139], v12 offset:7168\n"
"v_mfma_f32_16x16x32_bf16 v[164:167], v[108:111], v[184:187], v[164:167]\n"
"ds_read_b128 v[140:143], v12 offset:14336\n"
"buffer_load_dwordx4 v[204:207], v1, s[4:7], s60 offen offset:1024 sc1\n"
"v_mfma_f32_16x16x32_bf16 v[164:167], v[112:115], v[188:191], v[164:167]\n"
"ds_read_b128 v[144:147], v12 offset:21504\n"
"s_waitcnt lgkmcnt(4)\n"
"v_mfma_f32_16x16x32_bf16 v[168:171], v[116:119], v[176:179], v[168:171]\n"
"ds_read_b128 v[100:103], v12 offset:1024\n"
"s_lshl_b32 s68, s3, 11\n"
"v_mfma_f32_16x16x32_bf16 v[168:171], v[120:123], v[180:183], v[168:171]\n"
"ds_read_b128 v[104:107], v12 offset:8192\n"
"s_add_u32 s68, s68, 0x24000\n"
"v_mfma_f32_16x16x32_bf16 v[168:171], v[124:127], v[184:187], v[168:171]\n"
"ds_read_b128 v[108:111], v12 offset:15360\n"
"v_add_u32 v13, s68, v1\n"
"v_mfma_f32_16x16x32_bf16 v[168:171], v[128:131], v[188:191], v[168:171]\n"
"ds_read_b128 v[112:115], v12 offset:22528\n"
"s_waitcnt vmcnt(2) lgkmcnt(4)\n"
"v_mfma_f32_16x16x32_bf16 v[44:47], v[132:135], a[224:227], v[44:47]\n"
"ds_read_b128 v[116:119], v12 offset:2048\n"
"buffer_load_dwordx4 v[208:211], v1, s[4:7], s60 offen offset:2048 sc1\n"
"v_mfma_f32_16x16x32_bf16 v[44:47], v[136:139], a[232:235], v[44:47]\n"
"v_mov_b32 v16, 1.0\n"
"v_mov_b32 v17, 1.0\n"
"v_mfma_f32_16x16x32_bf16 v[44:47], v[140:143], a[240:243], v[44:47]\n"
"ds_read_b128 v[120:123], v12 offset:9216\n"
"v_mfma_f32_16x16x32_bf16 v[44:47], v[144:147], a[248:251], v[44:47]\n"
"v_mfma_f32_16x16x32_bf16 v[48:51], v[132:135], a[228:231], v[48:51]\n"
"ds_read_b128 v[124:127], v12 offset:16384\n"
"v_mfma_f32_16x16x32_bf16 v[48:51], v[136:139], a[236:239], v[48:51]\n"
"ds_write_b128 v13, v[164:167]\n"
"ds_write_b128 v13, v[168:171] offset:1024\n"
"v_mfma_f32_16x16x32_bf16 v[48:51], v[140:143], a[244:247], v[48:51]\n"
"ds_read_b128 v[128:131], v12 offset:23552\n"
"v_mfma_f32_16x16x32_bf16 v[48:51], v[144:147], a[252:255], v[48:51]\n"
"s_waitcnt lgkmcnt(6)\n"
"v_mfma_f32_16x16x32_bf16 v[52:55], v[100:103], a[224:227], v[52:55]\n"
"ds_read_b128 v[132:135], v12 offset:3072\n"
"v_mfma_f32_16x16x32_bf16 v[52:55], v[104:107], a[232:235], v[52:55]\n"
"buffer_load_dwordx4 v[212:215], v1, s[4:7], s60 offen offset:3072 sc1\n"
"s_add_u32 s60, s60, 0x1000\n"
"v_mfma_f32_16x16x32_bf16 v[52:55], v[108:111], a[240:243], v[52:55]\n"
"ds_read_b128 v[136:139], v12 offset:10240\n"
"v_mfma_f32_16x16x32_bf16 v[52:55], v[112:115], a[248:251], v[52:55]\n"
"v_mfma_f32_16x16x32_bf16 v[56:59], v[100:103], a[228:231], v[56:59]\n"
"ds_read_b128 v[140:143], v12 offset:17408\n"
"v_mfma_f32_16x16x32_bf16 v[56:59], v[104:107], a[236:239], v[56:59]\n"
"v_mfma_f32_16x16x32_bf16 v[56:59], v[108:111], a[244:247], v[56:59]\n"
"ds_read_b128 v[144:147], v12 offset:24576\n"
"buffer_load_dwordx4 v[216:219], v1, s[4:7], s60 offen sc1\n"
"v_mfma_f32_16x16x32_bf16 v[56:59], v[112:115], a[252:255], v[56:59]\n"
"s_waitcnt lgkmcnt(4)\n"
"v_mfma_f32_16x16x32_bf16 v[60:63], v[116:119], a[224:227], v[60:63]\n"
"ds_read_b128 v[100:103], v10 offset:32768\n"
"v_mfma_f32_16x16x32_bf16 v[60:63], v[120:123], a[232:235], v[60:63]\n"
"v_mfma_f32_16x16x32_bf16 v[60:63], v[124:127], a[240:243], v[60:63]\n"
"ds_read_b128 v[104:107], v10 offset:39936\n"
"v_mfma_f32_16x16x32_bf16 v[60:63], v[128:131], a[248:251], v[60:63]\n"
"buffer_load_dwordx4 v[220:223], v1, s[4:7], s60 offen offset:1024 sc1\n"
"v_mfma_f32_16x16x32_bf16 v[64:67], v[116:119], a[228:231], v[64:67]\n"
"ds_read_b128 v[108:111], v10 offset:47104\n"
"v_mfma_f32_16x16x32_bf16 v[64:67], v[120:123], a[236:239], v[64:67]\n"
"v_mfma_f32_16x16x32_bf16 v[64:67], v[124:127], a[244:247], v[64:67]\n"
"ds_read_b128 v[112:115], v10 offset:54272\n"
"v_mfma_f32_16x16x32_bf16 v[64:67], v[128:131], a[252:255], v[64:67]\n"
"s_waitcnt lgkmcnt(4)\n"
"v_mfma_f32_16x16x32_bf16 v[68:71], v[132:135], a[224:227], v[68:71]\n"
"ds_read_b128 v[116:119], v10 offset:33792\n"
"buffer_load_dwordx4 v[224:227], v1, s[4:7], s60 offen offset:2048 sc1\n"
"v_mfma_f32_16x16x32_bf16 v[68:71], v[136:139], a[232:235], v[68:71]\n"
"s_barrier\n"
"v_mfma_f32_16x16x32_bf16 v[68:71], v[140:143], a[240:243], v[68:71]\n"
"ds_read_b128 v[120:123], v10 offset:40960\n"
"v_lshlrev_b32 v13, 4, v15\n"
"v_mfma_f32_16x16x32_bf16 v[68:71], v[144:147], a[248:251], v[68:71]\n"
"v_add_u32 v13, 0x24000, v13\n"
"v_mfma_f32_16x16x32_bf16 v[72:75], v[132:135], a[228:231], v[72:75]\n"
"ds_read_b128 v[124:127], v10 offset:48128\n"
"v_mfma_f32_16x16x32_bf16 v[72:75], v[136:139], a[236:239], v[72:75]\n"
"ds_read_b128 v[176:179], v13\n"
"v_mfma_f32_16x16x32_bf16 v[72:75], v[140:143], a[244:247], v[72:75]\n"
"ds_read_b128 v[128:131], v10 offset:55296\n"
"v_mfma_f32_16x16x32_bf16 v[72:75], v[144:147], a[252:255], v[72:75]\n"
"ds_read_b128 v[180:183], v13 offset:128\n"
"s_waitcnt lgkmcnt(6)\n"
"v_mfma_f32_16x16x32_bf16 v[76:79], v[100:103], a[32:35], v[76:79]\n"
"ds_read_b128 v[132:135], v10 offset:34816\n"
"v_mfma_f32_16x16x32_bf16 v[76:79], v[104:107], a[40:43], v[76:79]\n"
"buffer_load_dwordx4 v[228:231], v1, s[4:7], s60 offen offset:3072 sc1\n"
"s_add_u32 s60, s60, 0x3000\n"
"v_mfma_f32_16x16x32_bf16 v[76:79], v[108:111], a[48:51], v[76:79]\n"
"ds_read_b128 v[136:139], v10 offset:41984\n"
"v_mfma_f32_16x16x32_bf16 v[76:79], v[112:115], a[56:59], v[76:79]\n"
"v_mfma_f32_16x16x32_bf16 v[80:83], v[100:103], a[36:39], v[80:83]\n"
"ds_read_b128 v[140:143], v10 offset:49152\n"
"v_mfma_f32_16x16x32_bf16 v[80:83], v[104:107], a[44:47], v[80:83]\n"
"ds_read_b128 v[184:187], v13 offset:1024\n"
"v_mfma_f32_16x16x32_bf16 v[80:83], v[108:111], a[52:55], v[80:83]\n"
"ds_read_b128 v[144:147], v10 offset:56320\n"
"buffer_load_dwordx4 v[232:235], v1, s[4:7], s60 offen sc1\n"
"ds_read_b128 v[188:191], v13 offset:1152\n"
"s_waitcnt lgkmcnt(6)\n"
"v_fmac_f32 v44, v172, v176\n"
"v_mfma_f32_16x16x32_bf16 v[80:83], v[112:115], a[60:63], v[80:83]\n"
"v_fmac_f32 v45, v172, v177\n"
"v_fmac_f32 v46, v172, v178\n"
"v_fmac_f32 v47, v172, v179\n"
"v_fma_f32 v44, v44, v174, v180\n"
"s_nop 0\n"
"v_mfma_f32_16x16x32_bf16 v[84:87], v[116:119], a[32:35], v[84:87]\n"
"ds_read_b128 v[100:103], v11 offset:4096\n"
"v_fma_f32 v45, v45, v174, v181\n"
"v_fma_f32 v46, v46, v174, v182\n"
"v_fma_f32 v47, v47, v174, v183\n"
"v_mul_f32 v164, 0xbfb8aa3b, v44\n"
"v_mfma_f32_16x16x32_bf16 v[84:87], v[120:123], a[40:43], v[84:87]\n"
"v_mul_f32 v165, 0xbfb8aa3b, v45\n"
"v_mul_f32 v166, 0xbfb8aa3b, v46\n"
"v_mul_f32 v167, 0xbfb8aa3b, v47\n"
"v_exp_f32 v164, v164\n"
"v_mfma_f32_16x16x32_bf16 v[84:87], v[124:127], a[48:51], v[84:87]\n"
"ds_read_b128 v[104:107], v11 offset:11264\n"
"v_exp_f32 v165, v165\n"
"v_exp_f32 v166, v166\n"
"v_mfma_f32_16x16x32_bf16 v[84:87], v[128:131], a[56:59], v[84:87]\n"
"buffer_load_dwordx4 v[236:239], v1, s[4:7], s60 offen offset:1024 sc1\n"
"v_exp_f32 v167, v167\n"
"v_add_f32 v164, 1.0, v164\n"
"v_add_f32 v165, 1.0, v165\n"
"v_mfma_f32_16x16x32_bf16 v[88:91], v[116:119], a[36:39], v[88:91]\n"
"ds_read_b128 v[108:111], v11 offset:18432\n"
"v_add_f32 v166, 1.0, v166\n"
"v_add_f32 v167, 1.0, v167\n"
"v_rcp_f32 v164, v164\n"
"v_mfma_f32_16x16x32_bf16 v[88:91], v[120:123], a[44:47], v[88:91]\n"
"v_rcp_f32 v165, v165\n"
"v_rcp_f32 v166, v166\n"
"v_mfma_f32_16x16x32_bf16 v[88:91], v[124:127], a[52:55], v[88:91]\n"
"ds_read_b128 v[112:115], v11 offset:25600\n"
"v_rcp_f32 v167, v167\n"
"v_mul_f32 v44, v44, v164\n"
"v_mul_f32 v45, v45, v165\n"
"v_mfma_f32_16x16x32_bf16 v[88:91], v[128:131], a[60:63], v[88:91]\n"
"v_mul_f32 v46, v46, v166\n"
"v_mul_f32 v47, v47, v167\n"
"v_cvt_pk_bf16_f32 v148, v44, v45\n"
"v_cvt_pk_bf16_f32 v149, v46, v47\n"
"s_waitcnt lgkmcnt(5)\n"
"v_mfma_f32_16x16x32_bf16 v[92:95], v[132:135], a[32:35], v[92:95]\n"
"ds_read_b128 v[116:119], v11 offset:5120\n"
"buffer_load_dwordx4 v[240:243], v1, s[4:7], s60 offen offset:2048 sc1\n"
"v_fmac_f32 v48, v173, v176\n"
"v_fmac_f32 v49, v173, v177\n"
"v_fmac_f32 v50, v173, v178\n"
"v_mfma_f32_16x16x32_bf16 v[92:95], v[136:139], a[40:43], v[92:95]\n"
"v_fmac_f32 v51, v173, v179\n"
"v_fma_f32 v48, v48, v175, v180\n"
"v_fma_f32 v49, v49, v175, v181\n"
"v_fma_f32 v50, v50, v175, v182\n"
"v_fma_f32 v51, v51, v175, v183\n"
"v_mfma_f32_16x16x32_bf16 v[92:95], v[140:143], a[48:51], v[92:95]\n"
"ds_read_b128 v[120:123], v11 offset:12288\n"
"v_mul_f32 v168, 0xbfb8aa3b, v48\n"
"v_mul_f32 v169, 0xbfb8aa3b, v49\n"
"v_mul_f32 v170, 0xbfb8aa3b, v50\n"
"v_mul_f32 v171, 0xbfb8aa3b, v51\n"
"v_mfma_f32_16x16x32_bf16 v[92:95], v[144:147], a[56:59], v[92:95]\n"
"v_exp_f32 v168, v168\n"
"v_exp_f32 v169, v169\n"
"v_mfma_f32_16x16x32_bf16 v[96:99], v[132:135], a[36:39], v[96:99]\n"
"ds_read_b128 v[124:127], v11 offset:19456\n"
"v_exp_f32 v170, v170\n"
"v_exp_f32 v171, v171\n"
"v_mfma_f32_16x16x32_bf16 v[96:99], v[136:139], a[44:47], v[96:99]\n"
"buffer_load_dwordx4 v[244:247], v1, s[4:7], s60 offen offset:3072 sc1\n"
"s_add_u32 s60, s60, 0x1000\n"
"v_add_f32 v168, 1.0, v168\n"
"v_add_f32 v169, 1.0, v169\n"
"v_add_f32 v170, 1.0, v170\n"
"v_mfma_f32_16x16x32_bf16 v[96:99], v[140:143], a[52:55], v[96:99]\n"
"ds_read_b128 v[128:131], v11 offset:26624\n"
"v_add_f32 v171, 1.0, v171\n"
"v_rcp_f32 v168, v168\n"
"v_mfma_f32_16x16x32_bf16 v[96:99], v[144:147], a[60:63], v[96:99]\n"
"v_rcp_f32 v169, v169\n"
"v_rcp_f32 v170, v170\n"
"s_waitcnt lgkmcnt(4)\n"
"v_mfma_f32_16x16x32_bf16 v[76:79], v[100:103], a[64:67], v[76:79]\n"
"ds_read_b128 v[132:135], v11 offset:6144\n"
"v_rcp_f32 v171, v171\n"
"v_mul_f32 v48, v48, v168\n"
"v_mul_f32 v49, v49, v169\n"
"v_mfma_f32_16x16x32_bf16 v[76:79], v[104:107], a[72:75], v[76:79]\n"
"v_mul_f32 v50, v50, v170\n"
"v_mul_f32 v51, v51, v171\n"
"v_cvt_pk_bf16_f32 v152, v48, v49\n"
"v_cvt_pk_bf16_f32 v153, v50, v51\n"
"v_mfma_f32_16x16x32_bf16 v[76:79], v[108:111], a[176:179], v[76:79]\n"
"ds_read_b128 v[136:139], v11 offset:13312\n"
"buffer_load_dwordx4 v[248:251], v1, s[4:7], s60 offen sc1\n"
"ds_read_b128 v[176:179], v13 offset:2048\n"
"v_mfma_f32_16x16x32_bf16 v[76:79], v[112:115], a[184:187], v[76:79]\n"
"ds_read_b128 v[180:183], v13 offset:2176\n"
"s_nop 0\n"
"v_fmac_f32 v52, v172, v184\n"
"v_fmac_f32 v53, v172, v185\n"
"v_fmac_f32 v54, v172, v186\n"
"v_mfma_f32_16x16x32_bf16 v[80:83], v[100:103], a[68:71], v[80:83]\n"
"ds_read_b128 v[140:143], v11 offset:20480\n"
"v_fmac_f32 v55, v172, v187\n"
"v_fma_f32 v52, v52, v174, v188\n"
"v_fma_f32 v53, v53, v174, v189\n"
"v_fma_f32 v54, v54, v174, v190\n"
"v_mfma_f32_16x16x32_bf16 v[80:83], v[104:107], a[76:79], v[80:83]\n"
"v_fma_f32 v55, v55, v174, v191\n"
"v_mul_f32 v164, 0xbfb8aa3b, v52\n"
"v_mul_f32 v165, 0xbfb8aa3b, v53\n"
"v_mul_f32 v166, 0xbfb8aa3b, v54\n"
"v_mul_f32 v167, 0xbfb8aa3b, v55\n"
"v_mfma_f32_16x16x32_bf16 v[80:83], v[108:111], a[180:183], v[80:83]\n"
"ds_read_b128 v[144:147], v11 offset:27648\n"
"v_exp_f32 v164, v164\n"
"v_exp_f32 v165, v165\n"
"v_mfma_f32_16x16x32_bf16 v[80:83], v[112:115], a[188:191], v[80:83]\n"
"buffer_load_dwordx4 v[252:255], v1, s[4:7], s60 offen offset:1024 sc1\n"
"v_exp_f32 v166, v166\n"
"s_waitcnt lgkmcnt(6)\n"
"v_mfma_f32_16x16x32_bf16 v[84:87], v[116:119], a[64:67], v[84:87]\n"
"ds_read_b128 v[100:103], v11 offset:32768\n"
"v_exp_f32 v167, v167\n"
"v_add_f32 v164, 1.0, v164\n"
"v_add_f32 v165, 1.0, v165\n"
"v_mfma_f32_16x16x32_bf16 v[84:87], v[120:123], a[72:75], v[84:87]\n"
"v_add_f32 v166, 1.0, v166\n"
"v_add_f32 v167, 1.0, v167\n"
"v_rcp_f32 v164, v164\n"
"v_mfma_f32_16x16x32_bf16 v[84:87], v[124:127], a[176:179], v[84:87]\n"
"ds_read_b128 v[104:107], v11 offset:39936\n"
"v_rcp_f32 v165, v165\n"
"v_rcp_f32 v166, v166\n"
"v_mfma_f32_16x16x32_bf16 v[84:87], v[128:131], a[184:187], v[84:87]\n"
"v_rcp_f32 v167, v167\n"
"v_mul_f32 v52, v52, v164\n"
"v_mul_f32 v53, v53, v165\n"
"v_mul_f32 v54, v54, v166\n"
"v_mfma_f32_16x16x32_bf16 v[88:91], v[116:119], a[68:71], v[88:91]\n"
"ds_read_b128 v[108:111], v11 offset:47104\n"
"buffer_load_dwordx4 a[0:3], v1, s[4:7], s60 offen offset:2048 sc1\n"
"v_mul_f32 v55, v55, v167\n"
"v_cvt_pk_bf16_f32 v150, v52, v53\n"
"v_cvt_pk_bf16_f32 v151, v54, v55\n"
"v_mfma_f32_16x16x32_bf16 v[88:91], v[120:123], a[76:79], v[88:91]\n"
"v_fmac_f32 v56, v173, v184\n"
"v_fmac_f32 v57, v173, v185\n"
"v_fmac_f32 v58, v173, v186\n"
"v_fmac_f32 v59, v173, v187\n"
"v_fma_f32 v56, v56, v175, v188\n"
"v_mfma_f32_16x16x32_bf16 v[88:91], v[124:127], a[180:183], v[88:91]\n"
"ds_read_b128 v[112:115], v11 offset:54272\n"
"v_fma_f32 v57, v57, v175, v189\n"
"v_fma_f32 v58, v58, v175, v190\n"
"v_fma_f32 v59, v59, v175, v191\n"
"v_mul_f32 v168, 0xbfb8aa3b, v56\n"
"v_mfma_f32_16x16x32_bf16 v[88:91], v[128:131], a[188:191], v[88:91]\n"
"v_mul_f32 v169, 0xbfb8aa3b, v57\n"
"v_mul_f32 v170, 0xbfb8aa3b, v58\n"
"v_mul_f32 v171, 0xbfb8aa3b, v59\n"
"s_waitcnt lgkmcnt(4)\n"
"v_mfma_f32_16x16x32_bf16 v[92:95], v[132:135], a[64:67], v[92:95]\n"
"ds_read_b128 v[116:119], v12 offset:4096\n"
"v_exp_f32 v168, v168\n"
"v_exp_f32 v169, v169\n"
"v_mfma_f32_16x16x32_bf16 v[92:95], v[136:139], a[72:75], v[92:95]\n"
"buffer_load_dwordx4 a[4:7], v1, s[4:7], s60 offen offset:3072 sc1\n"
"s_add_u32 s60, s60, 0x3000\n"
"v_exp_f32 v170, v170\n"
"v_mfma_f32_16x16x32_bf16 v[92:95], v[140:143], a[176:179], v[92:95]\n"
"ds_read_b128 v[120:123], v12 offset:11264\n"
"v_exp_f32 v171, v171\n"
"v_add_f32 v168, 1.0, v168\n"
"v_add_f32 v169, 1.0, v169\n"
"v_mfma_f32_16x16x32_bf16 v[92:95], v[144:147], a[184:187], v[92:95]\n"
"v_add_f32 v170, 1.0, v170\n"
"v_add_f32 v171, 1.0, v171\n"
"v_rcp_f32 v168, v168\n"
"v_mfma_f32_16x16x32_bf16 v[96:99], v[132:135], a[68:71], v[96:99]\n"
"ds_read_b128 v[124:127], v12 offset:18432\n"
"v_rcp_f32 v169, v169\n"
"v_rcp_f32 v170, v170\n"
"v_mfma_f32_16x16x32_bf16 v[96:99], v[136:139], a[76:79], v[96:99]\n"
"v_rcp_f32 v171, v171\n"
"v_mul_f32 v56, v56, v168\n"
"v_mul_f32 v57, v57, v169\n"
"v_mul_f32 v58, v58, v170\n"
"v_mfma_f32_16x16x32_bf16 v[96:99], v[140:143], a[180:183], v[96:99]\n"
"ds_read_b128 v[128:131], v12 offset:25600\n"
"buffer_load_dwordx4 a[8:11], v1, s[4:7], s60 offen sc1\n"
"v_mul_f32 v59, v59, v171\n"
"v_cvt_pk_bf16_f32 v154, v56, v57\n"
"v_cvt_pk_bf16_f32 v155, v58, v59\n"
"v_mfma_f32_16x16x32_bf16 v[96:99], v[144:147], a[188:191], v[96:99]\n"
"buffer_store_dwordx4 v[148:151], v1, s[4:7], s61 offen sc1\n"
"s_waitcnt lgkmcnt(4)\n"
"v_mfma_f32_16x16x32_bf16 v[76:79], v[100:103], a[192:195], v[76:79]\n"
"ds_read_b128 v[132:135], v11 offset:33792\n"
"buffer_store_dwordx4 v[152:155], v1, s[4:7], s61 offen offset:1024 sc1\n"
"s_add_u32 s61, s61, 0x1000\n"
"v_mfma_f32_16x16x32_bf16 v[76:79], v[104:107], a[200:203], v[76:79]\n"
"ds_read_b128 v[184:187], v13 offset:3072\n"
"v_mfma_f32_16x16x32_bf16 v[76:79], v[108:111], a[208:211], v[76:79]\n"
"ds_read_b128 v[136:139], v11 offset:40960\n"
"ds_read_b128 v[188:191], v13 offset:3200\n"
"s_nop 0\n"
"v_fmac_f32 v60, v172, v176\n"
"v_fmac_f32 v61, v172, v177\n"
"v_mfma_f32_16x16x32_bf16 v[76:79], v[112:115], a[216:219], v[76:79]\n"
"buffer_load_dwordx4 a[12:15], v1, s[4:7], s60 offen offset:1024 sc1\n"
"v_fmac_f32 v62, v172, v178\n"
"v_fmac_f32 v63, v172, v179\n"
"v_fma_f32 v60, v60, v174, v180\n"
"v_fma_f32 v61, v61, v174, v181\n"
"v_mfma_f32_16x16x32_bf16 v[80:83], v[100:103], a[196:199], v[80:83]\n"
"ds_read_b128 v[140:143], v11 offset:48128\n"
"v_fma_f32 v62, v62, v174, v182\n"
"v_fma_f32 v63, v63, v174, v183\n"
"v_mul_f32 v164, 0xbfb8aa3b, v60\n"
"v_mul_f32 v165, 0xbfb8aa3b, v61\n"
"v_mfma_f32_16x16x32_bf16 v[80:83], v[104:107], a[204:207], v[80:83]\n"
"v_mul_f32 v166, 0xbfb8aa3b, v62\n"
"v_mul_f32 v167, 0xbfb8aa3b, v63\n"
"v_exp_f32 v164, v164\n"
"v_mfma_f32_16x16x32_bf16 v[80:83], v[108:111], a[212:215], v[80:83]\n"
"ds_read_b128 v[144:147], v11 offset:55296\n"
"v_exp_f32 v165, v165\n"
"v_exp_f32 v166, v166\n"
"v_mfma_f32_16x16x32_bf16 v[80:83], v[112:115], a[220:223], v[80:83]\n"
"v_exp_f32 v167, v167\n"
"v_add_f32 v164, 1.0, v164\n"
"v_add_f32 v165, 1.0, v165\n"
"s_waitcnt lgkmcnt(6)\n"
"v_mfma_f32_16x16x32_bf16 v[76:79], v[116:119], a[224:227], v[76:79]\n"
"ds_read_b128 v[100:103], v12 offset:5120\n"
"buffer_load_dwordx4 a[16:19], v1, s[4:7], s60 offen offset:2048 sc1\n"
"v_add_f32 v166, 1.0, v166\n"
"v_add_f32 v167, 1.0, v167\n"
"v_mfma_f32_16x16x32_bf16 v[76:79], v[120:123], a[232:235], v[76:79]\n"
"v_rcp_f32 v164, v164\n"
"v_rcp_f32 v165, v165\n"
"v_mfma_f32_16x16x32_bf16 v[76:79], v[124:127], a[240:243], v[76:79]\n"
"ds_read_b128 v[104:107], v12 offset:12288\n"
"v_rcp_f32 v166, v166\n"
"v_rcp_f32 v167, v167\n"
"v_mfma_f32_16x16x32_bf16 v[76:79], v[128:131], a[248:251], v[76:79]\n"
"v_mul_f32 v60, v60, v164\n"
"v_mul_f32 v61, v61, v165\n"
"v_mul_f32 v62, v62, v166\n"
"v_mul_f32 v63, v63, v167\n"
"v_cvt_pk_bf16_f32 v156, v60, v61\n"
"v_mfma_f32_16x16x32_bf16 v[80:83], v[116:119], a[228:231], v[80:83]\n"
"ds_read_b128 v[108:111], v12 offset:19456\n"
"v_cvt_pk_bf16_f32 v157, v62, v63\n"
"v_fmac_f32 v64, v173, v176\n"
"v_fmac_f32 v65, v173, v177\n"
"v_fmac_f32 v66, v173, v178\n"
"v_mfma_f32_16x16x32_bf16 v[80:83], v[120:123], a[236:239], v[80:83]\n"
"buffer_load_dwordx4 a[20:23], v1, s[4:7], s60 offen offset:3072 sc1\n"
"s_add_u32 s60, s60, 0x1000\n"
"v_fmac_f32 v67, v173, v179\n"
"v_fma_f32 v64, v64, v175, v180\n"
"v_fma_f32 v65, v65, v175, v181\n"
"v_mfma_f32_16x16x32_bf16 v[80:83], v[124:127], a[244:247], v[80:83]\n"
"ds_read_b128 v[112:115], v12 offset:26624\n"
"v_fma_f32 v66, v66, v175, v182\n"
"v_fma_f32 v67, v67, v175, v183\n"
"v_mul_f32 v168, 0xbfb8aa3b, v64\n"
"v_mul_f32 v169, 0xbfb8aa3b, v65\n"
"v_mfma_f32_16x16x32_bf16 v[80:83], v[128:131], a[252:255], v[80:83]\n"
"v_mul_f32 v170, 0xbfb8aa3b, v66\n"
"v_mul_f32 v171, 0xbfb8aa3b, v67\n"
"v_exp_f32 v168, v168\n"
"s_waitcnt lgkmcnt(4)\n"
"v_mfma_f32_16x16x32_bf16 v[84:87], v[132:135], a[192:195], v[84:87]\n"
"ds_read_b128 v[116:119], v11 offset:34816\n"
"v_exp_f32 v169, v169\n"
"v_exp_f32 v170, v170\n"
"v_mfma_f32_16x16x32_bf16 v[84:87], v[136:139], a[200:203], v[84:87]\n"
"v_exp_f32 v171, v171\n"
"v_add_f32 v168, 1.0, v168\n"
"v_add_f32 v169, 1.0, v169\n"
"v_add_f32 v170, 1.0, v170\n"
"v_mfma_f32_16x16x32_bf16 v[84:87], v[140:143], a[208:211], v[84:87]\n"
"ds_read_b128 v[120:123], v11 offset:41984\n"
"buffer_load_dwordx4 a[24:27], v1, s[4:7], s60 offen sc1\n"
"v_add_f32 v171, 1.0, v171\n"
"v_rcp_f32 v168, v168\n"
"v_mfma_f32_16x16x32_bf16 v[84:87], v[144:147], a[216:219], v[84:87]\n"
"v_rcp_f32 v169, v169\n"
"v_rcp_f32 v170, v170\n"
"v_mfma_f32_16x16x32_bf16 v[88:91], v[132:135], a[196:199], v[88:91]\n"
"ds_read_b128 v[124:127], v11 offset:49152\n"
"v_rcp_f32 v171, v171\n"
"v_mul_f32 v64, v64, v168\n"
"v_mul_f32 v65, v65, v169\n"
"v_mfma_f32_16x16x32_bf16 v[88:91], v[136:139], a[204:207], v[88:91]\n"
"v_mul_f32 v66, v66, v170\n"
"v_mul_f32 v67, v67, v171\n"
"v_cvt_pk_bf16_f32 v160, v64, v65\n"
"v_cvt_pk_bf16_f32 v161, v66, v67\n"
"v_mfma_f32_16x16x32_bf16 v[88:91], v[140:143], a[212:215], v[88:91]\n"
"ds_read_b128 v[128:131], v11 offset:56320\n"
"ds_read_b128 v[176:179], v13 offset:4096\n"
"v_mfma_f32_16x16x32_bf16 v[88:91], v[144:147], a[220:223], v[88:91]\n"
"buffer_load_dwordx4 a[28:31], v1, s[4:7], s60 offen offset:1024 sc1\n"
"ds_read_b128 v[180:183], v13 offset:4224\n"
"s_nop 0\n"
"v_fmac_f32 v68, v172, v184\n"
"s_waitcnt lgkmcnt(6)\n"
"v_mfma_f32_16x16x32_bf16 v[84:87], v[100:103], a[224:227], v[84:87]\n"
"ds_read_b128 v[132:135], v12 offset:6144\n"
"v_fmac_f32 v69, v172, v185\n"
"v_fmac_f32 v70, v172, v186\n"
"v_fmac_f32 v71, v172, v187\n"
"v_fma_f32 v68, v68, v174, v188\n"
"v_mfma_f32_16x16x32_bf16 v[84:87], v[104:107], a[232:235], v[84:87]\n"
"v_fma_f32 v69, v69, v174, v189\n"
"v_fma_f32 v70, v70, v174, v190\n"
"v_fma_f32 v71, v71, v174, v191\n"
"v_mul_f32 v164, 0xbfb8aa3b, v68\n"
"v_mul_f32 v165, 0xbfb8aa3b, v69\n"
"v_mfma_f32_16x16x32_bf16 v[84:87], v[108:111], a[240:243], v[84:87]\n"
"ds_read_b128 v[136:139], v12 offset:13312\n"
"v_mul_f32 v166, 0xbfb8aa3b, v70\n"
"v_mul_f32 v167, 0xbfb8aa3b, v71\n"
"v_exp_f32 v164, v164\n"
"v_mfma_f32_16x16x32_bf16 v[84:87], v[112:115], a[248:251], v[84:87]\n"
"v_exp_f32 v165, v165\n"
"v_exp_f32 v166, v166\n"
"v_mfma_f32_16x16x32_bf16 v[88:91], v[100:103], a[228:231], v[88:91]\n"
"ds_read_b128 v[140:143], v12 offset:20480\n"
"buffer_load_dwordx4 a[32:35], v1, s[4:7], s60 offen offset:2048 sc1\n"
"v_exp_f32 v167, v167\n"
"v_add_f32 v164, 1.0, v164\n"
"v_mfma_f32_16x16x32_bf16 v[88:91], v[104:107], a[236:239], v[88:91]\n"
"v_add_f32 v165, 1.0, v165\n"
"v_add_f32 v166, 1.0, v166\n"
"v_add_f32 v167, 1.0, v167\n"
"v_rcp_f32 v164, v164\n"
"v_mfma_f32_16x16x32_bf16 v[88:91], v[108:111], a[244:247], v[88:91]\n"
"ds_read_b128 v[144:147], v12 offset:27648\n"
"v_rcp_f32 v165, v165\n"
"v_rcp_f32 v166, v166\n"
"v_mfma_f32_16x16x32_bf16 v[88:91], v[112:115], a[252:255], v[88:91]\n"
"v_rcp_f32 v167, v167\n"
"v_mul_f32 v68, v68, v164\n"
"v_mul_f32 v69, v69, v165\n"
"s_waitcnt lgkmcnt(6)\n"
"v_mfma_f32_16x16x32_bf16 v[92:95], v[116:119], a[192:195], v[92:95]\n"
"v_mul_f32 v70, v70, v166\n"
"v_mul_f32 v71, v71, v167\n"
"v_cvt_pk_bf16_f32 v158, v68, v69\n"
"v_cvt_pk_bf16_f32 v159, v70, v71\n"
"v_fmac_f32 v72, v173, v184\n"
"v_mfma_f32_16x16x32_bf16 v[92:95], v[120:123], a[200:203], v[92:95]\n"
"buffer_load_dwordx4 a[36:39], v1, s[4:7], s60 offen offset:3072 sc1\n"
"s_add_u32 s60, s60, 0x3000\n"
"v_fmac_f32 v73, v173, v185\n"
"v_fmac_f32 v74, v173, v186\n"
"v_fmac_f32 v75, v173, v187\n"
"v_mfma_f32_16x16x32_bf16 v[92:95], v[124:127], a[208:211], v[92:95]\n"
"v_fma_f32 v72, v72, v175, v188\n"
"v_fma_f32 v73, v73, v175, v189\n"
"v_fma_f32 v74, v74, v175, v190\n"
"v_fma_f32 v75, v75, v175, v191\n"
"v_mul_f32 v168, 0xbfb8aa3b, v72\n"
"v_mfma_f32_16x16x32_bf16 v[92:95], v[128:131], a[216:219], v[92:95]\n"
"v_mul_f32 v169, 0xbfb8aa3b, v73\n"
"v_mul_f32 v170, 0xbfb8aa3b, v74\n"
"v_mul_f32 v171, 0xbfb8aa3b, v75\n"
"v_exp_f32 v168, v168\n"
"v_mfma_f32_16x16x32_bf16 v[96:99], v[116:119], a[196:199], v[96:99]\n"
"v_exp_f32 v169, v169\n"
"v_exp_f32 v170, v170\n"
"v_mfma_f32_16x16x32_bf16 v[96:99], v[120:123], a[204:207], v[96:99]\n"
"v_exp_f32 v171, v171\n"
"v_add_f32 v168, 1.0, v168\n"
"v_add_f32 v169, 1.0, v169\n"
"v_add_f32 v170, 1.0, v170\n"
"v_mfma_f32_16x16x32_bf16 v[96:99], v[124:127], a[212:215], v[96:99]\n"
"buffer_load_dwordx4 a[40:43], v1, s[4:7], s60 offen sc1\n"
"v_add_f32 v171, 1.0, v171\n"
"v_rcp_f32 v168, v168\n"
"v_mfma_f32_16x16x32_bf16 v[96:99], v[128:131], a[220:223], v[96:99]\n"
"v_rcp_f32 v169, v169\n"
"v_rcp_f32 v170, v170\n"
"s_waitcnt lgkmcnt(0)\n"
"v_mfma_f32_16x16x32_bf16 v[92:95], v[132:135], a[224:227], v[92:95]\n"
"v_rcp_f32 v171, v171\n"
"v_mul_f32 v72, v72, v168\n"
"v_mul_f32 v73, v73, v169\n"
"v_mul_f32 v74, v74, v170\n"
"v_mfma_f32_16x16x32_bf16 v[92:95], v[136:139], a[232:235], v[92:95]\n"
"v_mul_f32 v75, v75, v171\n"
"v_cvt_pk_bf16_f32 v162, v72, v73\n"
"v_cvt_pk_bf16_f32 v163, v74, v75\n"
"v_mfma_f32_16x16x32_bf16 v[92:95], v[140:143], a[240:243], v[92:95]\n"
"buffer_store_dwordx4 v[156:159], v1, s[4:7], s61 offen sc1\n"
"v_mfma_f32_16x16x32_bf16 v[92:95], v[144:147], a[248:251], v[92:95]\n"
"buffer_store_dwordx4 v[160:163], v1, s[4:7], s61 offen offset:1024 sc1\n"
"v_mfma_f32_16x16x32_bf16 v[96:99], v[132:135], a[228:231], v[96:99]\n"
"v_mfma_f32_16x16x32_bf16 v[96:99], v[136:139], a[236:239], v[96:99]\n"
"v_mfma_f32_16x16x32_bf16 v[96:99], v[140:143], a[244:247], v[96:99]\n"
"v_mfma_f32_16x16x32_bf16 v[96:99], v[144:147], a[252:255], v[96:99]\n"
".Ladaln_block_T_F:\n"
"v_mov_b32 v172, 0xbfb8aa3b\n"
"v_mov_b32 v173, 0xbfb8aa3b\n"
"ds_read_b128 v[184:187], v13 offset:5120\n"
"ds_read_b128 v[188:191], v13 offset:5248\n"
"s_nop 0\n"
"v_pk_fma_f32 v[76:77], v[176:177], v[192:193], v[76:77]\n"
"v_pk_fma_f32 v[78:79], v[178:179], v[192:193], v[78:79]\n"
"v_pk_fma_f32 v[76:77], v[76:77], v[196:197], v[180:181]\n"
"v_pk_fma_f32 v[78:79], v[78:79], v[196:197], v[182:183]\n"
"v_pk_mul_f32 v[164:165], v[76:77], v[172:173]\n"
"v_pk_mul_f32 v[166:167], v[78:79], v[172:173]\n"
"v_exp_f32 v164, v164\n"
"v_exp_f32 v165, v165\n"
"v_exp_f32 v166, v166\n"
"v_exp_f32 v167, v167\n"
"v_pk_add_f32 v[164:165], v[164:165], v[16:17]\n"
"v_pk_add_f32 v[166:167], v[166:167], v[16:17]\n"
"v_rcp_f32 v164, v164\n"
"v_rcp_f32 v165, v165\n"
"v_rcp_f32 v166, v166\n"
"v_rcp_f32 v167, v167\n"
"v_pk_mul_f32 v[76:77], v[76:77], v[164:165]\n"
"v_pk_mul_f32 v[78:79], v[78:79], v[166:167]\n"
"v_cvt_pk_bf16_f32 v148, v76, v77\n"
"v_cvt_pk_bf16_f32 v149, v78, v79\n"
"v_pk_fma_f32 v[80:81], v[176:177], v[194:195], v[80:81]\n"
"v_pk_fma_f32 v[82:83], v[178:179], v[194:195], v[82:83]\n"
"v_pk_fma_f32 v[80:81], v[80:81], v[198:199], v[180:181]\n"
"v_pk_fma_f32 v[82:83], v[82:83], v[198:199], v[182:183]\n"
"v_pk_mul_f32 v[168:169], v[80:81], v[172:173]\n"
"v_pk_mul_f32 v[170:171], v[82:83], v[172:173]\n"
"v_exp_f32 v168, v168\n"
"v_exp_f32 v169, v169\n"
"v_exp_f32 v170, v170\n"
"v_exp_f32 v171, v171\n"
"v_pk_add_f32 v[168:169], v[168:169], v[16:17]\n"
"v_pk_add_f32 v[170:171], v[170:171], v[16:17]\n"
"v_rcp_f32 v168, v168\n"
"v_rcp_f32 v169, v169\n"
"v_rcp_f32 v170, v170\n"
"v_rcp_f32 v171, v171\n"
"v_pk_mul_f32 v[80:81], v[80:81], v[168:169]\n"
"v_pk_mul_f32 v[82:83], v[82:83], v[170:171]\n"
"v_cvt_pk_bf16_f32 v152, v80, v81\n"
"v_cvt_pk_bf16_f32 v153, v82, v83\n"
"ds_read_b128 v[176:179], v13 offset:6144\n"
"ds_read_b128 v[180:183], v13 offset:6272\n"
"s_waitcnt lgkmcnt(2)\n"
"v_pk_fma_f32 v[84:85], v[184:185], v[192:193], v[84:85]\n"
"v_pk_fma_f32 v[86:87], v[186:187], v[192:193], v[86:87]\n"
"v_pk_fma_f32 v[84:85], v[84:85], v[196:197], v[188:189]\n"
"v_pk_fma_f32 v[86:87], v[86:87], v[196:197], v[190:191]\n"
"v_pk_mul_f32 v[164:165], v[84:85], v[172:173]\n"
"v_pk_mul_f32 v[166:167], v[86:87], v[172:173]\n"
"v_exp_f32 v164, v164\n"
"v_exp_f32 v165, v165\n"
"v_exp_f32 v166, v166\n"
"v_exp_f32 v167, v167\n"
"v_pk_add_f32 v[164:165], v[164:165], v[16:17]\n"
"v_pk_add_f32 v[166:167], v[166:167], v[16:17]\n"
"s_waitcnt vmcnt(0)\n"
"s_barrier\n"
"buffer_store_dword v22, v23, s[4:7], s69 offen sc1\n"
"s_add_u32 s69, s69, 0xfffe8000\n"
"v_rcp_f32 v164, v164\n"
"v_rcp_f32 v165, v165\n"
"v_rcp_f32 v166, v166\n"
"v_rcp_f32 v167, v167\n"
"v_pk_mul_f32 v[84:85], v[84:85], v[164:165]\n"
"v_pk_mul_f32 v[86:87], v[86:87], v[166:167]\n"
"v_cvt_pk_bf16_f32 v150, v84, v85\n"
"v_cvt_pk_bf16_f32 v151, v86, v87\n"
"v_pk_fma_f32 v[88:89], v[184:185], v[194:195], v[88:89]\n"
"v_pk_fma_f32 v[90:91], v[186:187], v[194:195], v[90:91]\n"
"v_pk_fma_f32 v[88:89], v[88:89], v[198:199], v[188:189]\n"
"v_pk_fma_f32 v[90:91], v[90:91], v[198:199], v[190:191]\n"
"v_pk_mul_f32 v[168:169], v[88:89], v[172:173]\n"
"v_pk_mul_f32 v[170:171], v[90:91], v[172:173]\n"
"v_exp_f32 v168, v168\n"
"v_exp_f32 v169, v169\n"
"v_exp_f32 v170, v170\n"
"v_exp_f32 v171, v171\n"
"v_pk_add_f32 v[168:169], v[168:169], v[16:17]\n"
"v_pk_add_f32 v[170:171], v[170:171], v[16:17]\n"
"v_rcp_f32 v168, v168\n"
"v_rcp_f32 v169, v169\n"
"v_rcp_f32 v170, v170\n"
"v_rcp_f32 v171, v171\n"
"v_pk_mul_f32 v[88:89], v[88:89], v[168:169]\n"
"v_pk_mul_f32 v[90:91], v[90:91], v[170:171]\n"
"v_cvt_pk_bf16_f32 v154, v88, v89\n"
"v_cvt_pk_bf16_f32 v155, v90, v91\n"
"s_nop 0\n"
"buffer_store_dwordx4 v[148:151], v1, s[4:7], s66 offen sc1\n"
"buffer_store_dwordx4 v[152:155], v1, s[4:7], s66 offen offset:1024 sc1\n"
"s_waitcnt lgkmcnt(0)\n"
"v_pk_fma_f32 v[92:93], v[176:177], v[192:193], v[92:93]\n"
"v_pk_fma_f32 v[94:95], v[178:179], v[192:193], v[94:95]\n"
"v_pk_fma_f32 v[92:93], v[92:93], v[196:197], v[180:181]\n"
"v_pk_fma_f32 v[94:95], v[94:95], v[196:197], v[182:183]\n"
"v_pk_mul_f32 v[164:165], v[92:93], v[172:173]\n"
"v_pk_mul_f32 v[166:167], v[94:95], v[172:173]\n"
"v_exp_f32 v164, v164\n"
"v_exp_f32 v165, v165\n"
"v_exp_f32 v166, v166\n"
"v_exp_f32 v167, v167\n"
"v_pk_add_f32 v[164:165], v[164:165], v[16:17]\n"
"v_pk_add_f32 v[166:167], v[166:167], v[16:17]\n"
"v_rcp_f32 v164, v164\n"
"v_rcp_f32 v165, v165\n"
"v_rcp_f32 v166, v166\n"
"v_rcp_f32 v167, v167\n"
"v_pk_mul_f32 v[92:93], v[92:93], v[164:165]\n"
"v_pk_mul_f32 v[94:95], v[94:95], v[166:167]\n"
"v_cvt_pk_bf16_f32 v156, v92, v93\n"
"v_cvt_pk_bf16_f32 v157, v94, v95\n"
"v_pk_fma_f32 v[96:97], v[176:177], v[194:195], v[96:97]\n"
"v_pk_fma_f32 v[98:99], v[178:179], v[194:195], v[98:99]\n"
"v_pk_fma_f32 v[96:97], v[96:97], v[198:199], v[180:181]\n"
"v_pk_fma_f32 v[98:99], v[98:99], v[198:199], v[182:183]\n"
"v_pk_mul_f32 v[168:169], v[96:97], v[172:173]\n"
"v_pk_mul_f32 v[170:171], v[98:99], v[172:173]\n"
"v_exp_f32 v168, v168\n"
"v_exp_f32 v169, v169\n"
"v_exp_f32 v170, v170\n"
"v_exp_f32 v171, v171\n"
"v_pk_add_f32 v[168:169], v[168:169], v[16:17]\n"
"v_pk_add_f32 v[170:171], v[170:171], v[16:17]\n"
"v_rcp_f32 v168, v168\n"
"v_rcp_f32 v169, v169\n"
"v_rcp_f32 v170, v170\n"
"v_rcp_f32 v171, v171\n"
"v_pk_mul_f32 v[96:97], v[96:97], v[168:169]\n"
"v_pk_mul_f32 v[98:99], v[98:99], v[170:171]\n"
"v_cvt_pk_bf16_f32 v160, v96, v97\n"
"v_cvt_pk_bf16_f32 v161, v98, v99\n"
"s_nop 0\n"
"buffer_store_dwordx2 v[156:157], v19, s[4:7], s58 offen sc1\n"
"buffer_store_dwordx2 v[160:161], v19, s[4:7], s58 offen offset:1024 sc1\n"
"v_mul_u32_u24 v14, 0xccd, v0\n"
"v_lshrrev_b32 v14, 16, v14\n"
"v_mul_u32_u24 v16, 20, v14\n"
"v_sub_u32 v16, v0, v16\n"
"s_and_b32 s58, s51, 1\n"
"v_lshl_add_u32 v16, s58, 2, v16\n"
"v_subrev_u32 v17, 20, v16\n"
"v_min_u32 v16, v16, v17\n"
"v_min_u32 v14, 0, v14\n"
"v_mul_u32_u24 v14, 0, v14\n"
"v_lshl_add_u32 v14, v16, 4, v14\n"
"s_mul_i32 s56, s51, 0x140\n"
"s_add_u32 s56, s56, 0x23200\n"
"s_lshl_b32 s57, s3, 14\n"
"s_add_u32 s57, s57, 0x1bc00\n"
"v_add_u32 v16, s57, v15\n"
"v_and_b32 v18, 3, v0\n"
"s_and_b32 s65, s3, 3\n"
"v_lshl_add_u32 v18, s65, 2, v18\n"
"v_lshlrev_b32 v18, 7, v18\n"
"s_add_u32 s58, s70, 0xfffe8000\n"
"s_lshr_b32 s61, s50, 1\n"
"s_lshl_b32 s61, s61, 19\n"
"s_add_u32 s61, s61, 0x1000000\n"
"s_lshr_b32 s72, s61, 8\n"
"s_and_b32 s72, s72, 0x2000\n"
"s_xor_b32 s61, s61, s72\n"
"s_xor_b32 s61, s61, s101\n"
"s_mul_i32 s55, s3, 0x18000\n"
"s_add_u32 s55, s55, s61\n"
"s_lshl_b32 s65, s3, 14\n"
"s_add_u32 s65, s65, s61\n"
"s_add_u32 s65, s65, 0x60000\n"
"s_cmp_eq_u32 s3, 3\n"
"s_cmov_b32 s65, 0x40000000\n"
"v_sub_u32 v10, v2, v19\n"
"v_sub_u32 v11, v3, v19\n"
"v_sub_u32 v12, v4, v19\n"
"v_sub_u32 v13, v5, v19\n"
"s_mov_b32 s8, s44\n"
"s_and_b32 s9, s45, 0xffff\n"
"s_mov_b32 s10, 0x280000\n"
"s_mov_b32 s11, 0x20000\n"
"buffer_load_dwordx4 a[44:47], v1, s[4:7], s60 offen offset:1024 sc1\n"
"buffer_load_dwordx4 a[48:51], v1, s[4:7], s60 offen offset:2048 sc1\n"
"buffer_load_dwordx4 a[52:55], v1, s[4:7], s60 offen offset:3072 sc1\n"
"s_add_u32 s60, s60, 0x1000\n"
"buffer_load_dwordx4 a[56:59], v1, s[4:7], s60 offen sc1\n"
"buffer_load_dwordx4 a[60:63], v1, s[4:7], s60 offen offset:1024 sc1\n"
"buffer_load_dwordx4 a[64:67], v1, s[4:7], s60 offen offset:2048 sc1\n"
"buffer_load_dwordx4 a[68:71], v1, s[4:7], s60 offen offset:3072 sc1\n"
"s_add_u32 s60, s60, 0x3000\n"
"buffer_load_dwordx4 a[72:75], v1, s[4:7], s60 offen sc1\n"
"buffer_load_dwordx4 a[76:79], v1, s[4:7], s60 offen offset:1024 sc1\n"
"buffer_load_dwordx4 v[100:103], v1, s[4:7], s60 offen offset:2048 sc1\n"
"buffer_load_dwordx4 v[104:107], v1, s[4:7], s60 offen offset:3072 sc1\n"
"s_mov_b32 s60, s64\n"
"buffer_load_dwordx4 v[108:111], v1, s[4:7], s60 offen sc1\n"
"buffer_load_dwordx4 v[112:115], v1, s[4:7], s60 offen offset:1024 sc1\n"
"buffer_load_dwordx4 v[116:119], v1, s[4:7], s60 offen offset:2048 sc1\n"
"buffer_load_dwordx4 v[120:123], v1, s[4:7], s60 offen offset:3072 sc1\n"
"s_add_u32 s60, s60, 0x3000\n"
"buffer_load_dwordx4 v[124:127], v1, s[4:7], s60 offen sc1\n"
"buffer_load_dwordx4 v[128:131], v1, s[4:7], s60 offen offset:1024 sc1\n"
"buffer_load_dwordx4 v[132:135], v1, s[4:7], s60 offen offset:2048 sc1\n"
"buffer_load_dwordx4 v[136:139], v1, s[4:7], s60 offen offset:3072 sc1\n"
"s_add_u32 s60, s60, 0x1000\n"
"buffer_load_dwordx4 v[140:143], v1, s[4:7], s60 offen sc1\n"
"buffer_load_dwordx4 v[144:147], v1, s[4:7], s60 offen offset:1024 sc1\n"
"buffer_load_dword v20, v18, s[4:7], s70 offen sc1\n"
"s_waitcnt vmcnt(0)\n"
"s_barrier\n"
"buffer_store_dword v22, v23, s[4:7], s69 offen sc1\n"
"v_cmp_ne_u32 vcc, s82, v20\n"
"s_nop 0\n"
"s_cbranch_vccz .Ladaln_block_P_fa_R\n"
"s_mov_b32 s83, 0\n"
".Ladaln_block_P_fa:\n"
"buffer_load_dword v20, v18, s[4:7], s70 offen sc1\n"
"s_add_u32 s83, s83, 1\n"
"s_waitcnt vmcnt(0)\n"
"v_cmp_ne_u32 vcc, s82, v20\n"
"s_cmp_lt_u32 s83, 0x20000\n"
"s_cbranch_vccz .Ladaln_block_P_fa_R\n"
"s_sleep 0\n"
"s_cbranch_scc1 .Ladaln_block_P_fa\n"
".Ladaln_block_P_fa_R:\n"
"buffer_load_dwordx4 v[64:67], v2, s[4:7], s55 offen sc1\n"
"buffer_load_dwordx4 v[68:71], v3, s[4:7], s55 offen sc1\n"
"buffer_load_dwordx4 v[72:75], v4, s[4:7], s55 offen sc1\n"
"buffer_load_dwordx4 v[76:79], v5, s[4:7], s55 offen sc1\n"
"s_add_u32 s55, s55, 0x1000\n"
"buffer_load_dwordx4 v[80:83], v2, s[4:7], s55 offen sc1\n"
"buffer_load_dwordx4 v[84:87], v3, s[4:7], s55 offen sc1\n"
"buffer_load_dwordx4 v[88:91], v4, s[4:7], s55 offen sc1\n"
"buffer_load_dwordx4 v[92:95], v5, s[4:7], s55 offen sc1\n"
"s_add_u32 s55, s55, 0x3000\n"
"buffer_load_dwordx4 v[96:99], v2, s[4:7], s55 offen sc1\n"
"buffer_load_dwordx4 v[148:151], v3, s[4:7], s55 offen sc1\n"
"buffer_load_dwordx4 v[152:155], v4, s[4:7], s55 offen sc1\n"
"buffer_load_dwordx4 v[156:159], v5, s[4:7], s55 offen sc1\n"
"s_add_u32 s55, s55, 0x1000\n"
"s_waitcnt vmcnt(8)\n"
"v_mfma_f32_16x16x32_bf16 a[176:179], a[80:83], v[64:67], 0\n"
"buffer_load_dwordx4 v[160:163], v2, s[4:7], s55 offen sc1\n"
"v_mfma_f32_16x16x32_bf16 a[180:183], a[80:83], v[68:71], 0\n"
"v_mfma_f32_16x16x32_bf16 a[184:187], a[80:83], v[72:75], 0\n"
"v_mfma_f32_16x16x32_bf16 a[188:191], a[80:83], v[76:79], 0\n"
"v_mfma_f32_16x16x32_bf16 a[192:195], a[84:87], v[64:67], 0\n"
"buffer_load_dwordx4 v[164:167], v3, s[4:7], s55 offen sc1\n"
"v_mfma_f32_16x16x32_bf16 a[196:199], a[84:87], v[68:71], 0\n"
"v_mfma_f32_16x16x32_bf16 a[200:203], a[84:87], v[72:75], 0\n"
"v_mfma_f32_16x16x32_bf16 a[204:207], a[84:87], v[76:79], 0\n"
"v_mfma_f32_16x16x32_bf16 a[208:211], a[88:91], v[64:67], 0\n"
"v_mfma_f32_16x16x32_bf16 a[212:215], a[88:91], v[68:71], 0\n"
"buffer_load_dwordx4 v[168:171], v4, s[4:7], s55 offen sc1\n"
"v_mfma_f32_16x16x32_bf16 a[216:219], a[88:91], v[72:75], 0\n"
"v_mfma_f32_16x16x32_bf16 a[220:223], a[88:91], v[76:79], 0\n"
"v_mfma_f32_16x16x32_bf16 a[224:227], a[92:95], v[64:67], 0\n"
"v_mfma_f32_16x16x32_bf16 a[228:231], a[92:95], v[68:71], 0\n"
"v_mfma_f32_16x16x32_bf16 a[232:235], a[92:95], v[72:75], 0\n"
"buffer_load_dwordx4 v[172:175], v5, s[4:7], s55 offen sc1\n"
"s_add_u32 s55, s55, 0x3000\n"
"v_mfma_f32_16x16x32_bf16 a[236:239], a[92:95], v[76:79], 0\n"
"v_mfma_f32_16x16x32_bf16 a[240:243], a[96:99], v[64:67], 0\n"
"v_mfma_f32_16x16x32_bf16 a[244:247], a[96:99], v[68:71], 0\n"
"v_mfma_f32_16x16x32_bf16 a[248:251], a[96:99], v[72:75], 0\n"
"v_mfma_f32_16x16x32_bf16 a[252:255], a[96:99], v[76:79], 0\n"
"buffer_load_dwordx4 v[176:179], v2, s[4:7], s55 offen sc1\n"
"s_waitcnt vmcnt(9)\n"
"v_mfma_f32_16x16x32_bf16 a[176:179], a[100:103], v[80:83], a[176:179]\n"
"v_mfma_f32_16x16x32_bf16 a[180:183], a[100:103], v[84:87], a[180:183]\n"
"v_mfma_f32_16x16x32_bf16 a[184:187], a[100:103], v[88:91], a[184:187]\n"
"v_mfma_f32_16x16x32_bf16 a[188:191], a[100:103], v[92:95], a[188:191]\n"
"buffer_load_dwordx4 v[180:183], v3, s[4:7], s55 offen sc1\n"
"v_mfma_f32_16x16x32_bf16 a[192:195], a[104:107], v[80:83], a[192:195]\n"
"v_mfma_f32_16x16x32_bf16 a[196:199], a[104:107], v[84:87], a[196:199]\n"
"v_mfma_f32_16x16x32_bf16 a[200:203], a[104:107], v[88:91], a[200:203]\n"
"v_mfma_f32_16x16x32_bf16 a[204:207], a[104:107], v[92:95], a[204:207]\n"
"v_mfma_f32_16x16x32_bf16 a[208:211], a[108:111], v[80:83], a[208:211]\n"
"buffer_load_dwordx4 v[184:187], v4, s[4:7], s55 offen sc1\n"
"v_mfma_f32_16x16x32_bf16 a[212:215], a[108:111], v[84:87], a[212:215]\n"
"v_mfma_f32_16x16x32_bf16 a[216:219], a[108:111], v[88:91], a[216:219]\n"
"v_mfma_f32_16x16x32_bf16 a[220:223], a[108:111], v[92:95], a[220:223]\n"
"v_mfma_f32_16x16x32_bf16 a[224:227], a[112:115], v[80:83], a[224:227]\n"
"v_mfma_f32_16x16x32_bf16 a[228:231], a[112:115], v[84:87], a[228:231]\n"
"buffer_load_dwordx4 v[188:191], v5, s[4:7], s55 offen sc1\n"
"s_add_u32 s55, s55, 0x1000\n"
"v_mfma_f32_16x16x32_bf16 a[232:235], a[112:115], v[88:91], a[232:235]\n"
"v_mfma_f32_16x16x32_bf16 a[236:239], a[112:115], v[92:95], a[236:239]\n"
"v_mfma_f32_16x16x32_bf16 a[240:243], a[116:119], v[80:83], a[240:243]\n"
"v_mfma_f32_16x16x32_bf16 a[244:247], a[116:119], v[84:87], a[244:247]\n"
"v_mfma_f32_16x16x32_bf16 a[248:251], a[116:119], v[88:91], a[248:251]\n"
"v_mfma_f32_16x16x32_bf16 a[252:255], a[116:119], v[92:95], a[252:255]\n"
"s_waitcnt vmcnt(4)\n"
"v_mfma_f32_16x16x32_bf16 a[176:179], a[120:123], v[96:99], a[176:179]\n"
"buffer_load_dwordx4 v[192:195], v2, s[4:7], s55 offen sc1\n"
"v_mfma_f32_16x16x32_bf16 a[176:179], a[140:143], v[160:163], a[176:179]\n"
"v_mfma_f32_16x16x32_bf16 a[180:183], a[120:123], v[148:151], a[180:183]\n"
"v_mfma_f32_16x16x32_bf16 a[180:183], a[140:143], v[164:167], a[180:183]\n"
"buffer_load_dwordx4 v[196:199], v3, s[4:7], s55 offen sc1\n"
"v_mfma_f32_16x16x32_bf16 a[184:187], a[120:123], v[152:155], a[184:187]\n"
"v_mfma_f32_16x16x32_bf16 a[184:187], a[140:143], v[168:171], a[184:187]\n"
"v_mfma_f32_16x16x32_bf16 a[188:191], a[120:123], v[156:159], a[188:191]\n"
"buffer_load_dwordx4 v[64:67], v4, s[4:7], s55 offen sc1\n"
"v_mfma_f32_16x16x32_bf16 a[188:191], a[140:143], v[172:175], a[188:191]\n"
"v_mfma_f32_16x16x32_bf16 a[192:195], a[124:127], v[96:99], a[192:195]\n"
"v_mfma_f32_16x16x32_bf16 a[192:195], a[144:147], v[160:163], a[192:195]\n"
"buffer_load_dwordx4 v[68:71], v5, s[4:7], s55 offen sc1\n"
"s_add_u32 s55, s55, 0x3000\n"
"v_mfma_f32_16x16x32_bf16 a[196:199], a[124:127], v[148:151], a[196:199]\n"
"v_mfma_f32_16x16x32_bf16 a[196:199], a[144:147], v[164:167], a[196:199]\n"
"v_mfma_f32_16x16x32_bf16 a[200:203], a[124:127], v[152:155], a[200:203]\n"
"buffer_load_dwordx4 v[72:75], v2, s[4:7], s55 offen sc1\n"
"v_mfma_f32_16x16x32_bf16 a[200:203], a[144:147], v[168:171], a[200:203]\n"
"v_mfma_f32_16x16x32_bf16 a[204:207], a[124:127], v[156:159], a[204:207]\n"
"v_mfma_f32_16x16x32_bf16 a[204:207], a[144:147], v[172:175], a[204:207]\n"
"buffer_load_dwordx4 v[76:79], v3, s[4:7], s55 offen sc1\n"
"v_mfma_f32_16x16x32_bf16 a[208:211], a[128:131], v[96:99], a[208:211]\n"
"v_mfma_f32_16x16x32_bf16 a[208:211], a[148:151], v[160:163], a[208:211]\n"
"v_mfma_f32_16x16x32_bf16 a[212:215], a[128:131], v[148:151], a[212:215]\n"
"v_mfma_f32_16x16x32_bf16 a[212:215], a[148:151], v[164:167], a[212:215]\n"
"buffer_load_dwordx4 v[80:83], v4, s[4:7], s55 offen sc1\n"
"v_mfma_f32_16x16x32_bf16 a[216:219], a[128:131], v[152:155], a[216:219]\n"
"buffer_load_dword v20, v18, s[4:7], s58 offen sc1\n"
"v_mfma_f32_16x16x32_bf16 a[216:219], a[148:151], v[168:171], a[216:219]\n"
"v_mfma_f32_16x16x32_bf16 a[220:223], a[128:131], v[156:159], a[220:223]\n"
"buffer_load_dwordx4 v[84:87], v5, s[4:7], s55 offen sc1\n"
"s_add_u32 s55, s55, 0x1000\n"
"v_mfma_f32_16x16x32_bf16 a[220:223], a[148:151], v[172:175], a[220:223]\n"
"v_mfma_f32_16x16x32_bf16 a[224:227], a[132:135], v[96:99], a[224:227]\n"
"v_mfma_f32_16x16x32_bf16 a[224:227], a[152:155], v[160:163], a[224:227]\n"
"buffer_load_dwordx4 v[88:91], v2, s[4:7], s55 offen sc1\n"
"v_mfma_f32_16x16x32_bf16 a[228:231], a[132:135], v[148:151], a[228:231]\n"
"v_mfma_f32_16x16x32_bf16 a[228:231], a[152:155], v[164:167], a[228:231]\n"
"v_mfma_f32_16x16x32_bf16 a[232:235], a[132:135], v[152:155], a[232:235]\n"
"buffer_load_dwordx4 v[92:95], v3, s[4:7], s55 offen sc1\n"
"v_mfma_f32_16x16x32_bf16 a[232:235], a[152:155], v[168:171], a[232:235]\n"
"v_mfma_f32_16x16x32_bf16 a[236:239], a[132:135], v[156:159], a[236:239]\n"
"v_mfma_f32_16x16x32_bf16 a[236:239], a[152:155], v[172:175], a[236:239]\n"
"buffer_load_dwordx4 a[80:83], v4, s[4:7], s55 offen sc1\n"
"v_mfma_f32_16x16x32_bf16 a[240:243], a[136:139], v[96:99], a[240:243]\n"
"v_mfma_f32_16x16x32_bf16 a[240:243], a[156:159], v[160:163], a[240:243]\n"
"v_mfma_f32_16x16x32_bf16 a[244:247], a[136:139], v[148:151], a[244:247]\n"
"buffer_load_dwordx4 a[84:87], v5, s[4:7], s55 offen sc1\n"
"s_add_u32 s55, s55, 0x3000\n"
"v_mfma_f32_16x16x32_bf16 a[244:247], a[156:159], v[164:167], a[244:247]\n"
"v_mfma_f32_16x16x32_bf16 a[248:251], a[136:139], v[152:155], a[248:251]\n"
"v_mfma_f32_16x16x32_bf16 a[248:251], a[156:159], v[168:171], a[248:251]\n"
"v_mfma_f32_16x16x32_bf16 a[252:255], a[136:139], v[156:159], a[252:255]\n"
"v_mfma_f32_16x16x32_bf16 a[252:255], a[156:159], v[172:175], a[252:255]\n"
"s_waitcnt vmcnt(9)\n"
"s_waitcnt vmcnt(5)\n"
"v_cmp_ne_u32 vcc, s82, v20\n"
"s_nop 0\n"
"s_cbranch_vccnz .Ladaln_block_P_f\n"
".Ladaln_block_P_f_R:\n"
"v_mfma_f32_16x16x32_bf16 a[176:179], a[160:163], v[176:179], a[176:179]\n"
"buffer_load_dwordx4 a[88:91], v2, s[4:7], s55 offen sc1\n"
"v_mfma_f32_16x16x32_bf16 a[176:179], v[204:207], v[192:195], a[176:179]\n"
"v_mfma_f32_16x16x32_bf16 a[180:183], a[160:163], v[180:183], a[180:183]\n"
"v_mfma_f32_16x16x32_bf16 a[180:183], v[204:207], v[196:199], a[180:183]\n"
"v_mfma_f32_16x16x32_bf16 a[184:187], a[160:163], v[184:187], a[184:187]\n"
"buffer_load_dwordx4 a[92:95], v3, s[4:7], s55 offen sc1\n"
"v_mfma_f32_16x16x32_bf16 a[184:187], v[204:207], v[64:67], a[184:187]\n"
"v_mfma_f32_16x16x32_bf16 a[188:191], a[160:163], v[188:191], a[188:191]\n"
"v_mfma_f32_16x16x32_bf16 a[188:191], v[204:207], v[68:71], a[188:191]\n"
"v_mfma_f32_16x16x32_bf16 a[192:195], a[164:167], v[176:179], a[192:195]\n"
"v_mfma_f32_16x16x32_bf16 a[192:195], v[208:211], v[192:195], a[192:195]\n"
"buffer_load_dwordx4 a[96:99], v4, s[4:7], s55 offen sc1\n"
"v_mfma_f32_16x16x32_bf16 a[196:199], a[164:167], v[180:183], a[196:199]\n"
"v_mfma_f32_16x16x32_bf16 a[196:199], v[208:211], v[196:199], a[196:199]\n"
"v_mfma_f32_16x16x32_bf16 a[200:203], a[164:167], v[184:187], a[200:203]\n"
"v_mfma_f32_16x16x32_bf16 a[200:203], v[208:211], v[64:67], a[200:203]\n"
"v_mfma_f32_16x16x32_bf16 a[204:207], a[164:167], v[188:191], a[204:207]\n"
"buffer_load_dwordx4 a[100:103], v5, s[4:7], s55 offen sc1\n"
"s_add_u32 s55, s55, 0x1000\n"
"v_mfma_f32_16x16x32_bf16 a[204:207], v[208:211], v[68:71], a[204:207]\n"
"v_mfma_f32_16x16x32_bf16 a[208:211], a[168:171], v[176:179], a[208:211]\n"
"v_mfma_f32_16x16x32_bf16 a[208:211], v[212:215], v[192:195], a[208:211]\n"
"v_mfma_f32_16x16x32_bf16 a[212:215], a[168:171], v[180:183], a[212:215]\n"
"v_mfma_f32_16x16x32_bf16 a[212:215], v[212:215], v[196:199], a[212:215]\n"
"buffer_load_dwordx4 a[104:107], v2, s[4:7], s55 offen sc1\n"
"v_mfma_f32_16x16x32_bf16 a[216:219], a[168:171], v[184:187], a[216:219]\n"
"v_mfma_f32_16x16x32_bf16 a[216:219], v[212:215], v[64:67], a[216:219]\n"
"v_mfma_f32_16x16x32_bf16 a[220:223], a[168:171], v[188:191], a[220:223]\n"
"v_mfma_f32_16x16x32_bf16 a[220:223], v[212:215], v[68:71], a[220:223]\n"
"buffer_load_dwordx4 a[108:111], v3, s[4:7], s55 offen sc1\n"
"v_mfma_f32_16x16x32_bf16 a[224:227], a[172:175], v[176:179], a[224:227]\n"
"v_mfma_f32_16x16x32_bf16 a[224:227], v[216:219], v[192:195], a[224:227]\n"
"v_mfma_f32_16x16x32_bf16 a[228:231], a[172:175], v[180:183], a[228:231]\n"
"v_mfma_f32_16x16x32_bf16 a[228:231], v[216:219], v[196:199], a[228:231]\n"
"v_mfma_f32_16x16x32_bf16 a[232:235], a[172:175], v[184:187], a[232:235]\n"
"buffer_load_dwordx4 a[112:115], v4, s[4:7], s55 offen sc1\n"
"v_mfma_f32_16x16x32_bf16 a[232:235], v[216:219], v[64:67], a[232:235]\n"
"v_mfma_f32_16x16x32_bf16 a[236:239], a[172:175], v[188:191], a[236:239]\n"
"v_mfma_f32_16x16x32_bf16 a[236:239], v[216:219], v[68:71], a[236:239]\n"
"v_mfma_f32_16x16x32_bf16 a[240:243], v[200:203], v[176:179], a[240:243]\n"
"v_mfma_f32_16x16x32_bf16 a[240:243], v[220:223], v[192:195], a[240:243]\n"
"buffer_load_dwordx4 a[116:119], v5, s[4:7], s55 offen sc1\n"
"s_add_u32 s55, s55, 0x3000\n"
"v_mfma_f32_16x16x32_bf16 a[244:247], v[200:203], v[180:183], a[244:247]\n"
"v_mfma_f32_16x16x32_bf16 a[244:247], v[220:223], v[196:199], a[244:247]\n"
"v_mfma_f32_16x16x32_bf16 a[248:251], v[200:203], v[184:187], a[248:251]\n"
"v_mfma_f32_16x16x32_bf16 a[248:251], v[220:223], v[64:67], a[248:251]\n"
"v_mfma_f32_16x16x32_bf16 a[252:255], v[200:203], v[188:191], a[252:255]\n"
"v_mfma_f32_16x16x32_bf16 a[252:255], v[220:223], v[68:71], a[252:255]\n"
"s_waitcnt vmcnt(8)\n"
"v_mfma_f32_16x16x32_bf16 a[176:179], v[224:227], v[72:75], a[176:179]\n"
"buffer_load_dwordx4 v[96:99], v2, s[4:7], s55 offen sc1\n"
"v_mfma_f32_16x16x32_bf16 a[176:179], v[244:247], v[88:91], a[176:179]\n"
"v_mfma_f32_16x16x32_bf16 a[180:183], v[224:227], v[76:79], a[180:183]\n"
"v_mfma_f32_16x16x32_bf16 a[180:183], v[244:247], v[92:95], a[180:183]\n"
"v_mfma_f32_16x16x32_bf16 a[184:187], v[224:227], v[80:83], a[184:187]\n"
"buffer_load_dwordx4 v[148:151], v3, s[4:7], s55 offen sc1\n"
"v_mfma_f32_16x16x32_bf16 a[184:187], v[244:247], a[80:83], a[184:187]\n"
"v_mfma_f32_16x16x32_bf16 a[188:191], v[224:227], v[84:87], a[188:191]\n"
"v_mfma_f32_16x16x32_bf16 a[188:191], v[244:247], a[84:87], a[188:191]\n"
"v_mfma_f32_16x16x32_bf16 a[192:195], v[228:231], v[72:75], a[192:195]\n"
"v_mfma_f32_16x16x32_bf16 a[192:195], v[248:251], v[88:91], a[192:195]\n"
"buffer_load_dwordx4 v[152:155], v4, s[4:7], s55 offen sc1\n"
"v_mfma_f32_16x16x32_bf16 a[196:199], v[228:231], v[76:79], a[196:199]\n"
"v_mfma_f32_16x16x32_bf16 a[196:199], v[248:251], v[92:95], a[196:199]\n"
"v_mfma_f32_16x16x32_bf16 a[200:203], v[228:231], v[80:83], a[200:203]\n"
"v_mfma_f32_16x16x32_bf16 a[200:203], v[248:251], a[80:83], a[200:203]\n"
"v_mfma_f32_16x16x32_bf16 a[204:207], v[228:231], v[84:87], a[204:207]\n"
"buffer_load_dwordx4 v[156:159], v5, s[4:7], s55 offen sc1\n"
"s_add_u32 s55, s55, 0x1000\n"
"v_mfma_f32_16x16x32_bf16 a[204:207], v[248:251], a[84:87], a[204:207]\n"
"v_mfma_f32_16x16x32_bf16 a[208:211], v[232:235], v[72:75], a[208:211]\n"
"v_mfma_f32_16x16x32_bf16 a[208:211], v[252:255], v[88:91], a[208:211]\n"
"v_mfma_f32_16x16x32_bf16 a[212:215], v[232:235], v[76:79], a[212:215]\n"
"v_mfma_f32_16x16x32_bf16 a[212:215], v[252:255], v[92:95], a[212:215]\n"
"buffer_load_dwordx4 v[160:163], v2, s[4:7], s55 offen sc1\n"
"v_mfma_f32_16x16x32_bf16 a[216:219], v[232:235], v[80:83], a[216:219]\n"
"v_mfma_f32_16x16x32_bf16 a[216:219], v[252:255], a[80:83], a[216:219]\n"
"v_mfma_f32_16x16x32_bf16 a[220:223], v[232:235], v[84:87], a[220:223]\n"
"v_mfma_f32_16x16x32_bf16 a[220:223], v[252:255], a[84:87], a[220:223]\n"
"buffer_load_dwordx4 v[164:167], v3, s[4:7], s55 offen sc1\n"
"v_mfma_f32_16x16x32_bf16 a[224:227], v[236:239], v[72:75], a[224:227]\n"
"v_mfma_f32_16x16x32_bf16 a[224:227], a[0:3], v[88:91], a[224:227]\n"
"v_mfma_f32_16x16x32_bf16 a[228:231], v[236:239], v[76:79], a[228:231]\n"
"v_mfma_f32_16x16x32_bf16 a[228:231], a[0:3], v[92:95], a[228:231]\n"
"v_mfma_f32_16x16x32_bf16 a[232:235], v[236:239], v[80:83], a[232:235]\n"
"buffer_load_dwordx4 v[168:171], v4, s[4:7], s55 offen sc1\n"
"v_mfma_f32_16x16x32_bf16 a[232:235], a[0:3], a[80:83], a[232:235]\n"
"v_mfma_f32_16x16x32_bf16 a[236:239], v[236:239], v[84:87], a[236:239]\n"
"v_mfma_f32_16x16x32_bf16 a[236:239], a[0:3], a[84:87], a[236:239]\n"
"v_mfma_f32_16x16x32_bf16 a[240:243], v[240:243], v[72:75], a[240:243]\n"
"v_mfma_f32_16x16x32_bf16 a[240:243], a[4:7], v[88:91], a[240:243]\n"
"buffer_load_dwordx4 v[172:175], v5, s[4:7], s55 offen sc1\n"
"s_mov_b32 s55, s65\n"
"v_mfma_f32_16x16x32_bf16 a[244:247], v[240:243], v[76:79], a[244:247]\n"
"v_mfma_f32_16x16x32_bf16 a[244:247], a[4:7], v[92:95], a[244:247]\n"
"v_mfma_f32_16x16x32_bf16 a[248:251], v[240:243], v[80:83], a[248:251]\n"
"v_mfma_f32_16x16x32_bf16 a[248:251], a[4:7], a[80:83], a[248:251]\n"
"v_mfma_f32_16x16x32_bf16 a[252:255], v[240:243], v[84:87], a[252:255]\n"
"v_mfma_f32_16x16x32_bf16 a[252:255], a[4:7], a[84:87], a[252:255]\n"
"s_waitcnt vmcnt(8)\n"
"v_mfma_f32_16x16x32_bf16 a[176:179], a[8:11], a[88:91], a[176:179]\n"
"buffer_load_dwordx2 a[120:121], v10, s[4:7], s55 offen sc1\n"
"v_mfma_f32_16x16x32_bf16 a[176:179], a[28:31], a[104:107], a[176:179]\n"
"v_mfma_f32_16x16x32_bf16 a[180:183], a[8:11], a[92:95], a[180:183]\n"
"buffer_load_dwordx2 a[122:123], v10, s[4:7], s55 offen offset:512 sc1\n"
"v_mfma_f32_16x16x32_bf16 a[180:183], a[28:31], a[108:111], a[180:183]\n"
"v_mfma_f32_16x16x32_bf16 a[184:187], a[8:11], a[96:99], a[184:187]\n"
"buffer_load_dwordx2 a[124:125], v11, s[4:7], s55 offen sc1\n"
"v_mfma_f32_16x16x32_bf16 a[184:187], a[28:31], a[112:115], a[184:187]\n"
"v_mfma_f32_16x16x32_bf16 a[188:191], a[8:11], a[100:103], a[188:191]\n"
"buffer_load_dwordx2 a[126:127], v11, s[4:7], s55 offen offset:512 sc1\n"
"v_mfma_f32_16x16x32_bf16 a[188:191], a[28:31], a[116:119], a[188:191]\n"
"v_mfma_f32_16x16x32_bf16 a[192:195], a[12:15], a[88:91], a[192:195]\n"
"buffer_load_dwordx2 a[128:129], v12, s[4:7], s55 offen sc1\n"
"v_mfma_f32_16x16x32_bf16 a[192:195], a[32:35], a[104:107], a[192:195]\n"
"v_mfma_f32_16x16x32_bf16 a[196:199], a[12:15], a[92:95], a[196:199]\n"
"buffer_load_dwordx2 a[130:131], v12, s[4:7], s55 offen offset:512 sc1\n"
"v_mfma_f32_16x16x32_bf16 a[196:199], a[32:35], a[108:111], a[196:199]\n"
"v_mfma_f32_16x16x32_bf16 a[200:203], a[12:15], a[96:99], a[200:203]\n"
"buffer_load_dwordx2 a[132:133], v13, s[4:7], s55 offen sc1\n"
"v_mfma_f32_16x16x32_bf16 a[200:203], a[32:35], a[112:115], a[200:203]\n"
"v_mfma_f32_16x16x32_bf16 a[204:207], a[12:15], a[100:103], a[204:207]\n"
"buffer_load_dwordx2 a[134:135], v13, s[4:7], s55 offen offset:512 sc1\n"
"s_add_u32 s55, s55, 0x1000\n"
"v_mfma_f32_16x16x32_bf16 a[204:207], a[32:35], a[116:119], a[204:207]\n"
"v_mfma_f32_16x16x32_bf16 a[208:211], a[16:19], a[88:91], a[208:211]\n"
"buffer_load_dwordx2 a[136:137], v10, s[4:7], s55 offen sc1\n"
"v_mfma_f32_16x16x32_bf16 a[208:211], a[36:39], a[104:107], a[208:211]\n"
"v_mfma_f32_16x16x32_bf16 a[212:215], a[16:19], a[92:95], a[212:215]\n"
"v_mfma_f32_16x16x32_bf16 a[212:215], a[36:39], a[108:111], a[212:215]\n"
"buffer_load_dwordx2 a[138:139], v10, s[4:7], s55 offen offset:512 sc1\n"
"v_mfma_f32_16x16x32_bf16 a[216:219], a[16:19], a[96:99], a[216:219]\n"
"v_mfma_f32_16x16x32_bf16 a[216:219], a[36:39], a[112:115], a[216:219]\n"
"buffer_load_dwordx2 a[140:141], v11, s[4:7], s55 offen sc1\n"
"v_mfma_f32_16x16x32_bf16 a[220:223], a[16:19], a[100:103], a[220:223]\n"
"v_mfma_f32_16x16x32_bf16 a[220:223], a[36:39], a[116:119], a[220:223]\n"
"buffer_load_dwordx2 a[142:143], v11, s[4:7], s55 offen offset:512 sc1\n"
"v_mfma_f32_16x16x32_bf16 a[224:227], a[20:23], a[88:91], a[224:227]\n"
"v_mfma_f32_16x16x32_bf16 a[224:227], a[40:43], a[104:107], a[224:227]\n"
"buffer_load_dwordx2 a[144:145], v12, s[4:7], s55 offen sc1\n"
"v_mfma_f32_16x16x32_bf16 a[228:231], a[20:23], a[92:95], a[228:231]\n"
"v_mfma_f32_16x16x32_bf16 a[228:231], a[40:43], a[108:111], a[228:231]\n"
"buffer_load_dwordx2 a[146:147], v12, s[4:7], s55 offen offset:512 sc1\n"
"v_mfma_f32_16x16x32_bf16 a[232:235], a[20:23], a[96:99], a[232:235]\n"
"v_mfma_f32_16x16x32_bf16 a[232:235], a[40:43], a[112:115], a[232:235]\n"
"buffer_load_dwordx2 a[148:149], v13, s[4:7], s55 offen sc1\n"
"v_mfma_f32_16x16x32_bf16 a[236:239], a[20:23], a[100:103], a[236:239]\n"
"v_mfma_f32_16x16x32_bf16 a[236:239], a[40:43], a[116:119], a[236:239]\n"
"buffer_load_dwordx2 a[150:151], v13, s[4:7], s55 offen offset:512 sc1\n"
"s_add_u32 s55, s55, 0x3000\n"
"v_mfma_f32_16x16x32_bf16 a[240:243], a[24:27], a[88:91], a[240:243]\n"
"v_mfma_f32_16x16x32_bf16 a[240:243], a[44:47], a[104:107], a[240:243]\n"
"s_mov_b32 m0, s57\n"
"v_mfma_f32_16x16x32_bf16 a[244:247], a[24:27], a[92:95], a[244:247]\n"
"v_mfma_f32_16x16x32_bf16 a[244:247], a[44:47], a[108:111], a[244:247]\n"
"buffer_load_dwordx4 v14, s[4:7], s56 offen sc1 lds\n"
"v_mfma_f32_16x16x32_bf16 a[248:251], a[24:27], a[96:99], a[248:251]\n"
"v_mfma_f32_16x16x32_bf16 a[248:251], a[44:47], a[112:115], a[248:251]\n"
"v_mfma_f32_16x16x32_bf16 a[252:255], a[24:27], a[100:103], a[252:255]\n"
"v_mfma_f32_16x16x32_bf16 a[252:255], a[44:47], a[116:119], a[252:255]\n"
"s_waitcnt vmcnt(17)\n"
"v_mfma_f32_16x16x32_bf16 a[176:179], a[48:51], v[96:99], a[176:179]\n"
"v_mfma_f32_16x16x32_bf16 a[176:179], a[68:71], v[160:163], a[176:179]\n"
"v_mfma_f32_16x16x32_bf16 a[180:183], a[48:51], v[148:151], a[180:183]\n"
"v_mfma_f32_16x16x32_bf16 a[180:183], a[68:71], v[164:167], a[180:183]\n"
"v_mfma_f32_16x16x32_bf16 a[184:187], a[48:51], v[152:155], a[184:187]\n"
"v_mfma_f32_16x16x32_bf16 a[184:187], a[68:71], v[168:171], a[184:187]\n"
"v_mfma_f32_16x16x32_bf16 a[188:191], a[48:51], v[156:159], a[188:191]\n"
"v_mfma_f32_16x16x32_bf16 a[188:191], a[68:71], v[172:175], a[188:191]\n"
"v_mfma_f32_16x16x32_bf16 a[192:195], a[52:55], v[96:99], a[192:195]\n"
"v_mfma_f32_16x16x32_bf16 a[192:195], a[72:75], v[160:163], a[192:195]\n"
"v_mfma_f32_16x16x32_bf16 a[196:199], a[52:55], v[148:151], a[196:199]\n"
"v_mfma_f32_16x16x32_bf16 a[196:199], a[72:75], v[164:167], a[196:199]\n"
"v_mfma_f32_16x16x32_bf16 a[200:203], a[52:55], v[152:155], a[200:203]\n"
"v_mfma_f32_16x16x32_bf16 a[200:203], a[72:75], v[168:171], a[200:203]\n"
"v_mfma_f32_16x16x32_bf16 a[204:207], a[52:55], v[156:159], a[204:207]\n"
"v_mfma_f32_16x16x32_bf16 a[204:207], a[72:75], v[172:175], a[204:207]\n"
"v_mfma_f32_16x16x32_bf16 a[208:211], a[56:59], v[96:99], a[208:211]\n"
"v_mfma_f32_16x16x32_bf16 a[208:211], a[76:79], v[160:163], a[208:211]\n"
"v_mfma_f32_16x16x32_bf16 a[212:215], a[56:59], v[148:151], a[212:215]\n"
"v_mfma_f32_16x16x32_bf16 a[212:215], a[76:79], v[164:167], a[212:215]\n"
"v_mfma_f32_16x16x32_bf16 a[216:219], a[56:59], v[152:155], a[216:219]\n"
"v_mfma_f32_16x16x32_bf16 a[216:219], a[76:79], v[168:171], a[216:219]\n"
"v_mfma_f32_16x16x32_bf16 a[220:223], a[56:59], v[156:159], a[220:223]\n"
"v_mfma_f32_16x16x32_bf16 a[220:223], a[76:79], v[172:175], a[220:223]\n"
"v_mfma_f32_16x16x32_bf16 a[224:227], a[60:63], v[96:99], a[224:227]\n"
"s_waitcnt vmcnt(0)\n"
"ds_read_b128 v[44:47], v16\n"
"ds_read_b128 v[48:51], v16 offset:64\n"
"v_mfma_f32_16x16x32_bf16 a[224:227], v[100:103], v[160:163], a[224:227]\n"
"ds_read_b128 v[52:55], v16 offset:128\n"
"ds_read_b128 v[56:59], v16 offset:192\n"
"ds_read_b128 v[60:63], v16 offset:256\n"
"v_mfma_f32_16x16x32_bf16 a[228:231], a[60:63], v[148:151], a[228:231]\n"
"v_mfma_f32_16x16x32_bf16 a[228:231], v[100:103], v[164:167], a[228:231]\n"
"v_mfma_f32_16x16x32_bf16 a[232:235], a[60:63], v[152:155], a[232:235]\n"
"v_mfma_f32_16x16x32_bf16 a[232:235], v[100:103], v[168:171], a[232:235]\n"
"v_mfma_f32_16x16x32_bf16 a[236:239], a[60:63], v[156:159], a[236:239]\n"
"v_mfma_f32_16x16x32_bf16 a[236:239], v[100:103], v[172:175], a[236:239]\n"
"v_mfma_f32_16x16x32_bf16 a[240:243], a[64:67], v[96:99], a[240:243]\n"
"v_mfma_f32_16x16x32_bf16 a[240:243], v[104:107], v[160:163], a[240:243]\n"
"v_mfma_f32_16x16x32_bf16 a[244:247], a[64:67], v[148:151], a[244:247]\n"
"v_mfma_f32_16x16x32_bf16 a[244:247], v[104:107], v[164:167], a[244:247]\n"
"v_mfma_f32_16x16x32_bf16 a[248:251], a[64:67], v[152:155], a[248:251]\n"
"v_mfma_f32_16x16x32_bf16 a[248:251], v[104:107], v[168:171], a[248:251]\n"
"v_mfma_f32_16x16x32_bf16 a[252:255], a[64:67], v[156:159], a[252:255]\n"
"v_mfma_f32_16x16x32_bf16 a[252:255], v[104:107], v[172:175], a[252:255]\n"
"s_nop 0\n"
"v_mfma_f32_16x16x32_bf16 a[180:183], v[108:111], a[124:127], a[180:183]\n"
"v_mfma_f32_16x16x32_bf16 a[180:183], v[128:131], a[140:143], a[180:183]\n"
"v_mfma_f32_16x16x32_bf16 a[196:199], v[112:115], a[124:127], a[196:199]\n"
"v_mfma_f32_16x16x32_bf16 a[196:199], v[132:135], a[140:143], a[196:199]\n"
"v_mfma_f32_16x16x32_bf16 a[212:215], v[116:119], a[124:127], a[212:215]\n"
"v_mfma_f32_16x16x32_bf16 a[212:215], v[136:139], a[140:143], a[212:215]\n"
"v_mfma_f32_16x16x32_bf16 a[228:231], v[120:123], a[124:127], a[228:231]\n"
"v_mfma_f32_16x16x32_bf16 a[228:231], v[140:143], a[140:143], a[228:231]\n"
"v_mfma_f32_16x16x32_bf16 a[244:247], v[124:127], a[124:127], a[244:247]\n"
"v_mfma_f32_16x16x32_bf16 a[244:247], v[144:147], a[140:143], a[244:247]\n"
"ds_write_b128 v7, a[180:183]\n"
"v_mfma_f32_16x16x32_bf16 a[184:187], v[108:111], a[128:131], a[184:187]\n"
"v_mfma_f32_16x16x32_bf16 a[184:187], v[128:131], a[144:147], a[184:187]\n"
"ds_write_b128 v7, a[196:199] offset:1024\n"
"v_mfma_f32_16x16x32_bf16 a[200:203], v[112:115], a[128:131], a[200:203]\n"
"v_mfma_f32_16x16x32_bf16 a[200:203], v[132:135], a[144:147], a[200:203]\n"
"ds_write_b128 v7, a[212:215] offset:2048\n"
"v_mfma_f32_16x16x32_bf16 a[216:219], v[116:119], a[128:131], a[216:219]\n"
"v_mfma_f32_16x16x32_bf16 a[216:219], v[136:139], a[144:147], a[216:219]\n"
"ds_write_b128 v7, a[228:231] offset:3072\n"
"v_mfma_f32_16x16x32_bf16 a[232:235], v[120:123], a[128:131], a[232:235]\n"
"v_mfma_f32_16x16x32_bf16 a[232:235], v[140:143], a[144:147], a[232:235]\n"
"ds_write_b128 v7, a[244:247] offset:4096\n"
"v_mfma_f32_16x16x32_bf16 a[248:251], v[124:127], a[128:131], a[248:251]\n"
"v_mfma_f32_16x16x32_bf16 a[248:251], v[144:147], a[144:147], a[248:251]\n"
"ds_write_b128 v8, a[184:187] offset:5120\n"
"v_mfma_f32_16x16x32_bf16 a[188:191], v[108:111], a[132:135], a[188:191]\n"
"v_mfma_f32_16x16x32_bf16 a[188:191], v[128:131], a[148:151], a[188:191]\n"
"ds_write_b128 v8, a[200:203] offset:6144\n"
"v_mfma_f32_16x16x32_bf16 a[204:207], v[112:115], a[132:135], a[204:207]\n"
"v_mfma_f32_16x16x32_bf16 a[204:207], v[132:135], a[148:151], a[204:207]\n"
"ds_write_b128 v8, a[216:219] offset:7168\n"
"v_mfma_f32_16x16x32_bf16 a[220:223], v[116:119], a[132:135], a[220:223]\n"
"v_mfma_f32_16x16x32_bf16 a[220:223], v[136:139], a[148:151], a[220:223]\n"
"ds_write_b128 v8, a[232:235] offset:8192\n"
"v_mfma_f32_16x16x32_bf16 a[236:239], v[120:123], a[132:135], a[236:239]\n"
"v_mfma_f32_16x16x32_bf16 a[236:239], v[140:143], a[148:151], a[236:239]\n"
"ds_write_b128 v8, a[248:251] offset:9216\n"
"v_mfma_f32_16x16x32_bf16 a[252:255], v[124:127], a[132:135], a[252:255]\n"
"v_mfma_f32_16x16x32_bf16 a[252:255], v[144:147], a[148:151], a[252:255]\n"
"ds_write_b128 v9, a[188:191] offset:10240\n"
"v_mfma_f32_16x16x32_bf16 a[176:179], v[108:111], a[120:123], a[176:179]\n"
"v_mfma_f32_16x16x32_bf16 a[176:179], v[128:131], a[136:139], a[176:179]\n"
"ds_write_b128 v9, a[204:207] offset:11264\n"
"v_mfma_f32_16x16x32_bf16 a[192:195], v[112:115], a[120:123], a[192:195]\n"
"v_mfma_f32_16x16x32_bf16 a[192:195], v[132:135], a[136:139], a[192:195]\n"
"ds_write_b128 v9, a[220:223] offset:12288\n"
"v_mfma_f32_16x16x32_bf16 a[208:211], v[116:119], a[120:123], a[208:211]\n"
"v_mfma_f32_16x16x32_bf16 a[208:211], v[136:139], a[136:139], a[208:211]\n"
"ds_write_b128 v9, a[236:239] offset:13312\n"
"v_mfma_f32_16x16x32_bf16 a[224:227], v[120:123], a[120:123], a[224:227]\n"
"v_mfma_f32_16x16x32_bf16 a[224:227], v[140:143], a[136:139], a[224:227]\n"
"ds_write_b128 v9, a[252:255] offset:14336\n"
"v_mfma_f32_16x16x32_bf16 a[240:243], v[124:127], a[120:123], a[240:243]\n"
"v_mfma_f32_16x16x32_bf16 a[240:243], v[144:147], a[136:139], a[240:243]\n"
"s_waitcnt lgkmcnt(0)\n"
"s_barrier\n"
"ds_read_b128 v[116:119], v6\n"
"ds_read_b128 v[136:139], v6 offset:5120\n"
"ds_read_b128 v[156:159], v6 offset:10240\n"
"ds_read_b128 v[120:123], v6 offset:1024\n"
"ds_read_b128 v[140:143], v6 offset:6144\n"
"ds_read_b128 v[160:163], v6 offset:11264\n"
"ds_read_b128 v[124:127], v6 offset:2048\n"
"ds_read_b128 v[144:147], v6 offset:7168\n"
"ds_read_b128 v[164:167], v6 offset:12288\n"
"ds_read_b128 v[128:131], v6 offset:3072\n"
"ds_read_b128 v[148:151], v6 offset:8192\n"
"ds_read_b128 v[168:171], v6 offset:13312\n"
"ds_read_b128 v[132:135], v6 offset:4096\n"
"ds_read_b128 v[152:155], v6 offset:9216\n"
"ds_read_b128 v[172:175], v6 offset:14336\n"
"v_accvgpr_read_b32 v96, a176\n"
"v_accvgpr_read_b32 v97, a177\n"
"v_accvgpr_read_b32 v98, a178\n"
"v_accvgpr_read_b32 v99, a179\n"
"v_accvgpr_read_b32 v100, a192\n"
"v_accvgpr_read_b32 v101, a193\n"
"v_accvgpr_read_b32 v102, a194\n"
"v_accvgpr_read_b32 v103, a195\n"
"v_accvgpr_read_b32 v104, a208\n"
"v_accvgpr_read_b32 v105, a209\n"
"v_accvgpr_read_b32 v106, a210\n"
"v_accvgpr_read_b32 v107, a211\n"
"v_accvgpr_read_b32 v108, a224\n"
"v_accvgpr_read_b32 v109, a225\n"
"v_accvgpr_read_b32 v110, a226\n"
"v_accvgpr_read_b32 v111, a227\n"
"v_accvgpr_read_b32 v112, a240\n"
"v_accvgpr_read_b32 v113, a241\n"
"v_accvgpr_read_b32 v114, a242\n"
"v_accvgpr_read_b32 v115, a243\n"
"s_nop 0\n"
"s_waitcnt lgkmcnt(12)\n"
"v_pk_add_f32 v[96:97], v[96:97], v[116:117]\n"
"v_pk_add_f32 v[136:137], v[136:137], v[156:157]\n"
"v_pk_add_f32 v[98:99], v[98:99], v[118:119]\n"
"v_pk_add_f32 v[138:139], v[138:139], v[158:159]\n"
"v_pk_add_f32 v[96:97], v[96:97], v[136:137]\n"
"v_pk_add_f32 v[98:99], v[98:99], v[138:139]\n"
"v_fmac_f32 v24, v44, v96\n"
"v_fmac_f32 v25, v45, v97\n"
"v_fmac_f32 v26, v46, v98\n"
"v_fmac_f32 v27, v47, v99\n"
"v_cvt_pk_bf16_f32 v176, v24, v25\n"
"v_cvt_pk_bf16_f32 v177, v26, v27\n"
"s_nop 0\n"
"buffer_store_dwordx2 v[176:177], v21, s[8:11], s62 offen\n"
"s_waitcnt lgkmcnt(9)\n"
"v_pk_add_f32 v[100:101], v[100:101], v[120:121]\n"
"v_pk_add_f32 v[140:141], v[140:141], v[160:161]\n"
"v_pk_add_f32 v[102:103], v[102:103], v[122:123]\n"
"v_pk_add_f32 v[142:143], v[142:143], v[162:163]\n"
"v_pk_add_f32 v[100:101], v[100:101], v[140:141]\n"
"v_pk_add_f32 v[102:103], v[102:103], v[142:143]\n"
"v_fmac_f32 v28, v48, v100\n"
"v_fmac_f32 v29, v49, v101\n"
"v_fmac_f32 v30, v50, v102\n"
"v_fmac_f32 v31, v51, v103\n"
"v_cvt_pk_bf16_f32 v178, v28, v29\n"
"v_cvt_pk_bf16_f32 v179, v30, v31\n"
"s_nop 0\n"
"buffer_store_dwordx2 v[178:179], v21, s[8:11], s62 offen offset:32\n"
"s_waitcnt lgkmcnt(6)\n"
"v_pk_add_f32 v[104:105], v[104:105], v[124:125]\n"
"v_pk_add_f32 v[144:145], v[144:145], v[164:165]\n"
"v_pk_add_f32 v[106:107], v[106:107], v[126:127]\n"
"v_pk_add_f32 v[146:147], v[146:147], v[166:167]\n"
"v_pk_add_f32 v[104:105], v[104:105], v[144:145]\n"
"v_pk_add_f32 v[106:107], v[106:107], v[146:147]\n"
"v_fmac_f32 v32, v52, v104\n"
"v_fmac_f32 v33, v53, v105\n"
"v_fmac_f32 v34, v54, v106\n"
"v_fmac_f32 v35, v55, v107\n"
"v_cvt_pk_bf16_f32 v180, v32, v33\n"
"v_cvt_pk_bf16_f32 v181, v34, v35\n"
"s_nop 0\n"
"buffer_store_dwordx2 v[180:181], v21, s[8:11], s62 offen offset:64\n"
"s_waitcnt lgkmcnt(3)\n"
"v_pk_add_f32 v[108:109], v[108:109], v[128:129]\n"
"v_pk_add_f32 v[148:149], v[148:149], v[168:169]\n"
"v_pk_add_f32 v[110:111], v[110:111], v[130:131]\n"
"v_pk_add_f32 v[150:151], v[150:151], v[170:171]\n"
"v_pk_add_f32 v[108:109], v[108:109], v[148:149]\n"
"v_pk_add_f32 v[110:111], v[110:111], v[150:151]\n"
"v_fmac_f32 v36, v56, v108\n"
"v_fmac_f32 v37, v57, v109\n"
"v_fmac_f32 v38, v58, v110\n"
"v_fmac_f32 v39, v59, v111\n"
"v_cvt_pk_bf16_f32 v182, v36, v37\n"
"v_cvt_pk_bf16_f32 v183, v38, v39\n"
"s_nop 0\n"
"buffer_store_dwordx2 v[182:183], v21, s[8:11], s62 offen offset:96\n"
"s_waitcnt lgkmcnt(0)\n"
"v_pk_add_f32 v[112:113], v[112:113], v[132:133]\n"
"v_pk_add_f32 v[152:153], v[152:153], v[172:173]\n"
"v_pk_add_f32 v[114:115], v[114:115], v[134:135]\n"
"v_pk_add_f32 v[154:155], v[154:155], v[174:175]\n"
"v_pk_add_f32 v[112:113], v[112:113], v[152:153]\n"
"v_pk_add_f32 v[114:115], v[114:115], v[154:155]\n"
"v_fmac_f32 v40, v60, v112\n"
"v_fmac_f32 v41, v61, v113\n"
"v_fmac_f32 v42, v62, v114\n"
"v_fmac_f32 v43, v63, v115\n"
"v_cvt_pk_bf16_f32 v184, v40, v41\n"
"v_cvt_pk_bf16_f32 v185, v42, v43\n"
"s_nop 0\n"
"buffer_store_dwordx2 v[184:185], v21, s[8:11], s63 offen\n"
"s_cmp_eq_u32 s2, 0\n"
"s_cselect_b32 s56, 0, 0x40000000\n"
"buffer_store_dword v22, v23, s[4:7], s56 offen sc1\n"
"s_endpgm\n"
".Ladaln_block_A_DIAG:\n"
"v_exp_f32 v172, v172\n"
"v_exp_f32 v173, v173\n"
"v_exp_f32 v174, v174\n"
"v_cvt_pk_bf16_f32 v220, v172, v173\n"
"v_exp_f32 v175, v175\n"
"v_cndmask_b32 v240, v224, v8, s[50:51]\n"
"v_cndmask_b32 v241, v225, v8, s[52:53]\n"
"v_cndmask_b32 v242, v226, v8, s[54:55]\n"
"v_cndmask_b32 v243, v227, v8, s[56:57]\n"
"v_cvt_pk_bf16_f32 v221, v174, v175\n"
"v_exp_f32 v184, v184\n"
"v_exp_f32 v185, v185\n"
"v_exp_f32 v186, v186\n"
"v_cvt_pk_bf16_f32 v214, v184, v185\n"
"v_exp_f32 v187, v187\n"
"v_cndmask_b32 v244, v228, v8, s[50:51]\n"
"v_cndmask_b32 v245, v229, v8, s[52:53]\n"
"v_cndmask_b32 v246, v230, v8, s[54:55]\n"
"v_cndmask_b32 v247, v231, v8, s[56:57]\n"
"v_cvt_pk_bf16_f32 v215, v186, v187\n"
"v_mfma_f32_16x16x32_bf16 v[132:135], a[144:147], v[72:75], v[228:231]\n"
"v_exp_f32 v188, v188\n"
"s_add_u32 s60, s60, s64\n"
"v_mfma_f32_16x16x32_bf16 v[132:135], a[148:151], v[76:79], v[132:135]\n"
"s_cmp_eq_u32 s8, 15\n"
"s_cmov_b32 s60, s9\n"
"s_xor_b32 s62, s62, 0x8000\n"
"v_mfma_f32_16x16x32_bf16 v[132:135], a[152:155], v[80:83], v[132:135]\n"
"v_exp_f32 v189, v189\n"
"s_add_u32 m0, s63, 0x4000\n"
"v_mfma_f32_16x16x32_bf16 v[132:135], a[156:159], v[84:87], v[132:135]\n"
"buffer_load_dwordx4 v9, s[16:19], s61 offen sc1 lds\n"
"v_cvt_pk_bf16_f32 v222, v188, v189\n"
"s_mov_b32 s64, s65\n"
"v_mfma_f32_16x16x32_bf16 v[128:131], a[144:147], v[56:59], v[240:243]\n"
"v_exp_f32 v190, v190\n"
"s_mov_b32 s65, 0x10000\n"
"v_mfma_f32_16x16x32_bf16 v[128:131], a[148:151], v[60:63], v[128:131]\n"
"buffer_load_dwordx4 v9, s[16:19], s61 offen offset:1024 sc1 lds\n"
"s_cmp_eq_u32 s8, 14\n"
"s_cselect_b32 s30, s10, 0x40000000\n"
"v_mfma_f32_16x16x32_bf16 v[128:131], a[152:155], v[64:67], v[128:131]\n"
"v_exp_f32 v191, v191\n"
"v_mfma_f32_16x16x32_bf16 v[128:131], a[156:159], v[68:71], v[128:131]\n"
"buffer_load_dwordx4 v9, s[16:19], s61 offen offset:2048 sc1 lds\n"
"v_cvt_pk_bf16_f32 v223, v190, v191\n"
"v_xor_b32 v3, s67, v3\n"
"v_mfma_f32_16x16x32_bf16 v[148:151], a[160:163], v[72:75], v[244:247]\n"
"buffer_load_dword v21, v20, s[4:7], s30 offen sc1\n"
"s_cmp_eq_u32 s8, 14\n"
"s_cselect_b32 s31, s82, 0\n"
"v_mfma_f32_16x16x32_bf16 v[148:151], a[164:167], v[76:79], v[148:151]\n"
"buffer_load_dwordx4 v9, s[16:19], s61 offen offset:3072 sc1 lds\n"
"v_mfma_f32_16x16x32_bf16 v[148:151], a[168:171], v[80:83], v[148:151]\n"
"s_xor_b32 s63, s63, 0x8000\n"
"s_mov_b32 s61, s60\n"
"s_cmp_eq_u32 s8, 15\n"
"v_mfma_f32_16x16x32_bf16 v[148:151], a[172:175], v[84:87], v[148:151]\n"
"s_cmov_b64 s[16:17], s[0:1]\n"
"s_cmov_b32 s63, s47\n"
"s_cmp_eq_u32 s8, 16\n"
"v_mfma_f32_16x16x32_bf16 a[64:67], a[208:211], v[208:211], a[64:67]\n"
"v_exp_f32 v132, v132\n"
"s_cmov_b32 s18, 0\n"
"v_mfma_f32_16x16x32_bf16 a[64:67], a[212:215], v[212:215], a[64:67]\n"
"v_mov_b32 v194, 0\n"
"v_mov_b32 v195, 0\n"
"s_cmov_b32 s63, s78\n"
"v_mfma_f32_16x16x32_bf16 a[96:99], a[208:211], v[216:219], a[96:99]\n"
"v_cndmask_b32 v248, v232, v8, s[50:51]\n"
"s_add_u32 s8, s8, 1\n"
"s_waitcnt vmcnt(13)\n"
"v_mfma_f32_16x16x32_bf16 a[96:99], a[212:215], v[220:223], a[96:99]\n"
"s_barrier\n"
"v_cndmask_b32 v249, v233, v8, s[52:53]\n"
"v_mfma_f32_16x16x32_bf16 a[68:71], a[216:219], v[208:211], a[68:71]\n"
"v_cndmask_b32 v250, v234, v8, s[54:55]\n"
"v_cndmask_b32 v251, v235, v8, s[56:57]\n"
"v_mfma_f32_16x16x32_bf16 a[68:71], a[220:223], v[212:215], a[68:71]\n"
"ds_read_b128 a[208:211], v3\n"
"v_cndmask_b32 v252, v236, v8, s[50:51]\n"
"v_mfma_f32_16x16x32_bf16 a[100:103], a[216:219], v[216:219], a[100:103]\n"
"v_cndmask_b32 v253, v237, v8, s[52:53]\n"
"v_cndmask_b32 v254, v238, v8, s[54:55]\n"
"s_mov_b32 m0, s62\n"
"v_mfma_f32_16x16x32_bf16 a[100:103], a[220:223], v[220:223], a[100:103]\n"
"ds_read_b128 a[212:215], v3 offset:1024\n"
"buffer_load_dwordx4 v1, s[4:7], s60 offen sc1 lds\n"
"v_cndmask_b32 v255, v239, v8, s[56:57]\n"
"v_mfma_f32_16x16x32_bf16 a[72:75], a[224:227], v[208:211], a[72:75]\n"
"v_mov_b32 v184, v8\n"
"v_mov_b32 v185, v8\n"
"v_mfma_f32_16x16x32_bf16 a[72:75], a[228:231], v[212:215], a[72:75]\n"
"ds_read_b128 a[216:219], v3 offset:2048\n"
"v_mov_b32 v186, v8\n"
"v_mfma_f32_16x16x32_bf16 a[104:107], a[224:227], v[216:219], a[104:107]\n"
"v_mov_b32 v187, v8\n"
"v_mfma_f32_16x16x32_bf16 a[104:107], a[228:231], v[220:223], a[104:107]\n"
"ds_read_b128 a[220:223], v3 offset:3072\n"
"v_mfma_f32_16x16x32_bf16 a[76:79], a[232:235], v[208:211], a[76:79]\n"
"v_exp_f32 v133, v133\n"
"v_mfma_f32_16x16x32_bf16 a[76:79], a[236:239], v[212:215], a[76:79]\n"
"ds_read_b128 a[224:227], v3 offset:4096\n"
"v_cvt_pk_bf16_f32 v200, v132, v133\n"
"v_mfma_f32_16x16x32_bf16 a[108:111], a[232:235], v[216:219], a[108:111]\n"
"v_exp_f32 v134, v134\n"
"v_mfma_f32_16x16x32_bf16 a[108:111], a[236:239], v[220:223], a[108:111]\n"
"ds_read_b128 a[228:231], v3 offset:5120\n"
"buffer_load_dwordx4 v1, s[4:7], s60 offen offset:1024 sc1 lds\n"
"v_mfma_f32_16x16x32_bf16 a[80:83], a[240:243], v[208:211], a[80:83]\n"
"v_exp_f32 v135, v135\n"
"v_mfma_f32_16x16x32_bf16 a[80:83], a[244:247], v[212:215], a[80:83]\n"
"ds_read_b128 a[232:235], v3 offset:6144\n"
"v_cvt_pk_bf16_f32 v201, v134, v135\n"
"v_mfma_f32_16x16x32_bf16 a[112:115], a[240:243], v[216:219], a[112:115]\n"
"v_exp_f32 v128, v128\n"
"v_mfma_f32_16x16x32_bf16 a[112:115], a[244:247], v[220:223], a[112:115]\n"
"ds_read_b128 a[236:239], v3 offset:7168\n"
"v_mfma_f32_16x16x32_bf16 a[84:87], a[248:251], v[208:211], a[84:87]\n"
"v_exp_f32 v129, v129\n"
"v_mfma_f32_16x16x32_bf16 a[84:87], a[252:255], v[212:215], a[84:87]\n"
"ds_read_b128 a[240:243], v3 offset:8192\n"
"v_cvt_pk_bf16_f32 v192, v128, v129\n"
"v_mfma_f32_16x16x32_bf16 a[116:119], a[248:251], v[216:219], a[116:119]\n"
"v_exp_f32 v130, v130\n"
"v_mfma_f32_16x16x32_bf16 a[116:119], a[252:255], v[220:223], a[116:119]\n"
"ds_read_b128 a[244:247], v3 offset:9216\n"
"buffer_load_dwordx4 v1, s[4:7], s60 offen offset:2048 sc1 lds\n"
"v_mfma_f32_16x16x32_bf16 a[88:91], v[24:27], v[208:211], a[88:91]\n"
"v_exp_f32 v131, v131\n"
"v_mfma_f32_16x16x32_bf16 a[88:91], v[28:31], v[212:215], a[88:91]\n"
"ds_read_b128 a[248:251], v3 offset:10240\n"
"v_cvt_pk_bf16_f32 v193, v130, v131\n"
"v_mfma_f32_16x16x32_bf16 a[120:123], v[24:27], v[216:219], a[120:123]\n"
"v_exp_f32 v148, v148\n"
"v_mfma_f32_16x16x32_bf16 a[120:123], v[28:31], v[220:223], a[120:123]\n"
"ds_read_b128 a[252:255], v3 offset:11264\n"
"v_mfma_f32_16x16x32_bf16 a[92:95], v[32:35], v[208:211], a[92:95]\n"
"v_exp_f32 v149, v149\n"
"v_mfma_f32_16x16x32_bf16 a[92:95], v[36:39], v[212:215], a[92:95]\n"
"ds_read_b128 v[24:27], v3 offset:12288\n"
"v_cvt_pk_bf16_f32 v202, v148, v149\n"
"v_mfma_f32_16x16x32_bf16 a[124:127], v[32:35], v[216:219], a[124:127]\n"
"v_exp_f32 v150, v150\n"
"v_mfma_f32_16x16x32_bf16 a[124:127], v[36:39], v[220:223], a[124:127]\n"
"ds_read_b128 v[28:31], v3 offset:13312\n"
"buffer_load_dwordx4 v1, s[4:7], s60 offen offset:3072 sc1 lds\n"
"v_mfma_f32_16x16x32_bf16 a[136:139], v[4:7], v[208:211], a[136:139]\n"
"v_exp_f32 v151, v151\n"
"v_mfma_f32_16x16x32_bf16 a[136:139], v[4:7], v[212:215], a[136:139]\n"
"ds_read_b128 v[32:35], v3 offset:14336\n"
"v_cvt_pk_bf16_f32 v203, v150, v151\n"
"v_mfma_f32_16x16x32_bf16 a[140:143], v[4:7], v[216:219], a[140:143]\n"
"s_add_u32 m0, s62, 0x4000\n"
"v_mfma_f32_16x16x32_bf16 a[140:143], v[4:7], v[220:223], a[140:143]\n"
"ds_read_b128 v[36:39], v3 offset:15360\n"
"buffer_load_dwordx4 v9, s[4:7], s60 offen sc1 lds\n"
"v_mfma_f32_16x16x32_bf16 v[136:139], a[144:147], v[88:91], v[232:235]\n"
"v_mfma_f32_16x16x32_bf16 v[136:139], a[148:151], v[92:95], v[136:139]\n"
"v_xor_b32 v2, s66, v2\n"
"v_mfma_f32_16x16x32_bf16 v[136:139], a[152:155], v[96:99], v[136:139]\n"
"v_mfma_f32_16x16x32_bf16 v[136:139], a[156:159], v[100:103], v[136:139]\n"
"v_mfma_f32_16x16x32_bf16 v[140:143], a[144:147], v[104:107], v[236:239]\n"
"buffer_load_dwordx4 v9, s[4:7], s60 offen offset:1024 sc1 lds\n"
"v_mfma_f32_16x16x32_bf16 v[140:143], a[148:151], v[108:111], v[140:143]\n"
"v_mfma_f32_16x16x32_bf16 v[140:143], a[152:155], v[112:115], v[140:143]\n"
"s_waitcnt vmcnt(11) lgkmcnt(0)\n"
"v_mfma_f32_16x16x32_bf16 v[140:143], a[156:159], v[116:119], v[140:143]\n"
"s_barrier\n"
"v_mfma_f32_16x16x32_bf16 v[152:155], a[160:163], v[88:91], v[232:235]\n"
"v_exp_f32 v136, v136\n"
"v_mfma_f32_16x16x32_bf16 v[152:155], a[164:167], v[92:95], v[152:155]\n"
"ds_read_b128 a[144:147], v2\n"
"buffer_load_dwordx4 v9, s[4:7], s60 offen offset:2048 sc1 lds\n"
"v_mfma_f32_16x16x32_bf16 v[152:155], a[168:171], v[96:99], v[152:155]\n"
"ds_read_b128 a[148:151], v2 offset:1024\n"
"v_mfma_f32_16x16x32_bf16 v[152:155], a[172:175], v[100:103], v[152:155]\n"
"ds_read_b128 a[152:155], v2 offset:2048\n"
"v_mfma_f32_16x16x32_bf16 v[156:159], a[160:163], v[104:107], v[236:239]\n"
"ds_read_b128 a[156:159], v2 offset:3072\n"
"v_mfma_f32_16x16x32_bf16 v[156:159], a[164:167], v[108:111], v[156:159]\n"
"v_exp_f32 v137, v137\n"
"v_mfma_f32_16x16x32_bf16 v[156:159], a[168:171], v[112:115], v[156:159]\n"
"buffer_load_dwordx4 v9, s[4:7], s60 offen offset:3072 sc1 lds\n"
"v_cvt_pk_bf16_f32 v208, v136, v137\n"
"v_mfma_f32_16x16x32_bf16 v[156:159], a[172:175], v[116:119], v[156:159]\n"
"v_exp_f32 v138, v138\n"
"v_mfma_f32_16x16x32_bf16 v[168:171], a[176:179], v[88:91], v[248:251]\n"
"ds_read_b128 a[160:163], v2 offset:4096\n"
"s_mov_b32 m0, s63\n"
"s_mov_b32 s67, s66\n"
"v_mfma_f32_16x16x32_bf16 v[168:171], a[180:183], v[92:95], v[168:171]\n"
"ds_read_b128 a[164:167], v2 offset:5120\n"
"buffer_load_dwordx4 v1, s[16:19], s60 offen sc1 lds\n"
"s_mov_b32 s66, s68\n"
"v_mfma_f32_16x16x32_bf16 v[168:171], a[184:187], v[96:99], v[168:171]\n"
"ds_read_b128 a[168:171], v2 offset:6144\n"
"s_mov_b32 s68, 0x8000\n"
"v_mfma_f32_16x16x32_bf16 v[168:171], a[188:191], v[100:103], v[168:171]\n"
"ds_read_b128 a[172:175], v2 offset:7168\n"
"v_mfma_f32_16x16x32_bf16 v[172:175], a[176:179], v[104:107], v[236:239]\n"
"buffer_load_dwordx4 v1, s[16:19], s60 offen offset:1024 sc1 lds\n"
"v_exp_f32 v139, v139\n"
"v_mfma_f32_16x16x32_bf16 v[172:175], a[180:183], v[108:111], v[172:175]\n"
"v_cvt_pk_bf16_f32 v209, v138, v139\n"
"s_waitcnt vmcnt(11)\n"
"v_mfma_f32_16x16x32_bf16 v[172:175], a[184:187], v[112:115], v[172:175]\n"
"v_exp_f32 v140, v140\n"
"v_cmp_ne_u32 vcc, s31, v21\n"
"v_mfma_f32_16x16x32_bf16 v[172:175], a[188:191], v[116:119], v[172:175]\n"
"buffer_load_dwordx4 v1, s[16:19], s60 offen offset:2048 sc1 lds\n"
"s_cbranch_vccnz .Ladaln_block_P_prep2\n"
".Ladaln_block_P_prep2_R:\n"
"v_mfma_f32_16x16x32_bf16 v[188:191], a[192:195], v[104:107], v[252:255]\n"
"ds_read_b128 a[176:179], v2 offset:8192\n"
"v_mfma_f32_16x16x32_bf16 v[188:191], a[196:199], v[108:111], v[188:191]\n"
"ds_read_b128 a[180:183], v2 offset:9216\n"
"v_mfma_f32_16x16x32_bf16 v[188:191], a[200:203], v[112:115], v[188:191]\n"
"ds_read_b128 a[184:187], v2 offset:10240\n"
"buffer_load_dwordx4 v1, s[16:19], s60 offen offset:3072 sc1 lds\n"
"v_mfma_f32_16x16x32_bf16 v[188:191], a[204:207], v[116:119], v[188:191]\n"
"ds_read_b128 a[188:191], v2 offset:11264\n"
"v_mfma_f32_16x16x32_bf16 a[0:3], a[208:211], v[192:195], a[0:3]\n"
"ds_read_b128 a[192:195], v2 offset:12288\n"
"v_mfma_f32_16x16x32_bf16 a[32:35], a[208:211], v[200:203], a[32:35]\n"
"ds_read_b128 a[196:199], v2 offset:13312\n"
"v_mfma_f32_16x16x32_bf16 a[4:7], a[216:219], v[192:195], a[4:7]\n"
"ds_read_b128 a[200:203], v2 offset:14336\n"
"v_mfma_f32_16x16x32_bf16 a[36:39], a[216:219], v[200:203], a[36:39]\n"
"ds_read_b128 a[204:207], v2 offset:15360\n"
"v_mfma_f32_16x16x32_bf16 a[8:11], a[224:227], v[192:195], a[8:11]\n"
"v_exp_f32 v141, v141\n"
"v_mfma_f32_16x16x32_bf16 a[40:43], a[224:227], v[200:203], a[40:43]\n"
"v_cvt_pk_bf16_f32 v216, v140, v141\n"
"v_mfma_f32_16x16x32_bf16 a[12:15], a[232:235], v[192:195], a[12:15]\n"
"v_exp_f32 v142, v142\n"
"v_mfma_f32_16x16x32_bf16 a[44:47], a[232:235], v[200:203], a[44:47]\n"
"v_mfma_f32_16x16x32_bf16 a[16:19], a[240:243], v[192:195], a[16:19]\n"
"v_exp_f32 v143, v143\n"
"v_mfma_f32_16x16x32_bf16 a[48:51], a[240:243], v[200:203], a[48:51]\n"
"v_cvt_pk_bf16_f32 v217, v142, v143\n"
"v_mfma_f32_16x16x32_bf16 a[20:23], a[248:251], v[192:195], a[20:23]\n"
"v_exp_f32 v152, v152\n"
"v_mfma_f32_16x16x32_bf16 a[52:55], a[248:251], v[200:203], a[52:55]\n"
"v_mfma_f32_16x16x32_bf16 a[24:27], v[24:27], v[192:195], a[24:27]\n"
"v_exp_f32 v153, v153\n"
"v_mfma_f32_16x16x32_bf16 a[56:59], v[24:27], v[200:203], a[56:59]\n"
"v_cvt_pk_bf16_f32 v210, v152, v153\n"
"v_mfma_f32_16x16x32_bf16 a[28:31], v[32:35], v[192:195], a[28:31]\n"
"v_exp_f32 v154, v154\n"
"v_mfma_f32_16x16x32_bf16 a[60:63], v[32:35], v[200:203], a[60:63]\n"
"s_waitcnt lgkmcnt(0)\n"
"v_mfma_f32_16x16x32_bf16 a[128:131], v[4:7], v[192:195], a[128:131]\n"
"v_exp_f32 v155, v155\n"
"v_mfma_f32_16x16x32_bf16 a[132:135], v[4:7], v[200:203], a[132:135]\n"
"v_cvt_pk_bf16_f32 v211, v154, v155\n"
"v_exp_f32 v156, v156\n"
"v_exp_f32 v157, v157\n"
"s_nop 0\n"
"v_cvt_pk_bf16_f32 v218, v156, v157\n"
"v_exp_f32 v158, v158\n"
"v_exp_f32 v159, v159\n"
"s_nop 0\n"
"v_cvt_pk_bf16_f32 v219, v158, v159\n"
"v_exp_f32 v168, v168\n"
"v_exp_f32 v169, v169\n"
"s_nop 0\n"
"v_cvt_pk_bf16_f32 v212, v168, v169\n"
"v_exp_f32 v170, v170\n"
"v_exp_f32 v171, v171\n"
"s_nop 0\n"
"v_cvt_pk_bf16_f32 v213, v170, v171\n"
".Ladaln_block_A_DIAG_DONE:\n"
"s_mov_b32 s71, 1\n"
"s_mov_b32 s69, s70\n"
"s_cmp_lt_u32 s3, 2\n"
"s_cbranch_scc1 .Ladaln_block_A_CHK\n"
"v_exp_f32 v172, v172\n"
"v_exp_f32 v173, v173\n"
"v_exp_f32 v174, v174\n"
"v_cvt_pk_bf16_f32 v220, v172, v173\n"
"v_exp_f32 v175, v175\n"
"v_mov_b32 v44, 0\n"
"v_cvt_pk_bf16_f32 v221, v174, v175\n"
"v_exp_f32 v184, v184\n"
"v_exp_f32 v185, v185\n"
"v_exp_f32 v186, v186\n"
"v_cvt_pk_bf16_f32 v214, v184, v185\n"
"v_exp_f32 v187, v187\n"
"v_mov_b32 v45, 0\n"
"v_cvt_pk_bf16_f32 v215, v186, v187\n"
"v_exp_f32 v188, v188\n"
"v_exp_f32 v189, v189\n"
"v_exp_f32 v190, v190\n"
"v_cvt_pk_bf16_f32 v222, v188, v189\n"
"v_exp_f32 v191, v191\n"
"v_mov_b32 v46, 0\n"
"v_cvt_pk_bf16_f32 v223, v190, v191\n"
"v_mov_b32 v47, 0\n"
"v_accvgpr_read_b32 v48, a128\n"
"v_rcp_f32 v48, v48\n"
"v_accvgpr_read_b32 v49, a132\n"
"v_rcp_f32 v49, v49\n"
"v_mfma_f32_16x16x32_bf16 a[64:67], a[208:211], v[208:211], a[64:67]\n"
"v_accvgpr_read_b32 v128, a0\n"
"s_add_u32 m0, s63, 0x4000\n"
"v_accvgpr_read_b32 v129, a1\n"
"v_mfma_f32_16x16x32_bf16 a[64:67], a[212:215], v[212:215], a[64:67]\n"
"v_accvgpr_read_b32 v130, a2\n"
"v_accvgpr_read_b32 v131, a3\n"
"v_mfma_f32_16x16x32_bf16 a[96:99], a[208:211], v[216:219], a[96:99]\n"
"buffer_load_dwordx4 v9, s[16:19], s61 offen sc1 lds\n"
"v_accvgpr_read_b32 v132, a4\n"
"v_accvgpr_read_b32 v133, a5\n"
"v_mfma_f32_16x16x32_bf16 a[96:99], a[212:215], v[220:223], a[96:99]\n"
"v_accvgpr_read_b32 v134, a6\n"
"v_accvgpr_read_b32 v135, a7\n"
"v_mfma_f32_16x16x32_bf16 a[68:71], a[216:219], v[208:211], a[68:71]\n"
"v_mul_f32 v128, v128, v48\n"
"v_mul_f32 v129, v129, v48\n"
"v_mfma_f32_16x16x32_bf16 a[68:71], a[220:223], v[212:215], a[68:71]\n"
"v_mul_f32 v130, v130, v48\n"
"v_mul_f32 v131, v131, v48\n"
"v_mfma_f32_16x16x32_bf16 a[100:103], a[216:219], v[216:219], a[100:103]\n"
"v_mul_f32 v132, v132, v48\n"
"v_mul_f32 v133, v133, v48\n"
"v_mfma_f32_16x16x32_bf16 a[100:103], a[220:223], v[220:223], a[100:103]\n"
"v_mul_f32 v134, v134, v48\n"
"v_mul_f32 v135, v135, v48\n"
"v_mfma_f32_16x16x32_bf16 a[72:75], a[224:227], v[208:211], a[72:75]\n"
"v_cvt_pk_bf16_f32 v160, v128, v129\n"
"v_cvt_pk_bf16_f32 v161, v130, v131\n"
"v_mfma_f32_16x16x32_bf16 a[72:75], a[228:231], v[212:215], a[72:75]\n"
"buffer_load_dwordx4 v9, s[16:19], s61 offen offset:1024 sc1 lds\n"
"v_cvt_pk_bf16_f32 v162, v132, v133\n"
"v_cvt_pk_bf16_f32 v163, v134, v135\n"
"v_mfma_f32_16x16x32_bf16 a[104:107], a[224:227], v[216:219], a[104:107]\n"
"s_add_u32 s74, s72, 0\n"
"buffer_store_dwordx4 v[160:163], v1, s[4:7], s74 offen sc1\n"
"v_mfma_f32_16x16x32_bf16 a[104:107], a[228:231], v[220:223], a[104:107]\n"
"v_accvgpr_read_b32 v144, a8\n"
"v_accvgpr_read_b32 v145, a9\n"
"v_mfma_f32_16x16x32_bf16 a[76:79], a[232:235], v[208:211], a[76:79]\n"
"v_accvgpr_read_b32 v146, a10\n"
"v_accvgpr_read_b32 v147, a11\n"
"v_mfma_f32_16x16x32_bf16 a[76:79], a[236:239], v[212:215], a[76:79]\n"
"v_accvgpr_read_b32 v148, a12\n"
"v_accvgpr_read_b32 v149, a13\n"
"v_mfma_f32_16x16x32_bf16 a[108:111], a[232:235], v[216:219], a[108:111]\n"
"v_accvgpr_read_b32 v150, a14\n"
"v_accvgpr_read_b32 v151, a15\n"
"v_mfma_f32_16x16x32_bf16 a[108:111], a[236:239], v[220:223], a[108:111]\n"
"v_mul_f32 v144, v144, v48\n"
"v_mul_f32 v145, v145, v48\n"
"v_mfma_f32_16x16x32_bf16 a[80:83], a[240:243], v[208:211], a[80:83]\n"
"v_mul_f32 v146, v146, v48\n"
"v_mul_f32 v147, v147, v48\n"
"v_mfma_f32_16x16x32_bf16 a[80:83], a[244:247], v[212:215], a[80:83]\n"
"buffer_load_dwordx4 v9, s[16:19], s61 offen offset:2048 sc1 lds\n"
"v_mul_f32 v148, v148, v48\n"
"v_mul_f32 v149, v149, v48\n"
"v_mfma_f32_16x16x32_bf16 a[112:115], a[240:243], v[216:219], a[112:115]\n"
"v_mul_f32 v150, v150, v48\n"
"v_mul_f32 v151, v151, v48\n"
"v_mfma_f32_16x16x32_bf16 a[112:115], a[244:247], v[220:223], a[112:115]\n"
"v_cvt_pk_bf16_f32 v164, v144, v145\n"
"v_cvt_pk_bf16_f32 v165, v146, v147\n"
"v_mfma_f32_16x16x32_bf16 a[84:87], a[248:251], v[208:211], a[84:87]\n"
"v_cvt_pk_bf16_f32 v166, v148, v149\n"
"v_cvt_pk_bf16_f32 v167, v150, v151\n"
"v_mfma_f32_16x16x32_bf16 a[84:87], a[252:255], v[212:215], a[84:87]\n"
"s_add_u32 s74, s72, 0x1000\n"
"buffer_store_dwordx4 v[164:167], v1, s[4:7], s74 offen sc1\n"
"v_mfma_f32_16x16x32_bf16 a[116:119], a[248:251], v[216:219], a[116:119]\n"
"v_accvgpr_read_b32 v128, a16\n"
"v_accvgpr_read_b32 v129, a17\n"
"v_mfma_f32_16x16x32_bf16 a[116:119], a[252:255], v[220:223], a[116:119]\n"
"v_accvgpr_read_b32 v130, a18\n"
"v_accvgpr_read_b32 v131, a19\n"
"v_mfma_f32_16x16x32_bf16 a[88:91], v[24:27], v[208:211], a[88:91]\n"
"buffer_load_dwordx4 v9, s[16:19], s61 offen offset:3072 sc1 lds\n"
"v_accvgpr_read_b32 v132, a20\n"
"v_accvgpr_read_b32 v133, a21\n"
"v_mfma_f32_16x16x32_bf16 a[88:91], v[28:31], v[212:215], a[88:91]\n"
"v_accvgpr_read_b32 v134, a22\n"
"v_accvgpr_read_b32 v135, a23\n"
"v_mfma_f32_16x16x32_bf16 a[120:123], v[24:27], v[216:219], a[120:123]\n"
"v_mul_f32 v128, v128, v48\n"
"v_mul_f32 v129, v129, v48\n"
"v_mfma_f32_16x16x32_bf16 a[120:123], v[28:31], v[220:223], a[120:123]\n"
"s_add_u32 s60, s60, s64\n"
"s_xor_b32 s62, s62, 0x8000\n"
"s_mov_b32 s64, s65\n"
"s_mov_b32 s65, 0x10000\n"
"s_cmp_eq_u32 s8, 15\n"
"s_cmov_b32 s60, s9\n"
"s_xor_b32 s63, s63, 0x8000\n"
"s_mov_b32 s61, s60\n"
"s_cmp_eq_u32 s8, 15\n"
"s_cmov_b64 s[16:17], s[0:1]\n"
"s_cmov_b32 s63, s47\n"
"s_cmp_eq_u32 s8, 16\n"
"s_cmov_b32 s18, 0\n"
"s_cmov_b32 s63, s78\n"
"v_xor_b32 v3, s67, v3\n"
"v_xor_b32 v2, s66, v2\n"
"s_mov_b32 s67, s66\n"
"s_mov_b32 s66, s68\n"
"s_mov_b32 s68, 0x8000\n"
"s_and_b32 s46, s100, 0xfe\n"
"s_add_u32 s46, s46, 3\n"
"s_sub_u32 s46, s46, s3\n"
"s_lshl_b32 s46, s46, 15\n"
"s_add_u32 s46, s46, 0x3800000\n"
"s_lshr_b32 s75, s46, 8\n"
"s_and_b32 s75, s75, 0x2000\n"
"s_xor_b32 s46, s46, s75\n"
"s_xor_b32 s46, s46, s101\n"
"v_mul_f32 v130, v130, v48\n"
"v_mul_f32 v131, v131, v48\n"
"v_mfma_f32_16x16x32_bf16 a[92:95], v[32:35], v[208:211], a[92:95]\n"
"v_mul_f32 v132, v132, v48\n"
"v_mul_f32 v133, v133, v48\n"
"v_mfma_f32_16x16x32_bf16 a[92:95], v[36:39], v[212:215], a[92:95]\n"
"v_mul_f32 v134, v134, v48\n"
"v_mul_f32 v135, v135, v48\n"
"v_mfma_f32_16x16x32_bf16 a[124:127], v[32:35], v[216:219], a[124:127]\n"
"v_cvt_pk_bf16_f32 v160, v128, v129\n"
"v_cvt_pk_bf16_f32 v161, v130, v131\n"
"v_mfma_f32_16x16x32_bf16 a[124:127], v[36:39], v[220:223], a[124:127]\n"
"v_cvt_pk_bf16_f32 v162, v132, v133\n"
"v_cvt_pk_bf16_f32 v163, v134, v135\n"
"v_mfma_f32_16x16x32_bf16 a[136:139], v[4:7], v[208:211], a[136:139]\n"
"s_add_u32 s74, s72, 0x4000\n"
"buffer_store_dwordx4 v[160:163], v1, s[4:7], s74 offen sc1\n"
"v_mfma_f32_16x16x32_bf16 a[136:139], v[4:7], v[212:215], a[136:139]\n"
"v_accvgpr_read_b32 v144, a24\n"
"v_accvgpr_read_b32 v145, a25\n"
"v_mfma_f32_16x16x32_bf16 a[140:143], v[4:7], v[216:219], a[140:143]\n"
"v_accvgpr_read_b32 v146, a26\n"
"v_accvgpr_read_b32 v147, a27\n"
"v_mfma_f32_16x16x32_bf16 a[140:143], v[4:7], v[220:223], a[140:143]\n"
"v_accvgpr_read_b32 v148, a28\n"
"v_accvgpr_read_b32 v149, a29\n"
"v_accvgpr_read_b32 v150, a30\n"
"v_accvgpr_read_b32 v151, a31\n"
"v_mul_f32 v144, v144, v48\n"
"v_mul_f32 v145, v145, v48\n"
"v_mul_f32 v146, v146, v48\n"
"v_mul_f32 v147, v147, v48\n"
"v_mul_f32 v148, v148, v48\n"
"v_mul_f32 v149, v149, v48\n"
"v_mul_f32 v150, v150, v48\n"
"v_mul_f32 v151, v151, v48\n"
"v_cvt_pk_bf16_f32 v164, v144, v145\n"
"v_cvt_pk_bf16_f32 v165, v146, v147\n"
"v_cvt_pk_bf16_f32 v166, v148, v149\n"
"v_cvt_pk_bf16_f32 v167, v150, v151\n"
"s_add_u32 s74, s72, 0x5000\n"
"buffer_store_dwordx4 v[164:167], v1, s[4:7], s74 offen sc1\n"
"v_accvgpr_read_b32 v121, a128\n"
"v_accvgpr_read_b32 v122, a132\n"
"v_accvgpr_read_b32 v50, a136\n"
"v_accvgpr_read_b32 v51, a140\n"
"v_add_f32 v120, v121, v122\n"
"v_add_f32 v120, v120, v50\n"
"v_add_f32 v120, v120, v51\n"
"v_cmp_nge_f32 vcc, 0x67800000, v120\n"
"s_nop 0\n"
"s_cbranch_vccnz .Ladaln_block_S_E\n"
".Ladaln_block_S_E_R:\n"
"v_rcp_f32 v50, v50\n"
"v_rcp_f32 v51, v51\n"
"v_mov_b32 v122, v49\n"
"v_mov_b32 v123, v49\n"
"v_mov_b32 v124, v50\n"
"s_waitcnt vmcnt(20)\n"
"s_barrier\n"
"v_mov_b32 v125, v50\n"
"v_mov_b32 v126, v51\n"
"v_mov_b32 v127, v51\n"
"v_mfma_f32_16x16x32_bf16 a[0:3], v[44:47], v[44:47], 0\n"
"buffer_load_dwordx4 v[56:59], v1, s[4:7], s46 offen offset:1024 sc1\n"
"s_cmp_eq_u32 s8, 14\n"
"s_cselect_b32 s30, s10, 0x40000000\n"
"s_cmp_eq_u32 s8, 14\n"
"s_cselect_b32 s31, s82, 0\n"
"buffer_load_dword v21, v20, s[4:7], s30 offen sc1\n"
"s_add_u32 s8, s8, 1\n"
"s_mov_b32 m0, s62\n"
"buffer_load_dwordx4 v[72:75], v1, s[4:7], s46 offen sc1\n"
"buffer_load_dwordx4 v1, s[4:7], s60 offen sc1 lds\n"
"v_accvgpr_read_b32 v128, a32\n"
"v_accvgpr_read_b32 v129, a33\n"
"v_mfma_f32_16x16x32_bf16 a[4:7], v[44:47], v[44:47], 0\n"
"v_accvgpr_read_b32 v130, a34\n"
"v_accvgpr_read_b32 v131, a35\n"
"v_mfma_f32_16x16x32_bf16 a[8:11], v[44:47], v[44:47], 0\n"
"v_accvgpr_read_b32 v132, a36\n"
"v_accvgpr_read_b32 v133, a37\n"
"v_accvgpr_read_b32 v134, a38\n"
"v_accvgpr_read_b32 v135, a39\n"
"v_pk_mul_f32 v[128:129], v[128:129], v[122:123]\n"
"v_pk_mul_f32 v[130:131], v[130:131], v[122:123]\n"
"v_pk_mul_f32 v[132:133], v[132:133], v[122:123]\n"
"v_pk_mul_f32 v[134:135], v[134:135], v[122:123]\n"
"v_cvt_pk_bf16_f32 v160, v128, v129\n"
"v_cvt_pk_bf16_f32 v161, v130, v131\n"
"v_cvt_pk_bf16_f32 v162, v132, v133\n"
"v_cvt_pk_bf16_f32 v163, v134, v135\n"
"s_add_u32 s74, s72, 0\n"
"buffer_store_dwordx4 v[160:163], v1, s[4:7], s74 offen offset:1024 sc1\n"
"buffer_load_dwordx4 v[88:91], v1, s[4:7], s46 offen offset:3072 sc1\n"
"buffer_load_dwordx4 v[104:107], v1, s[4:7], s46 offen offset:2048 sc1\n"
"s_add_u32 s46, s46, 0x1000\n"
"v_mfma_f32_16x16x32_bf16 a[12:15], v[44:47], v[44:47], 0\n"
"v_accvgpr_read_b32 v144, a40\n"
"v_accvgpr_read_b32 v145, a41\n"
"v_mfma_f32_16x16x32_bf16 a[16:19], v[44:47], v[44:47], 0\n"
"buffer_load_dwordx4 v1, s[4:7], s60 offen offset:1024 sc1 lds\n"
"v_accvgpr_read_b32 v146, a42\n"
"v_accvgpr_read_b32 v147, a43\n"
"v_mfma_f32_16x16x32_bf16 a[20:23], v[44:47], v[44:47], 0\n"
"buffer_load_dwordx4 v[60:63], v1, s[4:7], s46 offen offset:1024 sc1\n"
"v_accvgpr_read_b32 v148, a44\n"
"v_accvgpr_read_b32 v149, a45\n"
"v_accvgpr_read_b32 v150, a46\n"
"v_accvgpr_read_b32 v151, a47\n"
"v_pk_mul_f32 v[144:145], v[144:145], v[122:123]\n"
"v_pk_mul_f32 v[146:147], v[146:147], v[122:123]\n"
"v_pk_mul_f32 v[148:149], v[148:149], v[122:123]\n"
"v_pk_mul_f32 v[150:151], v[150:151], v[122:123]\n"
"v_cvt_pk_bf16_f32 v164, v144, v145\n"
"v_cvt_pk_bf16_f32 v165, v146, v147\n"
"v_cvt_pk_bf16_f32 v166, v148, v149\n"
"v_cvt_pk_bf16_f32 v167, v150, v151\n"
"s_add_u32 s74, s72, 0x1000\n"
"buffer_store_dwordx4 v[164:167], v1, s[4:7], s74 offen offset:1024 sc1\n"
"buffer_load_dwordx4 v[76:79], v1, s[4:7], s46 offen sc1\n"
"v_mfma_f32_16x16x32_bf16 a[24:27], v[44:47], v[44:47], 0\n"
"v_accvgpr_read_b32 v128, a48\n"
"v_accvgpr_read_b32 v129, a49\n"
"v_mfma_f32_16x16x32_bf16 a[28:31], v[44:47], v[44:47], 0\n"
"v_accvgpr_read_b32 v130, a50\n"
"v_accvgpr_read_b32 v131, a51\n"
"v_mfma_f32_16x16x32_bf16 a[128:131], v[44:47], v[44:47], 0\n"
"buffer_load_dwordx4 v[92:95], v1, s[4:7], s46 offen offset:3072 sc1\n"
"buffer_load_dwordx4 v1, s[4:7], s60 offen offset:2048 sc1 lds\n"
"v_accvgpr_read_b32 v132, a52\n"
"v_accvgpr_read_b32 v133, a53\n"
"v_accvgpr_read_b32 v134, a54\n"
"v_accvgpr_read_b32 v135, a55\n"
"v_pk_mul_f32 v[128:129], v[128:129], v[122:123]\n"
"v_pk_mul_f32 v[130:131], v[130:131], v[122:123]\n"
"v_pk_mul_f32 v[132:133], v[132:133], v[122:123]\n"
"v_pk_mul_f32 v[134:135], v[134:135], v[122:123]\n"
"v_cvt_pk_bf16_f32 v160, v128, v129\n"
"v_cvt_pk_bf16_f32 v161, v130, v131\n"
"v_cvt_pk_bf16_f32 v162, v132, v133\n"
"v_cvt_pk_bf16_f32 v163, v134, v135\n"
"s_add_u32 s74, s72, 0x4000\n"
"buffer_store_dwordx4 v[160:163], v1, s[4:7], s74 offen offset:1024 sc1\n"
"buffer_load_dwordx4 v[108:111], v1, s[4:7], s46 offen offset:2048 sc1\n"
"s_add_u32 s46, s46, 0x3000\n"
"v_mfma_f32_16x16x32_bf16 a[32:35], v[44:47], v[44:47], 0\n"
"v_accvgpr_read_b32 v144, a56\n"
"v_accvgpr_read_b32 v145, a57\n"
"v_mfma_f32_16x16x32_bf16 a[36:39], v[44:47], v[44:47], 0\n"
"buffer_load_dwordx4 v[64:67], v1, s[4:7], s46 offen offset:1024 sc1\n"
"v_accvgpr_read_b32 v146, a58\n"
"v_accvgpr_read_b32 v147, a59\n"
"v_mfma_f32_16x16x32_bf16 a[40:43], v[44:47], v[44:47], 0\n"
"v_accvgpr_read_b32 v148, a60\n"
"v_accvgpr_read_b32 v149, a61\n"
"v_accvgpr_read_b32 v150, a62\n"
"v_accvgpr_read_b32 v151, a63\n"
"v_pk_mul_f32 v[144:145], v[144:145], v[122:123]\n"
"v_pk_mul_f32 v[146:147], v[146:147], v[122:123]\n"
"v_pk_mul_f32 v[148:149], v[148:149], v[122:123]\n"
"v_pk_mul_f32 v[150:151], v[150:151], v[122:123]\n"
"v_cvt_pk_bf16_f32 v164, v144, v145\n"
"v_cvt_pk_bf16_f32 v165, v146, v147\n"
"v_cvt_pk_bf16_f32 v166, v148, v149\n"
"v_cvt_pk_bf16_f32 v167, v150, v151\n"
"s_add_u32 s74, s72, 0x5000\n"
"buffer_store_dwordx4 v[164:167], v1, s[4:7], s74 offen offset:1024 sc1\n"
"buffer_load_dwordx4 v1, s[4:7], s60 offen offset:3072 sc1 lds\n"
"buffer_load_dwordx4 v[80:83], v1, s[4:7], s46 offen sc1\n"
"v_mfma_f32_16x16x32_bf16 a[44:47], v[44:47], v[44:47], 0\n"
"v_accvgpr_read_b32 v128, a64\n"
"v_accvgpr_read_b32 v129, a65\n"
"v_mfma_f32_16x16x32_bf16 a[48:51], v[44:47], v[44:47], 0\n"
"buffer_load_dwordx4 v[96:99], v1, s[4:7], s46 offen offset:3072 sc1\n"
"v_accvgpr_read_b32 v130, a66\n"
"v_accvgpr_read_b32 v131, a67\n"
"v_mfma_f32_16x16x32_bf16 a[52:55], v[44:47], v[44:47], 0\n"
"s_add_u32 m0, s62, 0x4000\n"
"v_accvgpr_read_b32 v132, a68\n"
"v_accvgpr_read_b32 v133, a69\n"
"v_accvgpr_read_b32 v134, a70\n"
"v_accvgpr_read_b32 v135, a71\n"
"v_pk_mul_f32 v[128:129], v[128:129], v[124:125]\n"
"v_pk_mul_f32 v[130:131], v[130:131], v[124:125]\n"
"v_pk_mul_f32 v[132:133], v[132:133], v[124:125]\n"
"v_pk_mul_f32 v[134:135], v[134:135], v[124:125]\n"
"v_cvt_pk_bf16_f32 v160, v128, v129\n"
"v_cvt_pk_bf16_f32 v161, v130, v131\n"
"v_cvt_pk_bf16_f32 v162, v132, v133\n"
"v_cvt_pk_bf16_f32 v163, v134, v135\n"
"s_add_u32 s74, s72, 0\n"
"buffer_store_dwordx4 v[160:163], v1, s[4:7], s74 offen offset:2048 sc1\n"
"buffer_load_dwordx4 v[112:115], v1, s[4:7], s46 offen offset:2048 sc1\n"
"s_add_u32 s46, s46, 0x1000\n"
"buffer_load_dwordx4 v9, s[4:7], s60 offen sc1 lds\n"
"s_waitcnt vmcnt(31) lgkmcnt(0)\n"
"s_barrier\n"
"buffer_load_dwordx4 v[68:71], v1, s[4:7], s46 offen offset:1024 sc1\n"
"ds_read_b128 a[144:147], v2\n"
"v_mfma_f32_16x16x32_bf16 a[56:59], v[44:47], v[44:47], 0\n"
"v_accvgpr_read_b32 v144, a72\n"
"v_accvgpr_read_b32 v145, a73\n"
"v_mfma_f32_16x16x32_bf16 a[60:63], v[44:47], v[44:47], 0\n"
"ds_read_b128 a[148:151], v2 offset:1024\n"
"v_accvgpr_read_b32 v146, a74\n"
"v_accvgpr_read_b32 v147, a75\n"
"v_mfma_f32_16x16x32_bf16 a[132:135], v[44:47], v[44:47], 0\n"
"buffer_load_dwordx4 v[84:87], v1, s[4:7], s46 offen sc1\n"
"v_accvgpr_read_b32 v148, a76\n"
"v_accvgpr_read_b32 v149, a77\n"
"v_accvgpr_read_b32 v150, a78\n"
"v_accvgpr_read_b32 v151, a79\n"
"v_pk_mul_f32 v[144:145], v[144:145], v[124:125]\n"
"v_pk_mul_f32 v[146:147], v[146:147], v[124:125]\n"
"v_pk_mul_f32 v[148:149], v[148:149], v[124:125]\n"
"v_pk_mul_f32 v[150:151], v[150:151], v[124:125]\n"
"v_cvt_pk_bf16_f32 v164, v144, v145\n"
"v_cvt_pk_bf16_f32 v165, v146, v147\n"
"v_cvt_pk_bf16_f32 v166, v148, v149\n"
"v_cvt_pk_bf16_f32 v167, v150, v151\n"
"s_add_u32 s74, s72, 0x1000\n"
"buffer_store_dwordx4 v[164:167], v1, s[4:7], s74 offen offset:2048 sc1\n"
"ds_read_b128 a[152:155], v2 offset:2048\n"
"buffer_load_dwordx4 v9, s[4:7], s60 offen offset:1024 sc1 lds\n"
"buffer_load_dwordx4 v[100:103], v1, s[4:7], s46 offen offset:3072 sc1\n"
"ds_read_b128 a[156:159], v2 offset:3072\n"
"ds_read_b128 a[160:163], v2 offset:4096\n"
"v_mfma_f32_16x16x32_bf16 a[64:67], v[44:47], v[44:47], 0\n"
"buffer_load_dwordx4 v[116:119], v1, s[4:7], s46 offen offset:2048 sc1\n"
"s_add_u32 s46, s46, 0x3000\n"
"v_accvgpr_read_b32 v128, a80\n"
"v_accvgpr_read_b32 v129, a81\n"
"v_mfma_f32_16x16x32_bf16 a[68:71], v[44:47], v[44:47], 0\n"
"ds_read_b128 a[164:167], v2 offset:5120\n"
"v_accvgpr_read_b32 v130, a82\n"
"v_accvgpr_read_b32 v131, a83\n"
"v_mfma_f32_16x16x32_bf16 a[72:75], v[44:47], v[44:47], 0\n"
"ds_read_b128 a[168:171], v2 offset:6144\n"
"v_accvgpr_read_b32 v132, a84\n"
"v_accvgpr_read_b32 v133, a85\n"
"v_accvgpr_read_b32 v134, a86\n"
"v_accvgpr_read_b32 v135, a87\n"
"v_pk_mul_f32 v[128:129], v[128:129], v[124:125]\n"
"v_pk_mul_f32 v[130:131], v[130:131], v[124:125]\n"
"v_pk_mul_f32 v[132:133], v[132:133], v[124:125]\n"
"v_pk_mul_f32 v[134:135], v[134:135], v[124:125]\n"
"v_cvt_pk_bf16_f32 v160, v128, v129\n"
"v_cvt_pk_bf16_f32 v161, v130, v131\n"
"v_cvt_pk_bf16_f32 v162, v132, v133\n"
"v_cvt_pk_bf16_f32 v163, v134, v135\n"
"s_add_u32 s74, s72, 0x4000\n"
"buffer_store_dwordx4 v[160:163], v1, s[4:7], s74 offen offset:2048 sc1\n"
"buffer_load_dwordx4 v9, s[4:7], s60 offen offset:2048 sc1 lds\n"
"ds_read_b128 a[172:175], v2 offset:7168\n"
"ds_read_b128 a[176:179], v2 offset:8192\n"
"v_mfma_f32_16x16x32_bf16 a[76:79], v[44:47], v[44:47], 0\n"
"v_accvgpr_read_b32 v144, a88\n"
"v_accvgpr_read_b32 v145, a89\n"
"v_mfma_f32_16x16x32_bf16 a[80:83], v[44:47], v[44:47], 0\n"
"ds_read_b128 a[180:183], v2 offset:9216\n"
"v_accvgpr_read_b32 v146, a90\n"
"v_accvgpr_read_b32 v147, a91\n"
"v_mfma_f32_16x16x32_bf16 a[84:87], v[44:47], v[44:47], 0\n"
"ds_read_b128 a[184:187], v2 offset:10240\n"
"v_accvgpr_read_b32 v148, a92\n"
"v_accvgpr_read_b32 v149, a93\n"
"v_accvgpr_read_b32 v150, a94\n"
"v_accvgpr_read_b32 v151, a95\n"
"v_pk_mul_f32 v[144:145], v[144:145], v[124:125]\n"
"v_pk_mul_f32 v[146:147], v[146:147], v[124:125]\n"
"v_pk_mul_f32 v[148:149], v[148:149], v[124:125]\n"
"v_pk_mul_f32 v[150:151], v[150:151], v[124:125]\n"
"v_cvt_pk_bf16_f32 v164, v144, v145\n"
"v_cvt_pk_bf16_f32 v165, v146, v147\n"
"v_cvt_pk_bf16_f32 v166, v148, v149\n"
"v_cvt_pk_bf16_f32 v167, v150, v151\n"
"s_add_u32 s74, s72, 0x5000\n"
"buffer_store_dwordx4 v[164:167], v1, s[4:7], s74 offen offset:2048 sc1\n"
"ds_read_b128 a[188:191], v2 offset:11264\n"
"buffer_load_dwordx4 v9, s[4:7], s60 offen offset:3072 sc1 lds\n"
"ds_read_b128 a[192:195], v2 offset:12288\n"
"v_mfma_f32_16x16x32_bf16 a[88:91], v[44:47], v[44:47], 0\n"
"v_accvgpr_read_b32 v128, a96\n"
"v_accvgpr_read_b32 v129, a97\n"
"v_mfma_f32_16x16x32_bf16 a[92:95], v[44:47], v[44:47], 0\n"
"ds_read_b128 a[196:199], v2 offset:13312\n"
"v_accvgpr_read_b32 v130, a98\n"
"v_accvgpr_read_b32 v131, a99\n"
"v_mfma_f32_16x16x32_bf16 a[136:139], v[44:47], v[44:47], 0\n"
"v_accvgpr_read_b32 v132, a100\n"
"v_accvgpr_read_b32 v133, a101\n"
"v_accvgpr_read_b32 v134, a102\n"
"v_accvgpr_read_b32 v135, a103\n"
"v_pk_mul_f32 v[128:129], v[128:129], v[126:127]\n"
"v_pk_mul_f32 v[130:131], v[130:131], v[126:127]\n"
"v_pk_mul_f32 v[132:133], v[132:133], v[126:127]\n"
"v_pk_mul_f32 v[134:135], v[134:135], v[126:127]\n"
"v_cvt_pk_bf16_f32 v160, v128, v129\n"
"v_cvt_pk_bf16_f32 v161, v130, v131\n"
"v_cvt_pk_bf16_f32 v162, v132, v133\n"
"v_cvt_pk_bf16_f32 v163, v134, v135\n"
"s_add_u32 s74, s72, 0\n"
"buffer_store_dwordx4 v[160:163], v1, s[4:7], s74 offen offset:3072 sc1\n"
"ds_read_b128 a[200:203], v2 offset:14336\n"
"s_mov_b32 m0, s63\n"
"ds_read_b128 a[204:207], v2 offset:15360\n"
"buffer_load_dwordx4 v1, s[16:19], s60 offen sc1 lds\n"
"v_mfma_f32_16x16x32_bf16 a[96:99], v[44:47], v[44:47], 0\n"
"v_accvgpr_read_b32 v144, a104\n"
"v_accvgpr_read_b32 v145, a105\n"
"v_mfma_f32_16x16x32_bf16 a[100:103], v[44:47], v[44:47], 0\n"
"v_accvgpr_read_b32 v146, a106\n"
"v_accvgpr_read_b32 v147, a107\n"
"v_accvgpr_read_b32 v148, a108\n"
"v_accvgpr_read_b32 v149, a109\n"
"v_accvgpr_read_b32 v150, a110\n"
"v_accvgpr_read_b32 v151, a111\n"
"v_pk_mul_f32 v[144:145], v[144:145], v[126:127]\n"
"v_pk_mul_f32 v[146:147], v[146:147], v[126:127]\n"
"v_pk_mul_f32 v[148:149], v[148:149], v[126:127]\n"
"v_pk_mul_f32 v[150:151], v[150:151], v[126:127]\n"
"v_cvt_pk_bf16_f32 v164, v144, v145\n"
"v_cvt_pk_bf16_f32 v165, v146, v147\n"
"v_cvt_pk_bf16_f32 v166, v148, v149\n"
"v_cvt_pk_bf16_f32 v167, v150, v151\n"
"s_add_u32 s74, s72, 0x1000\n"
"buffer_store_dwordx4 v[164:167], v1, s[4:7], s74 offen offset:3072 sc1\n"
"buffer_load_dwordx4 v1, s[16:19], s60 offen offset:1024 sc1 lds\n"
"v_mfma_f32_16x16x32_bf16 a[104:107], v[44:47], v[44:47], 0\n"
"v_accvgpr_read_b32 v128, a112\n"
"v_accvgpr_read_b32 v129, a113\n"
"v_mfma_f32_16x16x32_bf16 a[108:111], v[44:47], v[44:47], 0\n"
"v_accvgpr_read_b32 v130, a114\n"
"v_accvgpr_read_b32 v131, a115\n"
"v_accvgpr_read_b32 v132, a116\n"
"v_accvgpr_read_b32 v133, a117\n"
"v_accvgpr_read_b32 v134, a118\n"
"v_accvgpr_read_b32 v135, a119\n"
"v_pk_mul_f32 v[128:129], v[128:129], v[126:127]\n"
"v_pk_mul_f32 v[130:131], v[130:131], v[126:127]\n"
"v_pk_mul_f32 v[132:133], v[132:133], v[126:127]\n"
"v_pk_mul_f32 v[134:135], v[134:135], v[126:127]\n"
"v_cvt_pk_bf16_f32 v160, v128, v129\n"
"v_cvt_pk_bf16_f32 v161, v130, v131\n"
"v_cvt_pk_bf16_f32 v162, v132, v133\n"
"v_cvt_pk_bf16_f32 v163, v134, v135\n"
"s_add_u32 s74, s72, 0x4000\n"
"buffer_store_dwordx4 v[160:163], v1, s[4:7], s74 offen offset:3072 sc1\n"
"buffer_load_dwordx4 v1, s[16:19], s60 offen offset:2048 sc1 lds\n"
"v_mfma_f32_16x16x32_bf16 a[112:115], v[44:47], v[44:47], 0\n"
"v_accvgpr_read_b32 v144, a120\n"
"v_accvgpr_read_b32 v145, a121\n"
"v_mfma_f32_16x16x32_bf16 a[116:119], v[44:47], v[44:47], 0\n"
"v_accvgpr_read_b32 v146, a122\n"
"v_accvgpr_read_b32 v147, a123\n"
"v_accvgpr_read_b32 v148, a124\n"
"v_accvgpr_read_b32 v149, a125\n"
"v_accvgpr_read_b32 v150, a126\n"
"v_accvgpr_read_b32 v151, a127\n"
"v_pk_mul_f32 v[144:145], v[144:145], v[126:127]\n"
"v_pk_mul_f32 v[146:147], v[146:147], v[126:127]\n"
"v_pk_mul_f32 v[148:149], v[148:149], v[126:127]\n"
"v_pk_mul_f32 v[150:151], v[150:151], v[126:127]\n"
"v_cvt_pk_bf16_f32 v164, v144, v145\n"
"v_cvt_pk_bf16_f32 v165, v146, v147\n"
"v_cvt_pk_bf16_f32 v166, v148, v149\n"
"v_cvt_pk_bf16_f32 v167, v150, v151\n"
"s_add_u32 s74, s72, 0x5000\n"
"buffer_store_dwordx4 v[164:167], v1, s[4:7], s74 offen offset:3072 sc1\n"
"buffer_load_dwordx4 v1, s[16:19], s60 offen offset:3072 sc1 lds\n"
"v_mfma_f32_16x16x32_bf16 a[120:123], v[44:47], v[44:47], 0\n"
"v_xor_b32 v224, 0x80000000, v41\n"
"v_xor_b32 v225, 0x80000000, v41\n"
"v_mfma_f32_16x16x32_bf16 a[124:127], v[44:47], v[44:47], 0\n"
"v_xor_b32 v228, 0x80000000, v40\n"
"v_xor_b32 v229, 0x80000000, v40\n"
"v_mfma_f32_16x16x32_bf16 a[140:143], v[44:47], v[44:47], 0\n"
"v_xor_b32 v232, 0x80000000, v43\n"
"v_xor_b32 v233, 0x80000000, v43\n"
"v_xor_b32 v236, 0x80000000, v42\n"
"v_xor_b32 v237, 0x80000000, v42\n"
"v_mov_b32 v172, v8\n"
"v_mov_b32 v173, v8\n"
"v_mov_b64 v[226:227], v[224:225]\n"
"v_mov_b64 v[230:231], v[228:229]\n"
"v_mov_b64 v[234:235], v[232:233]\n"
"v_mov_b64 v[238:239], v[236:237]\n"
"v_mov_b64 v[174:175], v[172:173]\n"
"v_mov_b64 v[184:185], v[172:173]\n"
"v_mov_b64 v[186:187], v[172:173]\n"
"v_mov_b64 v[188:189], v[172:173]\n"
"v_mov_b64 v[190:191], v[172:173]\n"
"v_mov_b64 v[208:209], 0\n"
"v_mov_b64 v[216:217], 0\n"
"v_mov_b64 v[210:211], 0\n"
"v_mov_b64 v[218:219], 0\n"
"s_waitcnt vmcnt(39)\n"
"v_cmp_ne_u32 vcc, s31, v21\n"
"s_nop 0\n"
"s_cbranch_vccnz .Ladaln_block_P_prep3\n"
".Ladaln_block_P_prep3_R:\n"
"v_mov_b64 v[212:213], 0\n"
"s_waitcnt vmcnt(12) lgkmcnt(0)\n"
"s_nop 1\n"
"s_branch .Ladaln_block_A_CHK\n"
".Ladaln_block_A_FIRSTD:\n"
"s_nop 1\n"
"s_waitcnt lgkmcnt(0)\n"
"v_cvt_pk_bf16_f32 v68, v224, v225\n"
"v_cvt_pk_bf16_f32 v69, v226, v227\n"
"v_cvt_pk_bf16_f32 v70, v240, v241\n"
"v_cvt_pk_bf16_f32 v71, v242, v243\n"
"v_cvt_pk_bf16_f32 v84, v228, v229\n"
"v_cvt_pk_bf16_f32 v85, v230, v231\n"
"v_cvt_pk_bf16_f32 v86, v244, v245\n"
"v_cvt_pk_bf16_f32 v87, v246, v247\n"
"v_mfma_f32_16x16x32_bf16 v[128:131], a[144:147], v[56:59], 0\n"
"v_cvt_pk_bf16_f32 v100, v232, v233\n"
"v_mfma_f32_16x16x32_bf16 v[128:131], a[148:151], v[60:63], v[128:131]\n"
"v_cvt_pk_bf16_f32 v101, v234, v235\n"
"v_mfma_f32_16x16x32_bf16 v[128:131], a[152:155], v[64:67], v[128:131]\n"
"v_cvt_pk_bf16_f32 v102, v248, v249\n"
"v_mfma_f32_16x16x32_bf16 v[128:131], a[156:159], v[68:71], v[128:131]\n"
"v_cvt_pk_bf16_f32 v103, v250, v251\n"
"v_mfma_f32_16x16x32_bf16 v[132:135], a[144:147], v[72:75], 0\n"
"v_cvt_pk_bf16_f32 v116, v236, v237\n"
"v_mfma_f32_16x16x32_bf16 v[132:135], a[148:151], v[76:79], v[132:135]\n"
"v_cvt_pk_bf16_f32 v117, v238, v239\n"
"v_mfma_f32_16x16x32_bf16 v[132:135], a[152:155], v[80:83], v[132:135]\n"
"v_cvt_pk_bf16_f32 v118, v252, v253\n"
"v_mfma_f32_16x16x32_bf16 v[132:135], a[156:159], v[84:87], v[132:135]\n"
"v_cvt_pk_bf16_f32 v119, v254, v255\n"
"v_mfma_f32_16x16x32_bf16 v[136:139], a[144:147], v[88:91], 0\n"
"v_cndmask_b32 v128, v128, v8, s[50:51]\n"
"v_mfma_f32_16x16x32_bf16 v[136:139], a[148:151], v[92:95], v[136:139]\n"
"v_cndmask_b32 v129, v129, v8, s[52:53]\n"
"v_mfma_f32_16x16x32_bf16 v[136:139], a[152:155], v[96:99], v[136:139]\n"
"v_cndmask_b32 v130, v130, v8, s[54:55]\n"
"v_mfma_f32_16x16x32_bf16 v[136:139], a[156:159], v[100:103], v[136:139]\n"
"v_cndmask_b32 v131, v131, v8, s[56:57]\n"
"v_mfma_f32_16x16x32_bf16 v[140:143], a[144:147], v[104:107], 0\n"
"v_max3_f32 v120, v128, v129, v130\n"
"v_mfma_f32_16x16x32_bf16 v[140:143], a[148:151], v[108:111], v[140:143]\n"
"v_max_f32 v124, v120, v131\n"
"v_mfma_f32_16x16x32_bf16 v[140:143], a[152:155], v[112:115], v[140:143]\n"
"v_max_f32 v120, v120, v131\n"
"v_mfma_f32_16x16x32_bf16 v[140:143], a[156:159], v[116:119], v[140:143]\n"
"v_max3_f32 v121, v132, v133, v134\n"
"v_max_f32 v125, v121, v135\n"
"v_max_f32 v121, v121, v135\n"
"v_max3_f32 v122, v136, v137, v138\n"
"v_max_f32 v126, v122, v139\n"
"v_max_f32 v122, v122, v139\n"
"s_nop 1\n"
"v_max3_f32 v123, v140, v141, v142\n"
"v_max_f32 v127, v123, v143\n"
"v_max_f32 v123, v123, v143\n"
"v_permlane16_swap_b32 v120, v124\n"
"v_permlane16_swap_b32 v121, v125\n"
"v_permlane16_swap_b32 v122, v126\n"
"v_permlane16_swap_b32 v123, v127\n"
"v_max_f32 v120, v120, v124\n"
"v_max_f32 v121, v121, v125\n"
"v_max_f32 v122, v122, v126\n"
"v_max_f32 v123, v123, v127\n"
"v_max_f32 v124, v120, v124\n"
"v_max_f32 v125, v121, v125\n"
"v_max_f32 v126, v122, v126\n"
"v_max_f32 v127, v123, v127\n"
"v_permlane32_swap_b32 v120, v124\n"
"v_permlane32_swap_b32 v121, v125\n"
"v_permlane32_swap_b32 v122, v126\n"
"v_permlane32_swap_b32 v123, v127\n"
"v_max_f32 v120, v120, v124\n"
"v_max_f32 v121, v121, v125\n"
"v_max_f32 v122, v122, v126\n"
"v_max_f32 v123, v123, v127\n"
"v_xor_b32 v224, 0x80000000, v120\n"
"v_xor_b32 v225, 0x80000000, v120\n"
"v_xor_b32 v226, 0x80000000, v120\n"
"v_xor_b32 v227, 0x80000000, v120\n"
"v_xor_b32 v228, 0x80000000, v121\n"
"v_xor_b32 v229, 0x80000000, v121\n"
"v_xor_b32 v230, 0x80000000, v121\n"
"v_xor_b32 v231, 0x80000000, v121\n"
"v_cndmask_b32 v240, v224, v8, s[50:51]\n"
"v_cndmask_b32 v241, v225, v8, s[52:53]\n"
"v_cndmask_b32 v242, v226, v8, s[54:55]\n"
"v_cndmask_b32 v243, v227, v8, s[56:57]\n"
"v_cndmask_b32 v244, v228, v8, s[50:51]\n"
"v_cndmask_b32 v245, v229, v8, s[52:53]\n"
"v_cndmask_b32 v246, v230, v8, s[54:55]\n"
"v_cndmask_b32 v247, v231, v8, s[56:57]\n"
"v_mfma_f32_16x16x32_bf16 v[132:135], a[144:147], v[72:75], v[228:231]\n"
"ds_write_b128 v11, v[120:123]\n"
"v_xor_b32 v11, 0xc00, v11\n"
"v_mfma_f32_16x16x32_bf16 v[132:135], a[148:151], v[76:79], v[132:135]\n"
"v_xor_b32 v232, 0x80000000, v122\n"
"v_xor_b32 v233, 0x80000000, v122\n"
"v_xor_b32 v234, 0x80000000, v122\n"
"v_mfma_f32_16x16x32_bf16 v[132:135], a[152:155], v[80:83], v[132:135]\n"
"v_xor_b32 v235, 0x80000000, v122\n"
"v_xor_b32 v236, 0x80000000, v123\n"
"v_xor_b32 v237, 0x80000000, v123\n"
"v_mfma_f32_16x16x32_bf16 v[132:135], a[156:159], v[84:87], v[132:135]\n"
"v_xor_b32 v238, 0x80000000, v123\n"
"v_xor_b32 v239, 0x80000000, v123\n"
"s_add_u32 s61, s61, 0x8000\n"
"v_mfma_f32_16x16x32_bf16 v[128:131], a[144:147], v[56:59], v[240:243]\n"
"v_mov_b32 v194, 0\n"
"v_mov_b32 v195, 0\n"
"v_cndmask_b32 v248, v232, v8, s[50:51]\n"
"v_mfma_f32_16x16x32_bf16 v[128:131], a[148:151], v[60:63], v[128:131]\n"
"v_cndmask_b32 v249, v233, v8, s[52:53]\n"
"v_cndmask_b32 v250, v234, v8, s[54:55]\n"
"v_cndmask_b32 v251, v235, v8, s[56:57]\n"
"v_mfma_f32_16x16x32_bf16 v[128:131], a[152:155], v[64:67], v[128:131]\n"
"v_cndmask_b32 v252, v236, v8, s[50:51]\n"
"v_cndmask_b32 v253, v237, v8, s[52:53]\n"
"v_cndmask_b32 v254, v238, v8, s[54:55]\n"
"v_mfma_f32_16x16x32_bf16 v[128:131], a[156:159], v[68:71], v[128:131]\n"
"v_cndmask_b32 v255, v239, v8, s[56:57]\n"
"v_mov_b32 v184, v8\n"
"s_waitcnt lgkmcnt(0)\n"
"v_mfma_f32_16x16x32_bf16 v[148:151], a[160:163], v[72:75], v[244:247]\n"
"s_barrier\n"
"v_mov_b32 v185, v8\n"
"v_mov_b32 v186, v8\n"
"v_mfma_f32_16x16x32_bf16 v[148:151], a[164:167], v[76:79], v[148:151]\n"
"ds_read_b128 v[40:43], v11\n"
"v_mov_b32 v187, v8\n"
"v_and_b32 v19, 7, v0\n"
"v_mfma_f32_16x16x32_bf16 v[148:151], a[168:171], v[80:83], v[148:151]\n"
"ds_read_b128 a[208:211], v3\n"
"v_exp_f32 v132, v132\n"
"v_mfma_f32_16x16x32_bf16 v[148:151], a[172:175], v[84:87], v[148:151]\n"
"ds_read_b128 a[212:215], v3 offset:1024\n"
"s_cmp_eq_u32 s49, 0\n"
"s_cselect_b32 s74, s64, 0x8000\n"
"v_mfma_f32_16x16x32_bf16 v[136:139], a[144:147], v[88:91], v[232:235]\n"
"ds_read_b128 a[216:219], v3 offset:2048\n"
"v_exp_f32 v133, v133\n"
"v_mfma_f32_16x16x32_bf16 v[136:139], a[148:151], v[92:95], v[136:139]\n"
"ds_read_b128 a[220:223], v3 offset:3072\n"
"v_cvt_pk_bf16_f32 v200, v132, v133\n"
"s_add_u32 s61, s61, s74\n"
"v_mfma_f32_16x16x32_bf16 v[136:139], a[152:155], v[96:99], v[136:139]\n"
"ds_read_b128 a[224:227], v3 offset:4096\n"
"v_exp_f32 v134, v134\n"
"v_mfma_f32_16x16x32_bf16 v[136:139], a[156:159], v[100:103], v[136:139]\n"
"ds_read_b128 a[228:231], v3 offset:5120\n"
"s_mov_b32 s60, s61\n"
"s_xor_b32 s62, s62, 0x8000\n"
"v_mfma_f32_16x16x32_bf16 v[140:143], a[144:147], v[104:107], v[236:239]\n"
"ds_read_b128 a[232:235], v3 offset:6144\n"
"v_exp_f32 v135, v135\n"
"v_mfma_f32_16x16x32_bf16 v[140:143], a[148:151], v[108:111], v[140:143]\n"
"ds_read_b128 a[236:239], v3 offset:7168\n"
"v_cvt_pk_bf16_f32 v201, v134, v135\n"
"s_xor_b32 s63, s63, 0x8000\n"
"v_mfma_f32_16x16x32_bf16 v[140:143], a[152:155], v[112:115], v[140:143]\n"
"ds_read_b128 a[240:243], v3 offset:8192\n"
"v_exp_f32 v128, v128\n"
"v_mfma_f32_16x16x32_bf16 v[140:143], a[156:159], v[116:119], v[140:143]\n"
"ds_read_b128 a[244:247], v3 offset:9216\n"
"v_lshlrev_b32 v19, 7, v19\n"
"v_mfma_f32_16x16x32_bf16 v[152:155], a[160:163], v[88:91], v[232:235]\n"
"ds_read_b128 a[248:251], v3 offset:10240\n"
"v_exp_f32 v129, v129\n"
"v_mfma_f32_16x16x32_bf16 v[152:155], a[164:167], v[92:95], v[152:155]\n"
"ds_read_b128 a[252:255], v3 offset:11264\n"
"buffer_load_dword v18, v19, s[4:7], s75 offen sc1\n"
"v_cvt_pk_bf16_f32 v192, v128, v129\n"
"v_mfma_f32_16x16x32_bf16 v[152:155], a[168:171], v[96:99], v[152:155]\n"
"ds_read_b128 v[24:27], v3 offset:12288\n"
"v_exp_f32 v130, v130\n"
"v_mfma_f32_16x16x32_bf16 v[152:155], a[172:175], v[100:103], v[152:155]\n"
"ds_read_b128 v[28:31], v3 offset:13312\n"
"v_mfma_f32_16x16x32_bf16 v[156:159], a[160:163], v[104:107], v[236:239]\n"
"ds_read_b128 v[32:35], v3 offset:14336\n"
"v_exp_f32 v131, v131\n"
"v_mfma_f32_16x16x32_bf16 v[156:159], a[164:167], v[108:111], v[156:159]\n"
"ds_read_b128 v[36:39], v3 offset:15360\n"
"v_cvt_pk_bf16_f32 v193, v130, v131\n"
"v_mfma_f32_16x16x32_bf16 v[156:159], a[168:171], v[112:115], v[156:159]\n"
"v_xor_b32 v2, s66, v2\n"
"v_exp_f32 v148, v148\n"
"v_mfma_f32_16x16x32_bf16 v[156:159], a[172:175], v[116:119], v[156:159]\n"
"v_mfma_f32_16x16x32_bf16 v[168:171], a[176:179], v[88:91], v[248:251]\n"
"v_exp_f32 v149, v149\n"
"s_waitcnt vmcnt(0)\n"
"v_cmp_ne_u32 vcc, s82, v18\n"
"s_nop 0\n"
"s_cbranch_vccnz .Ladaln_block_P_kvbd\n"
".Ladaln_block_P_kvbd_R:\n"
"v_mfma_f32_16x16x32_bf16 v[168:171], a[180:183], v[92:95], v[168:171]\n"
"v_cvt_pk_bf16_f32 v202, v148, v149\n"
"s_mov_b32 m0, s62\n"
"v_mfma_f32_16x16x32_bf16 v[168:171], a[184:187], v[96:99], v[168:171]\n"
"buffer_load_dwordx4 v1, s[4:7], s60 offen sc1 lds\n"
"v_exp_f32 v150, v150\n"
"v_mfma_f32_16x16x32_bf16 v[168:171], a[188:191], v[100:103], v[168:171]\n"
"s_waitcnt lgkmcnt(0)\n"
"v_mfma_f32_16x16x32_bf16 v[172:175], a[176:179], v[104:107], v[236:239]\n"
"s_barrier\n"
"v_exp_f32 v151, v151\n"
"v_mfma_f32_16x16x32_bf16 v[172:175], a[180:183], v[108:111], v[172:175]\n"
"buffer_load_dwordx4 v1, s[4:7], s60 offen offset:1024 sc1 lds\n"
"v_cvt_pk_bf16_f32 v203, v150, v151\n"
"v_mfma_f32_16x16x32_bf16 v[172:175], a[184:187], v[112:115], v[172:175]\n"
"ds_read_b128 a[144:147], v2\n"
"v_exp_f32 v136, v136\n"
"v_mfma_f32_16x16x32_bf16 v[172:175], a[188:191], v[116:119], v[172:175]\n"
"ds_read_b128 a[148:151], v2 offset:1024\n"
"v_mfma_f32_16x16x32_bf16 v[188:191], a[192:195], v[104:107], v[252:255]\n"
"ds_read_b128 a[152:155], v2 offset:2048\n"
"v_exp_f32 v137, v137\n"
"v_mfma_f32_16x16x32_bf16 v[188:191], a[196:199], v[108:111], v[188:191]\n"
"ds_read_b128 a[156:159], v2 offset:3072\n"
"buffer_load_dwordx4 v1, s[4:7], s60 offen offset:2048 sc1 lds\n"
"v_cvt_pk_bf16_f32 v208, v136, v137\n"
"v_mfma_f32_16x16x32_bf16 v[188:191], a[200:203], v[112:115], v[188:191]\n"
"ds_read_b128 a[160:163], v2 offset:4096\n"
"v_exp_f32 v138, v138\n"
"v_mfma_f32_16x16x32_bf16 v[188:191], a[204:207], v[116:119], v[188:191]\n"
"ds_read_b128 a[164:167], v2 offset:5120\n"
"buffer_load_dwordx4 v1, s[4:7], s60 offen offset:3072 sc1 lds\n"
"v_mfma_f32_16x16x32_bf16 a[0:3], a[208:211], v[192:195], 0\n"
"ds_read_b128 a[168:171], v2 offset:6144\n"
"v_exp_f32 v139, v139\n"
"v_mfma_f32_16x16x32_bf16 a[32:35], a[208:211], v[200:203], 0\n"
"ds_read_b128 a[172:175], v2 offset:7168\n"
"v_cvt_pk_bf16_f32 v209, v138, v139\n"
"s_mov_b32 m0, s63\n"
"v_mfma_f32_16x16x32_bf16 a[4:7], a[216:219], v[192:195], 0\n"
"ds_read_b128 a[176:179], v2 offset:8192\n"
"v_exp_f32 v140, v140\n"
"v_mfma_f32_16x16x32_bf16 a[36:39], a[216:219], v[200:203], 0\n"
"ds_read_b128 a[180:183], v2 offset:9216\n"
"buffer_load_dwordx4 v1, s[16:19], s60 offen sc1 lds\n"
"v_mfma_f32_16x16x32_bf16 a[8:11], a[224:227], v[192:195], 0\n"
"ds_read_b128 a[184:187], v2 offset:10240\n"
"v_exp_f32 v141, v141\n"
"v_mfma_f32_16x16x32_bf16 a[40:43], a[224:227], v[200:203], 0\n"
"ds_read_b128 a[188:191], v2 offset:11264\n"
"buffer_load_dwordx4 v1, s[16:19], s60 offen offset:1024 sc1 lds\n"
"v_cvt_pk_bf16_f32 v216, v140, v141\n"
"v_mfma_f32_16x16x32_bf16 a[12:15], a[232:235], v[192:195], 0\n"
"ds_read_b128 a[192:195], v2 offset:12288\n"
"v_exp_f32 v142, v142\n"
"v_mfma_f32_16x16x32_bf16 a[44:47], a[232:235], v[200:203], 0\n"
"ds_read_b128 a[196:199], v2 offset:13312\n"
"buffer_load_dwordx4 v1, s[16:19], s60 offen offset:2048 sc1 lds\n"
"v_mfma_f32_16x16x32_bf16 a[16:19], a[240:243], v[192:195], 0\n"
"ds_read_b128 a[200:203], v2 offset:14336\n"
"v_exp_f32 v143, v143\n"
"v_mfma_f32_16x16x32_bf16 a[48:51], a[240:243], v[200:203], 0\n"
"ds_read_b128 a[204:207], v2 offset:15360\n"
"buffer_load_dwordx4 v1, s[16:19], s60 offen offset:3072 sc1 lds\n"
"v_cvt_pk_bf16_f32 v217, v142, v143\n"
"v_mfma_f32_16x16x32_bf16 a[20:23], a[248:251], v[192:195], 0\n"
"v_exp_f32 v152, v152\n"
"v_mfma_f32_16x16x32_bf16 a[52:55], a[248:251], v[200:203], 0\n"
"v_mfma_f32_16x16x32_bf16 a[24:27], v[24:27], v[192:195], 0\n"
"v_exp_f32 v153, v153\n"
"v_mfma_f32_16x16x32_bf16 a[56:59], v[24:27], v[200:203], 0\n"
"v_cvt_pk_bf16_f32 v210, v152, v153\n"
"v_mfma_f32_16x16x32_bf16 a[28:31], v[32:35], v[192:195], 0\n"
"v_exp_f32 v154, v154\n"
"v_mfma_f32_16x16x32_bf16 a[60:63], v[32:35], v[200:203], 0\n"
"v_mfma_f32_16x16x32_bf16 a[128:131], v[4:7], v[192:195], 0\n"
"v_exp_f32 v155, v155\n"
"s_waitcnt lgkmcnt(0)\n"
"v_mfma_f32_16x16x32_bf16 a[132:135], v[4:7], v[200:203], 0\n"
"v_cvt_pk_bf16_f32 v211, v154, v155\n"
"v_exp_f32 v156, v156\n"
"v_exp_f32 v157, v157\n"
"s_nop 0\n"
"v_cvt_pk_bf16_f32 v218, v156, v157\n"
"v_exp_f32 v158, v158\n"
"v_exp_f32 v159, v159\n"
"s_nop 0\n"
"v_cvt_pk_bf16_f32 v219, v158, v159\n"
"v_exp_f32 v168, v168\n"
"v_exp_f32 v169, v169\n"
"s_nop 0\n"
"v_cvt_pk_bf16_f32 v212, v168, v169\n"
"v_exp_f32 v170, v170\n"
"v_exp_f32 v171, v171\n"
"s_nop 0\n"
"v_cvt_pk_bf16_f32 v213, v170, v171\n"
"s_branch .Ladaln_block_A_FIRST_DONE\n"
".Ladaln_block_A_P0:\n"
"s_add_u32 m0, s62, 0x4000\n"
"s_mov_b32 s64, s65\n"
"buffer_load_dwordx4 v9, s[4:7], s60 offen sc1 lds\n"
"buffer_load_dwordx4 v9, s[4:7], s60 offen offset:1024 sc1 lds\n"
"buffer_load_dwordx4 v9, s[4:7], s60 offen offset:2048 sc1 lds\n"
"buffer_load_dwordx4 v9, s[4:7], s60 offen offset:3072 sc1 lds\n"
"s_mov_b32 s65, 0x10000\n"
"s_mov_b32 s67, s66\n"
"s_mov_b32 s66, s68\n"
"s_mov_b32 s68, 0x8000\n"
"s_waitcnt vmcnt(5)\n"
"s_cmp_eq_u32 s3, 2\n"
"s_cbranch_scc1 .Ladaln_block_A_DIAG_DONE\n"
"s_sub_u32 s69, s75, 1\n"
"s_branch .Ladaln_block_A_CHK\n"
".Ladaln_block_P_par:\n"
"s_mov_b32 s83, 0\n"
".Ladaln_block_P_par_L:\n"
"s_sleep 2\n"
"buffer_load_dwordx2 v[92:93], v101, s[4:7], s61 offen sc1\n"
"s_waitcnt vmcnt(0)\n"
"v_cmp_ne_u32 vcc, s82, v93\n"
"s_nop 0\n"
"s_cbranch_vccz .Ladaln_block_P_par_R\n"
"s_add_u32 s83, s83, 1\n"
"s_cmp_lt_u32 s83, 0x10000\n"
"s_cbranch_scc1 .Ladaln_block_P_par_L\n"
"s_branch .Ladaln_block_P_par_R\n"
".Ladaln_block_P_mod:\n"
"s_mov_b32 s83, 0\n"
".Ladaln_block_P_mod_L:\n"
"s_sleep 4\n"
"buffer_load_dwordx4 v[80:83], v31, s[4:7], s74 offen sc1\n"
"buffer_load_dwordx4 v[84:87], v31, s[4:7], s74 offen offset:2560 sc1\n"
"s_waitcnt vmcnt(0)\n"
"v_xor_b32 v81, s82, v81\n"
"v_xor_b32 v83, s82, v83\n"
"v_xor_b32 v85, s82, v85\n"
"v_xor_b32 v87, s82, v87\n"
"v_or3_b32 v38, v81, v83, v85\n"
"v_or_b32 v38, v38, v87\n"
"v_cmp_ne_u32 vcc, 0, v38\n"
"s_nop 0\n"
"s_cbranch_vccz .Ladaln_block_P_mod_R\n"
"s_add_u32 s83, s83, 1\n"
"s_cmp_lt_u32 s83, 0x10000\n"
"s_cbranch_scc1 .Ladaln_block_P_mod_L\n"
"s_branch .Ladaln_block_P_mod_R\n"
".Ladaln_block_P_h:\n"
"s_mov_b32 s83, 0\n"
".Ladaln_block_P_h_L:\n"
"s_sleep 2\n"
"buffer_load_dword v20, v25, s[4:7], s61 offen sc1\n"
"s_waitcnt vmcnt(0)\n"
"v_cmp_ne_u32 vcc, s82, v20\n"
"s_nop 0\n"
"s_cbranch_vccz .Ladaln_block_P_h_R\n"
"s_add_u32 s83, s83, 1\n"
"s_cmp_lt_u32 s83, 0x10000\n"
"s_cbranch_scc1 .Ladaln_block_P_h_L\n"
"s_branch .Ladaln_block_P_h_R\n"
".Ladaln_block_P_vote:\n"
"s_mov_b32 s83, 0\n"
".Ladaln_block_P_vote_L:\n"
"s_sleep 4\n"
"buffer_load_dwordx4 v[32:35], v1, s[4:7], s56 offen sc1\n"
"s_waitcnt vmcnt(0)\n"
"v_xor_b32 v33, s82, v33\n"
"v_xor_b32 v35, s82, v35\n"
"v_or_b32 v33, v33, v35\n"
"v_cmp_ne_u32 vcc, 0, v33\n"
"s_nop 0\n"
"s_cbranch_vccz .Ladaln_block_P_vote_R\n"
"s_add_u32 s83, s83, 1\n"
"s_cmp_lt_u32 s83, 0x10000\n"
"s_cbranch_scc1 .Ladaln_block_P_vote_L\n"
"s_branch .Ladaln_block_P_vote_R\n"
".Ladaln_block_P_kva:\n"
"s_mov_b32 s83, 0\n"
".Ladaln_block_P_kva_L:\n"
"s_sleep 2\n"
"buffer_load_dword v11, v20, s[4:7], s75 offen sc1\n"
"s_waitcnt vmcnt(0)\n"
"v_cmp_ne_u32 vcc, s82, v11\n"
"s_nop 0\n"
"s_cbranch_vccz .Ladaln_block_P_kva_R\n"
"s_add_u32 s83, s83, 1\n"
"s_cmp_lt_u32 s83, 0x10000\n"
"s_cbranch_scc1 .Ladaln_block_P_kva_L\n"
"s_branch .Ladaln_block_P_kva_R\n"
".Ladaln_block_P_kvb:\n"
"s_mov_b32 s83, 0\n"
".Ladaln_block_P_kvb_L:\n"
"s_sleep 2\n"
"buffer_load_dword v18, v19, s[4:7], s75 offen sc1\n"
"s_waitcnt vmcnt(0)\n"
"v_cmp_ne_u32 vcc, s82, v18\n"
"s_nop 0\n"
"s_cbranch_vccz .Ladaln_block_P_kvb_R\n"
"s_add_u32 s83, s83, 1\n"
"s_cmp_lt_u32 s83, 0x10000\n"
"s_cbranch_scc1 .Ladaln_block_P_kvb_L\n"
"s_branch .Ladaln_block_P_kvb_R\n"
".Ladaln_block_P_prep1:\n"
"s_mov_b32 s83, 0\n"
".Ladaln_block_P_prep1_L:\n"
"s_sleep 2\n"
"buffer_load_dword v21, v20, s[4:7], s30 offen sc1\n"
"s_waitcnt vmcnt(0)\n"
"v_cmp_ne_u32 vcc, s31, v21\n"
"s_nop 0\n"
"s_cbranch_vccz .Ladaln_block_P_prep1_R\n"
"s_add_u32 s83, s83, 1\n"
"s_cmp_lt_u32 s83, 0x10000\n"
"s_cbranch_scc1 .Ladaln_block_P_prep1_L\n"
"s_branch .Ladaln_block_P_prep1_R\n"
".Ladaln_block_P_prep2:\n"
"s_mov_b32 s83, 0\n"
".Ladaln_block_P_prep2_L:\n"
"s_sleep 2\n"
"buffer_load_dword v21, v20, s[4:7], s30 offen sc1\n"
"s_waitcnt vmcnt(0)\n"
"v_cmp_ne_u32 vcc, s31, v21\n"
"s_nop 0\n"
"s_cbranch_vccz .Ladaln_block_P_prep2_R\n"
"s_add_u32 s83, s83, 1\n"
"s_cmp_lt_u32 s83, 0x10000\n"
"s_cbranch_scc1 .Ladaln_block_P_prep2_L\n"
"s_branch .Ladaln_block_P_prep2_R\n"
".Ladaln_block_S_E:\n"
"s_sub_u32 s26, s72, 0x6000000\n"
"s_bfe_u32 s27, s26, 0x1000a\n"
"s_lshr_b32 s26, s26, 19\n"
"s_call_b64 s[42:43], .Ladaln_block_S_EXACT\n"
"v_accvgpr_read_b32 v48, a128\n"
"v_accvgpr_read_b32 v49, a132\n"
"v_accvgpr_read_b32 v50, a136\n"
"v_accvgpr_read_b32 v51, a140\n"
"v_rcp_f32 v48, v48\n"
"v_rcp_f32 v49, v49\n"
"v_accvgpr_read_b32 v128, a0\n"
"v_accvgpr_read_b32 v129, a1\n"
"v_accvgpr_read_b32 v130, a2\n"
"v_accvgpr_read_b32 v131, a3\n"
"v_accvgpr_read_b32 v132, a4\n"
"v_accvgpr_read_b32 v133, a5\n"
"v_accvgpr_read_b32 v134, a6\n"
"v_accvgpr_read_b32 v135, a7\n"
"v_mul_f32 v128, v128, v48\n"
"v_mul_f32 v129, v129, v48\n"
"v_mul_f32 v130, v130, v48\n"
"v_mul_f32 v131, v131, v48\n"
"v_mul_f32 v132, v132, v48\n"
"v_mul_f32 v133, v133, v48\n"
"v_mul_f32 v134, v134, v48\n"
"v_mul_f32 v135, v135, v48\n"
"v_cvt_pk_bf16_f32 v160, v128, v129\n"
"v_cvt_pk_bf16_f32 v161, v130, v131\n"
"v_cvt_pk_bf16_f32 v162, v132, v133\n"
"v_cvt_pk_bf16_f32 v163, v134, v135\n"
"s_add_u32 s74, s72, 0\n"
"buffer_store_dwordx4 v[160:163], v1, s[4:7], s74 offen sc1\n"
"v_accvgpr_read_b32 v144, a8\n"
"v_accvgpr_read_b32 v145, a9\n"
"v_accvgpr_read_b32 v146, a10\n"
"v_accvgpr_read_b32 v147, a11\n"
"v_accvgpr_read_b32 v148, a12\n"
"v_accvgpr_read_b32 v149, a13\n"
"v_accvgpr_read_b32 v150, a14\n"
"v_accvgpr_read_b32 v151, a15\n"
"v_mul_f32 v144, v144, v48\n"
"v_mul_f32 v145, v145, v48\n"
"v_mul_f32 v146, v146, v48\n"
"v_mul_f32 v147, v147, v48\n"
"v_mul_f32 v148, v148, v48\n"
"v_mul_f32 v149, v149, v48\n"
"v_mul_f32 v150, v150, v48\n"
"v_mul_f32 v151, v151, v48\n"
"v_cvt_pk_bf16_f32 v164, v144, v145\n"
"v_cvt_pk_bf16_f32 v165, v146, v147\n"
"v_cvt_pk_bf16_f32 v166, v148, v149\n"
"v_cvt_pk_bf16_f32 v167, v150, v151\n"
"s_add_u32 s74, s72, 0x1000\n"
"buffer_store_dwordx4 v[164:167], v1, s[4:7], s74 offen sc1\n"
"v_accvgpr_read_b32 v128, a16\n"
"v_accvgpr_read_b32 v129, a17\n"
"v_accvgpr_read_b32 v130, a18\n"
"v_accvgpr_read_b32 v131, a19\n"
"v_accvgpr_read_b32 v132, a20\n"
"v_accvgpr_read_b32 v133, a21\n"
"v_accvgpr_read_b32 v134, a22\n"
"v_accvgpr_read_b32 v135, a23\n"
"v_mul_f32 v128, v128, v48\n"
"v_mul_f32 v129, v129, v48\n"
"v_mul_f32 v130, v130, v48\n"
"v_mul_f32 v131, v131, v48\n"
"v_mul_f32 v132, v132, v48\n"
"v_mul_f32 v133, v133, v48\n"
"v_mul_f32 v134, v134, v48\n"
"v_mul_f32 v135, v135, v48\n"
"v_cvt_pk_bf16_f32 v160, v128, v129\n"
"v_cvt_pk_bf16_f32 v161, v130, v131\n"
"v_cvt_pk_bf16_f32 v162, v132, v133\n"
"v_cvt_pk_bf16_f32 v163, v134, v135\n"
"s_add_u32 s74, s72, 0x4000\n"
"buffer_store_dwordx4 v[160:163], v1, s[4:7], s74 offen sc1\n"
"v_accvgpr_read_b32 v144, a24\n"
"v_accvgpr_read_b32 v145, a25\n"
"v_accvgpr_read_b32 v146, a26\n"
"v_accvgpr_read_b32 v147, a27\n"
"v_accvgpr_read_b32 v148, a28\n"
"v_accvgpr_read_b32 v149, a29\n"
"v_accvgpr_read_b32 v150, a30\n"
"v_accvgpr_read_b32 v151, a31\n"
"v_mul_f32 v144, v144, v48\n"
"v_mul_f32 v145, v145, v48\n"
"v_mul_f32 v146, v146, v48\n"
"v_mul_f32 v147, v147, v48\n"
"v_mul_f32 v148, v148, v48\n"
"v_mul_f32 v149, v149, v48\n"
"v_mul_f32 v150, v150, v48\n"
"v_mul_f32 v151, v151, v48\n"
"v_cvt_pk_bf16_f32 v164, v144, v145\n"
"v_cvt_pk_bf16_f32 v165, v146, v147\n"
"v_cvt_pk_bf16_f32 v166, v148, v149\n"
"v_cvt_pk_bf16_f32 v167, v150, v151\n"
"s_add_u32 s74, s72, 0x5000\n"
"buffer_store_dwordx4 v[164:167], v1, s[4:7], s74 offen sc1\n"
"s_branch .Ladaln_block_S_E_R\n"
".Ladaln_block_P_prep3:\n"
"s_mov_b32 s83, 0\n"
".Ladaln_block_P_prep3_L:\n"
"s_sleep 2\n"
"buffer_load_dword v21, v20, s[4:7], s30 offen sc1\n"
"s_waitcnt vmcnt(0)\n"
"v_cmp_ne_u32 vcc, s31, v21\n"
"s_nop 0\n"
"s_cbranch_vccz .Ladaln_block_P_prep3_R\n"
"s_add_u32 s83, s83, 1\n"
"s_cmp_lt_u32 s83, 0x10000\n"
"s_cbranch_scc1 .Ladaln_block_P_prep3_L\n"
"s_branch .Ladaln_block_P_prep3_R\n"
".Ladaln_block_P_kvbd:\n"
"s_mov_b32 s83, 0\n"
".Ladaln_block_P_kvbd_L:\n"
"s_sleep 2\n"
"buffer_load_dword v18, v19, s[4:7], s75 offen sc1\n"
"s_waitcnt vmcnt(0)\n"
"v_cmp_ne_u32 vcc, s82, v18\n"
"s_nop 0\n"
"s_cbranch_vccz .Ladaln_block_P_kvbd_R\n"
"s_add_u32 s83, s83, 1\n"
"s_cmp_lt_u32 s83, 0x10000\n"
"s_cbranch_scc1 .Ladaln_block_P_kvbd_L\n"
"s_branch .Ladaln_block_P_kvbd_R\n"
".Ladaln_block_S_F:\n"
"s_sub_u32 s26, s73, 0x6000000\n"
"s_bfe_u32 s27, s26, 0x1000a\n"
"s_lshr_b32 s26, s26, 19\n"
"s_call_b64 s[42:43], .Ladaln_block_S_EXACT\n"
"v_accvgpr_read_b32 v224, a0\n"
"v_accvgpr_read_b32 v225, a1\n"
"v_accvgpr_read_b32 v226, a2\n"
"v_accvgpr_read_b32 v227, a3\n"
"v_accvgpr_read_b32 v228, a4\n"
"v_accvgpr_read_b32 v229, a5\n"
"v_accvgpr_read_b32 v230, a6\n"
"v_accvgpr_read_b32 v231, a7\n"
"v_accvgpr_read_b32 v232, a8\n"
"v_accvgpr_read_b32 v233, a9\n"
"v_accvgpr_read_b32 v234, a10\n"
"v_accvgpr_read_b32 v235, a11\n"
"v_accvgpr_read_b32 v236, a12\n"
"v_accvgpr_read_b32 v237, a13\n"
"v_accvgpr_read_b32 v238, a14\n"
"v_accvgpr_read_b32 v239, a15\n"
"v_accvgpr_read_b32 v240, a16\n"
"v_accvgpr_read_b32 v241, a17\n"
"v_accvgpr_read_b32 v242, a18\n"
"v_accvgpr_read_b32 v243, a19\n"
"v_accvgpr_read_b32 v244, a20\n"
"v_accvgpr_read_b32 v245, a21\n"
"v_accvgpr_read_b32 v246, a22\n"
"v_accvgpr_read_b32 v247, a23\n"
"v_accvgpr_read_b32 v248, a24\n"
"v_accvgpr_read_b32 v249, a25\n"
"v_accvgpr_read_b32 v250, a26\n"
"v_accvgpr_read_b32 v251, a27\n"
"v_accvgpr_read_b32 v252, a28\n"
"v_accvgpr_read_b32 v253, a29\n"
"v_accvgpr_read_b32 v254, a30\n"
"v_accvgpr_read_b32 v255, a31\n"
"v_accvgpr_read_b32 v192, a64\n"
"v_accvgpr_read_b32 v193, a65\n"
"v_accvgpr_read_b32 v194, a66\n"
"v_accvgpr_read_b32 v195, a67\n"
"v_accvgpr_read_b32 v196, a68\n"
"v_accvgpr_read_b32 v197, a69\n"
"v_accvgpr_read_b32 v198, a70\n"
"v_accvgpr_read_b32 v199, a71\n"
"v_accvgpr_read_b32 v200, a72\n"
"v_accvgpr_read_b32 v201, a73\n"
"v_accvgpr_read_b32 v202, a74\n"
"v_accvgpr_read_b32 v203, a75\n"
"v_accvgpr_read_b32 v204, a76\n"
"v_accvgpr_read_b32 v205, a77\n"
"v_accvgpr_read_b32 v206, a78\n"
"v_accvgpr_read_b32 v207, a79\n"
"v_accvgpr_read_b32 v208, a80\n"
"v_accvgpr_read_b32 v209, a81\n"
"v_accvgpr_read_b32 v210, a82\n"
"v_accvgpr_read_b32 v211, a83\n"
"v_accvgpr_read_b32 v212, a84\n"
"v_accvgpr_read_b32 v213, a85\n"
"v_accvgpr_read_b32 v214, a86\n"
"v_accvgpr_read_b32 v215, a87\n"
"v_accvgpr_read_b32 v216, a88\n"
"v_accvgpr_read_b32 v217, a89\n"
"v_accvgpr_read_b32 v218, a90\n"
"v_accvgpr_read_b32 v219, a91\n"
"v_accvgpr_read_b32 v220, a92\n"
"v_accvgpr_read_b32 v221, a93\n"
"v_accvgpr_read_b32 v222, a94\n"
"v_accvgpr_read_b32 v223, a95\n"
"v_mov_b32 v128, 0\n"
"v_mov_b32 v129, 0\n"
"v_mov_b32 v130, 0\n"
"v_mov_b32 v131, 0\n"
"v_mov_b32 v132, 0\n"
"v_mov_b32 v133, 0\n"
"v_mov_b32 v134, 0\n"
"v_mov_b32 v135, 0\n"
"v_mov_b32 v136, 0\n"
"v_mov_b32 v137, 0\n"
"v_mov_b32 v138, 0\n"
"v_mov_b32 v139, 0\n"
"v_mov_b32 v140, 0\n"
"v_mov_b32 v141, 0\n"
"v_mov_b32 v142, 0\n"
"v_mov_b32 v143, 0\n"
"v_mov_b32 v144, 0\n"
"v_mov_b32 v145, 0\n"
"v_mov_b32 v146, 0\n"
"v_mov_b32 v147, 0\n"
"v_mov_b32 v148, 0\n"
"v_mov_b32 v149, 0\n"
"v_mov_b32 v150, 0\n"
"v_mov_b32 v151, 0\n"
"v_mov_b32 v152, 0\n"
"v_mov_b32 v153, 0\n"
"v_mov_b32 v154, 0\n"
"v_mov_b32 v155, 0\n"
"v_mov_b32 v156, 0\n"
"v_mov_b32 v157, 0\n"
"v_mov_b32 v158, 0\n"
"v_mov_b32 v159, 0\n"
"v_mov_b32 v160, 0\n"
"v_mov_b32 v161, 0\n"
"v_mov_b32 v162, 0\n"
"v_mov_b32 v163, 0\n"
"v_mov_b32 v164, 0\n"
"v_mov_b32 v165, 0\n"
"v_mov_b32 v166, 0\n"
"v_mov_b32 v167, 0\n"
"v_mov_b32 v168, 0\n"
"v_mov_b32 v169, 0\n"
"v_mov_b32 v170, 0\n"
"v_mov_b32 v171, 0\n"
"v_mov_b32 v172, 0\n"
"v_mov_b32 v173, 0\n"
"v_mov_b32 v174, 0\n"
"v_mov_b32 v175, 0\n"
"v_mov_b32 v176, 0\n"
"v_mov_b32 v177, 0\n"
"v_mov_b32 v178, 0\n"
"v_mov_b32 v179, 0\n"
"v_mov_b32 v180, 0\n"
"v_mov_b32 v181, 0\n"
"v_mov_b32 v182, 0\n"
"v_mov_b32 v183, 0\n"
"v_mov_b32 v184, 0\n"
"v_mov_b32 v185, 0\n"
"v_mov_b32 v186, 0\n"
"v_mov_b32 v187, 0\n"
"v_mov_b32 v188, 0\n"
"v_mov_b32 v189, 0\n"
"v_mov_b32 v190, 0\n"
"v_mov_b32 v191, 0\n"
"v_accvgpr_read_b32 v48, a128\n"
"v_accvgpr_read_b32 v50, a136\n"
"s_branch .Ladaln_block_S_F_R\n"
".Ladaln_block_S_EXACT:\n"
"s_waitcnt vmcnt(0) lgkmcnt(0)\n"
"s_nop 7\n"
"s_mov_b32 s28, 0\n"
"v_mov_b32 v224, 0\n"
"v_mov_b32 v225, 0\n"
"v_mov_b32 v226, 0\n"
"v_mov_b32 v227, 0\n"
"v_mov_b32 v228, 0\n"
"v_mov_b32 v229, 0\n"
"v_mov_b32 v230, 0\n"
"v_mov_b32 v231, 0\n"
"v_mov_b32 v232, 0\n"
"v_mov_b32 v233, 0\n"
"v_mov_b32 v234, 0\n"
"v_mov_b32 v235, 0\n"
"v_mov_b32 v236, 0\n"
"v_mov_b32 v237, 0\n"
"v_mov_b32 v238, 0\n"
"v_mov_b32 v239, 0\n"
"v_mov_b32 v124, v8\n"
"v_mov_b32 v125, v8\n"
"v_mov_b32 v126, v8\n"
"v_mov_b32 v127, v8\n"
".Ladaln_block_S_PASS:\n"
"s_bfe_u32 s29, s100, 0x20001\n"
"s_and_b32 s41, s48, 3\n"
"s_lshl2_add_u32 s29, s29, s41\n"
"s_lshl_b32 s29, s29, 20\n"
"s_add_u32 s29, s29, 0x4000000\n"
"s_lshr_b32 s41, s29, 8\n"
"s_and_b32 s41, s41, 0x2000\n"
"s_xor_b32 s29, s29, s41\n"
"s_xor_b32 s29, s29, s101\n"
"s_mov_b32 s40, 0\n"
"v_and_b32 v52, 15, v0\n"
"v_lshrrev_b32 v53, 4, v0\n"
"v_lshlrev_b32 v53, 2, v53\n"
"v_sub_u32 v52, v52, v53\n"
"s_xor_b32 s41, s27, 0\n"
"s_lshl2_add_u32 s41, s26, s41\n"
"s_lshl_b32 s41, s41, 4\n"
"v_add_u32 v120, s41, v52\n"
"s_xor_b32 s41, s27, 1\n"
"s_lshl2_add_u32 s41, s26, s41\n"
"s_lshl_b32 s41, s41, 4\n"
"v_add_u32 v121, s41, v52\n"
"s_xor_b32 s41, s27, 2\n"
"s_lshl2_add_u32 s41, s26, s41\n"
"s_lshl_b32 s41, s41, 4\n"
"v_add_u32 v122, s41, v52\n"
"s_xor_b32 s41, s27, 3\n"
"s_lshl2_add_u32 s41, s26, s41\n"
"s_lshl_b32 s41, s41, 4\n"
"v_add_u32 v123, s41, v52\n"
".Ladaln_block_S_LOOP:\n"
"s_lshl_b32 s41, s40, 14\n"
"s_add_u32 s41, s41, s29\n"
"buffer_load_dwordx4 v[128:131], v1, s[4:7], s41 offen sc1\n"
"buffer_load_dwordx4 v[132:135], v1, s[4:7], s41 offen offset:1024 sc1\n"
"buffer_load_dwordx4 v[136:139], v1, s[4:7], s41 offen offset:2048 sc1\n"
"buffer_load_dwordx4 v[140:143], v1, s[4:7], s41 offen offset:3072 sc1\n"
"s_add_u32 s41, s41, 0x1000\n"
"buffer_load_dwordx4 v[144:147], v1, s[4:7], s41 offen sc1\n"
"buffer_load_dwordx4 v[148:151], v1, s[4:7], s41 offen offset:1024 sc1\n"
"buffer_load_dwordx4 v[152:155], v1, s[4:7], s41 offen offset:2048 sc1\n"
"buffer_load_dwordx4 v[156:159], v1, s[4:7], s41 offen offset:3072 sc1\n"
"s_add_u32 s41, s41, 0x1000\n"
"s_lshr_b32 s41, s40, 1\n"
"s_lshl_b32 s41, s41, 5\n"
"s_and_b32 s11, s40, 1\n"
"s_add_u32 s41, s41, s11\n"
"s_lshl_b32 s41, s41, 10\n"
"s_add_u32 s41, s41, s29\n"
"s_add_u32 s41, s41, 0x1000000\n"
"buffer_load_dwordx4 v[160:163], v1, s[4:7], s41 offen sc1\n"
"buffer_load_dwordx4 v[164:167], v1, s[4:7], s41 offen offset:2048 sc1\n"
"s_add_u32 s41, s41, 0x1000\n"
"buffer_load_dwordx4 v[168:171], v1, s[4:7], s41 offen sc1\n"
"buffer_load_dwordx4 v[172:175], v1, s[4:7], s41 offen offset:2048 sc1\n"
"s_add_u32 s41, s41, 0x3000\n"
"buffer_load_dwordx4 v[176:179], v1, s[4:7], s41 offen sc1\n"
"buffer_load_dwordx4 v[180:183], v1, s[4:7], s41 offen offset:2048 sc1\n"
"s_add_u32 s41, s41, 0x1000\n"
"buffer_load_dwordx4 v[184:187], v1, s[4:7], s41 offen sc1\n"
"buffer_load_dwordx4 v[188:191], v1, s[4:7], s41 offen offset:2048 sc1\n"
"s_add_u32 s41, s41, 0x3000\n"
"s_waitcnt vmcnt(0)\n"
"v_mfma_f32_16x16x32_bf16 v[192:195], v[128:131], v[56:59], v[224:227]\n"
"v_mfma_f32_16x16x32_bf16 v[192:195], v[132:135], v[60:63], v[192:195]\n"
"v_mfma_f32_16x16x32_bf16 v[192:195], v[136:139], v[64:67], v[192:195]\n"
"v_mfma_f32_16x16x32_bf16 v[192:195], v[140:143], v[68:71], v[192:195]\n"
"v_mfma_f32_16x16x32_bf16 v[196:199], v[128:131], v[72:75], v[228:231]\n"
"v_mfma_f32_16x16x32_bf16 v[196:199], v[132:135], v[76:79], v[196:199]\n"
"v_mfma_f32_16x16x32_bf16 v[196:199], v[136:139], v[80:83], v[196:199]\n"
"v_mfma_f32_16x16x32_bf16 v[196:199], v[140:143], v[84:87], v[196:199]\n"
"v_mfma_f32_16x16x32_bf16 v[200:203], v[128:131], v[88:91], v[232:235]\n"
"v_mfma_f32_16x16x32_bf16 v[200:203], v[132:135], v[92:95], v[200:203]\n"
"v_mfma_f32_16x16x32_bf16 v[200:203], v[136:139], v[96:99], v[200:203]\n"
"v_mfma_f32_16x16x32_bf16 v[200:203], v[140:143], v[100:103], v[200:203]\n"
"v_mfma_f32_16x16x32_bf16 v[204:207], v[128:131], v[104:107], v[236:239]\n"
"v_mfma_f32_16x16x32_bf16 v[204:207], v[132:135], v[108:111], v[204:207]\n"
"v_mfma_f32_16x16x32_bf16 v[204:207], v[136:139], v[112:115], v[204:207]\n"
"v_mfma_f32_16x16x32_bf16 v[204:207], v[140:143], v[116:119], v[204:207]\n"
"v_mfma_f32_16x16x32_bf16 v[208:211], v[144:147], v[56:59], v[224:227]\n"
"v_mfma_f32_16x16x32_bf16 v[208:211], v[148:151], v[60:63], v[208:211]\n"
"v_mfma_f32_16x16x32_bf16 v[208:211], v[152:155], v[64:67], v[208:211]\n"
"v_mfma_f32_16x16x32_bf16 v[208:211], v[156:159], v[68:71], v[208:211]\n"
"v_mfma_f32_16x16x32_bf16 v[212:215], v[144:147], v[72:75], v[228:231]\n"
"v_mfma_f32_16x16x32_bf16 v[212:215], v[148:151], v[76:79], v[212:215]\n"
"v_mfma_f32_16x16x32_bf16 v[212:215], v[152:155], v[80:83], v[212:215]\n"
"v_mfma_f32_16x16x32_bf16 v[212:215], v[156:159], v[84:87], v[212:215]\n"
"v_mfma_f32_16x16x32_bf16 v[216:219], v[144:147], v[88:91], v[232:235]\n"
"v_mfma_f32_16x16x32_bf16 v[216:219], v[148:151], v[92:95], v[216:219]\n"
"v_mfma_f32_16x16x32_bf16 v[216:219], v[152:155], v[96:99], v[216:219]\n"
"v_mfma_f32_16x16x32_bf16 v[216:219], v[156:159], v[100:103], v[216:219]\n"
"v_mfma_f32_16x16x32_bf16 v[220:223], v[144:147], v[104:107], v[236:239]\n"
"v_mfma_f32_16x16x32_bf16 v[220:223], v[148:151], v[108:111], v[220:223]\n"
"v_mfma_f32_16x16x32_bf16 v[220:223], v[152:155], v[112:115], v[220:223]\n"
"v_mfma_f32_16x16x32_bf16 v[220:223], v[156:159], v[116:119], v[220:223]\n"
"s_nop 7\n"
"v_cmp_gt_i32 vcc, 0, v120\n"
"s_nop 1\n"
"v_cndmask_b32 v192, v192, v8, vcc\n"
"v_cmp_gt_i32 vcc, 1, v120\n"
"s_nop 1\n"
"v_cndmask_b32 v193, v193, v8, vcc\n"
"v_cmp_gt_i32 vcc, 2, v120\n"
"s_nop 1\n"
"v_cndmask_b32 v194, v194, v8, vcc\n"
"v_cmp_gt_i32 vcc, 3, v120\n"
"s_nop 1\n"
"v_cndmask_b32 v195, v195, v8, vcc\n"
"v_cmp_gt_i32 vcc, 0, v121\n"
"s_nop 1\n"
"v_cndmask_b32 v196, v196, v8, vcc\n"
"v_cmp_gt_i32 vcc, 1, v121\n"
"s_nop 1\n"
"v_cndmask_b32 v197, v197, v8, vcc\n"
"v_cmp_gt_i32 vcc, 2, v121\n"
"s_nop 1\n"
"v_cndmask_b32 v198, v198, v8, vcc\n"
"v_cmp_gt_i32 vcc, 3, v121\n"
"s_nop 1\n"
"v_cndmask_b32 v199, v199, v8, vcc\n"
"v_cmp_gt_i32 vcc, 0, v122\n"
"s_nop 1\n"
"v_cndmask_b32 v200, v200, v8, vcc\n"
"v_cmp_gt_i32 vcc, 1, v122\n"
"s_nop 1\n"
"v_cndmask_b32 v201, v201, v8, vcc\n"
"v_cmp_gt_i32 vcc, 2, v122\n"
"s_nop 1\n"
"v_cndmask_b32 v202, v202, v8, vcc\n"
"v_cmp_gt_i32 vcc, 3, v122\n"
"s_nop 1\n"
"v_cndmask_b32 v203, v203, v8, vcc\n"
"v_cmp_gt_i32 vcc, 0, v123\n"
"s_nop 1\n"
"v_cndmask_b32 v204, v204, v8, vcc\n"
"v_cmp_gt_i32 vcc, 1, v123\n"
"s_nop 1\n"
"v_cndmask_b32 v205, v205, v8, vcc\n"
"v_cmp_gt_i32 vcc, 2, v123\n"
"s_nop 1\n"
"v_cndmask_b32 v206, v206, v8, vcc\n"
"v_cmp_gt_i32 vcc, 3, v123\n"
"s_nop 1\n"
"v_cndmask_b32 v207, v207, v8, vcc\n"
"v_cmp_gt_i32 vcc, 16, v120\n"
"s_nop 1\n"
"v_cndmask_b32 v208, v208, v8, vcc\n"
"v_cmp_gt_i32 vcc, 17, v120\n"
"s_nop 1\n"
"v_cndmask_b32 v209, v209, v8, vcc\n"
"v_cmp_gt_i32 vcc, 18, v120\n"
"s_nop 1\n"
"v_cndmask_b32 v210, v210, v8, vcc\n"
"v_cmp_gt_i32 vcc, 19, v120\n"
"s_nop 1\n"
"v_cndmask_b32 v211, v211, v8, vcc\n"
"v_cmp_gt_i32 vcc, 16, v121\n"
"s_nop 1\n"
"v_cndmask_b32 v212, v212, v8, vcc\n"
"v_cmp_gt_i32 vcc, 17, v121\n"
"s_nop 1\n"
"v_cndmask_b32 v213, v213, v8, vcc\n"
"v_cmp_gt_i32 vcc, 18, v121\n"
"s_nop 1\n"
"v_cndmask_b32 v214, v214, v8, vcc\n"
"v_cmp_gt_i32 vcc, 19, v121\n"
"s_nop 1\n"
"v_cndmask_b32 v215, v215, v8, vcc\n"
"v_cmp_gt_i32 vcc, 16, v122\n"
"s_nop 1\n"
"v_cndmask_b32 v216, v216, v8, vcc\n"
"v_cmp_gt_i32 vcc, 17, v122\n"
"s_nop 1\n"
"v_cndmask_b32 v217, v217, v8, vcc\n"
"v_cmp_gt_i32 vcc, 18, v122\n"
"s_nop 1\n"
"v_cndmask_b32 v218, v218, v8, vcc\n"
"v_cmp_gt_i32 vcc, 19, v122\n"
"s_nop 1\n"
"v_cndmask_b32 v219, v219, v8, vcc\n"
"v_cmp_gt_i32 vcc, 16, v123\n"
"s_nop 1\n"
"v_cndmask_b32 v220, v220, v8, vcc\n"
"v_cmp_gt_i32 vcc, 17, v123\n"
"s_nop 1\n"
"v_cndmask_b32 v221, v221, v8, vcc\n"
"v_cmp_gt_i32 vcc, 18, v123\n"
"s_nop 1\n"
"v_cndmask_b32 v222, v222, v8, vcc\n"
"v_cmp_gt_i32 vcc, 19, v123\n"
"s_nop 1\n"
"v_cndmask_b32 v223, v223, v8, vcc\n"
"v_subrev_u32 v120, 32, v120\n"
"v_subrev_u32 v121, 32, v121\n"
"v_subrev_u32 v122, 32, v122\n"
"v_subrev_u32 v123, 32, v123\n"
"s_cmp_lg_u32 s28, 0\n"
"s_cbranch_scc1 .Ladaln_block_S_SUM\n"
"v_max3_f32 v124, v124, v192, v193\n"
"v_max3_f32 v124, v124, v194, v195\n"
"v_max3_f32 v124, v124, v208, v209\n"
"v_max3_f32 v124, v124, v210, v211\n"
"v_max3_f32 v125, v125, v196, v197\n"
"v_max3_f32 v125, v125, v198, v199\n"
"v_max3_f32 v125, v125, v212, v213\n"
"v_max3_f32 v125, v125, v214, v215\n"
"v_max3_f32 v126, v126, v200, v201\n"
"v_max3_f32 v126, v126, v202, v203\n"
"v_max3_f32 v126, v126, v216, v217\n"
"v_max3_f32 v126, v126, v218, v219\n"
"v_max3_f32 v127, v127, v204, v205\n"
"v_max3_f32 v127, v127, v206, v207\n"
"v_max3_f32 v127, v127, v220, v221\n"
"v_max3_f32 v127, v127, v222, v223\n"
"s_branch .Ladaln_block_S_NEXT\n"
".Ladaln_block_S_SUM:\n"
"v_exp_f32 v192, v192\n"
"v_exp_f32 v193, v193\n"
"v_exp_f32 v194, v194\n"
"v_exp_f32 v195, v195\n"
"v_exp_f32 v196, v196\n"
"v_exp_f32 v197, v197\n"
"v_exp_f32 v198, v198\n"
"v_exp_f32 v199, v199\n"
"v_exp_f32 v200, v200\n"
"v_exp_f32 v201, v201\n"
"v_exp_f32 v202, v202\n"
"v_exp_f32 v203, v203\n"
"v_exp_f32 v204, v204\n"
"v_exp_f32 v205, v205\n"
"v_exp_f32 v206, v206\n"
"v_exp_f32 v207, v207\n"
"v_exp_f32 v208, v208\n"
"v_exp_f32 v209, v209\n"
"v_exp_f32 v210, v210\n"
"v_exp_f32 v211, v211\n"
"v_exp_f32 v212, v212\n"
"v_exp_f32 v213, v213\n"
"v_exp_f32 v214, v214\n"
"v_exp_f32 v215, v215\n"
"v_exp_f32 v216, v216\n"
"v_exp_f32 v217, v217\n"
"v_exp_f32 v218, v218\n"
"v_exp_f32 v219, v219\n"
"v_exp_f32 v220, v220\n"
"v_exp_f32 v221, v221\n"
"v_exp_f32 v222, v222\n"
"v_exp_f32 v223, v223\n"
"v_cvt_pk_bf16_f32 v240, v192, v193\n"
"v_cvt_pk_bf16_f32 v241, v194, v195\n"
"v_cvt_pk_bf16_f32 v242, v208, v209\n"
"v_cvt_pk_bf16_f32 v243, v210, v211\n"
"v_cvt_pk_bf16_f32 v244, v196, v197\n"
"v_cvt_pk_bf16_f32 v245, v198, v199\n"
"v_cvt_pk_bf16_f32 v246, v212, v213\n"
"v_cvt_pk_bf16_f32 v247, v214, v215\n"
"v_cvt_pk_bf16_f32 v248, v200, v201\n"
"v_cvt_pk_bf16_f32 v249, v202, v203\n"
"v_cvt_pk_bf16_f32 v250, v216, v217\n"
"v_cvt_pk_bf16_f32 v251, v218, v219\n"
"v_cvt_pk_bf16_f32 v252, v204, v205\n"
"v_cvt_pk_bf16_f32 v253, v206, v207\n"
"v_cvt_pk_bf16_f32 v254, v220, v221\n"
"v_cvt_pk_bf16_f32 v255, v222, v223\n"
"v_mfma_f32_16x16x32_bf16 a[0:3], v[160:163], v[240:243], a[0:3]\n"
"v_mfma_f32_16x16x32_bf16 a[4:7], v[164:167], v[240:243], a[4:7]\n"
"v_mfma_f32_16x16x32_bf16 a[8:11], v[168:171], v[240:243], a[8:11]\n"
"v_mfma_f32_16x16x32_bf16 a[12:15], v[172:175], v[240:243], a[12:15]\n"
"v_mfma_f32_16x16x32_bf16 a[16:19], v[176:179], v[240:243], a[16:19]\n"
"v_mfma_f32_16x16x32_bf16 a[20:23], v[180:183], v[240:243], a[20:23]\n"
"v_mfma_f32_16x16x32_bf16 a[24:27], v[184:187], v[240:243], a[24:27]\n"
"v_mfma_f32_16x16x32_bf16 a[28:31], v[188:191], v[240:243], a[28:31]\n"
"v_mfma_f32_16x16x32_bf16 a[128:131], v[4:7], v[240:243], a[128:131]\n"
"v_mfma_f32_16x16x32_bf16 a[32:35], v[160:163], v[244:247], a[32:35]\n"
"v_mfma_f32_16x16x32_bf16 a[36:39], v[164:167], v[244:247], a[36:39]\n"
"v_mfma_f32_16x16x32_bf16 a[40:43], v[168:171], v[244:247], a[40:43]\n"
"v_mfma_f32_16x16x32_bf16 a[44:47], v[172:175], v[244:247], a[44:47]\n"
"v_mfma_f32_16x16x32_bf16 a[48:51], v[176:179], v[244:247], a[48:51]\n"
"v_mfma_f32_16x16x32_bf16 a[52:55], v[180:183], v[244:247], a[52:55]\n"
"v_mfma_f32_16x16x32_bf16 a[56:59], v[184:187], v[244:247], a[56:59]\n"
"v_mfma_f32_16x16x32_bf16 a[60:63], v[188:191], v[244:247], a[60:63]\n"
"v_mfma_f32_16x16x32_bf16 a[132:135], v[4:7], v[244:247], a[132:135]\n"
"v_mfma_f32_16x16x32_bf16 a[64:67], v[160:163], v[248:251], a[64:67]\n"
"v_mfma_f32_16x16x32_bf16 a[68:71], v[164:167], v[248:251], a[68:71]\n"
"v_mfma_f32_16x16x32_bf16 a[72:75], v[168:171], v[248:251], a[72:75]\n"
"v_mfma_f32_16x16x32_bf16 a[76:79], v[172:175], v[248:251], a[76:79]\n"
"v_mfma_f32_16x16x32_bf16 a[80:83], v[176:179], v[248:251], a[80:83]\n"
"v_mfma_f32_16x16x32_bf16 a[84:87], v[180:183], v[248:251], a[84:87]\n"
"v_mfma_f32_16x16x32_bf16 a[88:91], v[184:187], v[248:251], a[88:91]\n"
"v_mfma_f32_16x16x32_bf16 a[92:95], v[188:191], v[248:251], a[92:95]\n"
"v_mfma_f32_16x16x32_bf16 a[136:139], v[4:7], v[248:251], a[136:139]\n"
"v_mfma_f32_16x16x32_bf16 a[96:99], v[160:163], v[252:255], a[96:99]\n"
"v_mfma_f32_16x16x32_bf16 a[100:103], v[164:167], v[252:255], a[100:103]\n"
"v_mfma_f32_16x16x32_bf16 a[104:107], v[168:171], v[252:255], a[104:107]\n"
"v_mfma_f32_16x16x32_bf16 a[108:111], v[172:175], v[252:255], a[108:111]\n"
"v_mfma_f32_16x16x32_bf16 a[112:115], v[176:179], v[252:255], a[112:115]\n"
"v_mfma_f32_16x16x32_bf16 a[116:119], v[180:183], v[252:255], a[116:119]\n"
"v_mfma_f32_16x16x32_bf16 a[120:123], v[184:187], v[252:255], a[120:123]\n"
"v_mfma_f32_16x16x32_bf16 a[124:127], v[188:191], v[252:255], a[124:127]\n"
"v_mfma_f32_16x16x32_bf16 a[140:143], v[4:7], v[252:255], a[140:143]\n"
".Ladaln_block_S_NEXT:\n"
"s_nop 7\n"
"s_add_u32 s40, s40, 1\n"
"s_lshl1_add_u32 s41, s26, 2\n"
"s_cmp_lt_u32 s40, s41\n"
"s_cbranch_scc1 .Ladaln_block_S_LOOP\n"
"s_cmp_lg_u32 s28, 0\n"
"s_cbranch_scc1 .Ladaln_block_S_DONE\n"
"s_mov_b32 s28, 1\n"
"v_mov_b32 v52, v124\n"
"v_mov_b32 v53, v125\n"
"v_mov_b32 v54, v126\n"
"v_mov_b32 v55, v127\n"
"v_permlane16_swap_b32 v124, v52\n"
"v_permlane16_swap_b32 v125, v53\n"
"v_permlane16_swap_b32 v126, v54\n"
"v_permlane16_swap_b32 v127, v55\n"
"v_max_f32 v124, v124, v52\n"
"v_max_f32 v125, v125, v53\n"
"v_max_f32 v126, v126, v54\n"
"v_max_f32 v127, v127, v55\n"
"v_mov_b32 v52, v124\n"
"v_mov_b32 v53, v125\n"
"v_mov_b32 v54, v126\n"
"v_mov_b32 v55, v127\n"
"v_permlane32_swap_b32 v124, v52\n"
"v_permlane32_swap_b32 v125, v53\n"
"v_permlane32_swap_b32 v126, v54\n"
"v_permlane32_swap_b32 v127, v55\n"
"v_max_f32 v124, v124, v52\n"
"v_max_f32 v125, v125, v53\n"
"v_max_f32 v126, v126, v54\n"
"v_max_f32 v127, v127, v55\n"
"v_xor_b32 v224, 0x80000000, v124\n"
"v_xor_b32 v225, 0x80000000, v124\n"
"v_xor_b32 v226, 0x80000000, v124\n"
"v_xor_b32 v227, 0x80000000, v124\n"
"v_xor_b32 v228, 0x80000000, v125\n"
"v_xor_b32 v229, 0x80000000, v125\n"
"v_xor_b32 v230, 0x80000000, v125\n"
"v_xor_b32 v231, 0x80000000, v125\n"
"v_xor_b32 v232, 0x80000000, v126\n"
"v_xor_b32 v233, 0x80000000, v126\n"
"v_xor_b32 v234, 0x80000000, v126\n"
"v_xor_b32 v235, 0x80000000, v126\n"
"v_xor_b32 v236, 0x80000000, v127\n"
"v_xor_b32 v237, 0x80000000, v127\n"
"v_xor_b32 v238, 0x80000000, v127\n"
"v_xor_b32 v239, 0x80000000, v127\n"
"v_accvgpr_write_b32 a0, 0\n"
"v_accvgpr_write_b32 a1, 0\n"
"v_accvgpr_write_b32 a2, 0\n"
"v_accvgpr_write_b32 a3, 0\n"
"v_accvgpr_write_b32 a4, 0\n"
"v_accvgpr_write_b32 a5, 0\n"
"v_accvgpr_write_b32 a6, 0\n"
"v_accvgpr_write_b32 a7, 0\n"
"v_accvgpr_write_b32 a8, 0\n"
"v_accvgpr_write_b32 a9, 0\n"
"v_accvgpr_write_b32 a10, 0\n"
"v_accvgpr_write_b32 a11, 0\n"
"v_accvgpr_write_b32 a12, 0\n"
"v_accvgpr_write_b32 a13, 0\n"
"v_accvgpr_write_b32 a14, 0\n"
"v_accvgpr_write_b32 a15, 0\n"
"v_accvgpr_write_b32 a16, 0\n"
"v_accvgpr_write_b32 a17, 0\n"
"v_accvgpr_write_b32 a18, 0\n"
"v_accvgpr_write_b32 a19, 0\n"
"v_accvgpr_write_b32 a20, 0\n"
"v_accvgpr_write_b32 a21, 0\n"
"v_accvgpr_write_b32 a22, 0\n"
"v_accvgpr_write_b32 a23, 0\n"
"v_accvgpr_write_b32 a24, 0\n"
"v_accvgpr_write_b32 a25, 0\n"
"v_accvgpr_write_b32 a26, 0\n"
"v_accvgpr_write_b32 a27, 0\n"
"v_accvgpr_write_b32 a28, 0\n"
"v_accvgpr_write_b32 a29, 0\n"
"v_accvgpr_write_b32 a30, 0\n"
"v_accvgpr_write_b32 a31, 0\n"
"v_accvgpr_write_b32 a32, 0\n"
"v_accvgpr_write_b32 a33, 0\n"
"v_accvgpr_write_b32 a34, 0\n"
"v_accvgpr_write_b32 a35, 0\n"
"v_accvgpr_write_b32 a36, 0\n"
"v_accvgpr_write_b32 a37, 0\n"
"v_accvgpr_write_b32 a38, 0\n"
"v_accvgpr_write_b32 a39, 0\n"
"v_accvgpr_write_b32 a40, 0\n"
"v_accvgpr_write_b32 a41, 0\n"
"v_accvgpr_write_b32 a42, 0\n"
"v_accvgpr_write_b32 a43, 0\n"
"v_accvgpr_write_b32 a44, 0\n"
"v_accvgpr_write_b32 a45, 0\n"
"v_accvgpr_write_b32 a46, 0\n"
"v_accvgpr_write_b32 a47, 0\n"
"v_accvgpr_write_b32 a48, 0\n"
"v_accvgpr_write_b32 a49, 0\n"
"v_accvgpr_write_b32 a50, 0\n"
"v_accvgpr_write_b32 a51, 0\n"
"v_accvgpr_write_b32 a52, 0\n"
"v_accvgpr_write_b32 a53, 0\n"
"v_accvgpr_write_b32 a54, 0\n"
"v_accvgpr_write_b32 a55, 0\n"
"v_accvgpr_write_b32 a56, 0\n"
"v_accvgpr_write_b32 a57, 0\n"
"v_accvgpr_write_b32 a58, 0\n"
"v_accvgpr_write_b32 a59, 0\n"
"v_accvgpr_write_b32 a60, 0\n"
"v_accvgpr_write_b32 a61, 0\n"
"v_accvgpr_write_b32 a62, 0\n"
"v_accvgpr_write_b32 a63, 0\n"
"v_accvgpr_write_b32 a64, 0\n"
"v_accvgpr_write_b32 a65, 0\n"
"v_accvgpr_write_b32 a66, 0\n"
"v_accvgpr_write_b32 a67, 0\n"
"v_accvgpr_write_b32 a68, 0\n"
"v_accvgpr_write_b32 a69, 0\n"
"v_accvgpr_write_b32 a70, 0\n"
"v_accvgpr_write_b32 a71, 0\n"
"v_accvgpr_write_b32 a72, 0\n"
"v_accvgpr_write_b32 a73, 0\n"
"v_accvgpr_write_b32 a74, 0\n"
"v_accvgpr_write_b32 a75, 0\n"
"v_accvgpr_write_b32 a76, 0\n"
"v_accvgpr_write_b32 a77, 0\n"
"v_accvgpr_write_b32 a78, 0\n"
"v_accvgpr_write_b32 a79, 0\n"
"v_accvgpr_write_b32 a80, 0\n"
"v_accvgpr_write_b32 a81, 0\n"
"v_accvgpr_write_b32 a82, 0\n"
"v_accvgpr_write_b32 a83, 0\n"
"v_accvgpr_write_b32 a84, 0\n"
"v_accvgpr_write_b32 a85, 0\n"
"v_accvgpr_write_b32 a86, 0\n"
"v_accvgpr_write_b32 a87, 0\n"
"v_accvgpr_write_b32 a88, 0\n"
"v_accvgpr_write_b32 a89, 0\n"
"v_accvgpr_write_b32 a90, 0\n"
"v_accvgpr_write_b32 a91, 0\n"
"v_accvgpr_write_b32 a92, 0\n"
"v_accvgpr_write_b32 a93, 0\n"
"v_accvgpr_write_b32 a94, 0\n"
"v_accvgpr_write_b32 a95, 0\n"
"v_accvgpr_write_b32 a96, 0\n"
"v_accvgpr_write_b32 a97, 0\n"
"v_accvgpr_write_b32 a98, 0\n"
"v_accvgpr_write_b32 a99, 0\n"
"v_accvgpr_write_b32 a100, 0\n"
"v_accvgpr_write_b32 a101, 0\n"
"v_accvgpr_write_b32 a102, 0\n"
"v_accvgpr_write_b32 a103, 0\n"
"v_accvgpr_write_b32 a104, 0\n"
"v_accvgpr_write_b32 a105, 0\n"
"v_accvgpr_write_b32 a106, 0\n"
"v_accvgpr_write_b32 a107, 0\n"
"v_accvgpr_write_b32 a108, 0\n"
"v_accvgpr_write_b32 a109, 0\n"
"v_accvgpr_write_b32 a110, 0\n"
"v_accvgpr_write_b32 a111, 0\n"
"v_accvgpr_write_b32 a112, 0\n"
"v_accvgpr_write_b32 a113, 0\n"
"v_accvgpr_write_b32 a114, 0\n"
"v_accvgpr_write_b32 a115, 0\n"
"v_accvgpr_write_b32 a116, 0\n"
"v_accvgpr_write_b32 a117, 0\n"
"v_accvgpr_write_b32 a118, 0\n"
"v_accvgpr_write_b32 a119, 0\n"
"v_accvgpr_write_b32 a120, 0\n"
"v_accvgpr_write_b32 a121, 0\n"
"v_accvgpr_write_b32 a122, 0\n"
"v_accvgpr_write_b32 a123, 0\n"
"v_accvgpr_write_b32 a124, 0\n"
"v_accvgpr_write_b32 a125, 0\n"
"v_accvgpr_write_b32 a126, 0\n"
"v_accvgpr_write_b32 a127, 0\n"
"v_accvgpr_write_b32 a128, 0\n"
"v_accvgpr_write_b32 a129, 0\n"
"v_accvgpr_write_b32 a130, 0\n"
"v_accvgpr_write_b32 a131, 0\n"
"v_accvgpr_write_b32 a132, 0\n"
"v_accvgpr_write_b32 a133, 0\n"
"v_accvgpr_write_b32 a134, 0\n"
"v_accvgpr_write_b32 a135, 0\n"
"v_accvgpr_write_b32 a136, 0\n"
"v_accvgpr_write_b32 a137, 0\n"
"v_accvgpr_write_b32 a138, 0\n"
"v_accvgpr_write_b32 a139, 0\n"
"v_accvgpr_write_b32 a140, 0\n"
"v_accvgpr_write_b32 a141, 0\n"
"v_accvgpr_write_b32 a142, 0\n"
"v_accvgpr_write_b32 a143, 0\n"
"s_branch .Ladaln_block_S_PASS\n"
".Ladaln_block_S_DONE:\n"
"s_setpc_b64 s[42:43]\n"
".Ladaln_block_P_st:\n"
"s_mov_b32 s83, 0\n"
".Ladaln_block_P_st_L:\n"
"s_sleep 1\n"
"buffer_load_dwordx4 v[148:151], v1, s[4:7], s67 offen sc1\n"
"buffer_load_dwordx4 v[152:155], v1, s[4:7], s67 offen offset:1024 sc1\n"
"buffer_load_dwordx4 v[156:159], v1, s[4:7], s67 offen offset:2048 sc1\n"
"buffer_load_dwordx4 v[160:163], v1, s[4:7], s67 offen offset:3072 sc1\n"
"s_waitcnt vmcnt(0)\n"
"v_xor_b32 v149, s82, v149\n"
"v_xor_b32 v151, s82, v151\n"
"v_xor_b32 v153, s82, v153\n"
"v_xor_b32 v155, s82, v155\n"
"v_xor_b32 v157, s82, v157\n"
"v_xor_b32 v159, s82, v159\n"
"v_xor_b32 v161, s82, v161\n"
"v_xor_b32 v163, s82, v163\n"
"v_or3_b32 v149, v149, v151, v153\n"
"v_or3_b32 v155, v155, v157, v159\n"
"v_or3_b32 v149, v149, v161, v163\n"
"v_or_b32 v149, v149, v155\n"
"v_cmp_ne_u32 vcc, 0, v149\n"
"s_nop 0\n"
"s_cbranch_vccz .Ladaln_block_P_st_R\n"
"s_add_u32 s83, s83, 1\n"
"s_cmp_lt_u32 s83, 0x10000\n"
"s_cbranch_scc1 .Ladaln_block_P_st_L\n"
"s_branch .Ladaln_block_P_st_R\n"
".Ladaln_block_P_f:\n"
"s_mov_b32 s83, 0\n"
".Ladaln_block_P_f_L:\n"
"s_sleep 1\n"
"buffer_load_dword v20, v18, s[4:7], s58 offen sc1\n"
"s_waitcnt vmcnt(0)\n"
"v_cmp_ne_u32 vcc, s82, v20\n"
"s_nop 0\n"
"s_cbranch_vccz .Ladaln_block_P_f_R\n"
"s_add_u32 s83, s83, 1\n"
"s_cmp_lt_u32 s83, 0x10000\n"
"s_cbranch_scc1 .Ladaln_block_P_f_L\n"
"s_branch .Ladaln_block_P_f_R\n"
".Ladaln_block_C_END:\n"
  :: "{s[0:1]}"(__builtin_amdgcn_kernarg_segment_ptr()), "{s2}"(blockIdx.x), "{v0}"(threadIdx.x), "{s3}"((unsigned)(size_t)lds) : CLOBBERS);
}
extern "C" void kernel_launch(void* const* d_in, const int* in_sizes, int n_in, void* d_out, int out_size, void* d_ws, size_t ws_size, hipStream_t stream) {
  adaln_block<<<256, 256, 0, stream>>>(d_in[0], d_in[1], d_in[2], d_in[3], d_in[4], d_in[5], d_in[7], d_in[8], d_in[9], d_in[10], d_out, d_ws);
}
```
